```python
import torch

def _softmax(x, axis=-1):
    return torch.softmax(x, dim=axis)

def _gelu(x):
    return torch.nn.functional.gelu(x, approximate='tanh')

def _silu(x):
    return torch.nn.functional.silu(x)

def _sigmoid(x):
    return torch.sigmoid(x)

def _layernorm(x, axis=-1, eps=1e-05):
    m = x.mean(dim=axis, keepdim=True)
    v = x.var(dim=axis, keepdim=True, unbiased=False)
    return (x - m) / torch.sqrt(v + eps)

def _scatter_add(tgt, idx, src, axis=0):
    return tgt.clone().index_add_(axis, idx, src)

def _segment_sum(src, seg, n):
    out = torch.zeros((n,) + src.shape[1:], dtype=src.dtype)
    return out.index_add_(0, seg, src)

def _pad(x, pw):
    flat = tuple((v for pair in reversed(pw) for v in pair))
    return torch.nn.functional.pad(x, flat)
kernel_name = 'interleave_layers__spectral_gfnet__memory_aug_recurrent_mem_tr__prompt'

def setup_inputs():

    _w = lambda a: (lambda t: t.to(torch.bfloat16) if t.is_floating_point() else t)(torch.as_tensor(a))

    def _A_setup():
        B, S, D = (1, 2048, 640)
        Df = D // 2 + 1
        return {'x': _w((lambda *s: torch.randn(*s, dtype=torch.bfloat16) * 0.05)(B, S, D)), 'w_r': _w((lambda *s: torch.randn(*s, dtype=torch.bfloat16) * 0.4)(S, Df) * 0.02), 'w_i': _w((lambda *s: torch.randn(*s, dtype=torch.bfloat16) * 0.4)(S, Df) * 0.02)}
    _A_in = _A_setup()

    def _B_setup():
        B, S, Dm = (1, 2048, 640)
        H, Dh = (32, 128)
        M, seg = (8, 32)
        seg = min(seg, S)
        S = S // seg * seg
        return {'x': _w((lambda *s: torch.randn(*s, dtype=torch.bfloat16) * 0.05)(B, S, Dm)), 'mem0': _w((lambda *s: torch.randn(*s, dtype=torch.bfloat16) * 0.2)(B, M, Dm)), 'Wq': _w((lambda *s: torch.randn(*s, dtype=torch.bfloat16) * 1.6)(Dm, H, Dh) * Dm ** (-0.5)), 'Wk': _w((lambda *s: torch.randn(*s, dtype=torch.bfloat16) * 1.6)(Dm, H, Dh) * Dm ** (-0.5)), 'Wv': _w((lambda *s: torch.randn(*s, dtype=torch.bfloat16) * 0.2)(Dm, H, Dh) * Dm ** (-0.5)), 'Wo': _w((lambda *s: torch.randn(*s, dtype=torch.bfloat16) * 0.2)(H, Dh, Dm) * (H * Dh) ** (-0.5)), 'seg_len': seg}
    _B_in = _B_setup()
    return {'A_x': _A_in['x'], 'A_w_r': _A_in['w_r'], 'A_w_i': _A_in['w_i'], 'B_x': _B_in['x'], 'B_mem0': _B_in['mem0'], 'B_Wq': _B_in['Wq'], 'B_Wk': _B_in['Wk'], 'B_Wv': _B_in['Wv'], 'B_Wo': _B_in['Wo'], 'B_seg_len': _B_in['seg_len']}

def reference(A_x, A_w_r, A_w_i, B_x, B_mem0, B_Wq, B_Wk, B_Wv, B_Wo, B_seg_len):

    def _A_ref(x, w_r, w_i):
        B, S, D = x.shape
        X = (lambda x, axes=(-2, -1): torch.fft.rfft2(x.float(), dim=axes))(x, axes=(-2, -1))
        W = w_r + 1j * w_i
        X = X * W
        y = (lambda x, s, axes=(-2, -1): torch.fft.irfft2(x, s=s, dim=axes).to(torch.bfloat16))(X, (S, D), axes=(-2, -1))
        return y

    def _B_ref(x, mem0, Wq, Wk, Wv, Wo, seg_len):
        B, S, Dm = x.shape
        H, Dh = (Wq.shape[1], Wq.shape[2])
        M = mem0.shape[1]
        scale = Dh ** (-0.5)
        n_seg = S // seg_len
        L = M + seg_len
        idx = torch.arange(L)
        mask = (idx[:, None] < M) | (idx[:, None] >= idx[None, :])
        mem = mem0
        outs = []
        for s in range(n_seg):
            xs = x[:, s * seg_len:(s + 1) * seg_len, :].clone()
            h = torch.concatenate([mem, xs], axis=1)
            q = torch.einsum('btd,dhe->bhte', h, Wq)
            k = torch.einsum('btd,dhe->bhte', h, Wk)
            v = torch.einsum('btd,dhe->bhte', h, Wv)
            scores = torch.einsum('bhqe,bhke->bhqk', q, k) * scale
            scores = torch.where(mask[None, None], scores, -1000000000.0)
            p = _softmax(scores, axis=-1)
            o = torch.einsum('bhqk,bhke->bhqe', p, v)
            o = torch.einsum('bhte,hed->btd', o, Wo)
            mem = o[:, :M, :].clone()
            outs.append(o[:, M:, :])
        return torch.concatenate(outs, axis=1)
    x = A_x
    x = x + _A_ref(_layernorm(x, axis=-1), A_w_r, A_w_i)
    x = x + _B_ref(_layernorm(x, axis=-1), B_mem0, B_Wq, B_Wk, B_Wv, B_Wo, B_seg_len)
    x = x + _A_ref(_layernorm(x, axis=-1), A_w_r, A_w_i)
    x = x + _B_ref(_layernorm(x, axis=-1), B_mem0, B_Wq, B_Wk, B_Wv, B_Wo, B_seg_len)
    return x
```

```cpp
#include <hip/hip_runtime.h>
#include <hip/hip_bf16.h>
#include <stdint.h>

typedef unsigned short u16;
typedef __attribute__((ext_vector_type(8))) __bf16 bf16x8;
typedef __attribute__((ext_vector_type(4))) short s16x4;
typedef __attribute__((ext_vector_type(4))) float f32x4;
typedef __attribute__((ext_vector_type(8))) float f32x8;
typedef __attribute__((ext_vector_type(16))) float f32x16;
typedef __attribute__((ext_vector_type(4))) unsigned u32x4;
typedef __attribute__((ext_vector_type(2))) unsigned u32x2;
typedef __attribute__((ext_vector_type(8))) unsigned u32x8;
typedef __attribute__((ext_vector_type(16))) __bf16 bf16x16;
typedef __attribute__((ext_vector_type(2))) unsigned short u16x2;
#define LAS __attribute__((address_space(3)))

constexpr int S_ = 2048, D_ = 640, DF = 321, H_ = 32, DH = 128, HD = 4096, NM = 8, SEG = 32, NSEG = 64;
constexpr int NQKV = 3 * HD;
constexpr int NOWN = 40;
constexpr int ZTR = NSEG;
constexpr size_t ZTSEG = (size_t)NOWN * H_ * 512;
constexpr unsigned PAIR = 16384, HALF = 8192;
constexpr unsigned W_MEM = 0, W_P = W_MEM + 24 * PAIR, W_T = W_P + 40 * PAIR, W_GO = W_T + 8 * PAIR, W_OUT = W_GO + PAIR, W_FZ = W_OUT + PAIR, W_FPT = W_FZ + 4096, W_FZ2 = W_FZ + 8192, W_FQ = W_FZ + 12288, W_H = W_FZ + PAIR, W_PL = W_H + PAIR, W_PROBE = W_PL + 6 * PAIR,
                   W_ZM = 2u * 1024 * 1024, W_END = W_ZM + 120 * PAIR;
static_assert(W_PROBE + 2 * PAIR <= 2u * 1024 * 1024 && W_PROBE % (2 * PAIR) == 0, "window");
constexpr int PTSEG = 32768 + 8192;

#define XA1 0
#define FAUX 0
#define WAUX 16
#define A2AUX 16
#define FPAD 0
constexpr int FS = 1024 + FPAD;
constexpr size_t al(size_t x) { return (x + 255) & ~(size_t)255; }
constexpr size_t OFF_CTL  = 0;
constexpr size_t OFF_WQKVT = 4096 + 4 * 1024 * 1024;
constexpr size_t OFF_WOT  = OFF_WQKVT + al((size_t)NQKV * D_ * 2);
constexpr size_t OFF_WFT  = OFF_WOT + al((size_t)D_ * HD * 2);
constexpr size_t OFF_TW640 = OFF_WFT + al((size_t)DF * S_ * 8);
constexpr size_t OFF_TW2048 = OFF_TW640 + al(640 * 8);
constexpr size_t OFF_F    = OFF_TW2048 + al(20 * 256 * 8);
constexpr size_t OFF_X1   = OFF_F + al((size_t)DF * FS * 16);
constexpr size_t OFF_X2   = OFF_X1 + al((size_t)S_ * D_ * 2);
constexpr size_t OFF_H    = OFF_X2 + al((size_t)S_ * D_ * 2);
constexpr size_t OFF_Z    = OFF_H + al((size_t)S_ * D_ * 2);
constexpr size_t OFF_Z2   = OFF_Z + al(ZTSEG * ZTR * 2);
constexpr size_t OFF_PT   = OFF_Z2 + al((size_t)NSEG * NOWN * 8192);
constexpr size_t OFF_QK   = OFF_PT + al((size_t)NSEG * PTSEG * 2);
constexpr size_t OFF_WIN  = OFF_QK + (size_t)NSEG * H_ * 16384;
constexpr size_t OFF_END  = OFF_WIN + (size_t)6 * 1024 * 1024;
static_assert(OFF_END <= (size_t)256 * 1024 * 1024, "workspace");

#define st128(v, r, vo, so, aux) do { const u32x4 d_ = (v); __builtin_amdgcn_raw_buffer_store_b128(d_, r, vo, so, aux); asm volatile("s_nop 1" :: "v"(d_)); } while (0)
__device__ __forceinline__ float bf2f(u16 v) { return __uint_as_float((unsigned)v << 16); }
__device__ __forceinline__ u16 f2bf(float f) { __bf16 b = (__bf16)f; return __builtin_bit_cast(u16, b); }
__device__ __forceinline__ float bfr(float f) { return (float)(__bf16)f; }
typedef __attribute__((ext_vector_type(2))) float f32x2;
typedef __attribute__((ext_vector_type(2))) __bf16 bf16x2;
__device__ __forceinline__ unsigned pack2(float a, float b) { return __builtin_bit_cast(unsigned, __builtin_convertvector((f32x2){a, b}, bf16x2)); }
__device__ __forceinline__ float lo2f(unsigned v) { return __uint_as_float(v << 16); }
__device__ __forceinline__ float hi2f(unsigned v) { return __uint_as_float(v & 0xFFFF0000u); }
__device__ __forceinline__ float fmx(float a, float b) { return __builtin_elementwise_maximum(a, b); }
__device__ __forceinline__ f32x4 mfma32(u32x4 a, u32x4 b, f32x4 c) {
    return __builtin_amdgcn_mfma_f32_16x16x32_bf16(__builtin_bit_cast(bf16x8, a), __builtin_bit_cast(bf16x8, b), c, 0, 0, 0);
}
__device__ __forceinline__ f32x4 mfma16(u32x2 a, u32x2 b, f32x4 c) {
    return __builtin_amdgcn_mfma_f32_16x16x16bf16_1k(__builtin_bit_cast(s16x4, a), __builtin_bit_cast(s16x4, b), c, 0, 0, 0);
}
__device__ __forceinline__ f32x4 smfmac(u32x4 a, u32x8 b, f32x4 c, int idx) {
    return __builtin_amdgcn_smfmac_f32_16x16x64_bf16(__builtin_bit_cast(bf16x8, a), __builtin_bit_cast(bf16x16, b), c, idx, 0, 0);
}
__device__ __forceinline__ int pi16(int k) { return ((k & 3) << 2) | ((k >> 1) & 2) | (k >> 3); }
__device__ __forceinline__ float addswap32(float a, float b) {
    auto r = __builtin_amdgcn_permlane32_swap(__float_as_uint(a), __float_as_uint(b), false, false);
    return __uint_as_float(r[0]) + __uint_as_float(r[1]);
}
__device__ __forceinline__ float xmax3216(float v) {
    auto r = __builtin_amdgcn_permlane32_swap(__float_as_uint(v), __float_as_uint(v), false, false);
    v = fmx(__uint_as_float(r[0]), __uint_as_float(r[1]));
    auto q = __builtin_amdgcn_permlane16_swap(__float_as_uint(v), __float_as_uint(v), false, false);
    return fmx(__uint_as_float(q[0]), __uint_as_float(q[1]));
}
__device__ __forceinline__ float xsum3216(float v) {
    auto r = __builtin_amdgcn_permlane32_swap(__float_as_uint(v), __float_as_uint(v), false, false);
    v = __uint_as_float(r[0]) + __uint_as_float(r[1]);
    auto q = __builtin_amdgcn_permlane16_swap(__float_as_uint(v), __float_as_uint(v), false, false);
    return __uint_as_float(q[0]) + __uint_as_float(q[1]);
}
template <int CTRL> __device__ __forceinline__ float dppf(float v) { return __uint_as_float((unsigned)__builtin_amdgcn_update_dpp(0, (int)__float_as_uint(v), CTRL, 0xF, 0xF, false)); }
__device__ __forceinline__ float wsum(float v) {
    v += dppf<0xB1>(v); v += dppf<0x4E>(v); v += dppf<0x141>(v); v += dppf<0x140>(v);
    return xsum3216(v);
}
__device__ __forceinline__ unsigned ror8(unsigned v) { return (unsigned)__builtin_amdgcn_update_dpp(0, (int)v, 0x128, 0xF, 0xF, false); }
__device__ __forceinline__ float ror8f(float v) { return __uint_as_float(ror8(__float_as_uint(v))); }
__device__ __forceinline__ unsigned nosent(unsigned v) {
    u16x2 lim = {0xFFFE, 0xFFFE};
    return __builtin_bit_cast(unsigned, __builtin_elementwise_min(__builtin_bit_cast(u16x2, v), lim));
}
__device__ __forceinline__ unsigned pkmax(unsigned a, unsigned b) {
    return __builtin_bit_cast(unsigned, __builtin_elementwise_max(__builtin_bit_cast(u16x2, a), __builtin_bit_cast(u16x2, b)));
}
__device__ __forceinline__ bool has_sent(u32x4 v) {
    unsigned m = pkmax(pkmax(v.x, v.y), pkmax(v.z, v.w));
    return (m & 0xFFFFu) == 0xFFFFu || (m >> 16) == 0xFFFFu;
}

__device__ __forceinline__ float2 cadd(float2 a, float2 b) { return make_float2(a.x + b.x, a.y + b.y); }
__device__ __forceinline__ float2 csub(float2 a, float2 b) { return make_float2(a.x - b.x, a.y - b.y); }
__device__ __forceinline__ float2 cmul(float2 a, float2 b) { return make_float2(a.x * b.x - a.y * b.y, a.x * b.y + a.y * b.x); }
template <int SG> __device__ __forceinline__ float2 muli(float2 a) { return SG < 0 ? make_float2(a.y, -a.x) : make_float2(-a.y, a.x); }
template <int SG> __device__ __forceinline__ float2 ctw(float2 a, float2 w) { if (SG > 0) w.y = -w.y; return cmul(a, w); }

template <int SG> __device__ __forceinline__ void dft4(float2& a, float2& b, float2& c, float2& d) {
    float2 t0 = cadd(a, c), t1 = csub(a, c), t2 = cadd(b, d), t3 = muli<SG>(csub(b, d));
    a = cadd(t0, t2); b = cadd(t1, t3); c = csub(t0, t2); d = csub(t1, t3);
}
template <int SG> __device__ __forceinline__ void dft8(float2 (&u)[8]) {
    float2 e0 = u[0], e1 = u[2], e2 = u[4], e3 = u[6], o0 = u[1], o1 = u[3], o2 = u[5], o3 = u[7];
    dft4<SG>(e0, e1, e2, e3); dft4<SG>(o0, o1, o2, o3);
    const float r = 0.70710678118654752440f;
    float2 t1 = muli<SG>(o1); o1 = make_float2((o1.x + t1.x) * r, (o1.y + t1.y) * r);
    o2 = muli<SG>(o2);
    float2 t3 = muli<SG>(o3); o3 = make_float2((t3.x - o3.x) * r, (t3.y - o3.y) * r);
    u[0] = cadd(e0, o0); u[4] = csub(e0, o0);
    u[1] = cadd(e1, o1); u[5] = csub(e1, o1);
    u[2] = cadd(e2, o2); u[6] = csub(e2, o2);
    u[3] = cadd(e3, o3); u[7] = csub(e3, o3);
}
template <int SG> __device__ __forceinline__ void dft5(float2& x0, float2& x1, float2& x2, float2& x3, float2& x4) {
    const float c1 = 0.30901699437494742410f, c2 = -0.80901699437494742410f, s1 = 0.95105651629515357212f, s2 = 0.58778525229247312917f;
    float2 t1 = cadd(x1, x4), t2 = cadd(x2, x3), t3 = csub(x1, x4), t4 = csub(x2, x3);
    float2 a1 = make_float2(x0.x + c1 * t1.x + c2 * t2.x, x0.y + c1 * t1.y + c2 * t2.y);
    float2 a2 = make_float2(x0.x + c2 * t1.x + c1 * t2.x, x0.y + c2 * t1.y + c1 * t2.y);
    float2 b1 = muli<SG>(make_float2(s1 * t3.x + s2 * t4.x, s1 * t3.y + s2 * t4.y));
    float2 b2 = muli<SG>(make_float2(s2 * t3.x - s1 * t4.x, s2 * t3.y - s1 * t4.y));
    x0 = make_float2(x0.x + t1.x + t2.x, x0.y + t1.y + t2.y);
    x1 = cadd(a1, b1); x4 = csub(a1, b1); x2 = cadd(a2, b2); x3 = csub(a2, b2);
}
template <int SG> __device__ __forceinline__ void dft10(float2 (&u)[10]) {
    float2 e[5] = {u[0], u[2], u[4], u[6], u[8]}, o[5] = {u[1], u[3], u[5], u[7], u[9]};
    dft5<SG>(e[0], e[1], e[2], e[3], e[4]); dft5<SG>(o[0], o[1], o[2], o[3], o[4]);
    const float wc[5] = {1.f, 0.80901699437494742410f, 0.30901699437494742410f, -0.30901699437494742410f, -0.80901699437494742410f};
    const float ws[5] = {0.f, 0.58778525229247312917f, 0.95105651629515357212f, 0.95105651629515357212f, 0.58778525229247312917f};
#pragma unroll
    for (int k = 0; k < 5; ++k) {
        float2 w = make_float2(wc[k], SG < 0 ? -ws[k] : ws[k]);
        float2 t = k == 0 ? o[0] : cmul(o[k], w);
        u[k] = cadd(e[k], t); u[k + 5] = csub(e[k], t);
    }
}
__device__ __forceinline__ void ln_row(float (&v)[10]) {
    float s = 0.f;
#pragma unroll
    for (int r = 0; r < 10; ++r) s += v[r];
    const float mean = wsum(s) * (1.f / 640.f);
    float q = 0.f;
#pragma unroll
    for (int r = 0; r < 10; ++r) { float d = v[r] - mean; q += d * d; }
    const float var = wsum(q) * (1.f / 640.f);
    const float mb = bfr(mean), rs = __builtin_amdgcn_rcpf(bfr(sqrtf(bfr(bfr(var) + 1e-5f))));
#pragma unroll
    for (int r = 0; r < 10; ++r) v[r] = bfr(bfr(v[r] - mb) * rs);
}

__device__ __forceinline__ __amdgpu_buffer_rsrc_t mkrsrc(const void* p, unsigned bytes) {
    return __builtin_amdgcn_make_buffer_rsrc((void*)p, 0, bytes, 0x00020000);
}
template <int NT> __device__ __forceinline__ void frag_tile(const u16* src, int C, int r0, int c0, u16* dst, unsigned gstride, bool perm, u16 (*t)[66]) {
    const int tid = threadIdx.x;
    if (tid < 256) {
        u32x4 v[2 * NT];
#pragma unroll
        for (int i = 0; i < 2 * NT; ++i) {
            const int id = tid + i * 256, r = id / (8 * NT), c8 = (id % (8 * NT)) * 8;
            v[i] = *(const u32x4*)(src + (size_t)(r0 + r) * C + c0 + c8);
        }
#pragma unroll
        for (int i = 0; i < 2 * NT; ++i) {
            const int id = tid + i * 256, r = id / (8 * NT), c8 = (id % (8 * NT)) * 8;
#pragma unroll
            for (int j = 0; j < 4; ++j) { t[c8 + 2 * j][r] = (u16)(v[i][j] & 0xFFFF); t[c8 + 2 * j + 1][r] = (u16)(v[i][j] >> 16); }
        }
    }
    __syncthreads();
    if (tid < 256) {
        const __amdgpu_buffer_rsrc_t rd = mkrsrc(dst, 0x7FFFFFFF);
#pragma unroll
        for (int i = 0; i < 2 * NT; ++i) {
            const int id = tid + i * 256, g = id >> 7, u = (id >> 6) & 1, fq = (id >> 4) & 3, fr = id & 15;
            const int c = 16 * g + (perm ? pi16(fr) : fr), r8 = 32 * u + 8 * fq;
            u32x4 v;
#pragma unroll
            for (int j = 0; j < 4; ++j) v[j] = *(const unsigned*)&t[c][r8 + 2 * j];
            st128(v, rd, (g * gstride + (id & 127) * 8) * 2u, 0, WAUX);
        }
    }
}
template <int NT> __device__ __forceinline__ void qkv_tile(int m, int b, const u16* W, char* ws, u16 (*t)[66]) {
    const int kb = b / (64 / NT), cb = (b % (64 / NT)) * NT;
    frag_tile<NT>(W, HD, kb * 64, cb * 64, (u16*)(ws + OFF_WQKVT) + ((size_t)(m * 256 + cb * 4) * 10 + kb) * 1024, 10240, false, t);
}
template <int NT> __device__ __forceinline__ void wo_tile(int b, const u16* W, char* ws, u16 (*t)[66]) {
    const int eb = b / (10 / NT), cb = (b % (10 / NT)) * NT;
    frag_tile<NT>(W, D_, eb * 64, cb * 64, (u16*)(ws + OFF_WOT) + ((size_t)((eb >> 1) * 40 + cb * 4) * 2 + (eb & 1)) * 1024, 2048, true, t);
}

struct RowLds { float2 bA[4][644]; float2 bB[4][644]; float2 tw[640]; };
__device__ __forceinline__ float2 tw640_at(int i) {
    float sn, cs; sincospif((float)(i > 320 ? 640 - i : -i) * (1.f / 320.f), &sn, &cs);
    return make_float2(cs, sn);
}
__device__ __forceinline__ void a1_unit(__amdgpu_buffer_rsrc_t rx, int row0, float4* F4, RowLds& L, int tid, int xa = 0) {
    const int wave = tid >> 6, lane = tid & 63;
    if (wave < 4) {
        char* st = (char*)L.bB + wave * 2560;
        const unsigned base = (unsigned)(row0 + 2 * wave) * (D_ * 2);
        const u32x4 c0 = __builtin_amdgcn_raw_buffer_load_b128(rx, lane * 16u, base, 16), c1 = __builtin_amdgcn_raw_buffer_load_b128(rx, lane * 16u + 1024u, base, 16);
        if (lane < 32) *(u32x4*)(st + 2048 + lane * 16) = __builtin_amdgcn_raw_buffer_load_b128(rx, lane * 16u + 2048u, base, 16);
        *(u32x4*)(st + lane * 16) = c0; *(u32x4*)(st + 1024 + lane * 16) = c1;
        float a[10], b[10];
#pragma unroll
        for (int r = 0; r < 10; ++r) { a[r] = bf2f(*(const u16*)(st + (lane + 64 * r) * 2)); b[r] = bf2f(*(const u16*)(st + 1280 + (lane + 64 * r) * 2)); }
        ln_row(a); ln_row(b);
        float2 u[10];
#pragma unroll
        for (int r = 0; r < 10; ++r) u[r] = make_float2(a[r], b[r]);
        dft10<-1>(u);
#pragma unroll
        for (int r = 0; r < 10; ++r) L.bA[wave][10 * lane + r] = u[r];
    }
    __syncthreads();
    const int f = tid / 80, i = tid % 80;
    if (tid < 320) {
        const int k = i % 10, j = (i / 10) * 80 + k;
        float2 u[8];
#pragma unroll
        for (int r = 0; r < 8; ++r) { u[r] = L.bA[f][i + 80 * r]; if (r) u[r] = ctw<-1>(u[r], L.tw[8 * k * r]); }
        dft8<-1>(u);
#pragma unroll
        for (int r = 0; r < 8; ++r) L.bB[f][j + 10 * r] = u[r];
    }
    __syncthreads();
    if (tid < 320) {
        float2 u[8];
#pragma unroll
        for (int r = 0; r < 8; ++r) { u[r] = L.bB[f][i + 80 * r]; if (r) u[r] = ctw<-1>(u[r], L.tw[i * r]); }
        dft8<-1>(u);
#pragma unroll
        for (int r = 0; r < 8; ++r) L.bA[f][i + 80 * r] = u[r];
    }
    __syncthreads();
    for (int it = tid; it < (xa == 2 ? 4 : DF * 4); it += blockDim.x) {
        const int k = it >> 2, ff = it & 3;
        if (xa == 3) __builtin_amdgcn_s_sleep(16);
        const float2 zk = L.bA[ff][k], zn = L.bA[ff][k == 0 ? 0 : 640 - k];
        const f32x4 o = {0.5f * (zk.x + zn.x), 0.5f * (zk.y - zn.y), 0.5f * (zk.y + zn.y), 0.5f * (zn.x - zk.x)};
        st128(__builtin_bit_cast(u32x4, o), mkrsrc(F4, DF * FS * 16), (unsigned)(k * FS + (row0 >> 1) + ff) * 16u, 0, FAUX);
    }
}
#define A3S(k) do {} while (0)
__device__ __forceinline__ void a3_unit(const float4* F4, const u16* x, int row0, __amdgpu_buffer_rsrc_t rx1, __amdgpu_buffer_rsrc_t rh, RowLds& L, const float4* tw, unsigned long long* dbg = nullptr) {
    const int tid = threadIdx.x, wave = tid >> 6, lane = tid & 63;
    float4 fv[3];
#pragma unroll
    for (int c = 0; c < 3; ++c) {
        const int it = min(tid + 512 * c, DF * 4 - 1);
        fv[c] = F4[(size_t)(it >> 2) * FS + (row0 >> 1) + (it & 3)];
    }
    const float4 twv = tw[min(tid, 319)];
#pragma unroll
    for (int c = 0; c < 3; ++c) {
        const int it = tid + 512 * c, k = it >> 2, ff = it & 3;
        float4 v = fv[c];
        if (k == 0 || k == 320) { v.y = 0.f; v.w = 0.f; }
        if (it < DF * 4) L.bA[ff][k] = make_float2(v.x - v.w, v.y + v.z);
        if (k > 0 && k < 320) L.bA[ff][640 - k] = make_float2(v.x + v.w, v.z - v.y);
    }
    if (tid < 320) ((float4*)L.tw)[tid] = twv;
    unsigned xa[10], xb[10];
    {
        const u16* xp = x + (size_t)(row0 + 2 * (wave & 3)) * D_ + lane;
#pragma unroll
        for (int r = 0; r < 10; ++r) { xa[r] = xp[64 * r]; xb[r] = xp[D_ + 64 * r]; }
    }
    __syncthreads();
    A3S(0);
    const int f = tid / 80, i = tid % 80;
    if (tid < 320) {
        float2 u[8];
#pragma unroll
        for (int r = 0; r < 8; ++r) u[r] = L.bA[f][i + 80 * r];
        dft8<1>(u);
#pragma unroll
        for (int r = 0; r < 8; ++r) L.bB[f][8 * i + r] = u[r];
    }
    __syncthreads();
    A3S(1);
    if (tid < 320) {
        const int k = i & 7, j = (i >> 3) * 64 + k;
        float2 u[8];
#pragma unroll
        for (int r = 0; r < 8; ++r) { u[r] = L.bB[f][i + 80 * r]; if (r) u[r] = ctw<1>(u[r], L.tw[10 * k * r]); }
        dft8<1>(u);
#pragma unroll
        for (int r = 0; r < 8; ++r) L.bA[f][j + 8 * r] = u[r];
    }
    __syncthreads();
    A3S(2);
    if (wave < 4) {
        float2 u[10];
#pragma unroll
        for (int r = 0; r < 10; ++r) { u[r] = L.bA[wave][lane + 64 * r]; if (r) u[r] = ctw<1>(u[r], L.tw[lane * r]); }
        dft10<1>(u);
        char* st = (char*)L.bB + wave * 5120;
        float a[10], b[10];
#pragma unroll
        for (int r = 0; r < 10; ++r) {
            a[r] = bfr(lo2f(xa[r]) + bfr(u[r].x));
            b[r] = bfr(lo2f(xb[r]) + bfr(u[r].y));
            *(u16*)(st + (lane + 64 * r) * 2) = f2bf(a[r]); *(u16*)(st + 1280 + (lane + 64 * r) * 2) = f2bf(b[r]);
        }
        A3S(3);
        ln_row(a); ln_row(b);
        A3S(4);
#pragma unroll
        for (int r = 0; r < 10; ++r) { *(u16*)(st + 2560 + (lane + 64 * r) * 2) = f2bf(a[r]); *(u16*)(st + 3840 + (lane + 64 * r) * 2) = f2bf(b[r]); }
        const unsigned base = (unsigned)(row0 + 2 * wave) * (D_ * 2);
#pragma unroll
        for (int c = 0; c < 3; ++c) {
            if (c < 2 || lane < 32) {
                const int id = c * 64 + lane, rr = id >= 80, ch = id - 80 * rr, row = (row0 + 2 * wave + rr) & 31;
                st128(*(const u32x4*)(st + 2560 + id * 16), rh, (unsigned)((ch >> 3) * 4096 + row * 128 + (((ch & 7) ^ (row & 7)) << 4)), (unsigned)(row0 >> 5) * 40960u, 16);
                st128(*(const u32x4*)(st + id * 16), rx1, id * 16u, base, 16);
            }
        }
    }
}

constexpr int FL_NEAR = 16;
constexpr int FL_ROT = 40;
__device__ __forceinline__ unsigned xcc_id() { unsigned x; asm volatile("s_getreg_b32 %0, hwreg(HW_REG_XCC_ID)" : "=s"(x)); return x & 15u; }
__global__ __launch_bounds__(320) void k1_kernel(const u16* x, const u16* Wq, const u16* Wk, const u16* wr, const u16* wi, char* ws, char* win) {
    __shared__ __attribute__((aligned(16))) RowLds L;
    const int b = (int)blockIdx.x - 16, tid = threadIdx.x;
    if (b < 0) {
        unsigned* best = (unsigned*)&L;
        const __amdgpu_buffer_rsrc_t rw = mkrsrc(win + (b < -8 ? W_PROBE : W_END), 2 * PAIR);
        const unsigned base = (blockIdx.x & 7u) * 128u + (tid >> 6) * 1664u;
        if (tid < 4) best[tid] = 0u;
        __syncthreads();
        unsigned mn[4] = {~0u, ~0u, ~0u, ~0u};
        for (int r = 0; r < 4; ++r) {
#pragma unroll
            for (int i = 0; i < 4; ++i) __builtin_amdgcn_raw_buffer_store_b32(0u, rw, base + i * HALF, 0, 16);
            asm volatile("s_waitcnt vmcnt(0)" ::: "memory");
#pragma unroll
            for (int i = 0; i < 4; ++i) {
                const unsigned long long t1 = __builtin_amdgcn_s_memtime();
                asm volatile("s_waitcnt lgkmcnt(0)" ::: "memory");
                unsigned v = __builtin_amdgcn_raw_buffer_load_b32(rw, base + i * HALF, 0, 16);
                asm volatile("s_waitcnt vmcnt(0)" : "+v"(v) :: "memory");
                const unsigned long long t2 = __builtin_amdgcn_s_memtime();
                mn[i] = min(mn[i], (unsigned)(t2 - t1) + v);
            }
        }
        if ((tid & 63) == 0) {
#pragma unroll
            for (int i = 0; i < 4; ++i) atomicAdd(&best[i], min(mn[i], 1000u));
        }
        __syncthreads();
        if (tid == 0) ((unsigned*)(ws + OFF_CTL))[FL_NEAR + (xcc_id() & 7u) + (blockIdx.x & 8u)] = min(best[1], best[3]) < min(best[0], best[2]) ? (best[3] < best[1] ? 3u : 1u) : (best[2] < best[0] ? 2u : 0u);
    } else if (b < 256) {
        L.tw[tid] = tw640_at(tid); L.tw[tid + 320] = tw640_at(tid + 320);
        a1_unit(mkrsrc(x, S_ * D_ * 2), b * 8, (float4*)(ws + OFF_F), L, tid);
    } else if (b < 256 + 128) {
        u16* tf = (u16*)&L;
        const int s0 = (b - 256) * 16;
        u32x4 v[5];
#pragma unroll
        for (int i = 0; i < 5; ++i) {
            const int id = min(tid + 320 * i, 1283);
            v[i] = ((const u32x4*)((id < 642 ? wr : wi) + (size_t)s0 * DF))[id < 642 ? id : id - 642];
        }
#pragma unroll
        for (int i = 0; i < 5; ++i) ((u32x4*)tf)[min(tid + 320 * i, 1283)] = v[i];
        __syncthreads();
        const __amdgpu_buffer_rsrc_t rd = mkrsrc(ws + OFF_WFT, DF * S_ * 4);
        for (int i = tid; i < DF * 4; i += 320) {
            const int k = i >> 2, sq = (i & 3) * 4;
            u32x4 o;
#pragma unroll
            for (int j = 0; j < 4; ++j) o[j] = (unsigned)tf[(sq + j) * DF + k] | ((unsigned)tf[16 * DF + (sq + j) * DF + k] << 16);
            st128(o, rd, (unsigned)(k * S_ + s0 + sq) * 4u, 0, WAUX);
        }
    } else if (b < 256 + 128 + 20) {
        const int j = b - 384, i = j * 256 + tid;
        if (tid < 256) {
            const int m = j < 7 ? 32 * (tid & 7) * (j + 1) : j < 14 ? 4 * (tid & 63) * (j - 6) : j < 17 ? tid * (j - 13) : (tid + 256) * (j - 16);
            double sn, cs; sincospi(-2.0 * (double)m / 2048.0, &sn, &cs);
            ((float2*)(ws + OFF_TW2048))[((j >> 1) * 256 + tid) * 2 + (j & 1)] = make_float2((float)cs, (float)sn);
            if (i < 640) ((float2*)(ws + OFF_TW640))[i] = tw640_at(i);
        }
        if (i == 0) *(unsigned*)(ws + OFF_CTL) = 0u;
    } else {
        const int t = b - 404;
        qkv_tile<4>(t / 160, t % 160, t < 160 ? Wq : Wk, ws, (u16 (*)[66])&L);
    }
}

#define PD(i) ((i) + ((i) >> 4))
template <int SG> __device__ __forceinline__ void fft2048(float2 (&u)[8], float2* b0, float2* b1, const float2 (&tw)[20], int tid) {
    dft8<SG>(u);
#pragma unroll
    for (int r = 0; r < 8; ++r) b0[PD(8 * tid + r)] = u[r];
    __syncthreads();
    {
        const int k = tid & 7, j = (tid >> 3) * 64 + k;
#pragma unroll
        for (int r = 0; r < 8; ++r) { u[r] = b0[PD(tid + 256 * r)]; if (r) u[r] = ctw<SG>(u[r], tw[r - 1]); }
        dft8<SG>(u);
#pragma unroll
        for (int r = 0; r < 8; ++r) b1[PD(j + 8 * r)] = u[r];
    }
    __syncthreads();
    {
        const int k = tid & 63, j = (tid >> 6) * 512 + k;
#pragma unroll
        for (int r = 0; r < 8; ++r) { u[r] = b1[PD(tid + 256 * r)]; if (r) u[r] = ctw<SG>(u[r], tw[6 + r]); }
        dft8<SG>(u);
#pragma unroll
        for (int r = 0; r < 8; ++r) b0[PD(j + 64 * r)] = u[r];
    }
    __syncthreads();
#pragma unroll
    for (int hh = 0; hh < 2; ++hh) {
        const int i = tid + 256 * hh;
        float2 v0 = b0[PD(i)], v1 = ctw<SG>(b0[PD(i + 512)], tw[14 + 3 * hh]), v2 = ctw<SG>(b0[PD(i + 1024)], tw[15 + 3 * hh]), v3 = ctw<SG>(b0[PD(i + 1536)], tw[16 + 3 * hh]);
        dft4<SG>(v0, v1, v2, v3);
        u[hh] = v0; u[hh + 2] = v1; u[hh + 4] = v2; u[hh + 6] = v3;
    }
}
#define A2S(k) do {} while (0)
__global__ __launch_bounds__(256) void a2_kernel(const u16* Wv, const u16* Wo, char* ws, char* win) {
    __shared__ float2 bb[2][2048 + 128];
    float2* b0 = bb[0]; float2* b1 = bb[1];
    const int tid = threadIdx.x;
    if (blockIdx.x >= DF) {
        const int t = blockIdx.x - DF;
        A2S(0);
        if (t < 160) qkv_tile<4>(2, t, Wv, ws, (u16 (*)[66])b0);
        else wo_tile<2>(t - 160, Wo, ws, (u16 (*)[66])b0);
        A2S(4);
        return;
    }
    A2S(0);
    const float4* twg = (const float4*)(ws + OFF_TW2048);
    float2* f = (float2*)(ws + OFF_F) + (size_t)blockIdx.x * (2 * FS);
    const unsigned* w = (const unsigned*)(ws + OFF_WFT) + (size_t)blockIdx.x * S_;
    float2 u[8], tw[20];
    unsigned wv[8];
#pragma unroll
    for (int r = 0; r < 8; ++r) u[r] = f[tid + 256 * r];
#pragma unroll
    for (int r = 0; r < 10; ++r) { const float4 t = twg[r * 256 + tid]; tw[2 * r] = make_float2(t.x, t.y); tw[2 * r + 1] = make_float2(t.z, t.w); }
#pragma unroll
    for (int r = 0; r < 8; ++r) wv[r] = w[tid + 256 * r];
    {
        const u32x4 sv = {0xFFFFFFFFu, 0xFFFFFFFFu, 0xFFFFFFFFu, 0xFFFFFFFFu};
        const __amdgpu_buffer_rsrc_t rw = mkrsrc(win, W_END);
        for (int g = blockIdx.x * 256 + tid; g < (int)((W_PROBE + W_END - W_ZM) / 16); g += DF * 256)
            st128(sv, rw, g * 16u + (g < (int)(W_PROBE / 16) ? 0u : W_ZM - W_PROBE), 0, A2AUX);
        if (blockIdx.x == 0 && tid >= 32 && tid < 192) ((u32x4*)(ws + OFF_CTL))[tid] = (u32x4){0u, 0u, 0u, 0u};
        if (blockIdx.x == 0 && tid == 0) ((unsigned*)(ws + OFF_CTL))[FL_ROT] = xcc_id();
    }
    A2S(1);
    fft2048<-1>(u, b0, b1, tw, tid);
    A2S(2);
    const float sc = 1.f / ((float)S_ * (float)D_);
#pragma unroll
    for (int r = 0; r < 8; ++r) u[r] = cmul(u[r], make_float2(lo2f(wv[r]) * sc, hi2f(wv[r]) * sc));
    fft2048<1>(u, b1, b0, tw, tid);
    A2S(3);
    {
        const __amdgpu_buffer_rsrc_t rf = mkrsrc(f, 2048 * 8);
#pragma unroll
        for (int r = 0; r < 8; ++r) __builtin_amdgcn_raw_buffer_store_b64((u32x2){__float_as_uint(u[r].x), __float_as_uint(u[r].y)}, rf, tid * 8u, r * 2048u, A2AUX);
    }
    A2S(4);
}

struct RecArgs {
    unsigned* ctl; char* win; const u16* mem0; const u16* xin; u16* out;
    float4* Fnext;
};
constexpr unsigned O_H = (unsigned)OFF_H, O_X1 = (unsigned)OFF_X1, O_Z = (unsigned)OFF_Z, O_Z2 = (unsigned)OFF_Z2, O_PT = (unsigned)OFF_PT, O_QK = (unsigned)OFF_QK;
#define MXV 0x00808080808080AAull
#define MXQ 0x0008080808080000ull
#define NTAIL 5
static_assert(NTAIL >= 3, "the owners release segments up to NSEG - 4");
constexpr int TAILS = NSEG - NTAIL;
static_assert(!((MXV | MXQ) >> TAILS) && !(MXV & 1) && !(MXQ & 3), "segment sets");
constexpr int NREC = 96 + NOWN, NQT = NREC + NOWN, NXT = NQT + 32, NWRK = NXT + 32;
constexpr unsigned SPIN_CAP = 400000u;
__device__ __forceinline__ bool spin_fail(unsigned& spins, unsigned* tmo) {
    if (++spins > SPIN_CAP) { __hip_atomic_store(tmo, 1u, __ATOMIC_RELAXED, __HIP_MEMORY_SCOPE_AGENT); return true; }
    return (spins & 1023u) == 0u && __builtin_amdgcn_readfirstlane(__hip_atomic_load(tmo, __ATOMIC_RELAXED, __HIP_MEMORY_SCOPE_AGENT));
}
#define SST(k) do {} while (0)
#define LSEL 1
#define RT(k) do {} while (0)
#define RTS() do {} while (0)
#define B3ATTR
#define MTA(k)
#define MTRT() do {} while (0)
#define MT(k) do {} while (0)
#define MTS() do {} while (0)
#define FC(x) do {} while (0)
#define DQC 1200
#define DVC 1200
#define M1 44
#define MZ 40
#define MZP 20
#define M2 234
#define LTP (LT0 + 15)
#define LTZ 340
#define PEN 280
#define KSL 1
#define SVO 64
#define DZP 55
#define LT0 342
#define SAD 1
#define PEN2 80
#define PEN1 50
#define PEN0 150
#define DDM2 0
#define DRA 700
#define TSLP 1
#define PFS 0
#define PFOFF 1
#define OPR 2
#define BSPARSE 0
#define ZLAG 4
#define TKP 0
#define DPS 12
#define RZ 63
#define RQ 63
#define R2 63
#define DXT 3000
#define SSW 99
#define LTM 12
#define XS0 10
#define XSU 8
#define XSM 32
#define LTU 4
#define LTD 1
#define QUIET 0
#define QUAD 1
#define LTS 3
#define XSS 3
#define TKS 3
#define LT1 450
#define FDL 2
#define ZLA 3
#define DPT2 3
#define NORND 0
#define LTX 24
#define TKE 200
#define DVO 6000
#define ZPS 4
#define HLA 8
#define PSLP 16
#define PFD 4
#define NPF 3
#define KSL2 1
#define KBQ 4
#define VB1 6
#define VB2 5
#define KR1 3
#define QB2 2
#define NB2 6
#define DZR 24
#define ML 150
#define MN 12
#define CF 190
#define DB 1500
#define DP 200
#define ZSL 1
#define NB1 3
#define DPT 3
#define DDMA 8
#define DZ2 800
#define QEARLY 2
#define OSLP 32
#define OSLP2 1
#define WSLP 64
#define DO0 12000
#define DW0 1200
#define DQT 9000
#define DQ4 700
#define ZEARLY 3
#define HSLP 4
#define RMAP 0
#define DIE 0
#define VMWAIT() do { asm volatile("" ::: "memory"); __builtin_amdgcn_s_waitcnt(0x0F70); asm volatile("" ::: "memory"); } while (0)
#define SB() __builtin_amdgcn_sched_barrier(0)
#define BAR() asm volatile("s_waitcnt lgkmcnt(0)\n\ts_barrier" ::: "memory")
#define PIN1(a) asm volatile("" : "+v"(a) :: "memory")
#define PIN2(a, b) asm volatile("" : "+v"(a), "+v"(b) :: "memory")
#define BAR0() asm volatile("s_barrier" ::: "memory")
__device__ __forceinline__ unsigned fresh_lane() { unsigned l; asm volatile("v_mbcnt_lo_u32_b32 %0, -1, 0\n\tv_mbcnt_hi_u32_b32 %0, -1, %0" : "=v"(l)); return l; }
__device__ __forceinline__ void wait_until(unsigned long long t) { while ((long long)(__builtin_amdgcn_s_memtime() - t) < 0) __builtin_amdgcn_s_sleep(1); }
struct Clk { unsigned ta, ma, cpq, qpc; };
__device__ __forceinline__ int sx24(unsigned v) { return (int)(v << 8) >> 8; }
#define WAIT_FINE \
        "s_memrealtime vcc\n\t" \
        "s_memtime s[100:101]\n\t" \
        "s_brev_b32 %[fi], -2\n\t" \
        "s_waitcnt lgkmcnt(0)\n\t" \
        "s_mov_b32 %[rp], vcc_lo\n\t" \
        "s_lshl_b32 vcc_hi, vcc_lo, 2\n\t" \
        "s_sub_u32 %[x], %[t], vcc_hi\n\t" \
        "s_bfe_i32 %[x], %[x], 0x180000\n\t" \
        "s_min_i32 %[x], %[x], 4000\n\t" \
        "s_max_i32 %[x], %[x], -4000\n\t" \
        "s_add_u32 %[t], vcc_hi, %[x]\n\t" \
        "s_mul_i32 %[x], %[x], %[cq]\n\t" \
        "s_ashr_i32 %[x], %[x], 8\n\t" \
        "s_sub_u32 %[tm], %[t], %[ta]\n\t" \
        "s_bfe_i32 %[tm], %[tm], 0x180000\n\t" \
        "s_min_i32 %[tm], %[tm], 1000000\n\t" \
        "s_max_i32 %[tm], %[tm], -1000000\n\t" \
        "s_mul_i32 %[tm], %[tm], %[cq]\n\t" \
        "s_ashr_i32 %[tm], %[tm], 8\n\t" \
        "s_add_u32 %[tm], %[tm], %[ma]\n\t" \
        "s_sub_u32 %[tm], %[tm], s100\n\t" \
        "s_lshr_b32 vcc_hi, %[cq], 4\n\t" \
        "s_sub_u32 vcc_hi, %[x], vcc_hi\n\t" \
        "s_max_i32 %[tm], %[tm], vcc_hi\n\t" \
        "s_min_i32 %[tm], %[tm], %[x]\n\t" \
        "s_add_u32 %[tm], %[tm], s100\n" \
        "1:\n\t" \
        "s_memrealtime vcc\n\t" \
        "s_memtime s[100:101]\n\t" \
        "s_waitcnt lgkmcnt(0)\n\t" \
        "s_cmp_eq_u32 vcc_lo, %[rp]\n\t" \
        "s_cbranch_scc1 2f\n\t" \
        "s_mov_b32 %[rp], vcc_lo\n\t" \
        "s_lshl_b32 %[x], vcc_lo, 2\n\t" \
        "s_sub_u32 %[x], %[t], %[x]\n\t" \
        "s_bfe_i32 %[x], %[x], 0x180000\n\t" \
        "s_mul_i32 %[x], %[x], %[cq]\n\t" \
        "s_ashr_i32 %[x], %[x], 8\n\t" \
        "s_add_u32 %[x], %[x], s100\n\t" \
        "s_sub_u32 s101, %[x], %[tm]\n\t" \
        "s_cmp_lt_i32 s101, %[fi]\n\t" \
        "s_cselect_b32 %[tm], %[x], %[tm]\n\t" \
        "s_mov_b32 %[fi], 0\n" \
        "2:\n\t" \
        "s_sub_u32 %[x], %[tm], s100\n\t" \
        "s_cmp_gt_i32 %[x], 260\n\t" \
        "s_cbranch_scc1 1b\n\t" \
        "s_cmp_eq_u32 %[fi], 0\n\t" \
        "s_cselect_b32 %[ta], %[t], %[ta]\n\t" \
        "s_cselect_b32 %[ma], %[tm], %[ma]\n" \
        "3:\n\t" \
        "s_memtime s[100:101]\n\t" \
        "s_waitcnt lgkmcnt(0)\n\t" \
        "s_sub_u32 %[x], %[tm], s100\n\t" \
        "s_cmp_gt_i32 %[x], 92\n\t" \
        "s_cbranch_scc1 3b\n\t" \
        "s_max_i32 %[x], %[x], 0\n\t" \
        "s_andn2_b32 %[x], %[x], 3\n\t" \
        "s_sub_u32 %[x], 104, %[x]\n\t" \
        "s_getpc_b64 vcc\n\t" \
        "s_add_u32 vcc_lo, vcc_lo, %[x]\n\t" \
        "s_addc_u32 vcc_hi, vcc_hi, 0\n\t" \
        "s_setpc_b64 vcc\n\t" \
        ".rept 23\n\t" \
        "s_nop 0\n\t" \
        ".endr\n\t"
#define WAIT_FINE_OUT [tm] "=&s"(tm), [rp] "=&s"(rp), [x] "=&s"(x), [fi] "=&s"(fi), [t] "+s"(t), [ta] "+s"(c.ta), [ma] "+s"(c.ma)
__device__ __forceinline__ void wait_fine(unsigned t, Clk& c) {
    unsigned tm, rp, x, fi;
    asm volatile(WAIT_FINE : WAIT_FINE_OUT : [cq] "s"(c.cpq) : "vcc", "scc", "s100", "s101", "memory");
}
__device__ __forceinline__ void wait_fine_bar(unsigned t, Clk& c) {
    unsigned tm, rp, x, fi;
    asm volatile(WAIT_FINE "s_barrier" : WAIT_FINE_OUT : [cq] "s"(c.cpq) : "vcc", "scc", "s100", "s101", "memory");
}
__device__ __forceinline__ void wait_cyc_bar(unsigned tm) {
    unsigned x;
    asm volatile(
        "3:\n\t"
        "s_memtime s[100:101]\n\t"
        "s_waitcnt lgkmcnt(0)\n\t"
        "s_sub_u32 %0, %1, s100\n\t"
        "s_cmp_gt_i32 %0, 92\n\t"
        "s_cbranch_scc1 3b\n\t"
        "s_max_i32 %0, %0, 0\n\t"
        "s_andn2_b32 %0, %0, 3\n\t"
        "s_sub_u32 %0, 104, %0\n\t"
        "s_getpc_b64 vcc\n\t"
        "s_add_u32 vcc_lo, vcc_lo, %0\n\t"
        "s_addc_u32 vcc_hi, vcc_hi, 0\n\t"
        "s_setpc_b64 vcc\n\t"
        ".rept 23\n\t"
        "s_nop 0\n\t"
        ".endr\n\t"
        "s_barrier"
        : "=&s"(x) : "s"(tm) : "vcc", "scc", "s100", "s101", "memory");
}
__device__ __forceinline__ unsigned cyc_at(const Clk& c, int d) {
    d = min(d, 20000);
    asm volatile("" : "+s"(d));
    return c.ma + (unsigned)((max(d, -4000) * (int)c.cpq) >> 8);
}
__device__ __forceinline__ void clk_rate(Clk& c, unsigned ta0, unsigned ma0) {
    const int dq = sx24(c.ta - ta0);
    if (dq > 200) {
        const float r = fminf(fmaxf((float)(c.ma - ma0) * __builtin_amdgcn_rcpf((float)dq), 1.f), 8.f);
        asm volatile("s_nop 1\n\tv_readfirstlane_b32 %0, %2\n\tv_readfirstlane_b32 %1, %3\n\ts_nop 3" : "=s"(c.cpq), "=s"(c.qpc)
                     : "v"((unsigned)(256.f * r + 0.5f)), "v"((unsigned)(65536.f * __builtin_amdgcn_rcpf(r))));
    }
}
__device__ __forceinline__ void clk_init(Clk& c) {
    c.ta = 4u * (unsigned)__builtin_amdgcn_s_memrealtime(); c.ma = (unsigned)__builtin_amdgcn_s_memtime();
    for (int i = 0; i < 2; ++i) {
        const unsigned ta0 = c.ta, ma0 = c.ma;
        wait_fine(c.ta + (i ? 360u : 60u), c);
        clk_rate(c, ta0, ma0);
    }
}
__device__ __forceinline__ unsigned sched_lane(int s) { const unsigned l = fresh_lane(); return (l & 31u) * 128u + (l >> 5) * 4u; }
__device__ __forceinline__ int read_sched(const RecArgs& a, __amdgpu_buffer_rsrc_t rwin, int s, unsigned vo, unsigned to, unsigned f, unsigned g0, int& lt, int& xs, unsigned& fc) {
    int d = (int)((f - g0) & 0xFFFFFFu);
    if (__builtin_expect(__any((f >> 24) != (unsigned)s) || s == 0, 0)) {
        for (unsigned spins = 0; __any((f >> 24) != (unsigned)s);) {
            FC(fc);
            if (spin_fail(spins, a.ctl)) break;
            __builtin_amdgcn_s_sleep(TSLP);
            asm volatile("" ::: "memory");
            if ((f >> 24) != (unsigned)s) f = __builtin_amdgcn_raw_buffer_load_b32(rwin, vo, to, 16);
        }
        const unsigned f0 = __builtin_amdgcn_readfirstlane(f) - 0x100000u;
        d = (int)((f - f0) & 0xFFFFFFu) + (int)((f0 - g0) & 0xFFFFFFu);
    }
    d = max(d, __builtin_amdgcn_update_dpp(0, d, 0xB1, 0xF, 0xF, true));
    d = max(d, __builtin_amdgcn_update_dpp(0, d, 0x4E, 0xF, 0xF, true));
    d = max(d, __builtin_amdgcn_update_dpp(0, d, 0x141, 0xF, 0xF, true));
    d = max(d, __builtin_amdgcn_update_dpp(0, d, 0x140, 0xF, 0xF, true));
    d = max(d, __builtin_amdgcn_update_dpp(0, d, 0x142, 0xA, 0xF, true));
    d = max(__builtin_amdgcn_readlane(d, 16), __builtin_amdgcn_readlane(d, 48));
    const int l0 = lt, x0 = xs;
    d -= 0x400000;
    const bool miss = d > l0 + min(x0 + LTM, LTX);
    if (s >= SAD) { lt = miss ? l0 : min(max(d, l0 - 1 - ((l0 - d) >> LTS)), l0 + LTU); xs = miss ? min(x0 + XSU, XSM) : max(x0 - 1 - (x0 >> XSS), 0); }
    if (s == 1) lt = LT0;
    return 0x400000 + (s < SAD ? d + PEN0 : miss ? d + PEN : l0 + x0) + M1 + M2 + x0;
}
__device__ __forceinline__ unsigned nosent1(unsigned v) { return min(v, 0xFFFFFFFEu); }
__device__ __forceinline__ unsigned smax(u32x4 v) { return max(v[1], v[3]); }
constexpr int QS = 272;
constexpr int TQS = 1152;
constexpr int L_Q = 10240, L_K = L_Q + 2 * TQS, L_S = L_K + 2 * TQS, L_TOK = L_S + 256, L_H = L_TOK + 49152, L_END = L_H + 2 * 40960;
typedef __attribute__((ext_vector_type(4))) short v4i16_t;
__device__ __forceinline__ u32x2 ldtr(const char* p) { return __builtin_bit_cast(u32x2, __builtin_amdgcn_ds_read_tr16_b64_v4i16((LAS v4i16_t*)p)); }
__device__ __forceinline__ u32x2 ldtr1(const LAS char* p) { return __builtin_bit_cast(u32x2, __builtin_amdgcn_ds_read_tr16_b64_v4i16((LAS v4i16_t*)p)); }
__device__ __forceinline__ u32x4 ldtr2(const LAS char* p0, const LAS char* p1) {
    const u32x2 x = __builtin_bit_cast(u32x2, __builtin_amdgcn_ds_read_tr16_b64_v4i16((LAS v4i16_t*)p0)), y = __builtin_bit_cast(u32x2, __builtin_amdgcn_ds_read_tr16_b64_v4i16((LAS v4i16_t*)p1));
    return (u32x4){x[0], x[1], y[0], y[1]};
}
__device__ __forceinline__ u32x4 ldtr2(const char* p0, const char* p1) { const u32x2 x = ldtr(p0), y = ldtr(p1); return (u32x4){x[0], x[1], y[0], y[1]}; }

__device__ __forceinline__ unsigned poll_lim(int& budget) {
    --budget;
    unsigned lim = budget > 0 ? 0xFFFFFFFEu : 0xFFFFFFFFu;
    asm volatile("" : "+s"(lim));
    return lim;
}
#define GATHER \
        "buffer_load_dwordx4 %[vo], %[rw], %[s0] offen sc1 lds\n\t" \
        "buffer_load_dwordx4 %[vo], %[rw], %[s0] offen offset:1024 sc1 lds\n\t" \
        "buffer_load_dwordx4 %[vo], %[rw], %[s0] offen offset:2048 sc1 lds\n\t" \
        "buffer_load_dwordx4 %[vo], %[rw], %[s0] offen offset:3072 sc1 lds\n\t" \
        "s_movk_i32 m0, 0x1000\n\t" \
        "s_nop 0\n\t" \
        "buffer_load_dwordx4 %[vo], %[rw], %[s4] offen sc1 lds\n\t" \
        "buffer_load_dwordx4 %[vo], %[rw], %[s4] offen offset:1024 sc1 lds\n\t" \
        "buffer_load_dwordx4 %[vo], %[rw], %[s4] offen offset:2048 sc1 lds\n\t" \
        "buffer_load_dwordx4 %[vo], %[rw], %[s4] offen offset:3072 sc1 lds\n\t" \
        "s_movk_i32 m0, 0x2000\n\t" \
        "s_nop 0\n\t" \
        "buffer_load_dwordx4 %[vo], %[rw], %[s1] offen sc1 lds\n\t" \
        "buffer_load_dwordx4 %[vo], %[rw], %[s1] offen offset:1024 sc1 lds"
#define GATHER_IN [vo] "v"(vo), [rw] "s"(rwin), [s0] "s"(so0), [s4] "s"(so0 + 4096u), [s1] "s"(so1)
__device__ __forceinline__ void gather_dma(__amdgpu_buffer_rsrc_t rwin, unsigned vo, unsigned so0, unsigned so1) {
    asm volatile("s_mov_b32 m0, 0\n\ts_nop 0\n\t" GATHER :: GATHER_IN : "m0", "memory");
}
__device__ __forceinline__ void gather_at(unsigned t, Clk& c, __amdgpu_buffer_rsrc_t rwin, unsigned vo, unsigned so0, unsigned so1) {
    unsigned tm, rp, x, fi;
    asm volatile("s_mov_b32 m0, 0\n\t" WAIT_FINE GATHER : WAIT_FINE_OUT : [cq] "s"(c.cpq), GATHER_IN : "vcc", "scc", "s100", "s101", "m0", "memory");
}
__device__ __forceinline__ void dma4k(__amdgpu_buffer_rsrc_t r, unsigned vo, unsigned so, unsigned lds) {
    asm volatile(
        "s_mov_b32 m0, %3\n\t"
        "s_nop 0\n\t"
        "buffer_load_dwordx4 %0, %1, %2 offen sc1 lds\n\t"
        "buffer_load_dwordx4 %0, %1, %2 offen offset:1024 sc1 lds\n\t"
        "buffer_load_dwordx4 %0, %1, %2 offen offset:2048 sc1 lds\n\t"
        "buffer_load_dwordx4 %0, %1, %2 offen offset:3072 sc1 lds"
        :: "v"(vo), "s"(r), "s"(so), "s"(lds) : "m0", "memory");
}
__device__ __forceinline__ int bs2(int c) { return (c & ~3) | ((c & 1) << 1) | ((c >> 1) & 1); }
__device__ __forceinline__ void gather_mem0(const RecArgs& a, char* memL, int t0, int nt) {
    for (int c = t0; c < 640; c += nt) {
        const unsigned* src = (const unsigned*)(a.mem0 + ((c & 6) + ((c >> 3) & 1)) * D_ + (c >> 4) * 16 + (c & 1) * 2);
        *(u32x4*)(memL + c * 16) = (u32x4){src[0], src[2], src[4], src[6]};
    }
}
__device__ __forceinline__ f32x2 hadd(f32x4 c) { return (f32x2){c[0], c[1]} + (f32x2){c[2], c[3]}; }
__device__ __forceinline__ unsigned hpack(f32x4 c) { const f32x2 y = hadd(c); return pack2(y[0], y[1]); }
__device__ __forceinline__ u32x8 cat8(u32x4 lo, u32x4 hi) { return (u32x8){lo[0], lo[1], lo[2], lo[3], hi[0], hi[1], hi[2], hi[3]}; }
__device__ __forceinline__ u32x8 ldw(const u16* p) { return cat8(*(const u32x4*)p, *(const u32x4*)(p + 512)); }
__device__ __forceinline__ u32x4 half8(const u32x8& v, int u) { return u ? (u32x4){v[4], v[5], v[6], v[7]} : (u32x4){v[0], v[1], v[2], v[3]}; }
__device__ __forceinline__ void dma1k(__amdgpu_buffer_rsrc_t r, unsigned vo, unsigned so, unsigned lds) {
    asm volatile("s_mov_b32 m0, %3\n\ts_nop 0\n\tbuffer_load_dwordx4 %0, %1, %2 offen sc1 lds" :: "v"(vo), "s"(r), "s"(so), "s"(lds) : "m0", "memory");
}
__device__ __forceinline__ void stage_h(__amdgpu_buffer_rsrc_t rws, int seg, unsigned lds, int t) {
    const unsigned wo = __builtin_amdgcn_readfirstlane(t >> 6) * 1024;
#pragma unroll
    for (int i = 0; i < 5; ++i) dma1k(rws, (t & 63) * 16u, O_H + (unsigned)seg * 40960u + wo + i * 8192u, lds + wo + i * 8192u);
}
__device__ __forceinline__ void wait_h(const RecArgs& a, __amdgpu_buffer_rsrc_t rfl, int seg, int& hrdy) {
    unsigned lim = 63u;
    if (__builtin_expect(hrdy <= seg, 0)) for (unsigned spins = 0;;) {
        asm volatile("" ::: "memory");
        const u32x4 f = __builtin_amdgcn_raw_buffer_load_b128(rfl, min(fresh_lane(), lim) * 128u, W_H, 16);
        const unsigned long long miss = __ballot((f[0] | f[1] | f[2] | f[3]) != 0u);
        hrdy = miss ? __builtin_ctzll(miss) : (int)lim + 1;
        if (hrdy > seg) break;
        if (spin_fail(spins, a.ctl)) break;
        lim = (unsigned)min(seg + HLA, 63);
        __builtin_amdgcn_s_sleep(HSLP);
    }
}
template <int CTRL> __device__ __forceinline__ unsigned dppu(unsigned v) { return (unsigned)__builtin_amdgcn_mov_dpp((int)v, CTRL, 0xF, 0xF, true); }
template <int CTRL> __device__ __forceinline__ u32x4 ror4(u32x4 v) { return (u32x4){dppu<CTRL>(v[0]), dppu<CTRL>(v[1]), dppu<CTRL>(v[2]), dppu<CTRL>(v[3])}; }
template <int N> struct IC { static constexpr int v = N; };
__device__ __forceinline__ float xmax16(float v) {
    auto q = __builtin_amdgcn_permlane16_swap(__float_as_uint(v), __float_as_uint(v), false, false);
    return fmx(__uint_as_float(q[0]), __uint_as_float(q[1]));
}
__device__ __forceinline__ float xsum16(float v) {
    auto q = __builtin_amdgcn_permlane16_swap(__float_as_uint(v), __float_as_uint(v), false, false);
    return __uint_as_float(q[0]) + __uint_as_float(q[1]);
}
__device__ __forceinline__ float addswap16(float a, float b) {
    auto r = __builtin_amdgcn_permlane16_swap(__float_as_uint(a), __float_as_uint(b), false, false);
    return __uint_as_float(r[0]) + __uint_as_float(r[1]);
}
#define LANES() \
    const int lane = (int)fresh_lane(), tid = w * 64 + lane, fr = lane & 15, fq = lane >> 4; \
    const int sidx = (lane & 2) ? 0xEEEE : 0x4444; \
    const int ma0 = fq * 256 + (fr & 1) * 128 + (fr >> 1) * 16; \
    const int prow = 16 * ((fr >> 2) & 1) + 4 * (fr >> 3) + (fr & 3), hoff = fr * 128; \
    (void)tid; (void)sidx; (void)ma0; (void)prow; (void)hoff

__device__ __forceinline__ int seg_after(unsigned long long m, int j) { m = j >= 63 ? 0ull : m >> (j + 1); return m ? j + 1 + __builtin_ctzll(m) : NSEG; }
__device__ __forceinline__ int seg_done(unsigned long long m, int f0, int f1) { return min(seg_after(~m, f0), seg_after(m, f1)) - 1; }
template <int OFF> __device__ __forceinline__ void ldfrag(u32x4& d, const LAS char* p) { asm volatile("ds_read_b128 %0, %1 offset:%2" : "=v"(d) : "v"(p), "n"(OFF) : "memory"); }
template <int U, class FM, class FV> __device__ __forceinline__ void project(const u32x8 (&wt)[2][10], f32x4 (&tc)[2][2], u32x4 (&bb)[8], const LAS char* c0, const LAS char* c1, const LAS char* n0, const LAS char* n1, FM mid, FV vm) {
    constexpr int t = U >> 2, k = U & 3, v = (U + 8) % 40;
    if (U == 32) mid();
    vm(IC<U>());
    const LAS char* p = U < 32 ? (k >> 1 ? c1 : c0) : (k >> 1 ? n1 : n0);
    if (U < 2) asm volatile("s_nop 0\n\ts_waitcnt lgkmcnt(7)\n\tv_mfma_f32_16x16x32_bf16 %0, %3, %2, 0\n\tv_mfma_f32_16x16x32_bf16 %1, %4, %2, 0\n\tds_read_b128 %2, %5 offset:%6"
                            : "=&v"(tc[0][k & 1]), "=&v"(tc[1][k & 1]), "+v"(bb[U & 7]) : "v"(half8(wt[0][t], k >> 1)), "v"(half8(wt[1][t], k >> 1)), "v"(p), "n"((v >> 2) * 4096 + (k & 1) * 2048) : "memory");
    else asm volatile(".rept %7\n\ts_nop 0\n\t.endr\n\ts_waitcnt lgkmcnt(7)\n\tv_mfma_f32_16x16x32_bf16 %0, %3, %2, %0\n\tv_mfma_f32_16x16x32_bf16 %1, %4, %2, %1\n\tds_read_b128 %2, %5 offset:%6"
                      : "+v"(tc[0][k & 1]), "+v"(tc[1][k & 1]), "+v"(bb[U & 7]) : "v"(half8(wt[0][t], k >> 1)), "v"(half8(wt[1][t], k >> 1)), "v"(p), "n"((v >> 2) * 4096 + (k & 1) * 2048), "n"(U < 8 ? 1 : 0) : "memory");
    if constexpr (U < 39) project<U + 1>(wt, tc, bb, c0, c1, n0, n1, mid, vm);
    else asm volatile("s_nop 7" : "+v"(tc[0][0]), "+v"(tc[0][1]), "+v"(tc[1][0]), "+v"(tc[1][1]));
}
__device__ __forceinline__ void project_end(u32x4 (&bb)[8]) {
    asm volatile("s_waitcnt lgkmcnt(0)" : "+v"(bb[0]), "+v"(bb[1]), "+v"(bb[2]), "+v"(bb[3]), "+v"(bb[4]), "+v"(bb[5]), "+v"(bb[6]), "+v"(bb[7]) :: "memory");
}
__device__ __forceinline__ void project0(u32x4 (&bb)[8], const LAS char* c0, const LAS char* c1) {
    ldfrag<0>(bb[0], c0); ldfrag<2048>(bb[1], c0); ldfrag<0>(bb[2], c1); ldfrag<2048>(bb[3], c1);
    ldfrag<4096>(bb[4], c0); ldfrag<6144>(bb[5], c0); ldfrag<4096>(bb[6], c1); ldfrag<6144>(bb[7], c1);
    project_end(bb);
}

__global__ __launch_bounds__(512, 2) B3ATTR void b3_kernel(RecArgs a) {
    extern __shared__ __attribute__((aligned(16))) char smem[];
    char* memL = smem;
    char* tokL = smem + L_TOK;
    char* hL = smem + L_H;
    const int w = __builtin_amdgcn_readfirstlane(threadIdx.x >> 6);
    const unsigned vx = blockIdx.x + __builtin_amdgcn_readfirstlane(a.ctl[FL_ROT]) + 4u * DIE;
    int bid;
    {
        const int j = blockIdx.x >> 3, x = vx & 3;
        if (!(vx & 4)) bid = j < 8 ? 8 * x + j : j < 16 ? 24 + 8 * x + j : j < 26 ? 80 + 10 * x + j : 255 - 4 * (j - 26) - x;
        else bid = j < 8 ? 64 + 8 * x + j : j < 16 ? NQT - 8 + 8 * x + j : j < 26 ? NREC - 16 + 10 * x + j : NXT - 26 + 6 * x + j;
    }
    const __amdgpu_buffer_rsrc_t rwin = mkrsrc(a.win, W_END);
    char* const ws = (char*)a.ctl;
    const __amdgpu_buffer_rsrc_t rws = mkrsrc(ws, (unsigned)OFF_WIN);
    unsigned long long tref = 0;
    const unsigned long long tstart = __builtin_amdgcn_s_memtime();
    const unsigned nh = __builtin_amdgcn_readfirstlane(a.ctl[FL_NEAR + 4 * DIE] & 1u) * HALF;
    const unsigned nz = __builtin_amdgcn_readfirstlane(a.ctl[FL_NEAR + 8 + 4 * DIE] & 1u) * HALF;
    auto quad = [&](int p) {
        const unsigned* f = a.ctl + FL_NEAR + p + 4 * DIE;
        return (unsigned)__builtin_amdgcn_readfirstlane((3u * (f[0] & 2u) + 2u * ((~f[1] & 2u) + (f[2] & 2u) + (~f[3] & 2u))) > 9u);
    };
    const unsigned mc = (vx & 3u) ^ quad(0);
    const unsigned qz = quad(8);
    auto zpair = [&](unsigned x, unsigned k) { return (((x >> 1) * 10u + k) * 2u + ((x & 1u) ^ qz)) * PAIR + nz; };
    const unsigned mo = W_MEM + mc * PAIR + nh, mo1 = W_MEM + (4u + (mc & 1u)) * PAIR + nh + (mc >> 1) * 2048u;
    const unsigned tro = W_T + (mc & 1u) * PAIR + nh + (mc >> 1) * 4096u;
    auto two = [&]() { const unsigned l = fresh_lane(); return l < 4 ? (l >> 1) * PAIR + (l & 1) * 4096u + nh : 0x40000000u; };
    const u32x4 SENT = {0xFFFFFFFFu, 0xFFFFFFFFu, 0xFFFFFFFFu, 0xFFFFFFFFu};
    const f32x4 z4 = {0.f, 0.f, 0.f, 0.f};
    int hrdy = 0, budget = (int)SPIN_CAP;
    unsigned fc0 = 0, fc1 = 0, fc2 = 0;
    Clk ck = {0u, 0u, 1600u, 10486u};
    SST(0);
    const bool xt = bid >= NXT && bid < NWRK;
    RowLds& L = *(RowLds*)smem;
    unsigned tpp = 0;
    int lt = LT1, xs = XS0;
    struct Tk { unsigned vo, tw, sl; const LAS char* sp; };
    auto tk_init = [&]() {
        const unsigned l = fresh_lane();
        Tk k = {l * 16u, two(), (l & 31u) * 128u + (l >> 5) * 4u, (const LAS char*)(memL + l * 64 + 4 + ((l >> 1) & 7) * 8)};
        PIN2(k.vo, k.tw); PIN2(k.sl, k.sp);
        return k;
    };
    auto sched2 = [&](Tk& k, int s, unsigned f) {
        const unsigned g0 = tpp - 0x400000u;
        tpp = g0 + (unsigned)read_sched(a, rwin, s, k.sl, tro + (unsigned)(s & 3) * 2u * PAIR, f, g0, lt, xs, fc1);
        k.sl &= ~4u;
        RT(6);
    };
    auto sched = [&](Tk& k, int s) { sched2(k, s, __builtin_amdgcn_raw_buffer_load_b32(rwin, k.sl, tro + (unsigned)(s & 3) * 2u * PAIR, 16)); };
    unsigned it = 0;
    auto fetch = [&](const Tk& k, int s, bool first, unsigned lim, unsigned dc, unsigned fo, bool tell) {
        const unsigned sb = (unsigned)(s & 3) * 6u * PAIR, ta0 = ck.ta, mm0 = ck.ma;
        if (__builtin_expect(s > 0, 1)) {
            if (__builtin_expect(first, 1)) { const unsigned te = cyc_at(ck, sx24(tpp - TKE - ck.ta)); while ((int)((unsigned)__builtin_amdgcn_s_memtime() - te) < 0) __builtin_amdgcn_s_sleep(1); }
            if (__builtin_expect(first, 1)) gather_at(tpp, ck, rwin, k.vo, mo + sb, mo1 + sb); else { const unsigned c = (mc + 1u + (it & 1u)) & 3u; gather_dma(rwin, k.vo, W_MEM + c * PAIR + nh + sb, W_MEM + (4u + (c & 1u)) * PAIR + nh + (c >> 1) * 2048u + sb); }
        }
        __builtin_amdgcn_s_setprio(0);
        RT(1);
        clk_rate(ck, ta0, mm0);
        unsigned tb = ck.ma - dc, tg = (unsigned)s << 24, so = W_T + (unsigned)(s & 3) * 2u * PAIR + fo;
        asm volatile("" : "+s"(tb), "+s"(tg), "+s"(so));
        asm volatile("s_waitcnt vmcnt(%1) lgkmcnt(0)\n\ts_barrier\n\ts_waitcnt vmcnt(%2)\n\ts_barrier\n\ts_waitcnt vmcnt(0)\n\ts_memtime %0\n\ts_barrier"
                     : "=s"(tref) : "n"(10 - NB1), "n"(10 - NB2) : "memory");
        MT(1);
        if (!tell) { RT(3); return; }
        unsigned x0 = *(const LAS unsigned*)k.sp, x1 = *(const LAS unsigned*)(k.sp + 4096), x2 = *(const LAS unsigned*)(k.sp + 6144);
        __builtin_amdgcn_s_waitcnt(0xC07F);
        asm volatile("" : "+s"(tref) :: "memory");
        const unsigned fw = ((ck.ta + ((((unsigned)tref - tb) * ck.qpc) >> 16)) & 0xFFFFFFu) | tg;
        __builtin_amdgcn_raw_buffer_store_b32(fw, rwin, k.tw, __any(max(max(x0, x1), x2) > lim) ? 0x40000000u : so, 16);
        RT(3);
    };
    if (bid >= 96 && (bid < NQT || bid >= NWRK)) {
        const int fi = bid >= NWRK ? 255 - bid : NQT + 15 - bid;
        for (int u = fi; u < 256; u += 56) {
            a3_unit((const float4*)(ws + OFF_F), a.xin, u * 8, mkrsrc(ws + OFF_X1, S_ * D_ * 2), mkrsrc(ws + OFF_H, S_ * D_ * 2), L, (const float4*)(ws + OFF_TW640));
            VMWAIT();
            __syncthreads();
            if (threadIdx.x == 0) __builtin_amdgcn_raw_buffer_store_b32(0u, rwin, 0u, W_H + (unsigned)((u >> 2) * 128 + (u & 3) * 4), 16);
            if (u < 96) SST(1);
            if (fi >= 56) break;
            if (u < 96) u += 40;
        }
    }
    if ((bid >= 32 && bid < 96) || xt) {
        const int hh = (xt ? bid - NXT : bid) & 31;
        u32x8 wt[2][10];
        const bool vt = bid >= 64;
        const unsigned long long mv = xt ? MXV : ~MXV;
        auto nextv = [&](int j) { return min(seg_after(mv, j), NSEG - 1); };
        const int jv1 = nextv(-1), jv2 = nextv(jv1);
        {
            LANES();
            if (!vt) gather_mem0(a, memL, tid, 512);
            if (xt || !vt) wait_until(tstart + (xt ? DXT : DVO));
            const u16* pv = w < 4 ? (const u16*)(ws + OFF_WQKVT) + (size_t)2 * HD * D_ + (size_t)(hh * 8 + w) * 10240 + lane * 8 : (const u16*)(ws + OFF_WOT) + (size_t)(hh * 40 + 10 * (w - 4)) * 2048 + lane * 8;
            const int sg = w < 4 ? 40960 : 1024, st = w < 4 ? 1024 : 2048;
#pragma unroll
            for (int t = 0; t < 10; ++t) { wt[0][t] = ldw(pv + t * st); wt[1][t] = ldw(pv + sg + t * st); }
            __builtin_amdgcn_s_waitcnt(0x0F70);
            if (tid < 8) *(unsigned*)(smem + L_S + tid * 4) = 0u;
            if (!vt && w == 7) clk_init(ck);
            SST(1);
            if (vt) {
                if (w == 0) { wait_h(a, rwin, jv2, hrdy); *(volatile LAS int*)(smem + L_S + 32) = hrdy; }
                BAR();
                hrdy = __builtin_amdgcn_readfirstlane(*(volatile LAS int*)(smem + L_S + 32));
                SST(2);
                stage_h(rws, jv1, L_H, tid);
                stage_h(rws, jv2, L_H + 40960, tid);
            }
        }
        unsigned zb = W_ZM;
        auto steps = [&](auto cc) {
            constexpr int C = decltype(cc)::v & 1;
            constexpr bool TK = decltype(cc)::v == 3;
            LANES();
            Tk tk = {0u, 0u, 0x40000000u, nullptr};
            if (TK) { tk = tk_init(); __builtin_amdgcn_s_setprio(TKP); }
            const unsigned zi = (unsigned)(hh * 128 + fr * 8) * 2u;
            unsigned zo0 = zpair(w & 3, fq) + zi, zo1 = zo0 + 8 * PAIR, zo2 = zpair(w & 3, 8 + (fq >> 1)) + zi + (fq & 1) * 8;
            const LAS char* va = (const LAS char*)(smem + L_Q + (fr & 1) * 544 + ((fr >> 1) & 1) * 1152 + (16 * fq + 2 * (fr >> 2)) * 4);
            LAS char* vw = (LAS char*)(smem + L_Q + (fq & 1) * 544 + (fq >> 1) * 1152 + (16 * w + bs2(fr)) * 4);
            const LAS char* mp0 = (const LAS char*)(memL + ma0);
            PIN2(zo0, zo1); PIN1(zo2);
            if (C == 0) { PIN2(mp0, vw); } else PIN1(va);
            u32x4 za = SENT, zc = SENT;
            u32x2 ze = {0u, 0u};
            auto put_z2 = [&](int s) {
                const unsigned z2b = s < NSEG ? O_Z2 + (unsigned)s * (NOWN * 8192u) + (unsigned)(10 * (w & 3) * 8192 + hh * 256) : 0x40000000u, l2 = fresh_lane();
                st128(za, rws, (l2 >> 4) * 8192u + (l2 & 15u) * 16u, z2b, 16);
                st128(zc, rws, (l2 >> 4) * 8192u + (l2 & 15u) * 16u, z2b + 4u * 8192u, 16);
                __builtin_amdgcn_raw_buffer_store_b64(ze, rws, (l2 >> 5) * 8192u + (l2 & 15u) * 16u + ((l2 >> 4) & 1u) * 8u, z2b + 8u * 8192u, 16);
            };
            for (int s = 0; s < NSEG; ++s) {
                RT(0);
                bool redo = false;
                unsigned sf = 0;
                do {
                    ++it;
                    if (C == 0) {
                        const unsigned lim = s > 0 ? poll_lim(budget) : ~0u;
                        asm volatile(
                            "v_mov_b64 v[200:201], 0\n\t"
                            "v_mov_b64 v[202:203], 0\n\t"
                            "v_mov_b64 v[204:205], 0\n\t"
                            "v_mov_b64 v[206:207], 0\n\t"
                            "v_mov_b32 v208, 0\n\t"
                            "s_setprio 2\n\t"
                            "s_waitcnt lgkmcnt(0)\n\t"
                            "s_barrier\n\t"
                            MTA(0)
                            "ds_read_b128 v[212:215], %[mp] offset:0\n\t"
                            "ds_read_b128 v[216:219], %[mp] offset:1024\n\t"
                            "ds_read_b128 v[220:223], %[mp] offset:2048\n\t"
                            "s_barrier\n\t"
                            "ds_read_b128 v[224:227], %[mp] offset:3072\n\t"
                            "ds_read_b128 v[228:231], %[mp] offset:4096\n\t"
                            "ds_read_b128 v[232:235], %[mp] offset:5120\n\t"
                            "s_waitcnt lgkmcnt(5)\n\t"
                            "v_smfmac_f32_16x16x64_bf16 v[200:203], v[212:215], %[w00], %[ix]\n\t"
                            "s_waitcnt lgkmcnt(4)\n\t"
                            "v_smfmac_f32_16x16x64_bf16 v[200:203], v[216:219], %[w01], %[ix]\n\t"
                            "s_barrier\n\t"
                            MTA(1)
                            "ds_read_b128 v[236:239], %[mp] offset:6144\n\t"
                            "s_waitcnt lgkmcnt(4)\n\t"
                            "v_smfmac_f32_16x16x64_bf16 v[200:203], v[220:223], %[w02], %[ix]\n\t"
                            "ds_read_b128 v[240:243], %[mp] offset:7168\n\t"
                            "s_waitcnt lgkmcnt(4)\n\t"
                            "v_smfmac_f32_16x16x64_bf16 v[200:203], v[224:227], %[w03], %[ix]\n\t"
                            "ds_read_b128 v[244:247], %[mp] offset:8192\n\t"
                            "s_waitcnt lgkmcnt(4)\n\t"
                            "v_smfmac_f32_16x16x64_bf16 v[200:203], v[228:231], %[w04], %[ix]\n\t"
                            "ds_read_b128 v[248:251], %[mp] offset:9216\n\t"
                            "s_waitcnt lgkmcnt(4)\n\t"
                            "v_smfmac_f32_16x16x64_bf16 v[200:203], v[232:235], %[w05], %[ix]\n\t"
                            "s_waitcnt lgkmcnt(3)\n\t"
                            "v_smfmac_f32_16x16x64_bf16 v[200:203], v[236:239], %[w06], %[ix]\n\t"
                            "s_waitcnt lgkmcnt(2)\n\t"
                            "v_smfmac_f32_16x16x64_bf16 v[200:203], v[240:243], %[w07], %[ix]\n\t"
                            "s_waitcnt lgkmcnt(1)\n\t"
                            "v_smfmac_f32_16x16x64_bf16 v[200:203], v[244:247], %[w08], %[ix]\n\t"
                            "s_waitcnt lgkmcnt(0)\n\t"
                            "v_smfmac_f32_16x16x64_bf16 v[200:203], v[248:251], %[w09], %[ix]\n\t"
                            "s_nop 2\n\t"
                            "v_smfmac_f32_16x16x64_bf16 v[204:207], v[212:215], %[w10], %[ix]\n\t"
                            "s_nop 2\n\t"
                            "v_smfmac_f32_16x16x64_bf16 v[204:207], v[216:219], %[w11], %[ix]\n\t"
                            "v_add_f32 v200, v200, v202\n\t"
                            "v_add_f32 v201, v201, v203\n\t"
                            "v_cvt_pk_bf16_f32 v200, v200, v201\n\t"
                            "ds_write_b32 %[vw], v200\n\t"
                            MTA(2)
                            "v_smfmac_f32_16x16x64_bf16 v[204:207], v[220:223], %[w12], %[ix]\n\t"
                            "v_max3_u32 v208, v208, v212, v213\n\t"
                            "v_max3_u32 v208, v208, v214, v215\n\t"
                            "v_smfmac_f32_16x16x64_bf16 v[204:207], v[224:227], %[w13], %[ix]\n\t"
                            "v_max3_u32 v208, v208, v216, v217\n\t"
                            "v_max3_u32 v208, v208, v218, v219\n\t"
                            "v_smfmac_f32_16x16x64_bf16 v[204:207], v[228:231], %[w14], %[ix]\n\t"
                            "v_max3_u32 v208, v208, v220, v221\n\t"
                            "v_max3_u32 v208, v208, v222, v223\n\t"
                            "v_smfmac_f32_16x16x64_bf16 v[204:207], v[232:235], %[w15], %[ix]\n\t"
                            "v_max3_u32 v208, v208, v224, v225\n\t"
                            "v_max3_u32 v208, v208, v226, v227\n\t"
                            "v_smfmac_f32_16x16x64_bf16 v[204:207], v[236:239], %[w16], %[ix]\n\t"
                            "v_max3_u32 v208, v208, v228, v229\n\t"
                            "v_max3_u32 v208, v208, v230, v231\n\t"
                            "v_smfmac_f32_16x16x64_bf16 v[204:207], v[240:243], %[w17], %[ix]\n\t"
                            "s_waitcnt lgkmcnt(0)\n\t"
                            "s_barrier\n\t"
                            MTA(5)
                            "v_smfmac_f32_16x16x64_bf16 v[204:207], v[244:247], %[w18], %[ix]\n\t"
                            "v_max3_u32 v208, v208, v232, v233\n\t"
                            "v_max3_u32 v208, v208, v234, v235\n\t"
                            "v_smfmac_f32_16x16x64_bf16 v[204:207], v[248:251], %[w19], %[ix]\n\t"
                            "v_max3_u32 v208, v208, v236, v237\n\t"
                            "v_max3_u32 v208, v208, v238, v239\n\t"
                            "v_max3_u32 v208, v208, v240, v241\n\t"
                            "v_max3_u32 v208, v208, v242, v243\n\t"
                            "v_max3_u32 v208, v208, v244, v245\n\t"
                            "v_max3_u32 v208, v208, v246, v247\n\t"
                            "v_max3_u32 v208, v208, v248, v249\n\t"
                            "v_max3_u32 v208, v208, v250, v251\n\t"
                            "v_cmp_lt_u32 vcc, %[lim], v208\n\t"
                            "v_add_f32 v204, v204, v206\n\t"
                            "v_add_f32 v205, v205, v207\n\t"
                            "v_cvt_pk_bf16_f32 v204, v204, v205\n\t"
                            "ds_write_b32 %[vw], v204 offset:256\n\t"
                            MTA(3)
                            "s_cbranch_vccz 1f\n\t"
                            "v_mov_b32 v208, %[it]\n\t"
                            "v_mov_b32 v209, 0\n\t"
                            "ds_write_b32 v209, v208 offset:%[ls]\n\t"
                            "1:\n\t"
                            "s_waitcnt vmcnt(0) lgkmcnt(0)\n\t"
                            "s_barrier\n\t"
                            "s_setprio 0\n\t"
                            :: [mp] "v"(mp0), [vw] "v"(vw), [ix] "v"(sidx), [lim] "s"(lim), [it] "s"(it), [ls] "n"(L_S),
                              [w00] "v"(wt[0][0]), [w01] "v"(wt[0][1]), [w02] "v"(wt[0][2]), [w03] "v"(wt[0][3]), [w04] "v"(wt[0][4]), [w05] "v"(wt[0][5]), [w06] "v"(wt[0][6]), [w07] "v"(wt[0][7]), [w08] "v"(wt[0][8]), [w09] "v"(wt[0][9]), [w10] "v"(wt[1][0]), [w11] "v"(wt[1][1]), [w12] "v"(wt[1][2]), [w13] "v"(wt[1][3]), [w14] "v"(wt[1][4]), [w15] "v"(wt[1][5]), [w16] "v"(wt[1][6]), [w17] "v"(wt[1][7]), [w18] "v"(wt[1][8]), [w19] "v"(wt[1][9])
                            : "v200", "v201", "v202", "v203", "v204", "v205", "v206", "v207", "v208", "v209", "v212", "v213", "v214", "v215", "v216", "v217", "v218", "v219", "v220", "v221", "v222", "v223", "v224", "v225", "v226", "v227", "v228", "v229", "v230", "v231", "v232", "v233", "v234", "v235", "v236", "v237", "v238", "v239", "v240", "v241", "v242", "v243", "v244", "v245", "v246", "v247", "v248", "v249", "v250", "v251", "vcc", "memory");
                        MT(4);
                        redo = __builtin_amdgcn_readfirstlane(*(volatile LAS unsigned*)(smem + L_S)) == it;
                        if (redo) FC(fc0);
                    } else {
                        f32x16 ca = {}, cb = {};
                        f32x8 cc = {};
                        asm volatile("" : "+{v[212:227]}"(ca), "+{v[228:243]}"(cb), "+{v[244:251]}"(cc));
                        unsigned sr = tro + (unsigned)(s & 3) * 2u * PAIR;
                        asm volatile("" : "+s"(sr));
                        if (TK) fetch(tk, s, !redo, s > 0 ? poll_lim(budget) : ~0u, DVC, (unsigned)hh * 128u + 4u, __builtin_expect(s == 0, 0));
                        else asm volatile("s_waitcnt lgkmcnt(0)\n\ts_barrier\n\ts_barrier\n\ts_barrier" ::: "memory");
                        unsigned vd, vv, zs = zb;
                        u32x4 af1;
                        asm volatile(
                            "s_setprio 1\n\t"
                            "s_waitcnt lgkmcnt(0)\n\t"
                            "s_barrier\n\t"
                            "ds_read_b64_tr_b16 v[192:193], %[va]\n\t"
                            "ds_read_b64_tr_b16 v[194:195], %[va] offset:32\n\t"
                            "s_waitcnt lgkmcnt(0)\n\t"
                            "v_smfmac_f32_16x16x64_bf16 v[212:215], v[192:195], %[w0], %[ix]\n\t"
                            "v_smfmac_f32_16x16x64_bf16 v[220:223], v[192:195], %[w2], %[ix]\n\t"
                            "v_smfmac_f32_16x16x64_bf16 v[216:219], v[192:195], %[w1], %[ix]\n\t"
                            "v_smfmac_f32_16x16x64_bf16 v[224:227], v[192:195], %[w3], %[ix]\n\t"
                            "s_barrier\n\t"
                            MTA(4)
                            "ds_read_b64_tr_b16 v[196:197], %[va] offset:256\n\t"
                            "v_smfmac_f32_16x16x64_bf16 v[228:231], v[192:195], %[w4], %[ix]\n\t"
                            "ds_read_b64_tr_b16 v[198:199], %[va] offset:288\n\t"
                            "v_smfmac_f32_16x16x64_bf16 v[236:239], v[192:195], %[w6], %[ix]\n\t"
                            "ds_read_b32 v210, %[vz] offset:%[ls]\n\t"
                            "v_smfmac_f32_16x16x64_bf16 v[232:235], v[192:195], %[w5], %[ix]\n\t"
                            "v_smfmac_f32_16x16x64_bf16 v[240:243], v[192:195], %[w7], %[ix]\n\t"
                            "v_smfmac_f32_16x16x64_bf16 v[244:247], v[192:195], %[w8], %[ix]\n\t"
                            "v_smfmac_f32_16x16x64_bf16 v[248:251], v[192:195], %[w9], %[ix]\n\t"
                            "buffer_load_dword %[sf], %[sl], %[rw], %[sr] offen sc1\n\t"
                            : [sf] "=&v"(sf), "+{v[212:227]}"(ca), "+{v[228:243]}"(cb), "+{v[244:251]}"(cc), "=&{v[196:199]}"(af1), "=&{v210}"(vv)
                            : [va] "v"(va), [vz] "v"(0u), [ls] "n"(L_S), [ix] "v"(sidx), [sl] "v"(tk.sl), [sr] "s"(sr), [rw] "s"(rwin),
                              [w0] "v"(wt[0][0]), [w1] "v"(wt[0][1]), [w2] "v"(wt[0][2]), [w3] "v"(wt[0][3]), [w4] "v"(wt[0][4]), [w5] "v"(wt[0][5]), [w6] "v"(wt[0][6]), [w7] "v"(wt[0][7]), [w8] "v"(wt[0][8]), [w9] "v"(wt[0][9])
                            : "v192", "v193", "v194", "v195", "memory");
                        asm volatile(
                            "s_waitcnt lgkmcnt(0)\n\t"
                            "v_smfmac_f32_16x16x64_bf16 v[212:215], v[196:199], %[w0], %[ix]\n\t"
                            "v_readfirstlane_b32 %[vd], v210\n\t"
                            "v_smfmac_f32_16x16x64_bf16 v[220:223], v[196:199], %[w2], %[ix]\n\t"
                            "s_cmp_eq_u32 %[vd], %[it]\n\t"
                            "s_cselect_b32 %[zs], 2.0, %[zs]\n\t"
                            "v_smfmac_f32_16x16x64_bf16 v[216:219], v[196:199], %[w1], %[ix]\n\t"
                            "s_nop 1\n\t"
                            "v_smfmac_f32_16x16x64_bf16 v[224:227], v[196:199], %[w3], %[ix]\n\t"
                            "v_add_f32 v212, v212, v214\n\t"
                            "v_smfmac_f32_16x16x64_bf16 v[228:231], v[196:199], %[w4], %[ix]\n\t"
                            "v_add_f32 v213, v213, v215\n\t"
                            "v_smfmac_f32_16x16x64_bf16 v[236:239], v[196:199], %[w6], %[ix]\n\t"
                            "v_add_f32 v220, v220, v222\n\t"
                            "v_smfmac_f32_16x16x64_bf16 v[232:235], v[196:199], %[w5], %[ix]\n\t"
                            "v_add_f32 v221, v221, v223\n\t"
                            "v_smfmac_f32_16x16x64_bf16 v[240:243], v[196:199], %[w7], %[ix]\n\t"
                            "v_cvt_pk_bf16_f32 v200, v212, v213\n\t"
                            "v_smfmac_f32_16x16x64_bf16 v[244:247], v[196:199], %[w8], %[ix]\n\t"
                            "v_cvt_pk_bf16_f32 v201, v220, v221\n\t"
                            "v_smfmac_f32_16x16x64_bf16 v[248:251], v[196:199], %[w9], %[ix]\n\t"
                            "v_add_f32 v216, v216, v218\n\t"
                            "v_add_f32 v217, v217, v219\n\t"
                            "v_add_f32 v224, v224, v226\n\t"
                            "v_add_f32 v225, v225, v227\n\t"
                            "v_cvt_pk_bf16_f32 v202, v216, v217\n\t"
                            "v_cvt_pk_bf16_f32 v203, v224, v225\n\t"
                            "v_permlane32_swap_b32 v200, v201\n\t"
                            "v_pk_add_f32 v[228:229], v[228:229], v[230:231]\n\t"
                            "v_permlane32_swap_b32 v202, v203\n\t"
                            "v_min_u32 v201, -2, v201\n\t"
                            "v_pk_add_f32 v[236:237], v[236:237], v[238:239]\n\t"
                            "v_min_u32 v203, -2, v203\n\t"
                            "v_permlane16_swap_b32 v200, v202\n\t"
                            "v_pk_add_f32 v[232:233], v[232:233], v[234:235]\n\t"
                            "v_permlane16_swap_b32 v201, v203\n\t"
                            "v_pk_add_f32 v[240:241], v[240:241], v[242:243]\n\t"
                            MTA(5)
                            "buffer_store_dwordx4 v[200:203], %[z0], %[rw], %[zs] offen sc1\n\t"
                            "v_cvt_pk_bf16_f32 v204, v228, v229\n\t"
                            "v_cvt_pk_bf16_f32 v205, v236, v237\n\t"
                            "v_cvt_pk_bf16_f32 v206, v232, v233\n\t"
                            "v_cvt_pk_bf16_f32 v207, v240, v241\n\t"
                            "v_permlane32_swap_b32 v204, v205\n\t"
                            "v_pk_add_f32 v[244:245], v[244:245], v[246:247]\n\t"
                            "v_permlane32_swap_b32 v206, v207\n\t"
                            "v_min_u32 v205, -2, v205\n\t"
                            "v_pk_add_f32 v[248:249], v[248:249], v[250:251]\n\t"
                            "v_min_u32 v207, -2, v207\n\t"
                            "v_permlane16_swap_b32 v204, v206\n\t"
                            "v_cvt_pk_bf16_f32 v208, v244, v245\n\t"
                            "v_permlane16_swap_b32 v205, v207\n\t"
                            "v_cvt_pk_bf16_f32 v209, v248, v249\n\t"
                            "buffer_store_dwordx4 v[204:207], %[z1], %[rw], %[zs] offen sc1\n\t"
                            "s_nop 0\n\t"
                            "v_permlane32_swap_b32 v208, v209\n\t"
                            "v_min_u32 v209, -2, v209\n\t"
                            "buffer_store_dwordx2 v[208:209], %[z2], %[rw], %[zs] offen sc1\n\t"
                            : [vd] "=&s"(vd), [zs] "+s"(zs), "=&{v[200:203]}"(za), "=&{v[204:207]}"(zc), "=&{v[208:209]}"(ze), "+{v[212:227]}"(ca), "+{v[228:243]}"(cb), "+{v[244:251]}"(cc), "+v"(sf)
                            : [ix] "v"(sidx), [it] "s"(it), [rw] "s"(rwin), [z0] "v"(zo0), [z1] "v"(zo1), [z2] "v"(zo2), "{v[196:199]}"(af1), "{v210}"(vv),
                              [w0] "v"(wt[1][0]), [w1] "v"(wt[1][1]), [w2] "v"(wt[1][2]), [w3] "v"(wt[1][3]), [w4] "v"(wt[1][4]), [w5] "v"(wt[1][5]), [w6] "v"(wt[1][6]), [w7] "v"(wt[1][7]), [w8] "v"(wt[1][8]), [w9] "v"(wt[1][9])
                            : "scc", "memory");
                        redo = vd == it;
                        RT(4);
                        MT(6);
                        tref = __builtin_amdgcn_s_memtime();
                        __builtin_amdgcn_s_setprio(TKP);
                        asm volatile("s_waitcnt vmcnt(3)" : "+v"(sf) :: "memory");
                    }
                } while (__builtin_expect(redo, 0));
                if (s < 8) SST(8 + s);
                if (C == 0) {
                    if (s > 0) {
                        __builtin_amdgcn_s_sleep(DZR);
                        const unsigned zq = zb == W_ZM ? W_ZM + 2u * NOWN * PAIR : zb - NOWN * PAIR;
                        unsigned s1;
                        asm volatile("v_mov_b32 %0, -1" : "=v"(s1));
                        st128(((u32x4){s1, s1, s1, s1}), rwin, zo0, zq, 16);
                        st128(((u32x4){s1, s1, s1, s1}), rwin, zo1, zq, 16);
                        __builtin_amdgcn_raw_buffer_store_b64((u32x2){s1, s1}, rwin, zo2, zq, 16);
                    }
                } else {
                    if (s > TAILS) __builtin_amdgcn_raw_buffer_store_b32((unsigned)s - 1u, rwin, 0u, W_FZ2 + (unsigned)(hh * 128 + (w & 3) * 4), 16);
                    if (TK && s + 1 < NSEG) sched2(tk, s, sf);
                    if (!TK) wait_until(tref + DZ2);
                    if (s + 1 < NSEG) put_z2(s);
                }
                if (budget <= 0) __hip_atomic_store(a.ctl, 1u, __ATOMIC_RELAXED, __HIP_MEMORY_SCOPE_AGENT);
                zb = zb == W_ZM + 2u * NOWN * PAIR ? W_ZM : zb + NOWN * PAIR;
                RT(5);
                RTS();
                MT(7);
                MTS();
            }
        };
        if (!vt) {
            if (w < 4) steps(IC<0>()); else if (w < 7) steps(IC<1>()); else steps(IC<3>());
        } else {
            asm volatile("s_waitcnt vmcnt(5) lgkmcnt(0)\n\ts_barrier" ::: "memory");
            SST(3);
            LANES();
            const int ni = __builtin_popcountll(mv) + 1;
            if (w < 4) {
                const LAS char* c0 = (const LAS char*)(hL + hoff + ((fq ^ (fr & 7)) << 4));
                const LAS char* c1 = (const LAS char*)(hL + hoff + (((4 + fq) ^ (fr & 7)) << 4));
                const LAS char* n0 = c0 + 40960; const LAS char* n1 = c1 + 40960;
                u32x4 bb[8];
                project0(bb, c0, c1);
                for (int i = 1; i < ni; ++i) {
                    if (vt) MT(0);
                    f32x4 tc[2][2];
                    project<0>(wt, tc, bb, c0, c1, n0, n1,
                        [&]() {
                            MT(1);
                            asm volatile("s_waitcnt vmcnt(0) lgkmcnt(0)\n\ts_barrier" ::: "memory");
                            MT(2);
                        }, [](auto) {});
                    { const LAS char* x = c0; c0 = n0; n0 = x; x = c1; c1 = n1; n1 = x; }
                    char* tb = tokL + (i & 1) * 32 * QS + (16 * w + 4 * fq) * 2;
#pragma unroll
                    for (int g = 0; g < 2; ++g)
#pragma unroll
                        for (int tt = 0; tt < 2; ++tt) *(u32x2*)(tb + (prow + 8 * tt) * QS + g * 128) = (u32x2){pack2(tc[g][tt][0], tc[g][tt][1]), pack2(tc[g][tt][2], tc[g][tt][3])};
                    asm volatile("" ::: "memory");
                    *(volatile LAS int*)(smem + L_S + 16 + w * 4) = i;
                    if (vt) { MT(3); const int s = i & 63; MTRT(); MTS(); }
                }
                project_end(bb);
            } else {
                __builtin_amdgcn_s_setprio(OPR);
                const unsigned hv = lane * 16u, fz_ = W_FZ + (unsigned)(hh * 128 + xt * 64 + (w - 4) * 4);
                unsigned fp = ~0u;
                int jo = jv1, jh = jv2;
                for (int i = 1; i < ni; ++i, jo = nextv(jo)) {
                    const int b = i + (i >= ZLAG);
                    if (__builtin_expect(i == 1 || i == ZLAG, 0)) {
                        VMWAIT();
                        if (i == ZLAG) {
                            jh = nextv(jh);
                            wait_h(a, rwin, jh, hrdy);
                            BAR0();
                            for (int t = 0; t < 10; ++t) dma1k(rws, hv, O_H + (unsigned)(jh * 40960 + (w - 4) * 1024 + t * 4096), (unsigned)(L_H + ((i + 1) & 1) * 40960 + (w - 4) * 1024 + t * 4096));
                            VMWAIT();
                        }
                    }
                    jh = nextv(jh);
                    wait_h(a, rwin, jh, hrdy);
                    if (b < ni) BAR0();
                    if (vt) MT(1);
                    for (;;) {
                        const u32x4 f = *(volatile LAS u32x4*)(smem + L_S + 16);
                        if (__builtin_amdgcn_readfirstlane(min(min(f[0], f[1]), min(f[2], f[3]))) >= (unsigned)i) break;
                        __builtin_amdgcn_s_sleep(1);
                    }
                    asm volatile("" ::: "memory");
                    if (vt) MT(0);
                    u32x4 vf[2][4];
#pragma unroll
                    for (int t = 0; t < 2; ++t)
#pragma unroll
                        for (int ks = 0; ks < 4; ++ks) vf[t][ks] = *(const u32x4*)(tokL + (i & 1) * 32 * QS + (16 * t + fr) * QS + ks * 64 + fq * 16);
                    const unsigned ztb = O_Z + (unsigned)(jo * ZTSEG) * 2u + (unsigned)(10 * (w - 4) * H_ + hh) * 1024u;
                    f32x4 c[2][2];
                    u32x4 r;
                    unsigned hs = O_H + (unsigned)(jh * 40960 + (w - 4) * 1024), hd = (unsigned)(L_H + ((b + 1) & 1) * 40960 + (w - 4) * 1024);
                    asm volatile("" : "+s"(hs), "+s"(hd));
#pragma unroll
                    for (int nt = 0; nt < 11; ++nt) {
                        auto& cc = c[nt & 1]; auto& cp = c[(nt + 1) & 1];
#pragma unroll
                        for (int ks = 0; ks < 4; ++ks) {
                            if (nt < 10) { cc[0] = mfma32(vf[0][ks], half8(wt[ks >> 1][nt], ks & 1), ks ? cc[0] : z4); PIN1(cc[0]); }
                            if (nt > 0 && ks >= 1 && ks < 3) { r[2 * ks - 2] = pack2(cp[ks - 1][0], cp[ks - 1][1]); PIN1(r); }
                            if (nt < 10) { cc[1] = mfma32(vf[1][ks], half8(wt[ks >> 1][nt], ks & 1), ks ? cc[1] : z4); PIN1(cc[1]); }
                            if (nt > 0 && ks >= 1 && ks < 3) { r[2 * ks - 1] = pack2(cp[ks - 1][2], cp[ks - 1][3]); PIN1(r); }
                            if (nt > 0 && ks == 2) st128(r, rws, lane * 16u, ztb + (unsigned)((nt - 1) * H_) * 1024u, 16);
                            if (nt < 5 && !(ks & 1)) dma1k(rws, hv, hs + (2 * nt + (ks >> 1)) * 4096u, hd + (2 * nt + (ks >> 1)) * 4096u);
                            if (nt == 5 && ks == 0) {
                                asm volatile("s_waitcnt vmcnt(14)" ::: "memory");
                                __builtin_amdgcn_raw_buffer_store_b32(fp, rwin, 0u, fz_, 16);
                            }
                            SB();
                        }
                    }
                    if (vt) MT(2);
                    fp = (unsigned)jo;
                    if (__builtin_expect(jo <= ZEARLY || i + 1 == ni, 0)) {
                        VMWAIT();
                        __builtin_amdgcn_raw_buffer_store_b32(fp, rwin, 0u, fz_, 16);
                    } else asm volatile("s_waitcnt vmcnt(7)" ::: "memory");
                    if (jo < 8) SST(8 + jo);
                    if (vt && (!BSPARSE || jo % 6 == 2)) { const int s = jo; RT(0); RTS(); }
                    if (vt) { MT(3); const int s = i & 63; MTRT(); MTS(); }
                }
            }
            __builtin_amdgcn_s_setprio(0);
            VMWAIT();
            BAR();
        }
    }
    const bool qt = bid >= NQT && bid < NWRK;
    if (bid < 32 || qt) {
        LANES();
        const int hh = qt ? (bid - NQT) & 31 : bid;
        const unsigned long long mq = xt ? MXQ : ~MXQ;
        auto nextq = [&](int j) { return min(seg_after(mq, j), NSEG - 1); };
        const int jq0 = qt ? nextq(1) : 0, jq1 = nextq(jq0);
        u32x8 wt[2][10];
        if (w > 0) {
            const unsigned long long t0 = __builtin_amdgcn_s_memtime();
            if (!qt) gather_mem0(a, memL, tid - 64, 448);
            if (w == 7) clk_init(ck);
            wait_until(t0 + DW0);
        }
        if (DQT && qt) wait_until(tstart + DQT);
        {
            const u16* pq = (const u16*)(ws + OFF_WQKVT) + (size_t)((w >> 2) * 256 + hh * 8 + 2 * (w & 3)) * 10240 + lane * 8;
#pragma unroll
            for (int t = 0; t < 10; ++t) { wt[0][t] = ldw(pq + t * 1024); wt[1][t] = ldw(pq + 10240 + t * 1024); }
        }
        if (tid < 4) *(unsigned*)(smem + L_S + tid * 4) = 0u;
        SST(1);
        if (w == 0) { wait_h(a, rwin, jq1, hrdy); *(volatile LAS int*)(smem + L_S + 32) = hrdy; }
        BAR();
        hrdy = __builtin_amdgcn_readfirstlane(*(volatile LAS int*)(smem + L_S + 32));
        SST(2);
        stage_h(rws, jq0, L_H, tid);
        stage_h(rws, jq1, L_H + 40960, tid);
        VMWAIT();
        BAR();
        SST(3);
        int qrdy = 0;
        {
            LANES();
            const unsigned to0 = (unsigned)((w ^ 4) >> 2) * 8192u + (unsigned)((w & 3) * 64 + (lane ^ ((fq & 1) * (w < 4 ? 8 : 5)))) * 16u, to1 = to0 + 4096u, hv = lane * 16u;
            const LAS char* c0 = (const LAS char*)(hL + hoff + ((fq ^ (fr & 7)) << 4));
            const LAS char* c1 = (const LAS char*)(hL + hoff + (((4 + fq) ^ (fr & 7)) << 4));
            u32x4 bb[8], g0 = SENT, g1 = SENT;
            f32x4 tc[2][2];
            auto pack = [&]() {
                g0 = (u32x4){pack2(tc[0][0][0], tc[0][0][1]), pack2(tc[0][0][2], tc[0][0][3]), pack2(tc[1][0][0], tc[1][0][1]), pack2(tc[1][0][2], tc[1][0][3])};
                g1 = (u32x4){pack2(tc[0][1][0], tc[0][1][1]), pack2(tc[0][1][2], tc[0][1][3]), pack2(tc[1][1][0], tc[1][1][1]), pack2(tc[1][1][2], tc[1][1][3])};
            };
            const LAS char* n0 = c0 + 40960; const LAS char* n1 = c1 + 40960;
            if (!qt) {
                project0(bb, c0, c1);
                for (int j = 0; j < 2; ++j) {
                    project<0>(wt, tc, bb, c0, c1, n0, n1, []() {}, [](auto) {});
                    pack();
                    *(u32x4*)(tokL + j * 16384 + to0) = g0; *(u32x4*)(tokL + j * 16384 + to1) = g1;
                    c0 = n0; c1 = n1;
                }
                project_end(bb);
            } else {
                project0(bb, c0, c1);
                unsigned hs = O_H + (unsigned)(jq1 * 40960 + w * 1024), hd = (unsigned)(L_H + 40960 + w * 1024), fv = tid == 0 ? 0u : 0x40000000u;
                const unsigned fq_ = W_FQ + (unsigned)(hh * 128 + xt * 64);
                const int jl = 63 - __builtin_clzll(mq);
                for (int jp = 1, j = jq0, jn = jq1;; jp = j, j = jn, jn = nextq(jn)) {
                    MT(0);
                    project<0>(wt, tc, bb, c0, c1, n0, n1,
                        [&]() {
                            wait_h(a, rwin, nextq(jn), hrdy);
                            hs = O_H + (unsigned)(nextq(jn) * 40960 + w * 1024); hd = (unsigned)(2 * L_H + 40960 + 2 * w * 1024) - hd;
                            unsigned fj = (unsigned)jp;
                            asm volatile("" : "+s"(hs), "+s"(hd));
                            PIN1(fj);
                            MT(1);
                            VMWAIT();
                            BAR();
                            MT(2);
                            if (w == 0) __builtin_amdgcn_raw_buffer_store_b32(fj, rwin, fv, fq_, 16);
                        },
                        [&](auto uu) {
                            constexpr int u = decltype(uu)::v;
                            if (u == 34 || u == 38 || u == 2 || u == 6 || u == 10) { constexpr int t = ((u + 6) % 40) / 4; dma1k(rws, hv, hs + t * 8192u, hd + t * 8192u); }
                        });
                    pack();
                    const unsigned qs = O_QK + (unsigned)(j * H_ + hh) * 16384u;
                    st128(g0, rws, to0, qs, 16);
                    st128(g1, rws, to1, qs, 16);
                    { const LAS char* x = c0; c0 = n0; n0 = x; x = c1; c1 = n1; n1 = x; }
                    if (__builtin_expect(j <= QEARLY || j == jl, 0)) {
                        VMWAIT();
                        BAR0();
                        if (tid == 0) __builtin_amdgcn_raw_buffer_store_b32((unsigned)j, rwin, 0u, fq_, 16);
                        if (j == jl) break;
                    }
                    MT(3);
                    if (j < 8) SST(8 + j);
                    if (!BSPARSE || j % 6 == 2) { const int s = j; RT(0); RTS(); MTRT(); MTS(); }
                }
                project_end(bb);
            }
        }
        SST(4);
        auto steps = [&](auto cc) {
        constexpr int C = decltype(cc)::v >> 1, R = decltype(cc)::v & 1;
        LANES();
        const LAS char* ka = (const LAS char*)(smem + L_K + (fr & 1) * TQS + (4 * fq + (fr >> 2)) * 8);
        const LAS char* qa = (const LAS char*)(smem + L_Q + (fr & 1) * 544 + ((fr >> 1) & 1) * 1152 + (16 * fq + 2 * (fr >> 2)) * 4);
        LAS char* qw = (LAS char*)(C < 2 ? smem + L_Q + (fq & 1) * 544 + (fq >> 1) * 1152 + (32 * w + 8 * (fr >> 2) + bs2(fr & 3)) * 4
                                         : smem + L_K + (fq >> 1) * TQS + (32 * (w & 3) + fr) * 8 + (fq & 1) * 4);
        const LAS char* mp0 = (const LAS char*)(memL + ma0);
        const int z = 16 * (fr >> 3) + 4 * (fr & 3) + 2 * ((fr >> 2) & 1);
        const int ko = ((z >> 4) * 256 + fq * 16 + ((z & 15) ^ ((fq & 1) * 5))) * 16;
        unsigned pa0 = (4u * C + 2u * (hh >> 4)) * PAIR + (unsigned)((hh & 15) * 512 + R * 256 + lane * 4);
        unsigned pa2 = ((8u + (fr >> 3)) * PAIR + C * 4096u + (unsigned)((hh >> 1) * 256 + R * 128 + (hh & 1) * 64 + fq * 16 + (fr & 6) * 2)) | (fr & 1) << 30;
        PIN2(ka, qw); PIN1(mp0);
        constexpr bool TK = C == 2 && R == 1;
        Tk tk = {0u, 0u, 0x40000000u, nullptr};
        if (TK) { tk = tk_init(); __builtin_amdgcn_s_setprio(TKP); }
        if (C < 2) { PIN2(qa, pa0); PIN1(pa2); }
        int qf_ = 0;
        int tbo = 0;
        for (int s = 0; s < NSEG; ++s) {
            RT(0);
            const char* tb = tokL + tbo;
            unsigned pb = __builtin_amdgcn_readfirstlane(s + 1 < NSEG ? W_P + (unsigned)(s & 3) * 10u * PAIR + nh : 0x40000000u);
            asm volatile("" : "+s"(pb));
            if (s == 0) BAR();
            u32x8 kx[2][2];
            if (C < 2) {
#pragma unroll
                for (int u = 0; u < 4; ++u) kx[u >> 1][u & 1] = cat8(*(const u32x4*)(tb + (ko ^ ((u >> 1) * 16)) + (u & 1) * 2048), *(const u32x4*)(tb + (ko ^ ((u >> 1) * 16)) + (u & 1) * 2048 + 1024));
                PIN2(kx[0][0], kx[0][1]); PIN2(kx[1][0], kx[1][1]);
            }
            bool redo = false;
            unsigned sf = 0;
            do {
                ++it;
                f32x4 a0 = z4, a1 = z4;
                PIN2(a0, a1);
                u32x4 m[10];
                if (C < 2) {
                    __builtin_amdgcn_s_setprio(2);
                    BAR();
                    MT(0);
#pragma unroll
                    for (int t = 0; t < NB1; ++t) { m[t] = *(const LAS u32x4*)(mp0 + t * 1024); SB(); }
                    BAR0();
#pragma unroll
                    for (int t = 0; t < 10; ++t) {
                        if (t == QB2) { BAR0(); MT(1); }
                        if (t + NB1 < 10) m[t + NB1] = *(const LAS u32x4*)(mp0 + (t + NB1) * 1024);
                        SB();
                        a0 = smfmac(m[t], wt[0][t], a0, sidx); a1 = smfmac(m[t], wt[1][t], a1, sidx);
                        PIN2(a0, a1);
                    }
                } else {
                    unsigned mx = 0;
                    asm volatile("" : "+{v[200:203]}"(a0), "+{v[204:207]}"(a1), "+{v208}"(mx));
                    unsigned lim = s > 0 ? poll_lim(budget) : ~0u, sr = tro + (unsigned)(s & 3) * 2u * PAIR;
                    asm volatile("" : "+s"(lim), "+s"(sr));
                    if (TK) fetch(tk, s, !redo, lim, DQC, (unsigned)hh * 128u, true);
                    else asm volatile("s_waitcnt lgkmcnt(0)\n\ts_barrier\n\ts_barrier\n\ts_barrier" ::: "memory");
                    asm volatile(
                        "ds_read_b128 v[212:215], %[mp] offset:0\n\t"
                        "ds_read_b128 v[216:219], %[mp] offset:1024\n\t"
                        "ds_read_b128 v[220:223], %[mp] offset:2048\n\t"
                        "ds_read_b128 v[224:227], %[mp] offset:3072\n\t"
                        "ds_read_b128 v[228:231], %[mp] offset:4096\n\t"
                        "ds_read_b128 v[232:235], %[mp] offset:5120\n\t"
                        "ds_read_b128 v[236:239], %[mp] offset:6144\n\t"
                        "ds_read_b128 v[240:243], %[mp] offset:7168\n\t"
                        "ds_read_b128 v[244:247], %[mp] offset:8192\n\t"
                        "ds_read_b128 v[248:251], %[mp] offset:9216\n\t"
                        MTA(4)
                        "s_waitcnt lgkmcnt(9)\n\t"
                        "v_smfmac_f32_16x16x64_bf16 v[200:203], v[212:215], %[w00], %[ix]\n\t"
                        "v_max3_u32 v208, v208, v212, v213\n\t"
                        "v_smfmac_f32_16x16x64_bf16 v[204:207], v[212:215], %[w10], %[ix]\n\t"
                        "s_waitcnt lgkmcnt(8)\n\t"
                        "v_max3_u32 v208, v208, v214, v215\n\t"
                        "v_smfmac_f32_16x16x64_bf16 v[200:203], v[216:219], %[w01], %[ix]\n\t"
                        "v_max3_u32 v208, v208, v216, v217\n\t"
                        "v_smfmac_f32_16x16x64_bf16 v[204:207], v[216:219], %[w11], %[ix]\n\t"
                        "s_waitcnt lgkmcnt(7)\n\t"
                        "v_max3_u32 v208, v208, v218, v219\n\t"
                        "v_smfmac_f32_16x16x64_bf16 v[200:203], v[220:223], %[w02], %[ix]\n\t"
                        "v_max3_u32 v208, v208, v220, v221\n\t"
                        "v_smfmac_f32_16x16x64_bf16 v[204:207], v[220:223], %[w12], %[ix]\n\t"
                        "s_waitcnt lgkmcnt(6)\n\t"
                        "v_max3_u32 v208, v208, v222, v223\n\t"
                        "v_smfmac_f32_16x16x64_bf16 v[200:203], v[224:227], %[w03], %[ix]\n\t"
                        "v_max3_u32 v208, v208, v224, v225\n\t"
                        "v_smfmac_f32_16x16x64_bf16 v[204:207], v[224:227], %[w13], %[ix]\n\t"
                        "s_waitcnt lgkmcnt(0)\n\t"
                        "v_max3_u32 v208, v208, v226, v227\n\t"
                        "v_smfmac_f32_16x16x64_bf16 v[200:203], v[228:231], %[w04], %[ix]\n\t"
                        "v_max3_u32 v208, v208, v228, v229\n\t"
                        "v_smfmac_f32_16x16x64_bf16 v[204:207], v[228:231], %[w14], %[ix]\n\t"
                        "v_max3_u32 v208, v208, v230, v231\n\t"
                        "v_smfmac_f32_16x16x64_bf16 v[200:203], v[232:235], %[w05], %[ix]\n\t"
                        "v_max3_u32 v208, v208, v232, v233\n\t"
                        "v_smfmac_f32_16x16x64_bf16 v[204:207], v[232:235], %[w15], %[ix]\n\t"
                        "v_max3_u32 v208, v208, v234, v235\n\t"
                        "v_smfmac_f32_16x16x64_bf16 v[200:203], v[236:239], %[w06], %[ix]\n\t"
                        "v_max3_u32 v208, v208, v236, v237\n\t"
                        "v_smfmac_f32_16x16x64_bf16 v[204:207], v[236:239], %[w16], %[ix]\n\t"
                        "v_max3_u32 v208, v208, v238, v239\n\t"
                        "v_smfmac_f32_16x16x64_bf16 v[200:203], v[240:243], %[w07], %[ix]\n\t"
                        "v_max3_u32 v208, v208, v240, v241\n\t"
                        "v_smfmac_f32_16x16x64_bf16 v[204:207], v[240:243], %[w17], %[ix]\n\t"
                        "v_max3_u32 v208, v208, v242, v243\n\t"
                        "v_smfmac_f32_16x16x64_bf16 v[200:203], v[244:247], %[w08], %[ix]\n\t"
                        "v_max3_u32 v208, v208, v244, v245\n\t"
                        "v_smfmac_f32_16x16x64_bf16 v[204:207], v[244:247], %[w18], %[ix]\n\t"
                        "v_max3_u32 v208, v208, v246, v247\n\t"
                        "v_smfmac_f32_16x16x64_bf16 v[200:203], v[248:251], %[w09], %[ix]\n\t"
                        "v_max3_u32 v208, v208, v248, v249\n\t"
                        "v_smfmac_f32_16x16x64_bf16 v[204:207], v[248:251], %[w19], %[ix]\n\t"
                        "v_max3_u32 v208, v208, v250, v251\n\t"
                        "buffer_load_dword %[sf], %[sl], %[rw], %[sr] offen sc1\n\t"
                        "v_cmp_lt_u32 vcc, %[lim], v208\n\t"
                        "s_nop 3\n\t"
                        "v_pk_add_f32 v[200:201], v[200:201], v[202:203]\n\t"
                        "v_pk_add_f32 v[204:205], v[204:205], v[206:207]\n\t"
                        "v_cvt_pk_bf16_f32 v200, v200, v201\n\t"
                        "v_cvt_pk_bf16_f32 v204, v204, v205\n\t"
                        "ds_write2_b32 %[qw], v200, v204 offset1:32\n\t"
                        MTA(2)
                        "s_cbranch_vccz 1f\n\t"
                        "v_mov_b32 v208, %[it]\n\t"
                        "v_mov_b32 v209, 0\n\t"
                        "ds_write_b32 v209, v208 offset:%[ls]\n\t"
                        "1:\n\t"
                        "s_waitcnt vmcnt(%[nv]) lgkmcnt(0)\n\t"
                        "s_barrier\n\t"
                        "s_waitcnt vmcnt(0)\n\t"
                        : [sf] "=&v"(sf), "+v"(qf_), "+{v[200:203]}"(a0), "+{v[204:207]}"(a1), "+{v208}"(mx)
                        : [mp] "v"(mp0), [qw] "v"(qw), [ix] "v"(sidx), [lim] "s"(lim), [it] "s"(it), [ls] "n"(L_S), [sl] "v"(tk.sl), [sr] "s"(sr), [rw] "s"(rwin), [nv] "n"(TK ? 2 : 1),
                          [w00] "v"(wt[0][0]), [w01] "v"(wt[0][1]), [w02] "v"(wt[0][2]), [w03] "v"(wt[0][3]), [w04] "v"(wt[0][4]), [w05] "v"(wt[0][5]), [w06] "v"(wt[0][6]), [w07] "v"(wt[0][7]), [w08] "v"(wt[0][8]), [w09] "v"(wt[0][9]), [w10] "v"(wt[1][0]), [w11] "v"(wt[1][1]), [w12] "v"(wt[1][2]), [w13] "v"(wt[1][3]), [w14] "v"(wt[1][4]), [w15] "v"(wt[1][5]), [w16] "v"(wt[1][6]), [w17] "v"(wt[1][7]), [w18] "v"(wt[1][8]), [w19] "v"(wt[1][9])
                        : "v209", "v212", "v213", "v214", "v215", "v216", "v217", "v218", "v219", "v220", "v221", "v222", "v223", "v224", "v225", "v226", "v227", "v228", "v229", "v230", "v231", "v232", "v233", "v234", "v235", "v236", "v237", "v238", "v239", "v240", "v241", "v242", "v243", "v244", "v245", "v246", "v247", "v248", "v249", "v250", "v251", "vcc", "memory");
                }
                if (C < 2) {
                    *(LAS unsigned*)qw = hpack(a0); *(LAS unsigned*)(qw + 16) = hpack(a1);
                    SB();
                    MT(2);
                    unsigned vd;
#define SC0 "v[212:215]"
#define SC1 "v[216:219]"
#define SC2 "v[220:223]"
#define DPP(op, d, ctl) op " " d ", " d ", " d " " ctl " row_mask:0xf bank_mask:0xf\n\t"
#define TAIL(A0, A2, B0, B2, C0, C2) \
                        "v_mov_b64 v[212:213], 0\n\tv_mov_b64 v[214:215], 0\n\tv_mov_b64 v[216:217], 0\n\tv_mov_b64 v[218:219], 0\n\tv_mov_b64 v[220:221], 0\n\tv_mov_b64 v[222:223], 0\n\t" \
                        "s_setprio 3\n\t" \
                        "s_waitcnt lgkmcnt(0)\n\t" \
                        "s_barrier\n\t" MTA(3) \
                        "ds_read_b64_tr_b16 v[224:225], %[qa]\n\t" \
                        "ds_read_b64_tr_b16 v[226:227], %[qa] offset:32\n\t" \
                        "ds_read_b64_tr_b16 v[228:229], %[qa] offset:256\n\t" \
                        "ds_read_b64_tr_b16 v[230:231], %[qa] offset:288\n\t" \
                        "ds_read_b64_tr_b16 v[232:233], %[ka]\n\t" \
                        "ds_read_b64_tr_b16 v[234:235], %[ka] offset:128\n\t" \
                        "ds_read_b64_tr_b16 v[236:237], %[ka] offset:256\n\t" \
                        "ds_read_b64_tr_b16 v[238:239], %[ka] offset:384\n\t" \
                        "ds_read_b64_tr_b16 v[240:241], %[ka] offset:512\n\t" \
                        "ds_read_b64_tr_b16 v[242:243], %[ka] offset:640\n\t" \
                        "s_waitcnt lgkmcnt(8)\n\t" \
                        "v_smfmac_f32_16x16x64_bf16 " SC0 ", v[224:227], %[k00], %[ix]\n\t" \
                        "ds_read_b64_tr_b16 v[244:245], %[ka] offset:768\n\t" \
                        "v_smfmac_f32_16x16x64_bf16 " SC1 ", v[224:227], %[k10], %[ix]\n\t" \
                        "ds_read_b64_tr_b16 v[246:247], %[ka] offset:896\n\t" \
                        "s_waitcnt lgkmcnt(8)\n\t" \
                        "v_smfmac_f32_16x16x64_bf16 " SC0 ", v[228:231], %[k01], %[ix]\n\t" \
                        "ds_read_b32 v248, %[vz] offset:%[ls]\n\t" \
                        "v_smfmac_f32_16x16x64_bf16 " SC1 ", v[228:231], %[k11], %[ix]\n\t" \
                        "s_waitcnt lgkmcnt(5)\n\t" \
                        "v_smfmac_f32_16x16x64_bf16 " SC2 ", v[224:227], v[232:239], %[ix]\n\t" \
                        "s_waitcnt lgkmcnt(1)\n\t" \
                        "v_smfmac_f32_16x16x64_bf16 " SC2 ", v[228:231], v[240:247], %[ix]\n\t" \
                        "s_waitcnt lgkmcnt(0)\n\t" \
                        MTA(4) "v_readfirstlane_b32 %[vd], v248\n\t" \
                        "v_add_f32 v232, " A0 ", " A2 "\n\t" \
                        "s_nop 0\n\t" \
                        "v_add_f32 v233, " B0 ", " B2 "\n\t" \
                        "v_max_f32 v235, v232, v233\n\t" \
                        "s_cmp_eq_u32 %[vd], %[it]\n\t" \
                        "s_cselect_b32 %[p0], 2.0, %[p0]\n\t" \
                        DPP("v_max_f32_dpp", "v235", "row_ror:8") \
                        "v_add_f32 v234, " C0 ", " C2 "\n\t" \
                        "v_max_f32 v235, v235, v234\n\t" \
                        "s_add_u32 %[p1], %[p0], 0x4000\n\t" \
                        "s_nop 0\n\t" DPP("v_max_f32_dpp", "v235", "row_ror:4") \
                        "s_nop 1\n\t" DPP("v_max_f32_dpp", "v235", "row_ror:2") \
                        "s_nop 1\n\t" DPP("v_max_f32_dpp", "v235", "row_ror:1") \
                        "v_mul_f32 v235, 0xbe0293ee, v235\n\t" \
                        "v_fmamk_f32 v232, v232, 0x3e0293ee, v235\n\t" \
                        "v_fmamk_f32 v233, v233, 0x3e0293ee, v235\n\t" \
                        "v_exp_f32 v232, v232\n\t" \
                        "v_exp_f32 v233, v233\n\t" \
                        "v_fmamk_f32 v234, v234, 0x3e0293ee, v235\n\t" \
                        "v_add_f32 v235, v232, v233\n\t" \
                        "v_exp_f32 v234, v234\n\t" \
                        "s_nop 0\n\t" DPP("v_add_f32_dpp", "v235", "row_ror:8") \
                        "v_add_f32 v235, v235, v234\n\t" \
                        "v_mov_b32_dpp v236, v234 quad_perm:[1,1,3,3] row_mask:0xf bank_mask:0xf\n\t" \
                        "s_nop 0\n\t" DPP("v_add_f32_dpp", "v235", "row_ror:4") \
                        "s_nop 1\n\t" DPP("v_add_f32_dpp", "v235", "row_ror:2") \
                        "s_nop 1\n\t" DPP("v_add_f32_dpp", "v235", "row_ror:1") \
                        "v_rcp_f32 v235, v235\n\t" \
                        "s_nop 0\n\t" \
                        "v_mul_f32_e64 v232, v232, v235 clamp\n\t" \
                        "v_mul_f32_e64 v233, v233, v235 clamp\n\t" \
                        "v_cvt_pk_bf16_f32 v232, v232, v233\n\t" \
                        MTA(5) "buffer_store_dword v232, %[pa], %[rw], %[p0] offen sc1\n\t" \
                        "v_mul_f32_e64 v234, v234, v235 clamp\n\t" \
                        "v_mul_f32_e64 v236, v236, v235 clamp\n\t" \
                        "buffer_store_dword v232, %[pa], %[rw], %[p1] offen sc1\n\t" \
                        "v_cvt_pk_bf16_f32 v234, v234, v236\n\t" \
                        "buffer_store_dword v234, %[pm], %[rw], %[p0] offen sc1\n\t" \
                        "s_setprio 0"
#define TAILIO : [vd] "=&s"(vd), [p0] "+s"(ps), [p1] "=&s"(ps1) \
                        : [qa] "v"(qa), [ka] "v"(ka), [vz] "v"(0u), [ls] "n"(L_S), [ix] "v"(sidx), [it] "s"(it), [pa] "v"(pa0), [pm] "v"(pa2), [rw] "s"(rwin), \
                          [k00] "v"(kx[0][0]), [k01] "v"(kx[0][1]), [k10] "v"(kx[1][0]), [k11] "v"(kx[1][1]) \
                        : "v212", "v213", "v214", "v215", "v216", "v217", "v218", "v219", "v220", "v221", "v222", "v223", "v224", "v225", "v226", "v227", "v228", "v229", "v230", "v231", "v232", \
                          "v233", "v234", "v235", "v236", "v237", "v238", "v239", "v240", "v241", "v242", "v243", "v244", "v245", "v246", "v247", "v248", "scc", "memory"
                    unsigned ps = pb, ps1;
                    if (R == 0) asm volatile(TAIL("v212", "v214", "v216", "v218", "v220", "v222") TAILIO);
                    else asm volatile(TAIL("v213", "v215", "v217", "v219", "v221", "v223") TAILIO);
                    redo = vd == it;
                    RT(4);
                    MT(6);
                } else {
                    MT(3);
                    if (TK) { __builtin_amdgcn_s_setprio(TKP); if (TKS) __builtin_amdgcn_s_sleep(TKS); }
                    redo = __builtin_amdgcn_readfirstlane(*(volatile LAS unsigned*)(smem + L_S)) == it;
                    if (redo) FC(fc0);
                }
            } while (__builtin_expect(redo, 0));
            if (s < 8) SST(8 + s);
            if (s >= 56) SST(s - 40);
            if (C + R == 1) {
                if (s > 0 && s + 1 < NSEG) {
                    __builtin_amdgcn_s_sleep(DDMA);
                    unsigned l2 = fresh_lane(), s1;
                    asm volatile("v_mov_b32 %0, -1" : "=v"(s1));
                    const unsigned base = W_P + (unsigned)((s - 1) & 3) * 10u * PAIR + nh;
                    asm volatile("s_waitcnt vmcnt(3)" ::: "memory");
                    st128(((u32x4){s1, s1, s1, s1}), rwin, (l2 >> 5) * PAIR + (l2 & 31u) * 16u, base + (unsigned)(R * 4 + 2 * (hh >> 4)) * PAIR + (unsigned)(hh & 15) * 512u, 16);
                    if (R) __builtin_amdgcn_raw_buffer_store_b64((u32x2){s1, s1}, rwin, (l2 >> 5) * PAIR + (l2 & 16u) * 256u + (l2 & 8u) * 16u + (l2 & 7u) * 8u, base + 8u * PAIR + (unsigned)((hh >> 1) * 256 + (hh & 1) * 64), 16);
                    else __builtin_amdgcn_raw_buffer_store_b32(s1, rwin, two(), W_T + (unsigned)((s + 2) & 3) * 2u * PAIR + (unsigned)hh * 128u, 16);
                }
            } else if (TK) {
                if (s + 1 < NSEG) sched2(tk, s, sf);
            } else if (C == 2 && w == 4) {
                if (s + 2 < NSEG) {
                    qrdy = max(qrdy, seg_done(MXQ, __builtin_amdgcn_readlane(qf_, 0), __builtin_amdgcn_readlane(qf_, 1)));
                    if (__builtin_expect(qrdy < s + 2, 0)) for (unsigned spins = 0;;) {
                        asm volatile("" ::: "memory");
                        const int f = (int)__builtin_amdgcn_raw_buffer_load_b32(rwin, (unsigned)(lane & 1) * 64u, W_FQ + (unsigned)hh * 128u, 16);
                        qrdy = seg_done(MXQ, __builtin_amdgcn_readlane(f, 0), __builtin_amdgcn_readlane(f, 1));
                        if (qrdy >= s + 2) break;
                        FC(fc2);
                        if (spin_fail(spins, a.ctl)) break;
                        __builtin_amdgcn_s_sleep(2);
                    }
                    const unsigned so = O_QK + (unsigned)(((s + 2) * H_ + hh) * 16384), lo = (unsigned)(L_TOK + (tbo ? tbo - 16384 : 32768)), l2 = fresh_lane();
#pragma unroll
                    for (int i = 0; i < 4; ++i) dma4k(rws, l2 * 16u, so + i * 4096u, lo + i * 4096u);
                    if (qrdy < min(s + 5, NSEG - 1))
                        asm volatile("buffer_load_dword %0, %1, %2, %3 offen sc1" : "+v"(qf_) : "v"((l2 & 1u) * 64u), "s"(rwin), "s"(W_FQ + (unsigned)hh * 128u) : "memory");
                }
            } else {
                asm volatile("s_waitcnt vmcnt(3)" ::: "memory");
                if (s > TAILS) __builtin_amdgcn_raw_buffer_store_b32((unsigned)s - 1u, rwin, 0u, W_FPT + (unsigned)(hh * 128 + (w & 3) * 4), 16);
                if (C == 2 && s + 1 < NSEG) __builtin_amdgcn_s_sleep(DPT);
                const float scale = 0.08838834764831845f, l2e = 1.44269504088896340736f * (NORND ? scale : 1.f);
                const int tq = (w >> 1) & 1, qh = w & 1;
                f32x4 sc[3] = {z4, z4, z4};
                u32x4 kt[2][4], qf[4], kf[4];
                const unsigned l2 = fresh_lane(), kto = ((l2 & 8u) * 512u + (l2 & 51u) * 16u + (l2 & 4u) * 32u) ^ ((l2 & 16u) * 5u), qto = ((l2 & 55u) * 16u + qh * 128u) ^ ((l2 & 16u) * 8u);
#pragma unroll
                for (int ks = 0; ks < 4; ++ks) {
                    qf[ks] = *(const u32x4*)(tb + 8192 + (tq * 4 + ks) * 1024 + qto);
                    kt[0][ks] = *(const u32x4*)(tb + ks * 1024 + kto);
                    kt[1][ks] = *(const u32x4*)(tb + ks * 1024 + (kto ^ 64u));
                    SB();
                }
#pragma unroll
                for (int ks = 0; ks < 4; ++ks) {
                    sc[0] = mfma32(kt[0][ks], qf[ks], sc[0]); PIN1(sc[0]);
                    kf[ks] = ldtr2(ka + ks * 256, ka + ks * 256 + 128);
                    SB();
                    sc[1] = mfma32(kt[1][ks], qf[ks], sc[1]); PIN1(sc[1]);
                }
#pragma unroll
                for (int ks = 0; ks < 4; ++ks) sc[2] = mfma32(kf[ks], qf[ks], sc[2]);
                const bool hi = (l2 & 8u) != 0u;
                float v[6];
#pragma unroll
                for (int r = 0; r < 4; ++r) v[r] = hi ? sc[1][r] : sc[0][r];
                v[4] = hi ? sc[2][2] : sc[2][0]; v[5] = hi ? sc[2][3] : sc[2][1];
#pragma unroll
                for (int r = 0; r < 6; r += 2) {
                    if (NORND) break;
                    const unsigned b0 = pack2(v[r], v[r + 1]);
                    const unsigned b1 = pack2(lo2f(b0) * scale, hi2f(b0) * scale);
                    v[r] = lo2f(b1); v[r + 1] = hi2f(b1);
                }
                const int lim = 8 * (w & 3) + (int)(l2 & 7u) - 8 * (int)(l2 >> 4) - (int)((l2 & 8u) >> 1);
#pragma unroll
                for (int r = 0; r < 4; ++r) if (r > lim) v[r] = NORND ? -1000000000.0f / scale : bfr(-1000000000.0f);
                float mx = fmx(fmx(fmx(fmx(fmx(v[0], v[1]), v[2]), v[3]), v[4]), v[5]);
                mx = xmax3216(fmx(mx, ror8f(mx)));
                const float mb = -mx * l2e;
#pragma unroll
                for (int r = 0; r < 6; ++r) v[r] = __builtin_amdgcn_exp2f(fmaf(v[r], l2e, mb));
                float sum = fmaf(v[4] + v[5], 0.5f, (v[0] + v[1]) + (v[2] + v[3]));
                sum = xsum3216(sum + ror8f(sum));
                const float inv = __builtin_amdgcn_rcpf(sum);
                const unsigned p0 = pack2(v[0] * inv, v[1] * inv), p1 = nosent1(pack2(v[2] * inv, v[3] * inv)), p2 = nosent1(pack2(v[4] * inv, v[5] * inv));
                const unsigned base = s + 1 < NSEG ? O_PT + (unsigned)(s * PTSEG) * 2u : W_PL;
                const __amdgpu_buffer_rsrc_t rp = s + 1 < NSEG ? rws : rwin;
                const unsigned oob = (l2 & 8u) << 27;
                st128(((u32x4){p0, p1, dppu<0x128>(p0), dppu<0x128>(p1)}), rp, (l2 * 16u) | oob, base + (unsigned)((hh * 2 + tq) * 1024 + qh * 128), 16);
                __builtin_amdgcn_raw_buffer_store_b64((u32x2){p2, dppu<0x128>(p2)}, rp, ((l2 & 7u) * 16u + (l2 >> 4) * 8u) | oob | (l2 & 32u) << 25, base + 65536u + (unsigned)((tq * 32 + hh) * 256 + qh * 128), 16);
            }
            tbo = tbo == 32768 ? 0 : tbo + 16384;
            if (C == 2 && __builtin_expect(budget <= 0, 0)) __hip_atomic_store(a.ctl, 1u, __ATOMIC_RELAXED, __HIP_MEMORY_SCOPE_AGENT);
            RT(5);
            RTS();
            MT(7);
            MTS();
        }
        };
        if (qt) {} else if (w == 0) steps(IC<0>()); else if (w == 1) steps(IC<1>()); else if (w == 2) steps(IC<2>()); else if (w == 3) steps(IC<3>()); else if (w < 7) steps(IC<4>()); else steps(IC<5>());
    } else if (bid >= 96 && bid < NREC) {
        LANES();
        const int o = bid - 96, hg = w & 3;
        const unsigned ztoff = (unsigned)((o * H_ + 8 * hg) * 64 + lane) * 16u;
        const unsigned zoff = zpair(o / 10, o % 10) + (unsigned)(lane * 16);
        const unsigned poff = (4u * (mc >> 1) + 2u * (hg >> 1) + (mc & 1u)) * PAIR + nh + (unsigned)((hg & 1) * 4096 + (fq >> 1) * 512 + (fr >> 2) * 128 + (fr & 1) * 64 + (fq & 1) * 32 + (fr & 2) * 8);
        const unsigned pmoff = (8u + (mc & 1u)) * PAIR + nh + (mc >> 1) * 4096u + (unsigned)(fq * 256 + (fr >> 3) * 128 + (fr & 2) * 32 + (fr & 4) * 8 + (fr & 1) * 16);
        const unsigned fzo = (unsigned)(8 * hg + (lane & 7)) * 128u + (unsigned)(o / 10) * 4u + (lane & 8) * 8u;
        const unsigned moff = (o < 32 ? (unsigned)o * 256u : 4u * PAIR + (unsigned)(o - 32) * 256u) + nh + (unsigned)(lane * 4);
        const unsigned mc2 = o < 32 ? 2u * PAIR : 2048u;
        char* op = smem + 8192;
        SST(2);
        if (w == 7) clk_init(ck);
        wait_until(tstart + DO0);
        if (w < 4) {
            u32x8 zf[4];
            int zr = -1;
            unsigned zb = W_ZM, zso = O_Z;
            const unsigned ra = (o < 32 ? (unsigned)fq * PAIR + (unsigned)o * 256u : (4u + (fq & 1)) * PAIR + (unsigned)(fq >> 1) * 2048u + (unsigned)(o - 32) * 256u) + nh + (unsigned)(fr * 16);
            auto zt_miss = [&](int f, int s) { return __any(f < s && ((lane >> 3) & 1) == (int)((MXV >> s) & 1)); };
            for (int s = 0;; ++s) {

                const unsigned pb = W_P + (unsigned)(s & 3) * 10u * PAIR;
                if (__builtin_expect(zr < s, 0)) for (unsigned spins = 0;;) {
                    asm volatile("" ::: "memory");
                    if (!zt_miss((int)__builtin_amdgcn_raw_buffer_load_b32(rwin, fzo, W_FZ, 16), s)) break;
                    FC(fc2);
                    if (spin_fail(spins, a.ctl)) break;
                    __builtin_amdgcn_s_sleep(ZPS);
                }
                if (s == 0) SST(1);
                asm volatile(
                    "buffer_load_dwordx4 v[160:163], %[zt], %[rs], %[so] offen sc1\n\t"
                    "buffer_load_dwordx4 v[164:167], %[zt], %[rs], %[so] offen offset:1024 sc1\n\t"
                    "buffer_load_dwordx4 v[168:171], %[zt], %[rs], %[so] offen offset:2048 sc1\n\t"
                    "buffer_load_dwordx4 v[172:175], %[zt], %[rs], %[so] offen offset:3072 sc1\n\t"
                    "buffer_load_dwordx4 v[176:179], %[zt], %[rs], %[s4] offen sc1\n\t"
                    "buffer_load_dwordx4 v[180:183], %[zt], %[rs], %[s4] offen offset:1024 sc1\n\t"
                    "buffer_load_dwordx4 v[184:187], %[zt], %[rs], %[s4] offen offset:2048 sc1\n\t"
                    "buffer_load_dwordx4 v[188:191], %[zt], %[rs], %[s4] offen offset:3072 sc1"
                    : "=&{v[160:167]}"(zf[0]), "=&{v[168:175]}"(zf[1]), "=&{v[176:183]}"(zf[2]), "=&{v[184:191]}"(zf[3])
                    : [zt] "v"(ztoff), [rs] "s"(rws), [so] "s"(zso), [s4] "s"(zso + 4096u) : "memory");
                zso = O_Z + (unsigned)((s + 1) * ZTSEG) * 2u;
                if (__builtin_expect(budget <= 0, 0)) __hip_atomic_store(a.ctl, 1u, __ATOMIC_RELAXED, __HIP_MEMORY_SCOPE_AGENT);
                RT(6);
                if (s + 1 == NSEG) break;
                const unsigned spa = (unsigned)((s & 1) * 2048 + (w < 3 ? w * 512 : 0) + lane * 8);
#define OST(r)
                unsigned long long t0_, t1_, t2_, t3_, t4_ = 0, bad;
                int tries = budget;
                unsigned po = poff, pm = pmoff, x0 = 4u * PAIR, x1 = 4096u;
                const unsigned mb = W_MEM + (unsigned)((s + 1) & 3) * 6u * PAIR;
                if (w < 3) asm volatile(
                    "s_barrier\n\t"
                    OST("%[t0]")
                    "v_mov_b64 v[96:97], 0\n\t"
                    "v_mov_b64 v[98:99], 0\n\t"
                    "v_mov_b32 v100, 0\n\t"
                    "s_barrier\n\t"
                    "1:\n\t"
                    "buffer_load_dwordx4 v[208:211], %[po], %[rw], %[pb] offen sc1\n\t"
                    "buffer_load_dwordx4 v[212:215], %[po], %[rw], %[pb] offen offset:1024 sc1\n\t"
                    "buffer_load_dwordx4 v[216:219], %[po], %[rw], %[pb] offen offset:2048 sc1\n\t"
                    "buffer_load_dwordx4 v[220:223], %[po], %[rw], %[pb] offen offset:3072 sc1\n\t"
                    OST("%[t1]")
                    "s_waitcnt vmcnt(3)\n\t"
                    OST("%[t3]")
                    "v_smfmac_f32_16x16x64_bf16 v[96:99], v[208:211], v[160:167], %[ix]\n\t"
                    "v_max3_u32 v100, v100, v208, v209\n\t"
                    "v_max3_u32 v100, v100, v210, v211\n\t"
                    "s_waitcnt vmcnt(2)\n\t"
                    "v_smfmac_f32_16x16x64_bf16 v[96:99], v[212:215], v[168:175], %[ix]\n\t"
                    "v_max3_u32 v100, v100, v212, v213\n\t"
                    "v_max3_u32 v100, v100, v214, v215\n\t"
                    "s_waitcnt vmcnt(1)\n\t"
                    "v_smfmac_f32_16x16x64_bf16 v[96:99], v[216:219], v[176:183], %[ix]\n\t"
                    "v_max3_u32 v100, v100, v216, v217\n\t"
                    "v_max3_u32 v100, v100, v218, v219\n\t"
                    "s_waitcnt vmcnt(0)\n\t"
                    "v_smfmac_f32_16x16x64_bf16 v[96:99], v[220:223], v[184:191], %[ix]\n\t"
                    "v_max3_u32 v100, v100, v220, v221\n\t"
                    "v_max3_u32 v100, v100, v222, v223\n\t"
                    "v_cmp_eq_u32 vcc, -1, v100\n\t"
                    OST("%[t2]")
                    "s_nop 4\n\t"
                    "v_pk_add_f32 v[96:97], v[96:97], v[98:99]\n\t"
                    "ds_write_b64 %[sp], v[96:97]\n\t"
                    "s_cbranch_vccz 2f\n\t"
                    "s_sub_i32 %[tr], %[tr], 1\n\t"
                    "s_cmp_lt_i32 %[tr], 1\n\t"
                    "s_cbranch_scc1 2f\n\t"
                    "v_xor_b32 %[po], %[x0], %[po]\n\t"
                    "s_xor_b32 %[x0], %[x0], 0x14000\n\t"
                    "s_waitcnt lgkmcnt(0)\n\t"
                    "v_mov_b64 v[96:97], 0\n\t"
                    "v_mov_b64 v[98:99], 0\n\t"
                    "v_mov_b32 v100, 0\n\t"
                    "s_branch 1b\n\t"
                    "2:\n\t"
                    "s_waitcnt lgkmcnt(0)\n\t"
                    "s_barrier\n\t"
                    "s_sleep 3\n\t"
                    : [tr] "+s"(tries), [po] "+v"(po), [x0] "+s"(x0), [t0] "=&s"(t0_), [t1] "=&s"(t1_), [t2] "=&s"(t2_), [t3] "=&s"(t3_)
                    : [sp] "v"(spa), [ix] "v"(sidx), [rw] "s"(rwin), [pb] "s"(pb), "{v[160:167]}"(zf[0]), "{v[168:175]}"(zf[1]), "{v[176:183]}"(zf[2]), "{v[184:191]}"(zf[3])
                    : "v96", "v97", "v98", "v99", "v100", "v208", "v209", "v210", "v211", "v212", "v213", "v214", "v215", "v216", "v217", "v218", "v219", "v220", "v221", "v222", "v223", "vcc", "scc", "memory");
                else asm volatile(
                    "s_barrier\n\t"
                    "buffer_load_dwordx4 v[128:131], %[zo], %[rw], %[zb] offen sc1\n\t"
                    "buffer_load_dwordx4 v[132:135], %[zo], %[rw], %[zb] offen offset:1024 sc1\n\t"
                    "buffer_load_dwordx4 v[136:139], %[zo], %[rw], %[zb] offen offset:2048 sc1\n\t"
                    "buffer_load_dwordx4 v[140:143], %[zo], %[rw], %[zb] offen offset:3072 sc1\n\t"
                    "buffer_load_dwordx4 v[144:147], %[zo], %[rw], %[z4] offen sc1\n\t"
                    "buffer_load_dwordx4 v[148:151], %[zo], %[rw], %[z4] offen offset:1024 sc1\n\t"
                    "buffer_load_dwordx4 v[152:155], %[zo], %[rw], %[z4] offen offset:2048 sc1\n\t"
                    "buffer_load_dwordx4 v[156:159], %[zo], %[rw], %[z4] offen offset:3072 sc1\n\t"
                    OST("%[t0]")
                    "v_mov_b64 v[96:97], 0\n\t"
                    "v_mov_b64 v[98:99], 0\n\t"
                    "v_mov_b32 v100, 0\n\t"
                    "s_barrier\n\t"
                    "s_sleep %[fd]\n\t"
                    "buffer_load_dwordx4 v[208:211], %[po], %[rw], %[pb] offen sc1\n\t"
                    "buffer_load_dwordx4 v[212:215], %[po], %[rw], %[pb] offen offset:1024 sc1\n\t"
                    "buffer_load_dwordx4 v[216:219], %[po], %[rw], %[pb] offen offset:2048 sc1\n\t"
                    "buffer_load_dwordx4 v[220:223], %[po], %[rw], %[pb] offen offset:3072 sc1\n\t"
                    "buffer_load_dwordx4 v[224:227], %[pm], %[rw], %[pb] offen sc1\n\t"
                    "buffer_load_dwordx4 v[228:231], %[pm], %[rw], %[pb] offen offset:1024 sc1\n\t"
                    "buffer_load_dwordx4 v[232:235], %[pm], %[rw], %[pb] offen offset:2048 sc1\n\t"
                    "buffer_load_dwordx4 v[236:239], %[pm], %[rw], %[pb] offen offset:3072 sc1\n\t"
                    OST("%[t1]")
                    "s_waitcnt vmcnt(8)\n\t"
                    "v_max3_u32 v101, v129, v131, v133\n\t"
                    "v_max3_u32 v101, v101, v135, v137\n\t"
                    "v_max3_u32 v101, v101, v139, v141\n\t"
                    "v_max3_u32 v101, v101, v143, v145\n\t"
                    "v_max3_u32 v101, v101, v147, v149\n\t"
                    "v_max3_u32 v101, v101, v151, v153\n\t"
                    "v_max3_u32 v101, v101, v155, v157\n\t"
                    "v_max_u32 v101, v101, v159\n\t"
                    "v_cmp_eq_u32 vcc, -1, v101\n\t"
                    "s_cbranch_vccz 3f\n\t"
                    "buffer_load_dwordx4 v[128:131], %[zo], %[rw], %[zb] offen sc1\n\t"
                    "buffer_load_dwordx4 v[132:135], %[zo], %[rw], %[zb] offen offset:1024 sc1\n\t"
                    "buffer_load_dwordx4 v[136:139], %[zo], %[rw], %[zb] offen offset:2048 sc1\n\t"
                    "buffer_load_dwordx4 v[140:143], %[zo], %[rw], %[zb] offen offset:3072 sc1\n\t"
                    "buffer_load_dwordx4 v[144:147], %[zo], %[rw], %[z4] offen sc1\n\t"
                    "buffer_load_dwordx4 v[148:151], %[zo], %[rw], %[z4] offen offset:1024 sc1\n\t"
                    "buffer_load_dwordx4 v[152:155], %[zo], %[rw], %[z4] offen offset:2048 sc1\n\t"
                    "buffer_load_dwordx4 v[156:159], %[zo], %[rw], %[z4] offen offset:3072 sc1\n\t"
                    "s_waitcnt vmcnt(0)\n\t"
                    "v_max3_u32 v101, v129, v131, v133\n\t"
                    "v_max3_u32 v101, v101, v135, v137\n\t"
                    "v_max3_u32 v101, v101, v139, v141\n\t"
                    "v_max3_u32 v101, v101, v143, v145\n\t"
                    "v_max3_u32 v101, v101, v147, v149\n\t"
                    "v_max3_u32 v101, v101, v151, v153\n\t"
                    "v_max3_u32 v101, v101, v155, v157\n\t"
                    "v_max_u32 v101, v101, v159\n\t"
                    "v_mov_b32 v100, v101\n\t"
                    "3:\n\t"
                    "v_permlane16_swap_b32 v128, v129\n\t"
                    "v_permlane16_swap_b32 v130, v131\n\t"
                    "v_permlane16_swap_b32 v132, v133\n\t"
                    "v_permlane16_swap_b32 v134, v135\n\t"
                    "v_permlane16_swap_b32 v136, v137\n\t"
                    "v_permlane16_swap_b32 v138, v139\n\t"
                    "v_permlane16_swap_b32 v140, v141\n\t"
                    "v_permlane16_swap_b32 v142, v143\n\t"
                    "v_permlane16_swap_b32 v144, v145\n\t"
                    "v_permlane16_swap_b32 v146, v147\n\t"
                    "v_permlane16_swap_b32 v148, v149\n\t"
                    "v_permlane16_swap_b32 v150, v151\n\t"
                    "v_permlane16_swap_b32 v152, v153\n\t"
                    "v_permlane16_swap_b32 v154, v155\n\t"
                    "v_permlane16_swap_b32 v156, v157\n\t"
                    "v_permlane16_swap_b32 v158, v159\n\t"
                    "s_mov_b32 vcc_lo, 0x00ff00ff\n\t"
                    "s_mov_b32 vcc_hi, 0x00ff00ff\n\t"
                    "s_waitcnt vmcnt(7)\n\t"
                    OST("%[t3]")
                    "v_smfmac_f32_16x16x64_bf16 v[96:99], v[208:211], v[160:167], %[ix]\n\t"
                    "v_max3_u32 v100, v100, v208, v209\n\t"
                    "v_max3_u32 v100, v100, v210, v211\n\t"
                    "s_waitcnt vmcnt(6)\n\t"
                    "v_smfmac_f32_16x16x64_bf16 v[96:99], v[212:215], v[168:175], %[ix]\n\t"
                    "v_max3_u32 v100, v100, v212, v213\n\t"
                    "v_max3_u32 v100, v100, v214, v215\n\t"
                    "s_waitcnt vmcnt(5)\n\t"
                    "v_smfmac_f32_16x16x64_bf16 v[96:99], v[216:219], v[176:183], %[ix]\n\t"
                    "v_max3_u32 v100, v100, v216, v217\n\t"
                    "v_max3_u32 v100, v100, v218, v219\n\t"
                    "s_waitcnt vmcnt(4)\n\t"
                    "v_smfmac_f32_16x16x64_bf16 v[96:99], v[220:223], v[184:191], %[ix]\n\t"
                    "v_max3_u32 v100, v100, v220, v221\n\t"
                    "v_max3_u32 v100, v100, v222, v223\n\t"
                    "s_waitcnt vmcnt(3)\n\t"
                    "v_smfmac_f32_16x16x64_bf16 v[96:99], v[224:227], v[128:135], %[ix]\n\t"
                    "v_max3_u32 v100, v100, v224, v225\n\t"
                    "v_max3_u32 v100, v100, v226, v227\n\t"
                    "s_waitcnt vmcnt(2)\n\t"
                    "v_smfmac_f32_16x16x64_bf16 v[96:99], v[228:231], v[136:143], %[ix]\n\t"
                    "s_barrier\n\t"
                    "ds_read_b64 v[102:103], %[sp]\n\t"
                    "ds_read_b64 v[104:105], %[sp] offset:512\n\t"
                    "ds_read_b64 v[106:107], %[sp] offset:1024\n\t"
                    "v_max3_u32 v100, v100, v228, v229\n\t"
                    "v_max3_u32 v100, v100, v230, v231\n\t"
                    "s_waitcnt vmcnt(1)\n\t"
                    "v_smfmac_f32_16x16x64_bf16 v[96:99], v[232:235], v[144:151], %[ix]\n\t"
                    "v_max3_u32 v100, v100, v232, v233\n\t"
                    "v_max3_u32 v100, v100, v234, v235\n\t"
                    "s_waitcnt vmcnt(0)\n\t"
                    "v_smfmac_f32_16x16x64_bf16 v[96:99], v[236:239], v[152:159], %[ix]\n\t"
                    "v_max3_u32 v100, v100, v236, v237\n\t"
                    "v_max3_u32 v100, v100, v238, v239\n\t"
                    OST("%[t2]")
                    "v_cmp_eq_u32 %[bad], -1, v100\n\t"
                    "s_waitcnt lgkmcnt(0)\n\t"
                    "v_add_f32 v102, v102, v104\n\t"
                    "v_add_f32 v103, v103, v105\n\t"
                    "v_add_f32 v102, v102, v106\n\t"
                    "v_add_f32 v103, v103, v107\n\t"
                    "v_add_f32 v96, v96, v102\n\t"
                    "v_add_f32 v97, v97, v103\n\t"
                    "v_add_f32 v96, v96, v98\n\t"
                    "v_add_f32 v97, v97, v99\n\t"
                    "s_cmp_lg_u64 %[bad], 0\n\t"
                    "s_cbranch_scc1 4f\n\t"
                    "2:\n\t"
                    "v_cndmask_b32_dpp v98, v97, v96, vcc row_ror:8 row_mask:0xf bank_mask:0xf\n\t"
                    "s_not_b64 vcc, vcc\n\t"
                    "v_cndmask_b32_dpp v99, v96, v97, vcc row_ror:8 row_mask:0xf bank_mask:0xf\n\t"
                    "v_cvt_pk_bf16_f32 v98, v98, v99\n\t"
                    "v_min_u32 v98, -2, v98\n\t"
                    "buffer_store_dword v98, %[mo], %[rw], %[m0] offen sc1\n\t"
                    "buffer_store_dword v98, %[mo], %[rw], %[m1] offen sc1\n\t"
                    "buffer_store_dword v98, %[mo], %[rw], %[m2] offen sc1\n\t"
                    "buffer_store_dword v98, %[mo], %[rw], %[m3] offen sc1\n\t"
                    OST("%[t4]")
                    "s_branch 5f\n\t"
                    "4:\n\t"
                    "s_sub_i32 %[tr], %[tr], 1\n\t"
                    "s_cmp_lt_i32 %[tr], 1\n\t"
                    "s_cbranch_scc1 2b\n\t"
                    "v_xor_b32 %[po], %[x0], %[po]\n\t"
                    "s_xor_b32 %[x0], %[x0], 0x14000\n\t"
                    "v_xor_b32 %[pm], %[x1], %[pm]\n\t"
                    "s_xor_b32 %[x1], %[x1], 0x5000\n\t"
                    "buffer_load_dwordx4 v[128:131], %[zo], %[rw], %[zb] offen sc1\n\t"
                    "buffer_load_dwordx4 v[132:135], %[zo], %[rw], %[zb] offen offset:1024 sc1\n\t"
                    "buffer_load_dwordx4 v[136:139], %[zo], %[rw], %[zb] offen offset:2048 sc1\n\t"
                    "buffer_load_dwordx4 v[140:143], %[zo], %[rw], %[zb] offen offset:3072 sc1\n\t"
                    "buffer_load_dwordx4 v[144:147], %[zo], %[rw], %[z4] offen sc1\n\t"
                    "buffer_load_dwordx4 v[148:151], %[zo], %[rw], %[z4] offen offset:1024 sc1\n\t"
                    "buffer_load_dwordx4 v[152:155], %[zo], %[rw], %[z4] offen offset:2048 sc1\n\t"
                    "buffer_load_dwordx4 v[156:159], %[zo], %[rw], %[z4] offen offset:3072 sc1\n\t"
                    "buffer_load_dwordx4 v[208:211], %[po], %[rw], %[pb] offen sc1\n\t"
                    "buffer_load_dwordx4 v[212:215], %[po], %[rw], %[pb] offen offset:1024 sc1\n\t"
                    "buffer_load_dwordx4 v[216:219], %[po], %[rw], %[pb] offen offset:2048 sc1\n\t"
                    "buffer_load_dwordx4 v[220:223], %[po], %[rw], %[pb] offen offset:3072 sc1\n\t"
                    "buffer_load_dwordx4 v[224:227], %[pm], %[rw], %[pb] offen sc1\n\t"
                    "buffer_load_dwordx4 v[228:231], %[pm], %[rw], %[pb] offen offset:1024 sc1\n\t"
                    "buffer_load_dwordx4 v[232:235], %[pm], %[rw], %[pb] offen offset:2048 sc1\n\t"
                    "buffer_load_dwordx4 v[236:239], %[pm], %[rw], %[pb] offen offset:3072 sc1\n\t"
                    "v_mov_b64 v[96:97], 0\n\t"
                    "v_mov_b64 v[98:99], 0\n\t"
                    "v_mov_b32 v100, 0\n\t"
                    "s_waitcnt vmcnt(0)\n\t"
                    "v_max3_u32 v101, v129, v131, v133\n\t"
                    "v_max3_u32 v101, v101, v135, v137\n\t"
                    "v_max3_u32 v101, v101, v139, v141\n\t"
                    "v_max3_u32 v101, v101, v143, v145\n\t"
                    "v_max3_u32 v101, v101, v147, v149\n\t"
                    "v_max3_u32 v101, v101, v151, v153\n\t"
                    "v_max3_u32 v101, v101, v155, v157\n\t"
                    "v_max_u32 v101, v101, v159\n\t"
                    "v_mov_b32 v100, v101\n\t"
                    "v_permlane16_swap_b32 v128, v129\n\t"
                    "v_permlane16_swap_b32 v130, v131\n\t"
                    "v_permlane16_swap_b32 v132, v133\n\t"
                    "v_permlane16_swap_b32 v134, v135\n\t"
                    "v_permlane16_swap_b32 v136, v137\n\t"
                    "v_permlane16_swap_b32 v138, v139\n\t"
                    "v_permlane16_swap_b32 v140, v141\n\t"
                    "v_permlane16_swap_b32 v142, v143\n\t"
                    "v_permlane16_swap_b32 v144, v145\n\t"
                    "v_permlane16_swap_b32 v146, v147\n\t"
                    "v_permlane16_swap_b32 v148, v149\n\t"
                    "v_permlane16_swap_b32 v150, v151\n\t"
                    "v_permlane16_swap_b32 v152, v153\n\t"
                    "v_permlane16_swap_b32 v154, v155\n\t"
                    "v_permlane16_swap_b32 v156, v157\n\t"
                    "v_permlane16_swap_b32 v158, v159\n\t"
                    "s_nop 1\n\t"
                    "v_smfmac_f32_16x16x64_bf16 v[96:99], v[208:211], v[160:167], %[ix]\n\t"
                    "v_max3_u32 v100, v100, v208, v209\n\t"
                    "v_max3_u32 v100, v100, v210, v211\n\t"
                    "v_smfmac_f32_16x16x64_bf16 v[96:99], v[212:215], v[168:175], %[ix]\n\t"
                    "v_max3_u32 v100, v100, v212, v213\n\t"
                    "v_max3_u32 v100, v100, v214, v215\n\t"
                    "v_smfmac_f32_16x16x64_bf16 v[96:99], v[216:219], v[176:183], %[ix]\n\t"
                    "v_max3_u32 v100, v100, v216, v217\n\t"
                    "v_max3_u32 v100, v100, v218, v219\n\t"
                    "v_smfmac_f32_16x16x64_bf16 v[96:99], v[220:223], v[184:191], %[ix]\n\t"
                    "v_max3_u32 v100, v100, v220, v221\n\t"
                    "v_max3_u32 v100, v100, v222, v223\n\t"
                    "v_smfmac_f32_16x16x64_bf16 v[96:99], v[224:227], v[128:135], %[ix]\n\t"
                    "v_max3_u32 v100, v100, v224, v225\n\t"
                    "v_max3_u32 v100, v100, v226, v227\n\t"
                    "v_smfmac_f32_16x16x64_bf16 v[96:99], v[228:231], v[136:143], %[ix]\n\t"
                    "v_max3_u32 v100, v100, v228, v229\n\t"
                    "v_max3_u32 v100, v100, v230, v231\n\t"
                    "v_smfmac_f32_16x16x64_bf16 v[96:99], v[232:235], v[144:151], %[ix]\n\t"
                    "v_max3_u32 v100, v100, v232, v233\n\t"
                    "v_max3_u32 v100, v100, v234, v235\n\t"
                    "v_smfmac_f32_16x16x64_bf16 v[96:99], v[236:239], v[152:159], %[ix]\n\t"
                    "v_max3_u32 v100, v100, v236, v237\n\t"
                    "v_max3_u32 v100, v100, v238, v239\n\t"
                    "v_cmp_eq_u32 %[bad], -1, v100\n\t"
                    "s_nop 5\n\t"
                    "v_add_f32 v96, v96, v102\n\t"
                    "v_add_f32 v97, v97, v103\n\t"
                    "v_add_f32 v96, v96, v98\n\t"
                    "v_add_f32 v97, v97, v99\n\t"
                    "s_cmp_lg_u64 %[bad], 0\n\t"
                    "s_cbranch_scc1 4b\n\t"
                    "s_branch 2b\n\t"
                    "5:\n\t"
                    : [tr] "+s"(tries), [po] "+v"(po), [pm] "+v"(pm), [x0] "+s"(x0), [x1] "+s"(x1), [bad] "=&s"(bad), [t0] "=&s"(t0_), [t1] "=&s"(t1_), [t2] "=&s"(t2_), [t3] "=&s"(t3_), [t4] "=&s"(t4_)
                    : [zo] "v"(zoff), [sp] "v"(spa), [ix] "v"(sidx), [rw] "s"(rwin), [zb] "s"(zb), [z4] "s"(zb + 4096u), [pb] "s"(pb), [mo] "v"(moff), [fd] "n"(FDL),
                      [m0] "s"(mb), [m1] "s"(mb + PAIR), [m2] "s"(mb + mc2), [m3] "s"(mb + mc2 + PAIR), "{v[160:167]}"(zf[0]), "{v[168:175]}"(zf[1]), "{v[176:183]}"(zf[2]), "{v[184:191]}"(zf[3])
                    : "v96", "v97", "v98", "v99", "v100", "v101", "v102", "v103", "v104", "v105", "v106", "v107", "v128", "v129", "v130", "v131", "v132", "v133", "v134", "v135", "v136", "v137", "v138", "v139", "v140", "v141", "v142", "v143", "v144", "v145", "v146", "v147", "v148", "v149", "v150", "v151", "v152", "v153", "v154", "v155", "v156", "v157", "v158", "v159", "v208", "v209", "v210", "v211", "v212", "v213", "v214", "v215", "v216", "v217", "v218", "v219", "v220", "v221", "v222", "v223", "v224", "v225", "v226", "v227", "v228", "v229", "v230", "v231", "v232", "v233", "v234", "v235", "v236", "v237", "v238", "v239", "vcc", "scc", "memory");
                budget = tries;
                if (w == 3) *(volatile LAS int*)(smem + 4100) = s;
                __builtin_amdgcn_s_sleep(DPS - 4);
                if (w == 3) {
                    st128(SENT, rwin, ra, W_MEM + (unsigned)(s & 3) * 6u * PAIR, 16);
                    if (lane == 0 && s > 1) __builtin_amdgcn_raw_buffer_store_b32((unsigned)s - 2u, rwin, 0u, W_GO + (unsigned)o * 128u, 16);
                }
                for (;;) {
                    const u32x2 x = *(volatile LAS u32x2*)(smem + 4096);
                    zr = __builtin_amdgcn_readfirstlane(x[0]);
                    if (__builtin_amdgcn_readfirstlane(x[1]) == s) break;
                    __builtin_amdgcn_s_sleep(2);
                }
                zb = zb == W_ZM + 2u * NOWN * PAIR ? W_ZM : zb + NOWN * PAIR;
            }
            asm volatile("s_waitcnt vmcnt(0)" : "+{v[160:167]}"(zf[0]), "+{v[168:175]}"(zf[1]), "+{v[176:183]}"(zf[2]), "+{v[184:191]}"(zf[3]) :: "memory");
            BAR0();
            BAR0();
            f32x4 c[2];
            for (unsigned spins = 0;;) {
                asm volatile("" ::: "memory");
                u32x4 pt[16], ptm[2][2], zm[2];
#pragma unroll
                for (int q = 0; q < 2; ++q) zm[q] = __builtin_amdgcn_raw_buffer_load_b128(rwin, zoff + hg * 2048u + q * 1024u, zb, 16);
#pragma unroll
                for (int q = 0; q < 16; ++q) pt[q] = __builtin_amdgcn_raw_buffer_load_b128(rwin, (unsigned)(16 * hg) * 1024u + lane * 16u, W_PL + q * 1024u, 16);
#pragma unroll
                for (int q = 0; q < 4; ++q) ptm[q >> 1][q & 1] = __builtin_amdgcn_raw_buffer_load_b128(rwin, (unsigned)(hg * 2048 + lane * 16), W_PL + 65536u + (q >> 1) * 8192u + (q & 1) * 1024u, 16);
                unsigned m = max(smax(zm[0]), smax(zm[1]));
#pragma unroll
                for (int t = 0; t < 2; ++t) {
                    c[t] = z4;
#pragma unroll
                    for (int q = 0; q < 8; ++q) { c[t] = mfma32(half8(zf[q >> 1], q & 1), pt[2 * q + t], c[t]); m = max(m, smax(pt[2 * q + t])); }
#pragma unroll
                    for (int q = 0; q < 2; ++q) { c[t] = mfma32((u32x4){zm[q][0], zm[q][2], zm[q][1], zm[q][3]}, ptm[t][q], c[t]); m = max(m, smax(ptm[t][q])); }
                }
                if (!__any(m == 0xFFFFFFFFu)) break;
                if (spin_fail(spins, a.ctl)) break;
            }
#pragma unroll
            for (int t = 0; t < 2; ++t)
#pragma unroll
                for (int r = 0; r < 4; ++r) *(float*)(op + hg * 2048 + (16 * t + fr) * 64 + pi16(4 * fq + r) * 4) = c[t][r];
            BAR();
        } else if (w == 4) {
            const unsigned fo = (unsigned)(lane & 31) * 128u + (unsigned)(o / 10) * 4u + (lane & 32) * 2u;
            int zk = -1;
            if (lane < 2) *(volatile LAS int*)(smem + 4096 + lane * 4) = -1;
            for (int s = 0; s < NSEG; ++s) {
                asm volatile("s_waitcnt lgkmcnt(0)\n\ts_barrier\n\ts_barrier" ::: "memory");
                int f = 0;
                const bool look = zk < min(s + ZLA, NSEG - 1);
                if (look) { __builtin_amdgcn_s_sleep(6); f = (int)__builtin_amdgcn_raw_buffer_load_b32(rwin, fo, W_FZ, 16); }
                SB();
                BAR0();
                if (look) {
                    f = min(f, __builtin_amdgcn_update_dpp(f, f, 0xB1, 0xF, 0xF, false)); f = min(f, __builtin_amdgcn_update_dpp(f, f, 0x4E, 0xF, 0xF, false));
                    f = min(f, __builtin_amdgcn_update_dpp(f, f, 0x141, 0xF, 0xF, false)); f = min(f, __builtin_amdgcn_update_dpp(f, f, 0x140, 0xF, 0xF, false));
                    zk = seg_done(MXV, min(__builtin_amdgcn_readlane(f, 0), __builtin_amdgcn_readlane(f, 16)), min(__builtin_amdgcn_readlane(f, 32), __builtin_amdgcn_readlane(f, 48)));
                    *(volatile LAS int*)(smem + 4096) = zk;
                }
            }
        } else if (w == 5) {
            for (int s = 0; s < 3 * NSEG; ++s) BAR0();
        } else if (w == 6) {
            const unsigned ooff = (unsigned)((S_ - SEG + (lane >> 1)) * D_ + o * 16 + (lane & 1) * 8) * 2u;
            for (int s = 0; s < 3 * NSEG - 2; ++s) BAR0();
            const u32x4 xr = __builtin_amdgcn_raw_buffer_load_b128(rws, ooff, O_X1, 16);
            asm volatile("s_barrier\n\ts_barrier ; 6" ::: "memory");
            const char* src = op + lane * 32;
            f32x4 x = *(const f32x4*)src, y = *(const f32x4*)(src + 16);
#pragma unroll
            for (int ww = 1; ww < 4; ++ww) { x += *(const f32x4*)(src + ww * 2048); y += *(const f32x4*)(src + ww * 2048 + 16); }
            const u32x4 r = {pack2(lo2f(xr[0]) + bfr(x[0]), hi2f(xr[0]) + bfr(x[1])), pack2(lo2f(xr[1]) + bfr(x[2]), hi2f(xr[1]) + bfr(x[3])),
                             pack2(lo2f(xr[2]) + bfr(y[0]), hi2f(xr[2]) + bfr(y[1])), pack2(lo2f(xr[3]) + bfr(y[2]), hi2f(xr[3]) + bfr(y[3]))};
            st128(r, mkrsrc(a.out, S_ * D_ * 2), ooff, 0u, 16);
            if (a.Fnext) {
                VMWAIT();
                if (lane == 0) __builtin_amdgcn_raw_buffer_store_b32(0u, rwin, 0u, W_OUT + (unsigned)(NSEG - 1) * 256u + (unsigned)o * 4u, 16);
            }
        } else {
            Tk tk = tk_init();
            __builtin_amdgcn_s_setprio(3);
            for (int s = 0; s < NSEG; ++s) {
                const unsigned ta0 = ck.ta, mm0 = ck.ma;
                unsigned tp = tpp + lt + xs + M1;
                if (s < SAD) { sched(tk, s); tp = tpp - M2 - XS0 - PEN1; }
                wait_fine_bar(tp - DZP, ck);
                wait_cyc_bar(cyc_at(ck, sx24(tp + (s + 1 < NSEG ? 0 : ML) - ck.ta)));
                clk_rate(ck, ta0, mm0);
                asm volatile("s_barrier ; 7" ::: "memory");
                if (s >= SAD && s + 1 < NSEG) sched(tk, s);
                RTS();
            }
        }
        SST(23);
    }
    LANES();
    for (int ph = 0; ph < 2; ++ph) {
        if (ph ? bid >= NQT && bid < NQT + NOWN : bid >= NREC && bid < NQT) {
            const int o = bid - (ph ? NQT : NREC), cg = o >> 1, sbeg = (ph ? TAILS : 0) + (o & 1), send = ph ? NSEG - 1 : TAILS;
            const unsigned ztoff = (unsigned)((2 * cg * H_ + 4 * w) * 64 + lane) * 16u;
            const unsigned ptoff = (unsigned)(8 * w * 64 + lane) * 16u, ptmoff = 65536u + (unsigned)(4 * w * 16 + lane) * 16u;
            const unsigned ooff = (unsigned)(((w & 1) * 16 + (lane >> 2)) * D_ + cg * 32 + (lane & 3) * 8) * 2u;
            const unsigned tfoff = lane < 16 ? W_FPT + (unsigned)((4 * w + (lane & 3)) * 128 + (lane >> 2) * 4) : W_FZ2 + (unsigned)((4 * w + (lane & 3)) * 128 + (cg / 5) * 4);
            int rdy = -1;
            if (ph) {
                if (w == 0) for (unsigned spins = 0;;) {
                    asm volatile("" ::: "memory");
                    if ((int)__builtin_amdgcn_readfirstlane(__builtin_amdgcn_raw_buffer_load_b32(rwin, 0u, W_GO + (unsigned)o * 128u, 16)) >= TAILS - 3) break;
                    if (spin_fail(spins, a.ctl)) break;
                    __builtin_amdgcn_s_sleep(OSLP);
                }
                __syncthreads();
                for (unsigned spins = 0;;) {
                    asm volatile("" ::: "memory");
                    if (!__any((int)__builtin_amdgcn_raw_buffer_load_b32(rwin, (unsigned)(4 * w + (lane & 3)) * 128u + (unsigned)(cg / 5) * 4u, W_FZ, 16) < NSEG - 2)) break;
                    if (spin_fail(spins, a.ctl)) break;
                    __builtin_amdgcn_s_sleep(8);
                }
            }
            auto wait_rdy = [&](int s) {
                for (unsigned spins = 0; rdy < s;) {
                    asm volatile("" ::: "memory");
                    if (!ph) {
                        rdy = (int)__builtin_amdgcn_readfirstlane(__builtin_amdgcn_raw_buffer_load_b32(rwin, 0u, W_GO + (unsigned)o * 128u, 16));
                    } else {
                        const int f = (int)__builtin_amdgcn_raw_buffer_load_b32(rwin, tfoff, 0u, 16);
                        if (!__any(f < s)) rdy = s;
                    }
                    if (rdy >= s) break;
                    if (spin_fail(spins, a.ctl)) break;
                    if (ph) __builtin_amdgcn_s_sleep(OSLP2); else __builtin_amdgcn_s_sleep(OSLP);
                }
            };
            for (int s = sbeg; s < send; s += 2) {
                RT(0);
                u32x4 xr, zf[2][4], zx[2], pt[8], ptm[2];
                if (!ph) wait_rdy(max(s - 2, 0));
                if (w >= 6) xr = __builtin_amdgcn_raw_buffer_load_b128(rws, ooff, O_X1 + (unsigned)(s * SEG * D_ * 2), 16);
#pragma unroll
                for (int q = 0; q < 8; ++q) zf[q >> 2][q & 3] = __builtin_amdgcn_raw_buffer_load_b128(rws, ztoff + (q & 3) * 1024u, O_Z + (unsigned)(s * ZTSEG) * 2u + (q >> 2) * (H_ * 1024u), 16);
                wait_rdy(s);
                RT(1);
                const unsigned ptb = O_PT + (unsigned)(s * PTSEG) * 2u;
#pragma unroll
                for (int q = 0; q < 8; ++q) pt[q] = __builtin_amdgcn_raw_buffer_load_b128(rws, ptoff + q * 1024u, ptb, 16);
#pragma unroll
                for (int q = 0; q < 2; ++q) {
                    zx[q] = __builtin_amdgcn_raw_buffer_load_b128(rws, tid * 16u, O_Z2 + (unsigned)(s * NOWN + 2 * cg + q) * 8192u, 16);
                    ptm[q] = __builtin_amdgcn_raw_buffer_load_b128(rws, ptmoff + q * 8192u, ptb, 16);
                }
                char* op = smem + ((s >> 1) & 1) * 32768;
#pragma unroll
                for (int u = 0; u < 4; ++u) {
                    const int tq = u >> 1, t = u & 1;
                    f32x4 c = z4;
#pragma unroll
                    for (int q = 0; q < 4; ++q) c = mfma32(zf[t][q], pt[2 * q + tq], c);
                    c = mfma32((u32x4){zx[t][0], zx[t][2], zx[t][1], zx[t][3]}, ptm[tq], c);
                    *(f32x4*)(op + w * 4096 + (16 * tq + fr) * 128 + (((t * 4 + fq) ^ (fr & 7)) << 4)) = c;
                }
                __syncthreads();
                if (w >= 6) {
                    const int tk = (w & 1) * 16 + (lane >> 2);
                    f32x2 x[4];
#pragma unroll
                    for (int q = 0; q < 4; ++q) {
                        const char* sp = op + tk * 128 + ((((lane & 2) * 2 + q) ^ (tk & 7)) << 4) + (lane & 1) * 8;
                        x[q] = *(const f32x2*)sp;
#pragma unroll
                        for (int ww = 1; ww < 8; ++ww) x[q] += *(const f32x2*)(sp + ww * 4096);
                    }
                    const u32x4 r = {pack2(lo2f(xr[0]) + bfr(x[0][0]), hi2f(xr[0]) + bfr(x[2][0])), pack2(lo2f(xr[1]) + bfr(x[1][0]), hi2f(xr[1]) + bfr(x[3][0])),
                                     pack2(lo2f(xr[2]) + bfr(x[0][1]), hi2f(xr[2]) + bfr(x[2][1])), pack2(lo2f(xr[3]) + bfr(x[1][1]), hi2f(xr[3]) + bfr(x[3][1]))};
                    if (!a.Fnext) st128(r, mkrsrc(a.out, S_ * D_ * 2), ooff, (unsigned)(s * SEG * D_ * 2), 0);
                    else {
                        st128(r, mkrsrc(a.out, S_ * D_ * 2), ooff, (unsigned)(s * SEG * D_ * 2), 16);
                        VMWAIT();
                        if (lane == 0) __builtin_amdgcn_raw_buffer_store_b32(0u, rwin, 0u, W_OUT + (unsigned)(s * 64 + 2 * cg + (w & 1)) * 4u, 16);
                    }
                }
                RT(2);
                RTS();
                if (s < 8) SST(8 + s);
                if (s >= 56) SST(s - 40);
            }
        }
        if (a.Fnext && ph == 0 && bid >= NQT + NOWN) {
            if (tid < 320) ((float4*)L.tw)[tid] = ((const float4*)(ws + OFF_TW640))[tid];
            for (int u = bid - (NQT + NOWN); u < 256; u += 40) {
                if (w == 0) {
                    for (unsigned spins = 0;;) {
                        asm volatile("" ::: "memory");
                        const unsigned f = __builtin_amdgcn_raw_buffer_load_b32(rwin, (unsigned)min(lane, NOWN - 1) * 4u, W_OUT + (unsigned)(u >> 2) * 256u, 16);
                        if (!__any(f != 0u)) break;
                        if (spin_fail(spins, a.ctl)) break;
                        if (u < 216) __builtin_amdgcn_s_sleep(WSLP); else __builtin_amdgcn_s_sleep(4);
                    }
                }
                __syncthreads();
                SST(24);
                a1_unit(mkrsrc(a.out, S_ * D_ * 2), u * 8, a.Fnext, L, tid, XA1);
                SST(25);
            }
        }
    }
    SST(31);
}

constexpr int L_DBG = 0;
extern "C" size_t dbg_offset(int i) {
    const size_t o[] = {OFF_CTL, OFF_WQKVT, OFF_WOT, OFF_WFT, OFF_TW640, OFF_TW2048, OFF_F, OFF_X1, OFF_X2, OFF_H, OFF_Z, OFF_WIN, OFF_WIN, OFF_PT, OFF_WIN, OFF_END};
    return o[i];
}

extern "C" void kernel_launch(void* const* d_in, const int* in_sizes, int n_in, void* d_out, int out_size, void* d_ws, size_t ws_size,
                              hipStream_t stream) {
    const u16* A_x = (const u16*)d_in[0];
    const u16* w_r = (const u16*)d_in[1];
    const u16* w_i = (const u16*)d_in[2];
    const u16* mem0 = (const u16*)d_in[4];
    const u16* Wq = (const u16*)d_in[5];
    const u16* Wk = (const u16*)d_in[6];
    const u16* Wv = (const u16*)d_in[7];
    const u16* Wo = (const u16*)d_in[8];
    char* ws = (char*)d_ws;
    u16* X2 = (u16*)(ws + OFF_X2);

    hipFuncSetAttribute((const void*)b3_kernel, hipFuncAttributeMaxDynamicSharedMemorySize, L_END + L_DBG);
    char* win = (char*)(((uintptr_t)(ws + OFF_WIN) + 0x1FFFFFu) & ~(uintptr_t)0x1FFFFFu);
    k1_kernel<<<16 + 404 + 320, 320, 0, stream>>>(A_x, Wq, Wk, w_r, w_i, ws, win);
    for (int layer = 0; layer < 2; ++layer) {
        const u16* xin = layer == 0 ? A_x : X2;
        u16* xout = layer == 0 ? X2 : (u16*)d_out;
        a2_kernel<<<layer == 0 ? DF + 160 + 320 : DF, 256, 0, stream>>>(Wv, Wo, ws, win);
        RecArgs ra{(unsigned*)ws, win, mem0, xin, xout, layer == 0 ? (float4*)(ws + OFF_F) : nullptr};
        b3_kernel<<<256, 512, L_END + L_DBG, stream>>>(ra);
    }
}
```

```cpp
#define TD
#define ENC 0
#include <hip/hip_runtime.h>
#include <hip/hip_bf16.h>
#include <stdint.h>

typedef unsigned short u16;
typedef __attribute__((ext_vector_type(8))) __bf16 bf16x8;
typedef __attribute__((ext_vector_type(4))) short s16x4;
typedef __attribute__((ext_vector_type(4))) float f32x4;
typedef __attribute__((ext_vector_type(8))) float f32x8;
typedef __attribute__((ext_vector_type(16))) float f32x16;
typedef __attribute__((ext_vector_type(4))) unsigned u32x4;
typedef __attribute__((ext_vector_type(2))) unsigned u32x2;
typedef __attribute__((ext_vector_type(8))) unsigned u32x8;
typedef __attribute__((ext_vector_type(16))) __bf16 bf16x16;
typedef __attribute__((ext_vector_type(2))) unsigned short u16x2;
#define LAS __attribute__((address_space(3)))

constexpr int S_ = 2048, D_ = 640, DF = 321, H_ = 32, DH = 128, HD = 4096, NM = 8, SEG = 32, NSEG = 64;
constexpr int NQKV = 3 * HD;
constexpr int NOWN = 40;
constexpr int ZTR = NSEG;
constexpr size_t ZTSEG = (size_t)NOWN * H_ * 512;
constexpr unsigned PAIR = 16384, HALF = 8192;
constexpr unsigned W_MEM = 0, W_P = W_MEM + 24 * PAIR, W_T = W_P + 40 * PAIR, W_GO = W_T + 8 * PAIR, W_OUT = W_GO + PAIR, W_FZ = W_OUT + PAIR, W_FPT = W_FZ + 4096, W_FZ2 = W_FZ + 8192, W_FQ = W_FZ + 12288, W_H = W_FZ + PAIR, W_PL = W_H + PAIR, W_PROBE = W_PL + 6 * PAIR,
                   W_ZM = 2u * 1024 * 1024, W_END = W_ZM + 120 * PAIR;
static_assert(W_PROBE + 2 * PAIR <= 2u * 1024 * 1024 && W_PROBE % (2 * PAIR) == 0, "window");
constexpr int PTSEG = 32768 + 8192;

#define XA1 0
#define FAUX 0
#define WAUX 16
#define A2AUX 16
#define FPAD 0
constexpr int FS = 1024 + FPAD;
constexpr size_t al(size_t x) { return (x + 255) & ~(size_t)255; }
constexpr size_t OFF_CTL  = 0;
constexpr size_t OFF_WQKVT = 4096 + 4 * 1024 * 1024;
constexpr size_t OFF_WOT  = OFF_WQKVT + al((size_t)NQKV * D_ * 2);
constexpr size_t OFF_WFT  = OFF_WOT + al((size_t)D_ * HD * 2);
constexpr size_t OFF_TW640 = OFF_WFT + al((size_t)DF * S_ * 8);
constexpr size_t OFF_TW2048 = OFF_TW640 + al(640 * 8);
constexpr size_t OFF_F    = OFF_TW2048 + al(20 * 256 * 8);
constexpr size_t OFF_X1   = OFF_F + al((size_t)DF * FS * 16);
constexpr size_t OFF_X2   = OFF_X1 + al((size_t)S_ * D_ * 2);
constexpr size_t OFF_H    = OFF_X2 + al((size_t)S_ * D_ * 2);
constexpr size_t OFF_Z    = OFF_H + al((size_t)S_ * D_ * 2);
constexpr size_t OFF_Z2   = OFF_Z + al(ZTSEG * ZTR * 2);
constexpr size_t OFF_PT   = OFF_Z2 + al((size_t)NSEG * NOWN * 8192);
constexpr size_t OFF_QK   = OFF_PT + al((size_t)NSEG * PTSEG * 2);
constexpr size_t OFF_WIN  = OFF_QK + (size_t)NSEG * H_ * 16384;
constexpr size_t OFF_END  = OFF_WIN + (size_t)6 * 1024 * 1024;
static_assert(OFF_END <= (size_t)256 * 1024 * 1024, "workspace");

#define st128(v, r, vo, so, aux) do { const u32x4 d_ = (v); __builtin_amdgcn_raw_buffer_store_b128(d_, r, vo, so, aux); asm volatile("s_nop 1" :: "v"(d_)); } while (0)
__device__ __forceinline__ float bf2f(u16 v) { return __uint_as_float((unsigned)v << 16); }
__device__ __forceinline__ u16 f2bf(float f) { __bf16 b = (__bf16)f; return __builtin_bit_cast(u16, b); }
__device__ __forceinline__ float bfr(float f) { return (float)(__bf16)f; }
typedef __attribute__((ext_vector_type(2))) float f32x2;
typedef __attribute__((ext_vector_type(2))) __bf16 bf16x2;
__device__ __forceinline__ unsigned pack2(float a, float b) { return __builtin_bit_cast(unsigned, __builtin_convertvector((f32x2){a, b}, bf16x2)); }
__device__ __forceinline__ float lo2f(unsigned v) { return __uint_as_float(v << 16); }
__device__ __forceinline__ float hi2f(unsigned v) { return __uint_as_float(v & 0xFFFF0000u); }
__device__ __forceinline__ float fmx(float a, float b) { return __builtin_elementwise_maximum(a, b); }
__device__ __forceinline__ f32x4 mfma32(u32x4 a, u32x4 b, f32x4 c) {
    return __builtin_amdgcn_mfma_f32_16x16x32_bf16(__builtin_bit_cast(bf16x8, a), __builtin_bit_cast(bf16x8, b), c, 0, 0, 0);
}
__device__ __forceinline__ f32x4 mfma16(u32x2 a, u32x2 b, f32x4 c) {
    return __builtin_amdgcn_mfma_f32_16x16x16bf16_1k(__builtin_bit_cast(s16x4, a), __builtin_bit_cast(s16x4, b), c, 0, 0, 0);
}
__device__ __forceinline__ f32x4 smfmac(u32x4 a, u32x8 b, f32x4 c, int idx) {
    return __builtin_amdgcn_smfmac_f32_16x16x64_bf16(__builtin_bit_cast(bf16x8, a), __builtin_bit_cast(bf16x16, b), c, idx, 0, 0);
}
__device__ __forceinline__ int pi16(int k) { return ((k & 3) << 2) | ((k >> 1) & 2) | (k >> 3); }
__device__ __forceinline__ float addswap32(float a, float b) {
    auto r = __builtin_amdgcn_permlane32_swap(__float_as_uint(a), __float_as_uint(b), false, false);
    return __uint_as_float(r[0]) + __uint_as_float(r[1]);
}
__device__ __forceinline__ float xmax3216(float v) {
    auto r = __builtin_amdgcn_permlane32_swap(__float_as_uint(v), __float_as_uint(v), false, false);
    v = fmx(__uint_as_float(r[0]), __uint_as_float(r[1]));
    auto q = __builtin_amdgcn_permlane16_swap(__float_as_uint(v), __float_as_uint(v), false, false);
    return fmx(__uint_as_float(q[0]), __uint_as_float(q[1]));
}
__device__ __forceinline__ float xsum3216(float v) {
    auto r = __builtin_amdgcn_permlane32_swap(__float_as_uint(v), __float_as_uint(v), false, false);
    v = __uint_as_float(r[0]) + __uint_as_float(r[1]);
    auto q = __builtin_amdgcn_permlane16_swap(__float_as_uint(v), __float_as_uint(v), false, false);
    return __uint_as_float(q[0]) + __uint_as_float(q[1]);
}
template <int CTRL> __device__ __forceinline__ float dppf(float v) { return __uint_as_float((unsigned)__builtin_amdgcn_update_dpp(0, (int)__float_as_uint(v), CTRL, 0xF, 0xF, false)); }
__device__ __forceinline__ float wsum(float v) {
    v += dppf<0xB1>(v); v += dppf<0x4E>(v); v += dppf<0x141>(v); v += dppf<0x140>(v);
    return xsum3216(v);
}
__device__ __forceinline__ unsigned ror8(unsigned v) { return (unsigned)__builtin_amdgcn_update_dpp(0, (int)v, 0x128, 0xF, 0xF, false); }
__device__ __forceinline__ float ror8f(float v) { return __uint_as_float(ror8(__float_as_uint(v))); }
__device__ __forceinline__ unsigned nosent(unsigned v) {
    u16x2 lim = {0xFFFE, 0xFFFE};
    return __builtin_bit_cast(unsigned, __builtin_elementwise_min(__builtin_bit_cast(u16x2, v), lim));
}
__device__ __forceinline__ unsigned pkmax(unsigned a, unsigned b) {
    return __builtin_bit_cast(unsigned, __builtin_elementwise_max(__builtin_bit_cast(u16x2, a), __builtin_bit_cast(u16x2, b)));
}
__device__ __forceinline__ bool has_sent(u32x4 v) {
    unsigned m = pkmax(pkmax(v.x, v.y), pkmax(v.z, v.w));
    return (m & 0xFFFFu) == 0xFFFFu || (m >> 16) == 0xFFFFu;
}

__device__ __forceinline__ float2 cadd(float2 a, float2 b) { return make_float2(a.x + b.x, a.y + b.y); }
__device__ __forceinline__ float2 csub(float2 a, float2 b) { return make_float2(a.x - b.x, a.y - b.y); }
__device__ __forceinline__ float2 cmul(float2 a, float2 b) { return make_float2(a.x * b.x - a.y * b.y, a.x * b.y + a.y * b.x); }
template <int SG> __device__ __forceinline__ float2 muli(float2 a) { return SG < 0 ? make_float2(a.y, -a.x) : make_float2(-a.y, a.x); }
template <int SG> __device__ __forceinline__ float2 ctw(float2 a, float2 w) { if (SG > 0) w.y = -w.y; return cmul(a, w); }

template <int SG> __device__ __forceinline__ void dft4(float2& a, float2& b, float2& c, float2& d) {
    float2 t0 = cadd(a, c), t1 = csub(a, c), t2 = cadd(b, d), t3 = muli<SG>(csub(b, d));
    a = cadd(t0, t2); b = cadd(t1, t3); c = csub(t0, t2); d = csub(t1, t3);
}
template <int SG> __device__ __forceinline__ void dft8(float2 (&u)[8]) {
    float2 e0 = u[0], e1 = u[2], e2 = u[4], e3 = u[6], o0 = u[1], o1 = u[3], o2 = u[5], o3 = u[7];
    dft4<SG>(e0, e1, e2, e3); dft4<SG>(o0, o1, o2, o3);
    const float r = 0.70710678118654752440f;
    float2 t1 = muli<SG>(o1); o1 = make_float2((o1.x + t1.x) * r, (o1.y + t1.y) * r);
    o2 = muli<SG>(o2);
    float2 t3 = muli<SG>(o3); o3 = make_float2((t3.x - o3.x) * r, (t3.y - o3.y) * r);
    u[0] = cadd(e0, o0); u[4] = csub(e0, o0);
    u[1] = cadd(e1, o1); u[5] = csub(e1, o1);
    u[2] = cadd(e2, o2); u[6] = csub(e2, o2);
    u[3] = cadd(e3, o3); u[7] = csub(e3, o3);
}
template <int SG> __device__ __forceinline__ void dft5(float2& x0, float2& x1, float2& x2, float2& x3, float2& x4) {
    const float c1 = 0.30901699437494742410f, c2 = -0.80901699437494742410f, s1 = 0.95105651629515357212f, s2 = 0.58778525229247312917f;
    float2 t1 = cadd(x1, x4), t2 = cadd(x2, x3), t3 = csub(x1, x4), t4 = csub(x2, x3);
    float2 a1 = make_float2(x0.x + c1 * t1.x + c2 * t2.x, x0.y + c1 * t1.y + c2 * t2.y);
    float2 a2 = make_float2(x0.x + c2 * t1.x + c1 * t2.x, x0.y + c2 * t1.y + c1 * t2.y);
    float2 b1 = muli<SG>(make_float2(s1 * t3.x + s2 * t4.x, s1 * t3.y + s2 * t4.y));
    float2 b2 = muli<SG>(make_float2(s2 * t3.x - s1 * t4.x, s2 * t3.y - s1 * t4.y));
    x0 = make_float2(x0.x + t1.x + t2.x, x0.y + t1.y + t2.y);
    x1 = cadd(a1, b1); x4 = csub(a1, b1); x2 = cadd(a2, b2); x3 = csub(a2, b2);
}
template <int SG> __device__ __forceinline__ void dft10(float2 (&u)[10]) {
    float2 e[5] = {u[0], u[2], u[4], u[6], u[8]}, o[5] = {u[1], u[3], u[5], u[7], u[9]};
    dft5<SG>(e[0], e[1], e[2], e[3], e[4]); dft5<SG>(o[0], o[1], o[2], o[3], o[4]);
    const float wc[5] = {1.f, 0.80901699437494742410f, 0.30901699437494742410f, -0.30901699437494742410f, -0.80901699437494742410f};
    const float ws[5] = {0.f, 0.58778525229247312917f, 0.95105651629515357212f, 0.95105651629515357212f, 0.58778525229247312917f};
#pragma unroll
    for (int k = 0; k < 5; ++k) {
        float2 w = make_float2(wc[k], SG < 0 ? -ws[k] : ws[k]);
        float2 t = k == 0 ? o[0] : cmul(o[k], w);
        u[k] = cadd(e[k], t); u[k + 5] = csub(e[k], t);
    }
}
__device__ __forceinline__ void ln_row(float (&v)[10]) {
    float s = 0.f;
#pragma unroll
    for (int r = 0; r < 10; ++r) s += v[r];
    const float mean = wsum(s) * (1.f / 640.f);
    float q = 0.f;
#pragma unroll
    for (int r = 0; r < 10; ++r) { float d = v[r] - mean; q += d * d; }
    const float var = wsum(q) * (1.f / 640.f);
    const float mb = bfr(mean), rs = __builtin_amdgcn_rcpf(bfr(sqrtf(bfr(bfr(var) + 1e-5f))));
#pragma unroll
    for (int r = 0; r < 10; ++r) v[r] = bfr(bfr(v[r] - mb) * rs);
}

__device__ __forceinline__ __amdgpu_buffer_rsrc_t mkrsrc(const void* p, unsigned bytes) {
    return __builtin_amdgcn_make_buffer_rsrc((void*)p, 0, bytes, 0x00020000);
}
template <int NT> __device__ __forceinline__ void frag_tile(const u16* src, int C, int r0, int c0, u16* dst, unsigned gstride, bool perm, u16 (*t)[66]) {
    const int tid = threadIdx.x;
    if (tid < 256) {
        u32x4 v[2 * NT];
#pragma unroll
        for (int i = 0; i < 2 * NT; ++i) {
            const int id = tid + i * 256, r = id / (8 * NT), c8 = (id % (8 * NT)) * 8;
            v[i] = *(const u32x4*)(src + (size_t)(r0 + r) * C + c0 + c8);
        }
#pragma unroll
        for (int i = 0; i < 2 * NT; ++i) {
            const int id = tid + i * 256, r = id / (8 * NT), c8 = (id % (8 * NT)) * 8;
#pragma unroll
            for (int j = 0; j < 4; ++j) { t[c8 + 2 * j][r] = (u16)(v[i][j] & 0xFFFF); t[c8 + 2 * j + 1][r] = (u16)(v[i][j] >> 16); }
        }
    }
    __syncthreads();
    if (tid < 256) {
        const __amdgpu_buffer_rsrc_t rd = mkrsrc(dst, 0x7FFFFFFF);
#pragma unroll
        for (int i = 0; i < 2 * NT; ++i) {
            const int id = tid + i * 256, g = id >> 7, u = (id >> 6) & 1, fq = (id >> 4) & 3, fr = id & 15;
            const int c = 16 * g + (perm ? pi16(fr) : fr), r8 = 32 * u + 8 * fq;
            u32x4 v;
#pragma unroll
            for (int j = 0; j < 4; ++j) v[j] = *(const unsigned*)&t[c][r8 + 2 * j];
            st128(v, rd, (g * gstride + (id & 127) * 8) * 2u, 0, WAUX);
        }
    }
}
template <int NT> __device__ __forceinline__ void qkv_tile(int m, int b, const u16* W, char* ws, u16 (*t)[66]) {
    const int kb = b / (64 / NT), cb = (b % (64 / NT)) * NT;
    frag_tile<NT>(W, HD, kb * 64, cb * 64, (u16*)(ws + OFF_WQKVT) + ((size_t)(m * 256 + cb * 4) * 10 + kb) * 1024, 10240, false, t);
}
template <int NT> __device__ __forceinline__ void wo_tile(int b, const u16* W, char* ws, u16 (*t)[66]) {
    const int eb = b / (10 / NT), cb = (b % (10 / NT)) * NT;
    frag_tile<NT>(W, D_, eb * 64, cb * 64, (u16*)(ws + OFF_WOT) + ((size_t)((eb >> 1) * 40 + cb * 4) * 2 + (eb & 1)) * 1024, 2048, true, t);
}

struct RowLds { float2 bA[4][644]; float2 bB[4][644]; float2 tw[640]; };
__device__ __forceinline__ float2 tw640_at(int i) {
    float sn, cs; sincospif((float)(i > 320 ? 640 - i : -i) * (1.f / 320.f), &sn, &cs);
    return make_float2(cs, sn);
}
__device__ __forceinline__ void a1_unit(__amdgpu_buffer_rsrc_t rx, int row0, float4* F4, RowLds& L, int tid, int xa = 0) {
    const int wave = tid >> 6, lane = tid & 63;
    if (wave < 4) {
        char* st = (char*)L.bB + wave * 2560;
        const unsigned base = (unsigned)(row0 + 2 * wave) * (D_ * 2);
        const u32x4 c0 = __builtin_amdgcn_raw_buffer_load_b128(rx, lane * 16u, base, 16), c1 = __builtin_amdgcn_raw_buffer_load_b128(rx, lane * 16u + 1024u, base, 16);
        if (lane < 32) *(u32x4*)(st + 2048 + lane * 16) = __builtin_amdgcn_raw_buffer_load_b128(rx, lane * 16u + 2048u, base, 16);
        *(u32x4*)(st + lane * 16) = c0; *(u32x4*)(st + 1024 + lane * 16) = c1;
        float a[10], b[10];
#pragma unroll
        for (int r = 0; r < 10; ++r) { a[r] = bf2f(*(const u16*)(st + (lane + 64 * r) * 2)); b[r] = bf2f(*(const u16*)(st + 1280 + (lane + 64 * r) * 2)); }
        ln_row(a); ln_row(b);
        float2 u[10];
#pragma unroll
        for (int r = 0; r < 10; ++r) u[r] = make_float2(a[r], b[r]);
        dft10<-1>(u);
#pragma unroll
        for (int r = 0; r < 10; ++r) L.bA[wave][10 * lane + r] = u[r];
    }
    __syncthreads();
    const int f = tid / 80, i = tid % 80;
    if (tid < 320) {
        const int k = i % 10, j = (i / 10) * 80 + k;
        float2 u[8];
#pragma unroll
        for (int r = 0; r < 8; ++r) { u[r] = L.bA[f][i + 80 * r]; if (r) u[r] = ctw<-1>(u[r], L.tw[8 * k * r]); }
        dft8<-1>(u);
#pragma unroll
        for (int r = 0; r < 8; ++r) L.bB[f][j + 10 * r] = u[r];
    }
    __syncthreads();
    if (tid < 320) {
        float2 u[8];
#pragma unroll
        for (int r = 0; r < 8; ++r) { u[r] = L.bB[f][i + 80 * r]; if (r) u[r] = ctw<-1>(u[r], L.tw[i * r]); }
        dft8<-1>(u);
#pragma unroll
        for (int r = 0; r < 8; ++r) L.bA[f][i + 80 * r] = u[r];
    }
    __syncthreads();
    for (int it = tid; it < (xa == 2 ? 4 : DF * 4); it += blockDim.x) {
        const int k = it >> 2, ff = it & 3;
        if (xa == 3) __builtin_amdgcn_s_sleep(16);
        const float2 zk = L.bA[ff][k], zn = L.bA[ff][k == 0 ? 0 : 640 - k];
        const f32x4 o = {0.5f * (zk.x + zn.x), 0.5f * (zk.y - zn.y), 0.5f * (zk.y + zn.y), 0.5f * (zn.x - zk.x)};
        st128(__builtin_bit_cast(u32x4, o), mkrsrc(F4, DF * FS * 16), (unsigned)(k * FS + (row0 >> 1) + ff) * 16u, 0, FAUX);
    }
}
#define A3S(k) do {} while (0)
__device__ __forceinline__ void a3_unit(const float4* F4, const u16* x, int row0, __amdgpu_buffer_rsrc_t rx1, __amdgpu_buffer_rsrc_t rh, RowLds& L, const float4* tw, unsigned long long* dbg = nullptr) {
    const int tid = threadIdx.x, wave = tid >> 6, lane = tid & 63;
    float4 fv[3];
#pragma unroll
    for (int c = 0; c < 3; ++c) {
        const int it = min(tid + 512 * c, DF * 4 - 1);
        fv[c] = F4[(size_t)(it >> 2) * FS + (row0 >> 1) + (it & 3)];
    }
    const float4 twv = tw[min(tid, 319)];
#pragma unroll
    for (int c = 0; c < 3; ++c) {
        const int it = tid + 512 * c, k = it >> 2, ff = it & 3;
        float4 v = fv[c];
        if (k == 0 || k == 320) { v.y = 0.f; v.w = 0.f; }
        if (it < DF * 4) L.bA[ff][k] = make_float2(v.x - v.w, v.y + v.z);
        if (k > 0 && k < 320) L.bA[ff][640 - k] = make_float2(v.x + v.w, v.z - v.y);
    }
    if (tid < 320) ((float4*)L.tw)[tid] = twv;
    unsigned xa[10], xb[10];
    {
        const u16* xp = x + (size_t)(row0 + 2 * (wave & 3)) * D_ + lane;
#pragma unroll
        for (int r = 0; r < 10; ++r) { xa[r] = xp[64 * r]; xb[r] = xp[D_ + 64 * r]; }
    }
    __syncthreads();
    A3S(0);
    const int f = tid / 80, i = tid % 80;
    if (tid < 320) {
        float2 u[8];
#pragma unroll
        for (int r = 0; r < 8; ++r) u[r] = L.bA[f][i + 80 * r];
        dft8<1>(u);
#pragma unroll
        for (int r = 0; r < 8; ++r) L.bB[f][8 * i + r] = u[r];
    }
    __syncthreads();
    A3S(1);
    if (tid < 320) {
        const int k = i & 7, j = (i >> 3) * 64 + k;
        float2 u[8];
#pragma unroll
        for (int r = 0; r < 8; ++r) { u[r] = L.bB[f][i + 80 * r]; if (r) u[r] = ctw<1>(u[r], L.tw[10 * k * r]); }
        dft8<1>(u);
#pragma unroll
        for (int r = 0; r < 8; ++r) L.bA[f][j + 8 * r] = u[r];
    }
    __syncthreads();
    A3S(2);
    if (wave < 4) {
        float2 u[10];
#pragma unroll
        for (int r = 0; r < 10; ++r) { u[r] = L.bA[wave][lane + 64 * r]; if (r) u[r] = ctw<1>(u[r], L.tw[lane * r]); }
        dft10<1>(u);
        char* st = (char*)L.bB + wave * 5120;
        float a[10], b[10];
#pragma unroll
        for (int r = 0; r < 10; ++r) {
            a[r] = bfr(lo2f(xa[r]) + bfr(u[r].x));
            b[r] = bfr(lo2f(xb[r]) + bfr(u[r].y));
            *(u16*)(st + (lane + 64 * r) * 2) = f2bf(a[r]); *(u16*)(st + 1280 + (lane + 64 * r) * 2) = f2bf(b[r]);
        }
        A3S(3);
        ln_row(a); ln_row(b);
        A3S(4);
#pragma unroll
        for (int r = 0; r < 10; ++r) { *(u16*)(st + 2560 + (lane + 64 * r) * 2) = f2bf(a[r]); *(u16*)(st + 3840 + (lane + 64 * r) * 2) = f2bf(b[r]); }
        const unsigned base = (unsigned)(row0 + 2 * wave) * (D_ * 2);
#pragma unroll
        for (int c = 0; c < 3; ++c) {
            if (c < 2 || lane < 32) {
                const int id = c * 64 + lane, rr = id >= 80, ch = id - 80 * rr, row = (row0 + 2 * wave + rr) & 31;
                st128(*(const u32x4*)(st + 2560 + id * 16), rh, (unsigned)((ch >> 3) * 4096 + row * 128 + (((ch & 7) ^ (row & 7)) << 4)), (unsigned)(row0 >> 5) * 40960u, 16);
                st128(*(const u32x4*)(st + id * 16), rx1, id * 16u, base, 16);
            }
        }
    }
}

constexpr int FL_NEAR = 16;
constexpr int FL_ROT = 40;
__device__ __forceinline__ unsigned xcc_id() { unsigned x; asm volatile("s_getreg_b32 %0, hwreg(HW_REG_XCC_ID)" : "=s"(x)); return x & 15u; }
__global__ __launch_bounds__(320) void k1_kernel(const u16* x, const u16* Wq, const u16* Wk, const u16* wr, const u16* wi, char* ws, char* win) {
    __shared__ __attribute__((aligned(16))) RowLds L;
    const int b = (int)blockIdx.x - 16, tid = threadIdx.x;
    if (tid == 0 && blockIdx.x < 256) ((unsigned long long*)(ws + 4096 + 524288))[blockIdx.x] = __builtin_amdgcn_s_memrealtime();
    if (b < 0) {
        unsigned* best = (unsigned*)&L;
        const __amdgpu_buffer_rsrc_t rw = mkrsrc(win + (b < -8 ? W_PROBE : W_END), 2 * PAIR);
        const unsigned base = (blockIdx.x & 7u) * 128u + (tid >> 6) * 1664u;
        if (tid < 4) best[tid] = 0u;
        __syncthreads();
        unsigned mn[4] = {~0u, ~0u, ~0u, ~0u};
        for (int r = 0; r < 4; ++r) {
#pragma unroll
            for (int i = 0; i < 4; ++i) __builtin_amdgcn_raw_buffer_store_b32(0u, rw, base + i * HALF, 0, 16);
            asm volatile("s_waitcnt vmcnt(0)" ::: "memory");
#pragma unroll
            for (int i = 0; i < 4; ++i) {
                const unsigned long long t1 = __builtin_amdgcn_s_memtime();
                asm volatile("s_waitcnt lgkmcnt(0)" ::: "memory");
                unsigned v = __builtin_amdgcn_raw_buffer_load_b32(rw, base + i * HALF, 0, 16);
                asm volatile("s_waitcnt vmcnt(0)" : "+v"(v) :: "memory");
                const unsigned long long t2 = __builtin_amdgcn_s_memtime();
                mn[i] = min(mn[i], (unsigned)(t2 - t1) + v);
            }
        }
        if ((tid & 63) == 0) {
#pragma unroll
            for (int i = 0; i < 4; ++i) atomicAdd(&best[i], min(mn[i], 1000u));
        }
        __syncthreads();
        if (tid == 0) ((unsigned*)(ws + OFF_CTL))[FL_NEAR + (xcc_id() & 7u) + (blockIdx.x & 8u)] = min(best[1], best[3]) < min(best[0], best[2]) ? (best[3] < best[1] ? 3u : 1u) : (best[2] < best[0] ? 2u : 0u);
    } else if (b < 256) {
        L.tw[tid] = tw640_at(tid); L.tw[tid + 320] = tw640_at(tid + 320);
        a1_unit(mkrsrc(x, S_ * D_ * 2), b * 8, (float4*)(ws + OFF_F), L, tid);
    } else if (b < 256 + 128) {
        u16* tf = (u16*)&L;
        const int s0 = (b - 256) * 16;
        u32x4 v[5];
#pragma unroll
        for (int i = 0; i < 5; ++i) {
            const int id = min(tid + 320 * i, 1283);
            v[i] = ((const u32x4*)((id < 642 ? wr : wi) + (size_t)s0 * DF))[id < 642 ? id : id - 642];
        }
#pragma unroll
        for (int i = 0; i < 5; ++i) ((u32x4*)tf)[min(tid + 320 * i, 1283)] = v[i];
        __syncthreads();
        const __amdgpu_buffer_rsrc_t rd = mkrsrc(ws + OFF_WFT, DF * S_ * 4);
        for (int i = tid; i < DF * 4; i += 320) {
            const int k = i >> 2, sq = (i & 3) * 4;
            u32x4 o;
#pragma unroll
            for (int j = 0; j < 4; ++j) o[j] = (unsigned)tf[(sq + j) * DF + k] | ((unsigned)tf[16 * DF + (sq + j) * DF + k] << 16);
            st128(o, rd, (unsigned)(k * S_ + s0 + sq) * 4u, 0, WAUX);
        }
    } else if (b < 256 + 128 + 20) {
        const int j = b - 384, i = j * 256 + tid;
        if (tid < 256) {
            const int m = j < 7 ? 32 * (tid & 7) * (j + 1) : j < 14 ? 4 * (tid & 63) * (j - 6) : j < 17 ? tid * (j - 13) : (tid + 256) * (j - 16);
            double sn, cs; sincospi(-2.0 * (double)m / 2048.0, &sn, &cs);
            ((float2*)(ws + OFF_TW2048))[((j >> 1) * 256 + tid) * 2 + (j & 1)] = make_float2((float)cs, (float)sn);
            if (i < 640) ((float2*)(ws + OFF_TW640))[i] = tw640_at(i);
        }
        if (i == 0) *(unsigned*)(ws + OFF_CTL) = 0u;
    } else {
        const int t = b - 404;
        qkv_tile<4>(t / 160, t % 160, t < 160 ? Wq : Wk, ws, (u16 (*)[66])&L);
    }
}

#define PD(i) ((i) + ((i) >> 4))
template <int SG> __device__ __forceinline__ void fft2048(float2 (&u)[8], float2* b0, float2* b1, const float2 (&tw)[20], int tid) {
    dft8<SG>(u);
#pragma unroll
    for (int r = 0; r < 8; ++r) b0[PD(8 * tid + r)] = u[r];
    __syncthreads();
    {
        const int k = tid & 7, j = (tid >> 3) * 64 + k;
#pragma unroll
        for (int r = 0; r < 8; ++r) { u[r] = b0[PD(tid + 256 * r)]; if (r) u[r] = ctw<SG>(u[r], tw[r - 1]); }
        dft8<SG>(u);
#pragma unroll
        for (int r = 0; r < 8; ++r) b1[PD(j + 8 * r)] = u[r];
    }
    __syncthreads();
    {
        const int k = tid & 63, j = (tid >> 6) * 512 + k;
#pragma unroll
        for (int r = 0; r < 8; ++r) { u[r] = b1[PD(tid + 256 * r)]; if (r) u[r] = ctw<SG>(u[r], tw[6 + r]); }
        dft8<SG>(u);
#pragma unroll
        for (int r = 0; r < 8; ++r) b0[PD(j + 64 * r)] = u[r];
    }
    __syncthreads();
#pragma unroll
    for (int hh = 0; hh < 2; ++hh) {
        const int i = tid + 256 * hh;
        float2 v0 = b0[PD(i)], v1 = ctw<SG>(b0[PD(i + 512)], tw[14 + 3 * hh]), v2 = ctw<SG>(b0[PD(i + 1024)], tw[15 + 3 * hh]), v3 = ctw<SG>(b0[PD(i + 1536)], tw[16 + 3 * hh]);
        dft4<SG>(v0, v1, v2, v3);
        u[hh] = v0; u[hh + 2] = v1; u[hh + 4] = v2; u[hh + 6] = v3;
    }
}
#define A2S(k) do {} while (0)
__global__ __launch_bounds__(256) void a2_kernel(const u16* Wv, const u16* Wo, char* ws, char* win) {
    __shared__ float2 bb[2][2048 + 128];
    float2* b0 = bb[0]; float2* b1 = bb[1];
    const int tid = threadIdx.x;
    if (blockIdx.x >= DF) {
        const int t = blockIdx.x - DF;
        A2S(0);
        if (t < 160) qkv_tile<4>(2, t, Wv, ws, (u16 (*)[66])b0);
        else wo_tile<2>(t - 160, Wo, ws, (u16 (*)[66])b0);
        A2S(4);
        return;
    }
    A2S(0);
    const float4* twg = (const float4*)(ws + OFF_TW2048);
    float2* f = (float2*)(ws + OFF_F) + (size_t)blockIdx.x * (2 * FS);
    const unsigned* w = (const unsigned*)(ws + OFF_WFT) + (size_t)blockIdx.x * S_;
    float2 u[8], tw[20];
    unsigned wv[8];
#pragma unroll
    for (int r = 0; r < 8; ++r) u[r] = f[tid + 256 * r];
#pragma unroll
    for (int r = 0; r < 10; ++r) { const float4 t = twg[r * 256 + tid]; tw[2 * r] = make_float2(t.x, t.y); tw[2 * r + 1] = make_float2(t.z, t.w); }
#pragma unroll
    for (int r = 0; r < 8; ++r) wv[r] = w[tid + 256 * r];
    {
        const u32x4 sv = {0xFFFFFFFFu, 0xFFFFFFFFu, 0xFFFFFFFFu, 0xFFFFFFFFu};
        const __amdgpu_buffer_rsrc_t rw = mkrsrc(win, W_END);
        for (int g = blockIdx.x * 256 + tid; g < (int)((W_PROBE + W_END - W_ZM) / 16); g += DF * 256)
            st128(sv, rw, g * 16u + (g < (int)(W_PROBE / 16) ? 0u : W_ZM - W_PROBE), 0, A2AUX);
        if (blockIdx.x == 0 && tid >= 32 && tid < 192) ((u32x4*)(ws + OFF_CTL))[tid] = (u32x4){0u, 0u, 0u, 0u};
        if (blockIdx.x == 0 && tid == 0) ((unsigned*)(ws + OFF_CTL))[FL_ROT] = xcc_id();
    }
    A2S(1);
    fft2048<-1>(u, b0, b1, tw, tid);
    A2S(2);
    const float sc = 1.f / ((float)S_ * (float)D_);
#pragma unroll
    for (int r = 0; r < 8; ++r) u[r] = cmul(u[r], make_float2(lo2f(wv[r]) * sc, hi2f(wv[r]) * sc));
    fft2048<1>(u, b1, b0, tw, tid);
    A2S(3);
    {
        const __amdgpu_buffer_rsrc_t rf = mkrsrc(f, 2048 * 8);
#pragma unroll
        for (int r = 0; r < 8; ++r) __builtin_amdgcn_raw_buffer_store_b64((u32x2){__float_as_uint(u[r].x), __float_as_uint(u[r].y)}, rf, tid * 8u, r * 2048u, A2AUX);
    }
    A2S(4);
}

struct RecArgs {
    unsigned* ctl; char* win; const u16* mem0; const u16* xin; u16* out;
    float4* Fnext;
};
constexpr unsigned O_H = (unsigned)OFF_H, O_X1 = (unsigned)OFF_X1, O_Z = (unsigned)OFF_Z, O_Z2 = (unsigned)OFF_Z2, O_PT = (unsigned)OFF_PT, O_QK = (unsigned)OFF_QK;
#define MXV 0x00808080808080AAull
#define MXQ 0x0008080808080000ull
#define NTAIL 5
static_assert(NTAIL >= 3, "the owners release segments up to NSEG - 4");
constexpr int TAILS = NSEG - NTAIL;
static_assert(!((MXV | MXQ) >> TAILS) && !(MXV & 1) && !(MXQ & 3), "segment sets");
constexpr int NREC = 96 + NOWN, NQT = NREC + NOWN, NXT = NQT + 32, NWRK = NXT + 32;
constexpr unsigned SPIN_CAP = 400000u;
__device__ __forceinline__ bool spin_fail(unsigned& spins, unsigned* tmo) {
    if (++spins > SPIN_CAP) { __hip_atomic_store(tmo, 1u, __ATOMIC_RELAXED, __HIP_MEMORY_SCOPE_AGENT); return true; }
    return (spins & 1023u) == 0u && __builtin_amdgcn_readfirstlane(__hip_atomic_load(tmo, __ATOMIC_RELAXED, __HIP_MEMORY_SCOPE_AGENT));
}
#define SST(k) do {} while (0)
#define LSEL 1
#define RT(k) do {} while (0)
#define RTS() do {} while (0)
#define B3ATTR
#define MTA(k)
#define MTRT() do {} while (0)
#define MT(k) do {} while (0)
#define MTS() do {} while (0)
#define FC(x) do {} while (0)
#define DQC 1200
#define DVC 1200
#define M1 44
#define MZ 40
#define MZP 20
#define M2 234
#define LTP (LT0 + 15)
#define LTZ 340
#define PEN 280
#define KSL 1
#define SVO 64
#define DZP 55
#define LT0 342
#define SAD 1
#define PEN2 80
#define PEN1 50
#define PEN0 150
#define DDM2 0
#define DRA 700
#define TSLP 1
#define PFS 0
#define PFOFF 1
#define OPR 2
#define BSPARSE 0
#define ZLAG 4
#define TKP 0
#define DPS 12
#define RZ 63
#define RQ 63
#define R2 63
#define DXT 3000
#define SSW 99
#define LTM 12
#define XS0 10
#define XSU 8
#define XSM 32
#define LTU 4
#define LTD 1
#define QUIET 0
#define QUAD 1
#define LTS 3
#define XSS 3
#define TKS 3
#define LT1 450
#define FDL 2
#define ZLA 3
#define DPT2 3
#define NORND 0
#define LTX 24
#define TKE 200
#define DVO 6000
#define ZPS 4
#define HLA 8
#define PSLP 16
#define PFD 4
#define NPF 3
#define KSL2 1
#define KBQ 4
#define VB1 6
#define VB2 5
#define KR1 3
#define QB2 2
#define NB2 6
#define DZR 24
#define ML 150
#define MN 12
#define CF 190
#define DB 1500
#define DP 200
#define ZSL 1
#define NB1 3
#define DPT 3
#define DDMA 8
#define DZ2 800
#define QEARLY 2
#define OSLP 32
#define OSLP2 1
#define WSLP 64
#define DO0 12000
#define DW0 1200
#define DQT 9000
#define DQ4 700
#define ZEARLY 3
#define HSLP 4
#define RMAP 0
#define DIE 0
#define VMWAIT() do { asm volatile("" ::: "memory"); __builtin_amdgcn_s_waitcnt(0x0F70); asm volatile("" ::: "memory"); } while (0)
#define SB() __builtin_amdgcn_sched_barrier(0)
#define BAR() asm volatile("s_waitcnt lgkmcnt(0)\n\ts_barrier" ::: "memory")
#define PIN1(a) asm volatile("" : "+v"(a) :: "memory")
#define PIN2(a, b) asm volatile("" : "+v"(a), "+v"(b) :: "memory")
#define BAR0() asm volatile("s_barrier" ::: "memory")
__device__ __forceinline__ unsigned fresh_lane() { unsigned l; asm volatile("v_mbcnt_lo_u32_b32 %0, -1, 0\n\tv_mbcnt_hi_u32_b32 %0, -1, %0" : "=v"(l)); return l; }
__device__ __forceinline__ void wait_until(unsigned long long t) { while ((long long)(__builtin_amdgcn_s_memtime() - t) < 0) __builtin_amdgcn_s_sleep(1); }
struct Clk { unsigned ta, ma, cpq, qpc; };
__device__ __forceinline__ int sx24(unsigned v) { return (int)(v << 8) >> 8; }
#define WAIT_FINE \
        "s_memrealtime vcc\n\t" \
        "s_memtime s[100:101]\n\t" \
        "s_brev_b32 %[fi], -2\n\t" \
        "s_waitcnt lgkmcnt(0)\n\t" \
        "s_mov_b32 %[rp], vcc_lo\n\t" \
        "s_lshl_b32 vcc_hi, vcc_lo, 2\n\t" \
        "s_sub_u32 %[x], %[t], vcc_hi\n\t" \
        "s_bfe_i32 %[x], %[x], 0x180000\n\t" \
        "s_min_i32 %[x], %[x], 4000\n\t" \
        "s_max_i32 %[x], %[x], -4000\n\t" \
        "s_add_u32 %[t], vcc_hi, %[x]\n\t" \
        "s_mul_i32 %[x], %[x], %[cq]\n\t" \
        "s_ashr_i32 %[x], %[x], 8\n\t" \
        "s_sub_u32 %[tm], %[t], %[ta]\n\t" \
        "s_bfe_i32 %[tm], %[tm], 0x180000\n\t" \
        "s_min_i32 %[tm], %[tm], 1000000\n\t" \
        "s_max_i32 %[tm], %[tm], -1000000\n\t" \
        "s_mul_i32 %[tm], %[tm], %[cq]\n\t" \
        "s_ashr_i32 %[tm], %[tm], 8\n\t" \
        "s_add_u32 %[tm], %[tm], %[ma]\n\t" \
        "s_sub_u32 %[tm], %[tm], s100\n\t" \
        "s_lshr_b32 vcc_hi, %[cq], 4\n\t" \
        "s_sub_u32 vcc_hi, %[x], vcc_hi\n\t" \
        "s_max_i32 %[tm], %[tm], vcc_hi\n\t" \
        "s_min_i32 %[tm], %[tm], %[x]\n\t" \
        "s_add_u32 %[tm], %[tm], s100\n" \
        "1:\n\t" \
        "s_memrealtime vcc\n\t" \
        "s_memtime s[100:101]\n\t" \
        "s_waitcnt lgkmcnt(0)\n\t" \
        "s_cmp_eq_u32 vcc_lo, %[rp]\n\t" \
        "s_cbranch_scc1 2f\n\t" \
        "s_mov_b32 %[rp], vcc_lo\n\t" \
        "s_lshl_b32 %[x], vcc_lo, 2\n\t" \
        "s_sub_u32 %[x], %[t], %[x]\n\t" \
        "s_bfe_i32 %[x], %[x], 0x180000\n\t" \
        "s_mul_i32 %[x], %[x], %[cq]\n\t" \
        "s_ashr_i32 %[x], %[x], 8\n\t" \
        "s_add_u32 %[x], %[x], s100\n\t" \
        "s_sub_u32 s101, %[x], %[tm]\n\t" \
        "s_cmp_lt_i32 s101, %[fi]\n\t" \
        "s_cselect_b32 %[tm], %[x], %[tm]\n\t" \
        "s_mov_b32 %[fi], 0\n" \
        "2:\n\t" \
        "s_sub_u32 %[x], %[tm], s100\n\t" \
        "s_cmp_gt_i32 %[x], 260\n\t" \
        "s_cbranch_scc1 1b\n\t" \
        "s_cmp_eq_u32 %[fi], 0\n\t" \
        "s_cselect_b32 %[ta], %[t], %[ta]\n\t" \
        "s_cselect_b32 %[ma], %[tm], %[ma]\n" \
        "3:\n\t" \
        "s_memtime s[100:101]\n\t" \
        "s_waitcnt lgkmcnt(0)\n\t" \
        "s_sub_u32 %[x], %[tm], s100\n\t" \
        "s_cmp_gt_i32 %[x], 92\n\t" \
        "s_cbranch_scc1 3b\n\t" \
        "s_max_i32 %[x], %[x], 0\n\t" \
        "s_andn2_b32 %[x], %[x], 3\n\t" \
        "s_sub_u32 %[x], 104, %[x]\n\t" \
        "s_getpc_b64 vcc\n\t" \
        "s_add_u32 vcc_lo, vcc_lo, %[x]\n\t" \
        "s_addc_u32 vcc_hi, vcc_hi, 0\n\t" \
        "s_setpc_b64 vcc\n\t" \
        ".rept 23\n\t" \
        "s_nop 0\n\t" \
        ".endr\n\t"
#define WAIT_FINE_OUT [tm] "=&s"(tm), [rp] "=&s"(rp), [x] "=&s"(x), [fi] "=&s"(fi), [t] "+s"(t), [ta] "+s"(c.ta), [ma] "+s"(c.ma)
__device__ __forceinline__ void wait_fine(unsigned t, Clk& c) {
    unsigned tm, rp, x, fi;
    asm volatile(WAIT_FINE : WAIT_FINE_OUT : [cq] "s"(c.cpq) : "vcc", "scc", "s100", "s101", "memory");
}
__device__ __forceinline__ void wait_fine_bar(unsigned t, Clk& c) {
    unsigned tm, rp, x, fi;
    asm volatile(WAIT_FINE "s_barrier" : WAIT_FINE_OUT : [cq] "s"(c.cpq) : "vcc", "scc", "s100", "s101", "memory");
}
__device__ __forceinline__ void wait_cyc_bar(unsigned tm) {
    unsigned x;
    asm volatile(
        "3:\n\t"
        "s_memtime s[100:101]\n\t"
        "s_waitcnt lgkmcnt(0)\n\t"
        "s_sub_u32 %0, %1, s100\n\t"
        "s_cmp_gt_i32 %0, 92\n\t"
        "s_cbranch_scc1 3b\n\t"
        "s_max_i32 %0, %0, 0\n\t"
        "s_andn2_b32 %0, %0, 3\n\t"
        "s_sub_u32 %0, 104, %0\n\t"
        "s_getpc_b64 vcc\n\t"
        "s_add_u32 vcc_lo, vcc_lo, %0\n\t"
        "s_addc_u32 vcc_hi, vcc_hi, 0\n\t"
        "s_setpc_b64 vcc\n\t"
        ".rept 23\n\t"
        "s_nop 0\n\t"
        ".endr\n\t"
        "s_barrier"
        : "=&s"(x) : "s"(tm) : "vcc", "scc", "s100", "s101", "memory");
}
__device__ __forceinline__ unsigned cyc_at(const Clk& c, int d) {
    d = min(d, 20000);
    asm volatile("" : "+s"(d));
    return c.ma + (unsigned)((max(d, -4000) * (int)c.cpq) >> 8);
}
__device__ __forceinline__ void clk_rate(Clk& c, unsigned ta0, unsigned ma0) {
    const int dq = sx24(c.ta - ta0);
    if (dq > 200) {
        const float r = fminf(fmaxf((float)(c.ma - ma0) * __builtin_amdgcn_rcpf((float)dq), 1.f), 8.f);
        asm volatile("s_nop 1\n\tv_readfirstlane_b32 %0, %2\n\tv_readfirstlane_b32 %1, %3\n\ts_nop 3" : "=s"(c.cpq), "=s"(c.qpc)
                     : "v"((unsigned)(256.f * r + 0.5f)), "v"((unsigned)(65536.f * __builtin_amdgcn_rcpf(r))));
    }
}
__device__ __forceinline__ void clk_init(Clk& c) {
    c.ta = 4u * (unsigned)__builtin_amdgcn_s_memrealtime(); c.ma = (unsigned)__builtin_amdgcn_s_memtime();
    for (int i = 0; i < 2; ++i) {
        const unsigned ta0 = c.ta, ma0 = c.ma;
        wait_fine(c.ta + (i ? 360u : 60u), c);
        clk_rate(c, ta0, ma0);
    }
}
__device__ __forceinline__ unsigned sched_lane(int s) { const unsigned l = fresh_lane(); return (l & 31u) * 128u + (l >> 5) * 4u; }
__device__ __forceinline__ int read_sched(const RecArgs& a, __amdgpu_buffer_rsrc_t rwin, int s, unsigned vo, unsigned to, unsigned f, unsigned g0, int& lt, int& xs, unsigned& fc) {
    int d = (int)((f - g0) & 0xFFFFFFu);
    if (__builtin_expect(__any((f >> 24) != (unsigned)s) || s == 0, 0)) {
        for (unsigned spins = 0; __any((f >> 24) != (unsigned)s);) {
            FC(fc);
            if (spin_fail(spins, a.ctl)) break;
            __builtin_amdgcn_s_sleep(TSLP);
            asm volatile("" ::: "memory");
            if ((f >> 24) != (unsigned)s) f = __builtin_amdgcn_raw_buffer_load_b32(rwin, vo, to, 16);
        }
        const unsigned f0 = __builtin_amdgcn_readfirstlane(f) - 0x100000u;
        d = (int)((f - f0) & 0xFFFFFFu) + (int)((f0 - g0) & 0xFFFFFFu);
    }
    d = max(d, __builtin_amdgcn_update_dpp(0, d, 0xB1, 0xF, 0xF, true));
    d = max(d, __builtin_amdgcn_update_dpp(0, d, 0x4E, 0xF, 0xF, true));
    d = max(d, __builtin_amdgcn_update_dpp(0, d, 0x141, 0xF, 0xF, true));
    d = max(d, __builtin_amdgcn_update_dpp(0, d, 0x140, 0xF, 0xF, true));
    d = max(d, __builtin_amdgcn_update_dpp(0, d, 0x142, 0xA, 0xF, true));
    d = max(__builtin_amdgcn_readlane(d, 16), __builtin_amdgcn_readlane(d, 48));
    const int l0 = lt, x0 = xs;
    d -= 0x400000;
    const bool miss = d > l0 + min(x0 + LTM, LTX);
    if (s >= SAD) { lt = miss ? l0 : min(max(d, l0 - 1 - ((l0 - d) >> LTS)), l0 + LTU); xs = miss ? min(x0 + XSU, XSM) : max(x0 - 1 - (x0 >> XSS), 0); }
    if (s == 1) lt = LT0;
    return 0x400000 + (s < SAD ? d + PEN0 : miss ? d + PEN : l0 + x0) + M1 + M2 + x0;
}
__device__ __forceinline__ unsigned nosent1(unsigned v) { return min(v, 0xFFFFFFFEu); }
__device__ __forceinline__ unsigned smax(u32x4 v) { return max(v[1], v[3]); }
constexpr int QS = 272;
constexpr int TQS = 1152;
constexpr int L_Q = 10240, L_K = L_Q + 2 * TQS, L_S = L_K + 2 * TQS, L_TOK = L_S + 256, L_H = L_TOK + 49152, L_END = L_H + 2 * 40960;
typedef __attribute__((ext_vector_type(4))) short v4i16_t;
__device__ __forceinline__ u32x2 ldtr(const char* p) { return __builtin_bit_cast(u32x2, __builtin_amdgcn_ds_read_tr16_b64_v4i16((LAS v4i16_t*)p)); }
__device__ __forceinline__ u32x2 ldtr1(const LAS char* p) { return __builtin_bit_cast(u32x2, __builtin_amdgcn_ds_read_tr16_b64_v4i16((LAS v4i16_t*)p)); }
__device__ __forceinline__ u32x4 ldtr2(const LAS char* p0, const LAS char* p1) {
    const u32x2 x = __builtin_bit_cast(u32x2, __builtin_amdgcn_ds_read_tr16_b64_v4i16((LAS v4i16_t*)p0)), y = __builtin_bit_cast(u32x2, __builtin_amdgcn_ds_read_tr16_b64_v4i16((LAS v4i16_t*)p1));
    return (u32x4){x[0], x[1], y[0], y[1]};
}
__device__ __forceinline__ u32x4 ldtr2(const char* p0, const char* p1) { const u32x2 x = ldtr(p0), y = ldtr(p1); return (u32x4){x[0], x[1], y[0], y[1]}; }

__device__ __forceinline__ unsigned poll_lim(int& budget) {
    --budget;
    unsigned lim = budget > 0 ? 0xFFFFFFFEu : 0xFFFFFFFFu;
    asm volatile("" : "+s"(lim));
    return lim;
}
#define GATHER \
        "buffer_load_dwordx4 %[vo], %[rw], %[s0] offen sc1 lds\n\t" \
        "buffer_load_dwordx4 %[vo], %[rw], %[s0] offen offset:1024 sc1 lds\n\t" \
        "buffer_load_dwordx4 %[vo], %[rw], %[s0] offen offset:2048 sc1 lds\n\t" \
        "buffer_load_dwordx4 %[vo], %[rw], %[s0] offen offset:3072 sc1 lds\n\t" \
        "s_movk_i32 m0, 0x1000\n\t" \
        "s_nop 0\n\t" \
        "buffer_load_dwordx4 %[vo], %[rw], %[s4] offen sc1 lds\n\t" \
        "buffer_load_dwordx4 %[vo], %[rw], %[s4] offen offset:1024 sc1 lds\n\t" \
        "buffer_load_dwordx4 %[vo], %[rw], %[s4] offen offset:2048 sc1 lds\n\t" \
        "buffer_load_dwordx4 %[vo], %[rw], %[s4] offen offset:3072 sc1 lds\n\t" \
        "s_movk_i32 m0, 0x2000\n\t" \
        "s_nop 0\n\t" \
        "buffer_load_dwordx4 %[vo], %[rw], %[s1] offen sc1 lds\n\t" \
        "buffer_load_dwordx4 %[vo], %[rw], %[s1] offen offset:1024 sc1 lds"
#define GATHER_IN [vo] "v"(vo), [rw] "s"(rwin), [s0] "s"(so0), [s4] "s"(so0 + 4096u), [s1] "s"(so1)
__device__ __forceinline__ void gather_dma(__amdgpu_buffer_rsrc_t rwin, unsigned vo, unsigned so0, unsigned so1) {
    asm volatile("s_mov_b32 m0, 0\n\ts_nop 0\n\t" GATHER :: GATHER_IN : "m0", "memory");
}
__device__ __forceinline__ void gather_at(unsigned t, Clk& c, __amdgpu_buffer_rsrc_t rwin, unsigned vo, unsigned so0, unsigned so1) {
    unsigned tm, rp, x, fi;
    asm volatile("s_mov_b32 m0, 0\n\t" WAIT_FINE GATHER : WAIT_FINE_OUT : [cq] "s"(c.cpq), GATHER_IN : "vcc", "scc", "s100", "s101", "m0", "memory");
}
__device__ __forceinline__ void dma4k(__amdgpu_buffer_rsrc_t r, unsigned vo, unsigned so, unsigned lds) {
    asm volatile(
        "s_mov_b32 m0, %3\n\t"
        "s_nop 0\n\t"
        "buffer_load_dwordx4 %0, %1, %2 offen sc1 lds\n\t"
        "buffer_load_dwordx4 %0, %1, %2 offen offset:1024 sc1 lds\n\t"
        "buffer_load_dwordx4 %0, %1, %2 offen offset:2048 sc1 lds\n\t"
        "buffer_load_dwordx4 %0, %1, %2 offen offset:3072 sc1 lds"
        :: "v"(vo), "s"(r), "s"(so), "s"(lds) : "m0", "memory");
}
__device__ __forceinline__ int bs2(int c) { return (c & ~3) | ((c & 1) << 1) | ((c >> 1) & 1); }
__device__ __forceinline__ void gather_mem0(const RecArgs& a, char* memL, int t0, int nt) {
    for (int c = t0; c < 640; c += nt) {
        const unsigned* src = (const unsigned*)(a.mem0 + ((c & 6) + ((c >> 3) & 1)) * D_ + (c >> 4) * 16 + (c & 1) * 2);
        *(u32x4*)(memL + c * 16) = (u32x4){src[0], src[2], src[4], src[6]};
    }
}
__device__ __forceinline__ f32x2 hadd(f32x4 c) { return (f32x2){c[0], c[1]} + (f32x2){c[2], c[3]}; }
__device__ __forceinline__ unsigned hpack(f32x4 c) { const f32x2 y = hadd(c); return pack2(y[0], y[1]); }
__device__ __forceinline__ u32x8 cat8(u32x4 lo, u32x4 hi) { return (u32x8){lo[0], lo[1], lo[2], lo[3], hi[0], hi[1], hi[2], hi[3]}; }
__device__ __forceinline__ u32x8 ldw(const u16* p) { return cat8(*(const u32x4*)p, *(const u32x4*)(p + 512)); }
__device__ __forceinline__ u32x4 half8(const u32x8& v, int u) { return u ? (u32x4){v[4], v[5], v[6], v[7]} : (u32x4){v[0], v[1], v[2], v[3]}; }
__device__ __forceinline__ void dma1k(__amdgpu_buffer_rsrc_t r, unsigned vo, unsigned so, unsigned lds) {
    asm volatile("s_mov_b32 m0, %3\n\ts_nop 0\n\tbuffer_load_dwordx4 %0, %1, %2 offen sc1 lds" :: "v"(vo), "s"(r), "s"(so), "s"(lds) : "m0", "memory");
}
__device__ __forceinline__ void stage_h(__amdgpu_buffer_rsrc_t rws, int seg, unsigned lds, int t) {
    const unsigned wo = __builtin_amdgcn_readfirstlane(t >> 6) * 1024;
#pragma unroll
    for (int i = 0; i < 5; ++i) dma1k(rws, (t & 63) * 16u, O_H + (unsigned)seg * 40960u + wo + i * 8192u, lds + wo + i * 8192u);
}
__device__ __forceinline__ void wait_h(const RecArgs& a, __amdgpu_buffer_rsrc_t rfl, int seg, int& hrdy) {
    unsigned lim = 63u;
    if (__builtin_expect(hrdy <= seg, 0)) for (unsigned spins = 0;;) {
        asm volatile("" ::: "memory");
        const u32x4 f = __builtin_amdgcn_raw_buffer_load_b128(rfl, min(fresh_lane(), lim) * 128u, W_H, 16);
        const unsigned long long miss = __ballot((f[0] | f[1] | f[2] | f[3]) != 0u);
        hrdy = miss ? __builtin_ctzll(miss) : (int)lim + 1;
        if (hrdy > seg) break;
        if (spin_fail(spins, a.ctl)) break;
        lim = (unsigned)min(seg + HLA, 63);
        __builtin_amdgcn_s_sleep(HSLP);
    }
}
template <int CTRL> __device__ __forceinline__ unsigned dppu(unsigned v) { return (unsigned)__builtin_amdgcn_mov_dpp((int)v, CTRL, 0xF, 0xF, true); }
template <int CTRL> __device__ __forceinline__ u32x4 ror4(u32x4 v) { return (u32x4){dppu<CTRL>(v[0]), dppu<CTRL>(v[1]), dppu<CTRL>(v[2]), dppu<CTRL>(v[3])}; }
template <int N> struct IC { static constexpr int v = N; };
__device__ __forceinline__ float xmax16(float v) {
    auto q = __builtin_amdgcn_permlane16_swap(__float_as_uint(v), __float_as_uint(v), false, false);
    return fmx(__uint_as_float(q[0]), __uint_as_float(q[1]));
}
__device__ __forceinline__ float xsum16(float v) {
    auto q = __builtin_amdgcn_permlane16_swap(__float_as_uint(v), __float_as_uint(v), false, false);
    return __uint_as_float(q[0]) + __uint_as_float(q[1]);
}
__device__ __forceinline__ float addswap16(float a, float b) {
    auto r = __builtin_amdgcn_permlane16_swap(__float_as_uint(a), __float_as_uint(b), false, false);
    return __uint_as_float(r[0]) + __uint_as_float(r[1]);
}
#define LANES() \
    const int lane = (int)fresh_lane(), tid = w * 64 + lane, fr = lane & 15, fq = lane >> 4; \
    const int sidx = (lane & 2) ? 0xEEEE : 0x4444; \
    const int ma0 = fq * 256 + (fr & 1) * 128 + (fr >> 1) * 16; \
    const int prow = 16 * ((fr >> 2) & 1) + 4 * (fr >> 3) + (fr & 3), hoff = fr * 128; \
    (void)tid; (void)sidx; (void)ma0; (void)prow; (void)hoff

__device__ __forceinline__ int seg_after(unsigned long long m, int j) { m = j >= 63 ? 0ull : m >> (j + 1); return m ? j + 1 + __builtin_ctzll(m) : NSEG; }
__device__ __forceinline__ int seg_done(unsigned long long m, int f0, int f1) { return min(seg_after(~m, f0), seg_after(m, f1)) - 1; }
template <int OFF> __device__ __forceinline__ void ldfrag(u32x4& d, const LAS char* p) { asm volatile("ds_read_b128 %0, %1 offset:%2" : "=v"(d) : "v"(p), "n"(OFF) : "memory"); }
template <int U, class FM, class FV> __device__ __forceinline__ void project(const u32x8 (&wt)[2][10], f32x4 (&tc)[2][2], u32x4 (&bb)[8], const LAS char* c0, const LAS char* c1, const LAS char* n0, const LAS char* n1, FM mid, FV vm) {
    constexpr int t = U >> 2, k = U & 3, v = (U + 8) % 40;
    if (U == 32) mid();
    vm(IC<U>());
    const LAS char* p = U < 32 ? (k >> 1 ? c1 : c0) : (k >> 1 ? n1 : n0);
    if (U < 2) asm volatile("s_nop 0\n\ts_waitcnt lgkmcnt(7)\n\tv_mfma_f32_16x16x32_bf16 %0, %3, %2, 0\n\tv_mfma_f32_16x16x32_bf16 %1, %4, %2, 0\n\tds_read_b128 %2, %5 offset:%6"
                            : "=&v"(tc[0][k & 1]), "=&v"(tc[1][k & 1]), "+v"(bb[U & 7]) : "v"(half8(wt[0][t], k >> 1)), "v"(half8(wt[1][t], k >> 1)), "v"(p), "n"((v >> 2) * 4096 + (k & 1) * 2048) : "memory");
    else asm volatile(".rept %7\n\ts_nop 0\n\t.endr\n\ts_waitcnt lgkmcnt(7)\n\tv_mfma_f32_16x16x32_bf16 %0, %3, %2, %0\n\tv_mfma_f32_16x16x32_bf16 %1, %4, %2, %1\n\tds_read_b128 %2, %5 offset:%6"
                      : "+v"(tc[0][k & 1]), "+v"(tc[1][k & 1]), "+v"(bb[U & 7]) : "v"(half8(wt[0][t], k >> 1)), "v"(half8(wt[1][t], k >> 1)), "v"(p), "n"((v >> 2) * 4096 + (k & 1) * 2048), "n"(U < 8 ? 1 : 0) : "memory");
    if constexpr (U < 39) project<U + 1>(wt, tc, bb, c0, c1, n0, n1, mid, vm);
    else asm volatile("s_nop 7" : "+v"(tc[0][0]), "+v"(tc[0][1]), "+v"(tc[1][0]), "+v"(tc[1][1]));
}
__device__ __forceinline__ void project_end(u32x4 (&bb)[8]) {
    asm volatile("s_waitcnt lgkmcnt(0)" : "+v"(bb[0]), "+v"(bb[1]), "+v"(bb[2]), "+v"(bb[3]), "+v"(bb[4]), "+v"(bb[5]), "+v"(bb[6]), "+v"(bb[7]) :: "memory");
}
__device__ __forceinline__ void project0(u32x4 (&bb)[8], const LAS char* c0, const LAS char* c1) {
    ldfrag<0>(bb[0], c0); ldfrag<2048>(bb[1], c0); ldfrag<0>(bb[2], c1); ldfrag<2048>(bb[3], c1);
    ldfrag<4096>(bb[4], c0); ldfrag<6144>(bb[5], c0); ldfrag<4096>(bb[6], c1); ldfrag<6144>(bb[7], c1);
    project_end(bb);
}

__global__ __launch_bounds__(512, 2) B3ATTR void b3_kernel(RecArgs a) {
    extern __shared__ __attribute__((aligned(16))) char smem[];
    char* memL = smem;
    char* tokL = smem + L_TOK;
    char* hL = smem + L_H;
    const int w = __builtin_amdgcn_readfirstlane(threadIdx.x >> 6);
    const unsigned vx = blockIdx.x + __builtin_amdgcn_readfirstlane(a.ctl[FL_ROT]) + 4u * DIE;
    int bid;
    {
        const int j = blockIdx.x >> 3, x = vx & 3;
        if (!(vx & 4)) bid = j < 8 ? 8 * x + j : j < 16 ? 24 + 8 * x + j : j < 26 ? 80 + 10 * x + j : 255 - 4 * (j - 26) - x;
        else bid = j < 8 ? 64 + 8 * x + j : j < 16 ? NQT - 8 + 8 * x + j : j < 26 ? NREC - 16 + 10 * x + j : NXT - 26 + 6 * x + j;
    }
    const __amdgpu_buffer_rsrc_t rwin = mkrsrc(a.win, W_END);
    char* const ws = (char*)a.ctl;
    const __amdgpu_buffer_rsrc_t rws = mkrsrc(ws, (unsigned)OFF_WIN);
    unsigned long long tref = 0;
    const unsigned long long tstart = __builtin_amdgcn_s_memtime();
    const unsigned nh = __builtin_amdgcn_readfirstlane(a.ctl[FL_NEAR + 4 * DIE] & 1u) * HALF;
    const unsigned nz = __builtin_amdgcn_readfirstlane(a.ctl[FL_NEAR + 8 + 4 * DIE] & 1u) * HALF;
    auto quad = [&](int p) {
        const unsigned* f = a.ctl + FL_NEAR + p + 4 * DIE;
        return (unsigned)__builtin_amdgcn_readfirstlane((3u * (f[0] & 2u) + 2u * ((~f[1] & 2u) + (f[2] & 2u) + (~f[3] & 2u))) > 9u);
    };
    const unsigned mc = (vx & 3u) ^ quad(0);
    const unsigned qz = quad(8);
    auto zpair = [&](unsigned x, unsigned k) { return (((x >> 1) * 10u + k) * 2u + ((x & 1u) ^ qz)) * PAIR + nz; };
    const unsigned mo = W_MEM + mc * PAIR + nh, mo1 = W_MEM + (4u + (mc & 1u)) * PAIR + nh + (mc >> 1) * 2048u;
    const unsigned tro = W_T + (mc & 1u) * PAIR + nh + (mc >> 1) * 4096u;
    auto two = [&]() { const unsigned l = fresh_lane(); return l < 4 ? (l >> 1) * PAIR + (l & 1) * 4096u + nh : 0x40000000u; };
    const u32x4 SENT = {0xFFFFFFFFu, 0xFFFFFFFFu, 0xFFFFFFFFu, 0xFFFFFFFFu};
    const f32x4 z4 = {0.f, 0.f, 0.f, 0.f};
    int hrdy = 0, budget = (int)SPIN_CAP;
    unsigned fc0 = 0, fc1 = 0, fc2 = 0;
    Clk ck = {0u, 0u, 1600u, 10486u};
    SST(0);
    if (threadIdx.x == 0) ((unsigned long long*)(ws + 4096 + 524288))[(a.Fnext ? 256 : 768) + blockIdx.x] = __builtin_amdgcn_s_memrealtime();
    const bool xt = bid >= NXT && bid < NWRK;
    RowLds& L = *(RowLds*)smem;
    unsigned tpp = 0;
    int lt = LT1, xs = XS0;
    struct Tk { unsigned vo, tw, sl; const LAS char* sp; };
    auto tk_init = [&]() {
        const unsigned l = fresh_lane();
        Tk k = {l * 16u, two(), (l & 31u) * 128u + (l >> 5) * 4u, (const LAS char*)(memL + l * 64 + 4 + ((l >> 1) & 7) * 8)};
        PIN2(k.vo, k.tw); PIN2(k.sl, k.sp);
        return k;
    };
    auto sched2 = [&](Tk& k, int s, unsigned f) {
        const unsigned g0 = tpp - 0x400000u;
        tpp = g0 + (unsigned)read_sched(a, rwin, s, k.sl, tro + (unsigned)(s & 3) * 2u * PAIR, f, g0, lt, xs, fc1);
        k.sl &= ~4u;
        RT(6);
    };
    auto sched = [&](Tk& k, int s) { sched2(k, s, __builtin_amdgcn_raw_buffer_load_b32(rwin, k.sl, tro + (unsigned)(s & 3) * 2u * PAIR, 16)); };
    unsigned it = 0;
    auto fetch = [&](const Tk& k, int s, bool first, unsigned lim, unsigned dc, unsigned fo, bool tell) {
        const unsigned sb = (unsigned)(s & 3) * 6u * PAIR, ta0 = ck.ta, mm0 = ck.ma;
        if (__builtin_expect(s > 0, 1)) {
            if (__builtin_expect(first, 1)) { const unsigned te = cyc_at(ck, sx24(tpp - TKE - ck.ta)); while ((int)((unsigned)__builtin_amdgcn_s_memtime() - te) < 0) __builtin_amdgcn_s_sleep(1); }
            if (__builtin_expect(first, 1)) gather_at(tpp, ck, rwin, k.vo, mo + sb, mo1 + sb); else { const unsigned c = (mc + 1u + (it & 1u)) & 3u; gather_dma(rwin, k.vo, W_MEM + c * PAIR + nh + sb, W_MEM + (4u + (c & 1u)) * PAIR + nh + (c >> 1) * 2048u + sb); }
        }
        __builtin_amdgcn_s_setprio(0);
        RT(1);
        clk_rate(ck, ta0, mm0);
        unsigned tb = ck.ma - dc, tg = (unsigned)s << 24, so = W_T + (unsigned)(s & 3) * 2u * PAIR + fo;
        asm volatile("" : "+s"(tb), "+s"(tg), "+s"(so));
        asm volatile("s_waitcnt vmcnt(%1) lgkmcnt(0)\n\ts_barrier\n\ts_waitcnt vmcnt(%2)\n\ts_barrier\n\ts_waitcnt vmcnt(0)\n\ts_memtime %0\n\ts_barrier"
                     : "=s"(tref) : "n"(10 - NB1), "n"(10 - NB2) : "memory");
        MT(1);
        if (!tell) { RT(3); return; }
        unsigned x0 = *(const LAS unsigned*)k.sp, x1 = *(const LAS unsigned*)(k.sp + 4096), x2 = *(const LAS unsigned*)(k.sp + 6144);
        __builtin_amdgcn_s_waitcnt(0xC07F);
        asm volatile("" : "+s"(tref) :: "memory");
        const unsigned fw = ((ck.ta + ((((unsigned)tref - tb) * ck.qpc) >> 16)) & 0xFFFFFFu) | tg;
        __builtin_amdgcn_raw_buffer_store_b32(fw, rwin, k.tw, __any(max(max(x0, x1), x2) > lim) ? 0x40000000u : so, 16);
        RT(3);
    };
    if (bid >= 96 && (bid < NQT || bid >= NWRK)) {
        const int fi = bid >= NWRK ? 255 - bid : NQT + 15 - bid;
        for (int u = fi; u < 256; u += 56) {
            a3_unit((const float4*)(ws + OFF_F), a.xin, u * 8, mkrsrc(ws + OFF_X1, S_ * D_ * 2), mkrsrc(ws + OFF_H, S_ * D_ * 2), L, (const float4*)(ws + OFF_TW640));
            VMWAIT();
            __syncthreads();
            if (threadIdx.x == 0) __builtin_amdgcn_raw_buffer_store_b32(0u, rwin, 0u, W_H + (unsigned)((u >> 2) * 128 + (u & 3) * 4), 16);
            if (u < 96) SST(1);
            if (fi >= 56) break;
            if (u < 96) u += 40;
        }
    }
    if ((bid >= 32 && bid < 96) || xt) {
        const int hh = (xt ? bid - NXT : bid) & 31;
        u32x8 wt[2][10];
        const bool vt = bid >= 64;
        const unsigned long long mv = xt ? MXV : ~MXV;
        auto nextv = [&](int j) { return min(seg_after(mv, j), NSEG - 1); };
        const int jv1 = nextv(-1), jv2 = nextv(jv1);
        {
            LANES();
            if (!vt) gather_mem0(a, memL, tid, 512);
            if (xt || !vt) wait_until(tstart + (xt ? DXT : DVO));
            const u16* pv = w < 4 ? (const u16*)(ws + OFF_WQKVT) + (size_t)2 * HD * D_ + (size_t)(hh * 8 + w) * 10240 + lane * 8 : (const u16*)(ws + OFF_WOT) + (size_t)(hh * 40 + 10 * (w - 4)) * 2048 + lane * 8;
            const int sg = w < 4 ? 40960 : 1024, st = w < 4 ? 1024 : 2048;
#pragma unroll
            for (int t = 0; t < 10; ++t) { wt[0][t] = ldw(pv + t * st); wt[1][t] = ldw(pv + sg + t * st); }
            __builtin_amdgcn_s_waitcnt(0x0F70);
            if (tid < 8) *(unsigned*)(smem + L_S + tid * 4) = 0u;
            if (!vt && w == 7) clk_init(ck);
            SST(1);
            if (vt) {
                if (w == 0) { wait_h(a, rwin, jv2, hrdy); *(volatile LAS int*)(smem + L_S + 32) = hrdy; }
                BAR();
                hrdy = __builtin_amdgcn_readfirstlane(*(volatile LAS int*)(smem + L_S + 32));
                SST(2);
                stage_h(rws, jv1, L_H, tid);
                stage_h(rws, jv2, L_H + 40960, tid);
            }
        }
        unsigned zb = W_ZM;
        auto steps = [&](auto cc) {
            constexpr int C = decltype(cc)::v & 1;
            constexpr bool TK = decltype(cc)::v == 3;
            LANES();
            Tk tk = {0u, 0u, 0x40000000u, nullptr};
            if (TK) { tk = tk_init(); __builtin_amdgcn_s_setprio(TKP); }
            const unsigned zi = (unsigned)(hh * 128 + fr * 8) * 2u;
            unsigned zo0 = zpair(w & 3, fq) + zi, zo1 = zo0 + 8 * PAIR, zo2 = zpair(w & 3, 8 + (fq >> 1)) + zi + (fq & 1) * 8;
            const LAS char* va = (const LAS char*)(smem + L_Q + (fr & 1) * 544 + ((fr >> 1) & 1) * 1152 + (16 * fq + 2 * (fr >> 2)) * 4);
            LAS char* vw = (LAS char*)(smem + L_Q + (fq & 1) * 544 + (fq >> 1) * 1152 + (16 * w + bs2(fr)) * 4);
            const LAS char* mp0 = (const LAS char*)(memL + ma0);
            PIN2(zo0, zo1); PIN1(zo2);
            if (C == 0) { PIN2(mp0, vw); } else PIN1(va);
            u32x4 za = SENT, zc = SENT;
            u32x2 ze = {0u, 0u};
            auto put_z2 = [&](int s) {
                const unsigned z2b = s < NSEG ? O_Z2 + (unsigned)s * (NOWN * 8192u) + (unsigned)(10 * (w & 3) * 8192 + hh * 256) : 0x40000000u, l2 = fresh_lane();
                st128(za, rws, (l2 >> 4) * 8192u + (l2 & 15u) * 16u, z2b, 16);
                st128(zc, rws, (l2 >> 4) * 8192u + (l2 & 15u) * 16u, z2b + 4u * 8192u, 16);
                __builtin_amdgcn_raw_buffer_store_b64(ze, rws, (l2 >> 5) * 8192u + (l2 & 15u) * 16u + ((l2 >> 4) & 1u) * 8u, z2b + 8u * 8192u, 16);
            };
            for (int s = 0; s < NSEG; ++s) {
                RT(0);
                bool redo = false;
                unsigned sf = 0;
                do {
                    ++it;
                    if (C == 0) {
                        const unsigned lim = s > 0 ? poll_lim(budget) : ~0u;
                        asm volatile(
                            "v_mov_b64 v[200:201], 0\n\t"
                            "v_mov_b64 v[202:203], 0\n\t"
                            "v_mov_b64 v[204:205], 0\n\t"
                            "v_mov_b64 v[206:207], 0\n\t"
                            "v_mov_b32 v208, 0\n\t"
                            "s_setprio 2\n\t"
                            "s_waitcnt lgkmcnt(0)\n\t"
                            "s_barrier\n\t"
                            MTA(0)
                            "ds_read_b128 v[212:215], %[mp] offset:0\n\t"
                            "ds_read_b128 v[216:219], %[mp] offset:1024\n\t"
                            "ds_read_b128 v[220:223], %[mp] offset:2048\n\t"
                            "s_barrier\n\t"
                            "ds_read_b128 v[224:227], %[mp] offset:3072\n\t"
                            "ds_read_b128 v[228:231], %[mp] offset:4096\n\t"
                            "ds_read_b128 v[232:235], %[mp] offset:5120\n\t"
                            "s_waitcnt lgkmcnt(5)\n\t"
                            "v_smfmac_f32_16x16x64_bf16 v[200:203], v[212:215], %[w00], %[ix]\n\t"
                            "s_waitcnt lgkmcnt(4)\n\t"
                            "v_smfmac_f32_16x16x64_bf16 v[200:203], v[216:219], %[w01], %[ix]\n\t"
                            "s_barrier\n\t"
                            MTA(1)
                            "ds_read_b128 v[236:239], %[mp] offset:6144\n\t"
                            "s_waitcnt lgkmcnt(4)\n\t"
                            "v_smfmac_f32_16x16x64_bf16 v[200:203], v[220:223], %[w02], %[ix]\n\t"
                            "ds_read_b128 v[240:243], %[mp] offset:7168\n\t"
                            "s_waitcnt lgkmcnt(4)\n\t"
                            "v_smfmac_f32_16x16x64_bf16 v[200:203], v[224:227], %[w03], %[ix]\n\t"
                            "ds_read_b128 v[244:247], %[mp] offset:8192\n\t"
                            "s_waitcnt lgkmcnt(4)\n\t"
                            "v_smfmac_f32_16x16x64_bf16 v[200:203], v[228:231], %[w04], %[ix]\n\t"
                            "ds_read_b128 v[248:251], %[mp] offset:9216\n\t"
                            "s_waitcnt lgkmcnt(4)\n\t"
                            "v_smfmac_f32_16x16x64_bf16 v[200:203], v[232:235], %[w05], %[ix]\n\t"
                            "s_waitcnt lgkmcnt(3)\n\t"
                            "v_smfmac_f32_16x16x64_bf16 v[200:203], v[236:239], %[w06], %[ix]\n\t"
                            "s_waitcnt lgkmcnt(2)\n\t"
                            "v_smfmac_f32_16x16x64_bf16 v[200:203], v[240:243], %[w07], %[ix]\n\t"
                            "s_waitcnt lgkmcnt(1)\n\t"
                            "v_smfmac_f32_16x16x64_bf16 v[200:203], v[244:247], %[w08], %[ix]\n\t"
                            "s_waitcnt lgkmcnt(0)\n\t"
                            "v_smfmac_f32_16x16x64_bf16 v[200:203], v[248:251], %[w09], %[ix]\n\t"
                            "s_nop 2\n\t"
                            "v_smfmac_f32_16x16x64_bf16 v[204:207], v[212:215], %[w10], %[ix]\n\t"
                            "s_nop 2\n\t"
                            "v_smfmac_f32_16x16x64_bf16 v[204:207], v[216:219], %[w11], %[ix]\n\t"
                            "v_add_f32 v200, v200, v202\n\t"
                            "v_add_f32 v201, v201, v203\n\t"
                            "v_cvt_pk_bf16_f32 v200, v200, v201\n\t"
                            "ds_write_b32 %[vw], v200\n\t"
                            MTA(2)
                            "v_smfmac_f32_16x16x64_bf16 v[204:207], v[220:223], %[w12], %[ix]\n\t"
                            "v_max3_u32 v208, v208, v212, v213\n\t"
                            "v_max3_u32 v208, v208, v214, v215\n\t"
                            "v_smfmac_f32_16x16x64_bf16 v[204:207], v[224:227], %[w13], %[ix]\n\t"
                            "v_max3_u32 v208, v208, v216, v217\n\t"
                            "v_max3_u32 v208, v208, v218, v219\n\t"
                            "v_smfmac_f32_16x16x64_bf16 v[204:207], v[228:231], %[w14], %[ix]\n\t"
                            "v_max3_u32 v208, v208, v220, v221\n\t"
                            "v_max3_u32 v208, v208, v222, v223\n\t"
                            "v_smfmac_f32_16x16x64_bf16 v[204:207], v[232:235], %[w15], %[ix]\n\t"
                            "v_max3_u32 v208, v208, v224, v225\n\t"
                            "v_max3_u32 v208, v208, v226, v227\n\t"
                            "v_smfmac_f32_16x16x64_bf16 v[204:207], v[236:239], %[w16], %[ix]\n\t"
                            "v_max3_u32 v208, v208, v228, v229\n\t"
                            "v_max3_u32 v208, v208, v230, v231\n\t"
                            "v_smfmac_f32_16x16x64_bf16 v[204:207], v[240:243], %[w17], %[ix]\n\t"
                            "s_waitcnt lgkmcnt(0)\n\t"
                            "s_barrier\n\t"
                            MTA(5)
                            "v_smfmac_f32_16x16x64_bf16 v[204:207], v[244:247], %[w18], %[ix]\n\t"
                            "v_max3_u32 v208, v208, v232, v233\n\t"
                            "v_max3_u32 v208, v208, v234, v235\n\t"
                            "v_smfmac_f32_16x16x64_bf16 v[204:207], v[248:251], %[w19], %[ix]\n\t"
                            "v_max3_u32 v208, v208, v236, v237\n\t"
                            "v_max3_u32 v208, v208, v238, v239\n\t"
                            "v_max3_u32 v208, v208, v240, v241\n\t"
                            "v_max3_u32 v208, v208, v242, v243\n\t"
                            "v_max3_u32 v208, v208, v244, v245\n\t"
                            "v_max3_u32 v208, v208, v246, v247\n\t"
                            "v_max3_u32 v208, v208, v248, v249\n\t"
                            "v_max3_u32 v208, v208, v250, v251\n\t"
                            "v_cmp_lt_u32 vcc, %[lim], v208\n\t"
                            "v_add_f32 v204, v204, v206\n\t"
                            "v_add_f32 v205, v205, v207\n\t"
                            "v_cvt_pk_bf16_f32 v204, v204, v205\n\t"
                            "ds_write_b32 %[vw], v204 offset:256\n\t"
                            MTA(3)
                            "s_cbranch_vccz 1f\n\t"
                            "v_mov_b32 v208, %[it]\n\t"
                            "v_mov_b32 v209, 0\n\t"
                            "ds_write_b32 v209, v208 offset:%[ls]\n\t"
                            "1:\n\t"
                            "s_waitcnt vmcnt(0) lgkmcnt(0)\n\t"
                            "s_barrier\n\t"
                            "s_setprio 0\n\t"
                            :: [mp] "v"(mp0), [vw] "v"(vw), [ix] "v"(sidx), [lim] "s"(lim), [it] "s"(it), [ls] "n"(L_S),
                              [w00] "v"(wt[0][0]), [w01] "v"(wt[0][1]), [w02] "v"(wt[0][2]), [w03] "v"(wt[0][3]), [w04] "v"(wt[0][4]), [w05] "v"(wt[0][5]), [w06] "v"(wt[0][6]), [w07] "v"(wt[0][7]), [w08] "v"(wt[0][8]), [w09] "v"(wt[0][9]), [w10] "v"(wt[1][0]), [w11] "v"(wt[1][1]), [w12] "v"(wt[1][2]), [w13] "v"(wt[1][3]), [w14] "v"(wt[1][4]), [w15] "v"(wt[1][5]), [w16] "v"(wt[1][6]), [w17] "v"(wt[1][7]), [w18] "v"(wt[1][8]), [w19] "v"(wt[1][9])
                            : "v200", "v201", "v202", "v203", "v204", "v205", "v206", "v207", "v208", "v209", "v212", "v213", "v214", "v215", "v216", "v217", "v218", "v219", "v220", "v221", "v222", "v223", "v224", "v225", "v226", "v227", "v228", "v229", "v230", "v231", "v232", "v233", "v234", "v235", "v236", "v237", "v238", "v239", "v240", "v241", "v242", "v243", "v244", "v245", "v246", "v247", "v248", "v249", "v250", "v251", "vcc", "memory");
                        MT(4);
                        redo = __builtin_amdgcn_readfirstlane(*(volatile LAS unsigned*)(smem + L_S)) == it;
                        if (redo) FC(fc0);
                    } else {
                        f32x16 ca = {}, cb = {};
                        f32x8 cc = {};
                        asm volatile("" : "+{v[212:227]}"(ca), "+{v[228:243]}"(cb), "+{v[244:251]}"(cc));
                        unsigned sr = tro + (unsigned)(s & 3) * 2u * PAIR;
                        asm volatile("" : "+s"(sr));
                        if (TK) fetch(tk, s, !redo, s > 0 ? poll_lim(budget) : ~0u, DVC, (unsigned)hh * 128u + 4u, __builtin_expect(s == 0, 0));
                        else asm volatile("s_waitcnt lgkmcnt(0)\n\ts_barrier\n\ts_barrier\n\ts_barrier" ::: "memory");
                        unsigned vd, vv, zs = zb;
                        u32x4 af1;
                        asm volatile(
                            "s_setprio 1\n\t"
                            "s_waitcnt lgkmcnt(0)\n\t"
                            "s_barrier\n\t"
                            "ds_read_b64_tr_b16 v[192:193], %[va]\n\t"
                            "ds_read_b64_tr_b16 v[194:195], %[va] offset:32\n\t"
                            "s_waitcnt lgkmcnt(0)\n\t"
                            "v_smfmac_f32_16x16x64_bf16 v[212:215], v[192:195], %[w0], %[ix]\n\t"
                            "v_smfmac_f32_16x16x64_bf16 v[220:223], v[192:195], %[w2], %[ix]\n\t"
                            "v_smfmac_f32_16x16x64_bf16 v[216:219], v[192:195], %[w1], %[ix]\n\t"
                            "v_smfmac_f32_16x16x64_bf16 v[224:227], v[192:195], %[w3], %[ix]\n\t"
                            "s_barrier\n\t"
                            MTA(4)
                            "ds_read_b64_tr_b16 v[196:197], %[va] offset:256\n\t"
                            "v_smfmac_f32_16x16x64_bf16 v[228:231], v[192:195], %[w4], %[ix]\n\t"
                            "ds_read_b64_tr_b16 v[198:199], %[va] offset:288\n\t"
                            "v_smfmac_f32_16x16x64_bf16 v[236:239], v[192:195], %[w6], %[ix]\n\t"
                            "ds_read_b32 v210, %[vz] offset:%[ls]\n\t"
                            "v_smfmac_f32_16x16x64_bf16 v[232:235], v[192:195], %[w5], %[ix]\n\t"
                            "v_smfmac_f32_16x16x64_bf16 v[240:243], v[192:195], %[w7], %[ix]\n\t"
                            "v_smfmac_f32_16x16x64_bf16 v[244:247], v[192:195], %[w8], %[ix]\n\t"
                            "v_smfmac_f32_16x16x64_bf16 v[248:251], v[192:195], %[w9], %[ix]\n\t"
                            "buffer_load_dword %[sf], %[sl], %[rw], %[sr] offen sc1\n\t"
                            : [sf] "=&v"(sf), "+{v[212:227]}"(ca), "+{v[228:243]}"(cb), "+{v[244:251]}"(cc), "=&{v[196:199]}"(af1), "=&{v210}"(vv)
                            : [va] "v"(va), [vz] "v"(0u), [ls] "n"(L_S), [ix] "v"(sidx), [sl] "v"(tk.sl), [sr] "s"(sr), [rw] "s"(rwin),
                              [w0] "v"(wt[0][0]), [w1] "v"(wt[0][1]), [w2] "v"(wt[0][2]), [w3] "v"(wt[0][3]), [w4] "v"(wt[0][4]), [w5] "v"(wt[0][5]), [w6] "v"(wt[0][6]), [w7] "v"(wt[0][7]), [w8] "v"(wt[0][8]), [w9] "v"(wt[0][9])
                            : "v192", "v193", "v194", "v195", "memory");
                        asm volatile(
                            "s_waitcnt lgkmcnt(0)\n\t"
                            "v_smfmac_f32_16x16x64_bf16 v[212:215], v[196:199], %[w0], %[ix]\n\t"
                            "v_readfirstlane_b32 %[vd], v210\n\t"
                            "v_smfmac_f32_16x16x64_bf16 v[220:223], v[196:199], %[w2], %[ix]\n\t"
                            "s_cmp_eq_u32 %[vd], %[it]\n\t"
                            "s_cselect_b32 %[zs], 2.0, %[zs]\n\t"
                            "v_smfmac_f32_16x16x64_bf16 v[216:219], v[196:199], %[w1], %[ix]\n\t"
                            "s_nop 1\n\t"
                            "v_smfmac_f32_16x16x64_bf16 v[224:227], v[196:199], %[w3], %[ix]\n\t"
                            "v_add_f32 v212, v212, v214\n\t"
                            "v_smfmac_f32_16x16x64_bf16 v[228:231], v[196:199], %[w4], %[ix]\n\t"
                            "v_add_f32 v213, v213, v215\n\t"
                            "v_smfmac_f32_16x16x64_bf16 v[236:239], v[196:199], %[w6], %[ix]\n\t"
                            "v_add_f32 v220, v220, v222\n\t"
                            "v_smfmac_f32_16x16x64_bf16 v[232:235], v[196:199], %[w5], %[ix]\n\t"
                            "v_add_f32 v221, v221, v223\n\t"
                            "v_smfmac_f32_16x16x64_bf16 v[240:243], v[196:199], %[w7], %[ix]\n\t"
                            "v_cvt_pk_bf16_f32 v200, v212, v213\n\t"
                            "v_smfmac_f32_16x16x64_bf16 v[244:247], v[196:199], %[w8], %[ix]\n\t"
                            "v_cvt_pk_bf16_f32 v201, v220, v221\n\t"
                            "v_smfmac_f32_16x16x64_bf16 v[248:251], v[196:199], %[w9], %[ix]\n\t"
                            "v_add_f32 v216, v216, v218\n\t"
                            "v_add_f32 v217, v217, v219\n\t"
                            "v_add_f32 v224, v224, v226\n\t"
                            "v_add_f32 v225, v225, v227\n\t"
                            "v_cvt_pk_bf16_f32 v202, v216, v217\n\t"
                            "v_cvt_pk_bf16_f32 v203, v224, v225\n\t"
                            "v_permlane32_swap_b32 v200, v201\n\t"
                            "v_pk_add_f32 v[228:229], v[228:229], v[230:231]\n\t"
                            "v_permlane32_swap_b32 v202, v203\n\t"
                            "v_min_u32 v201, -2, v201\n\t"
                            "v_pk_add_f32 v[236:237], v[236:237], v[238:239]\n\t"
                            "v_min_u32 v203, -2, v203\n\t"
                            "v_permlane16_swap_b32 v200, v202\n\t"
                            "v_pk_add_f32 v[232:233], v[232:233], v[234:235]\n\t"
                            "v_permlane16_swap_b32 v201, v203\n\t"
                            "v_pk_add_f32 v[240:241], v[240:241], v[242:243]\n\t"
                            MTA(5)
                            "buffer_store_dwordx4 v[200:203], %[z0], %[rw], %[zs] offen sc1\n\t"
                            "v_cvt_pk_bf16_f32 v204, v228, v229\n\t"
                            "v_cvt_pk_bf16_f32 v205, v236, v237\n\t"
                            "v_cvt_pk_bf16_f32 v206, v232, v233\n\t"
                            "v_cvt_pk_bf16_f32 v207, v240, v241\n\t"
                            "v_permlane32_swap_b32 v204, v205\n\t"
                            "v_pk_add_f32 v[244:245], v[244:245], v[246:247]\n\t"
                            "v_permlane32_swap_b32 v206, v207\n\t"
                            "v_min_u32 v205, -2, v205\n\t"
                            "v_pk_add_f32 v[248:249], v[248:249], v[250:251]\n\t"
                            "v_min_u32 v207, -2, v207\n\t"
                            "v_permlane16_swap_b32 v204, v206\n\t"
                            "v_cvt_pk_bf16_f32 v208, v244, v245\n\t"
                            "v_permlane16_swap_b32 v205, v207\n\t"
                            "v_cvt_pk_bf16_f32 v209, v248, v249\n\t"
                            "buffer_store_dwordx4 v[204:207], %[z1], %[rw], %[zs] offen sc1\n\t"
                            "s_nop 0\n\t"
                            "v_permlane32_swap_b32 v208, v209\n\t"
                            "v_min_u32 v209, -2, v209\n\t"
                            "buffer_store_dwordx2 v[208:209], %[z2], %[rw], %[zs] offen sc1\n\t"
                            : [vd] "=&s"(vd), [zs] "+s"(zs), "=&{v[200:203]}"(za), "=&{v[204:207]}"(zc), "=&{v[208:209]}"(ze), "+{v[212:227]}"(ca), "+{v[228:243]}"(cb), "+{v[244:251]}"(cc), "+v"(sf)
                            : [ix] "v"(sidx), [it] "s"(it), [rw] "s"(rwin), [z0] "v"(zo0), [z1] "v"(zo1), [z2] "v"(zo2), "{v[196:199]}"(af1), "{v210}"(vv),
                              [w0] "v"(wt[1][0]), [w1] "v"(wt[1][1]), [w2] "v"(wt[1][2]), [w3] "v"(wt[1][3]), [w4] "v"(wt[1][4]), [w5] "v"(wt[1][5]), [w6] "v"(wt[1][6]), [w7] "v"(wt[1][7]), [w8] "v"(wt[1][8]), [w9] "v"(wt[1][9])
                            : "scc", "memory");
                        redo = vd == it;
                        RT(4);
                        MT(6);
                        tref = __builtin_amdgcn_s_memtime();
                        __builtin_amdgcn_s_setprio(TKP);
                        asm volatile("s_waitcnt vmcnt(3)" : "+v"(sf) :: "memory");
                    }
                } while (__builtin_expect(redo, 0));
                if (s < 8) SST(8 + s);
                if (C == 0) {
                    if (s > 0) {
                        __builtin_amdgcn_s_sleep(DZR);
                        const unsigned zq = zb == W_ZM ? W_ZM + 2u * NOWN * PAIR : zb - NOWN * PAIR;
                        unsigned s1;
                        asm volatile("v_mov_b32 %0, -1" : "=v"(s1));
                        st128(((u32x4){s1, s1, s1, s1}), rwin, zo0, zq, 16);
                        st128(((u32x4){s1, s1, s1, s1}), rwin, zo1, zq, 16);
                        __builtin_amdgcn_raw_buffer_store_b64((u32x2){s1, s1}, rwin, zo2, zq, 16);
                    }
                } else {
                    if (s > TAILS) __builtin_amdgcn_raw_buffer_store_b32((unsigned)s - 1u, rwin, 0u, W_FZ2 + (unsigned)(hh * 128 + (w & 3) * 4), 16);
                    if (TK && s + 1 < NSEG) sched2(tk, s, sf);
                    if (!TK) wait_until(tref + DZ2);
                    if (s + 1 < NSEG) put_z2(s);
                }
                if (budget <= 0) __hip_atomic_store(a.ctl, 1u, __ATOMIC_RELAXED, __HIP_MEMORY_SCOPE_AGENT);
                zb = zb == W_ZM + 2u * NOWN * PAIR ? W_ZM : zb + NOWN * PAIR;
                RT(5);
                RTS();
                MT(7);
                MTS();
            }
        };
        if (!vt) {
            if (w < 4) steps(IC<0>()); else if (w < 7) steps(IC<1>()); else steps(IC<3>());
        } else {
            asm volatile("s_waitcnt vmcnt(5) lgkmcnt(0)\n\ts_barrier" ::: "memory");
            SST(3);
            LANES();
            const int ni = __builtin_popcountll(mv) + 1;
            if (w < 4) {
                const LAS char* c0 = (const LAS char*)(hL + hoff + ((fq ^ (fr & 7)) << 4));
                const LAS char* c1 = (const LAS char*)(hL + hoff + (((4 + fq) ^ (fr & 7)) << 4));
                const LAS char* n0 = c0 + 40960; const LAS char* n1 = c1 + 40960;
                u32x4 bb[8];
                project0(bb, c0, c1);
                for (int i = 1; i < ni; ++i) {
                    if (vt) MT(0);
                    f32x4 tc[2][2];
                    project<0>(wt, tc, bb, c0, c1, n0, n1,
                        [&]() {
                            MT(1);
                            asm volatile("s_waitcnt vmcnt(0) lgkmcnt(0)\n\ts_barrier" ::: "memory");
                            MT(2);
                        }, [](auto) {});
                    { const LAS char* x = c0; c0 = n0; n0 = x; x = c1; c1 = n1; n1 = x; }
                    char* tb = tokL + (i & 1) * 32 * QS + (16 * w + 4 * fq) * 2;
#pragma unroll
                    for (int g = 0; g < 2; ++g)
#pragma unroll
                        for (int tt = 0; tt < 2; ++tt) *(u32x2*)(tb + (prow + 8 * tt) * QS + g * 128) = (u32x2){pack2(tc[g][tt][0], tc[g][tt][1]), pack2(tc[g][tt][2], tc[g][tt][3])};
                    asm volatile("" ::: "memory");
                    *(volatile LAS int*)(smem + L_S + 16 + w * 4) = i;
                    if (vt) { MT(3); const int s = i & 63; MTRT(); MTS(); }
                }
                project_end(bb);
            } else {
                __builtin_amdgcn_s_setprio(OPR);
                const unsigned hv = lane * 16u, fz_ = W_FZ + (unsigned)(hh * 128 + xt * 64 + (w - 4) * 4);
                unsigned fp = ~0u;
                int jo = jv1, jh = jv2;
                for (int i = 1; i < ni; ++i, jo = nextv(jo)) {
                    const int b = i + (i >= ZLAG);
                    if (__builtin_expect(i == 1 || i == ZLAG, 0)) {
                        VMWAIT();
                        if (i == ZLAG) {
                            jh = nextv(jh);
                            wait_h(a, rwin, jh, hrdy);
                            BAR0();
                            for (int t = 0; t < 10; ++t) dma1k(rws, hv, O_H + (unsigned)(jh * 40960 + (w - 4) * 1024 + t * 4096), (unsigned)(L_H + ((i + 1) & 1) * 40960 + (w - 4) * 1024 + t * 4096));
                            VMWAIT();
                        }
                    }
                    jh = nextv(jh);
                    wait_h(a, rwin, jh, hrdy);
                    if (b < ni) BAR0();
                    if (vt) MT(1);
                    for (;;) {
                        const u32x4 f = *(volatile LAS u32x4*)(smem + L_S + 16);
                        if (__builtin_amdgcn_readfirstlane(min(min(f[0], f[1]), min(f[2], f[3]))) >= (unsigned)i) break;
                        __builtin_amdgcn_s_sleep(1);
                    }
                    asm volatile("" ::: "memory");
                    if (vt) MT(0);
                    u32x4 vf[2][4];
#pragma unroll
                    for (int t = 0; t < 2; ++t)
#pragma unroll
                        for (int ks = 0; ks < 4; ++ks) vf[t][ks] = *(const u32x4*)(tokL + (i & 1) * 32 * QS + (16 * t + fr) * QS + ks * 64 + fq * 16);
                    const unsigned ztb = O_Z + (unsigned)(jo * ZTSEG) * 2u + (unsigned)(10 * (w - 4) * H_ + hh) * 1024u;
                    f32x4 c[2][2];
                    u32x4 r;
                    unsigned hs = O_H + (unsigned)(jh * 40960 + (w - 4) * 1024), hd = (unsigned)(L_H + ((b + 1) & 1) * 40960 + (w - 4) * 1024);
                    asm volatile("" : "+s"(hs), "+s"(hd));
#pragma unroll
                    for (int nt = 0; nt < 11; ++nt) {
                        auto& cc = c[nt & 1]; auto& cp = c[(nt + 1) & 1];
#pragma unroll
                        for (int ks = 0; ks < 4; ++ks) {
                            if (nt < 10) { cc[0] = mfma32(vf[0][ks], half8(wt[ks >> 1][nt], ks & 1), ks ? cc[0] : z4); PIN1(cc[0]); }
                            if (nt > 0 && ks >= 1 && ks < 3) { r[2 * ks - 2] = pack2(cp[ks - 1][0], cp[ks - 1][1]); PIN1(r); }
                            if (nt < 10) { cc[1] = mfma32(vf[1][ks], half8(wt[ks >> 1][nt], ks & 1), ks ? cc[1] : z4); PIN1(cc[1]); }
                            if (nt > 0 && ks >= 1 && ks < 3) { r[2 * ks - 1] = pack2(cp[ks - 1][2], cp[ks - 1][3]); PIN1(r); }
                            if (nt > 0 && ks == 2) st128(r, rws, lane * 16u, ztb + (unsigned)((nt - 1) * H_) * 1024u, 16);
                            if (nt < 5 && !(ks & 1)) dma1k(rws, hv, hs + (2 * nt + (ks >> 1)) * 4096u, hd + (2 * nt + (ks >> 1)) * 4096u);
                            if (nt == 5 && ks == 0) {
                                asm volatile("s_waitcnt vmcnt(14)" ::: "memory");
                                __builtin_amdgcn_raw_buffer_store_b32(fp, rwin, 0u, fz_, 16);
                            }
                            SB();
                        }
                    }
                    if (vt) MT(2);
                    fp = (unsigned)jo;
                    if (__builtin_expect(jo <= ZEARLY || i + 1 == ni, 0)) {
                        VMWAIT();
                        __builtin_amdgcn_raw_buffer_store_b32(fp, rwin, 0u, fz_, 16);
                    } else asm volatile("s_waitcnt vmcnt(7)" ::: "memory");
                    if (jo < 8) SST(8 + jo);
                    if (vt && (!BSPARSE || jo % 6 == 2)) { const int s = jo; RT(0); RTS(); }
                    if (vt) { MT(3); const int s = i & 63; MTRT(); MTS(); }
                }
            }
            __builtin_amdgcn_s_setprio(0);
            VMWAIT();
            BAR();
        }
    }
    const bool qt = bid >= NQT && bid < NWRK;
    if (bid < 32 || qt) {
        LANES();
        const int hh = qt ? (bid - NQT) & 31 : bid;
        const unsigned long long mq = xt ? MXQ : ~MXQ;
        auto nextq = [&](int j) { return min(seg_after(mq, j), NSEG - 1); };
        const int jq0 = qt ? nextq(1) : 0, jq1 = nextq(jq0);
        u32x8 wt[2][10];
        if (w > 0) {
            const unsigned long long t0 = __builtin_amdgcn_s_memtime();
            if (!qt) gather_mem0(a, memL, tid - 64, 448);
            if (w == 7) clk_init(ck);
            wait_until(t0 + DW0);
        }
        if (DQT && qt) wait_until(tstart + DQT);
        {
            const u16* pq = (const u16*)(ws + OFF_WQKVT) + (size_t)((w >> 2) * 256 + hh * 8 + 2 * (w & 3)) * 10240 + lane * 8;
#pragma unroll
            for (int t = 0; t < 10; ++t) { wt[0][t] = ldw(pq + t * 1024); wt[1][t] = ldw(pq + 10240 + t * 1024); }
        }
        if (tid < 4) *(unsigned*)(smem + L_S + tid * 4) = 0u;
        SST(1);
        if (w == 0) { wait_h(a, rwin, jq1, hrdy); *(volatile LAS int*)(smem + L_S + 32) = hrdy; }
        BAR();
        hrdy = __builtin_amdgcn_readfirstlane(*(volatile LAS int*)(smem + L_S + 32));
        SST(2);
        stage_h(rws, jq0, L_H, tid);
        stage_h(rws, jq1, L_H + 40960, tid);
        VMWAIT();
        BAR();
        SST(3);
        int qrdy = 0;
        {
            LANES();
            const unsigned to0 = (unsigned)((w ^ 4) >> 2) * 8192u + (unsigned)((w & 3) * 64 + (lane ^ ((fq & 1) * (w < 4 ? 8 : 5)))) * 16u, to1 = to0 + 4096u, hv = lane * 16u;
            const LAS char* c0 = (const LAS char*)(hL + hoff + ((fq ^ (fr & 7)) << 4));
            const LAS char* c1 = (const LAS char*)(hL + hoff + (((4 + fq) ^ (fr & 7)) << 4));
            u32x4 bb[8], g0 = SENT, g1 = SENT;
            f32x4 tc[2][2];
            auto pack = [&]() {
                g0 = (u32x4){pack2(tc[0][0][0], tc[0][0][1]), pack2(tc[0][0][2], tc[0][0][3]), pack2(tc[1][0][0], tc[1][0][1]), pack2(tc[1][0][2], tc[1][0][3])};
                g1 = (u32x4){pack2(tc[0][1][0], tc[0][1][1]), pack2(tc[0][1][2], tc[0][1][3]), pack2(tc[1][1][0], tc[1][1][1]), pack2(tc[1][1][2], tc[1][1][3])};
            };
            const LAS char* n0 = c0 + 40960; const LAS char* n1 = c1 + 40960;
            if (!qt) {
                project0(bb, c0, c1);
                for (int j = 0; j < 2; ++j) {
                    project<0>(wt, tc, bb, c0, c1, n0, n1, []() {}, [](auto) {});
                    pack();
                    *(u32x4*)(tokL + j * 16384 + to0) = g0; *(u32x4*)(tokL + j * 16384 + to1) = g1;
                    c0 = n0; c1 = n1;
                }
                project_end(bb);
            } else {
                project0(bb, c0, c1);
                unsigned hs = O_H + (unsigned)(jq1 * 40960 + w * 1024), hd = (unsigned)(L_H + 40960 + w * 1024), fv = tid == 0 ? 0u : 0x40000000u;
                const unsigned fq_ = W_FQ + (unsigned)(hh * 128 + xt * 64);
                const int jl = 63 - __builtin_clzll(mq);
                for (int jp = 1, j = jq0, jn = jq1;; jp = j, j = jn, jn = nextq(jn)) {
                    MT(0);
                    project<0>(wt, tc, bb, c0, c1, n0, n1,
                        [&]() {
                            wait_h(a, rwin, nextq(jn), hrdy);
                            hs = O_H + (unsigned)(nextq(jn) * 40960 + w * 1024); hd = (unsigned)(2 * L_H + 40960 + 2 * w * 1024) - hd;
                            unsigned fj = (unsigned)jp;
                            asm volatile("" : "+s"(hs), "+s"(hd));
                            PIN1(fj);
                            MT(1);
                            VMWAIT();
                            BAR();
                            MT(2);
                            if (w == 0) __builtin_amdgcn_raw_buffer_store_b32(fj, rwin, fv, fq_, 16);
                        },
                        [&](auto uu) {
                            constexpr int u = decltype(uu)::v;
                            if (u == 34 || u == 38 || u == 2 || u == 6 || u == 10) { constexpr int t = ((u + 6) % 40) / 4; dma1k(rws, hv, hs + t * 8192u, hd + t * 8192u); }
                        });
                    pack();
                    const unsigned qs = O_QK + (unsigned)(j * H_ + hh) * 16384u;
                    st128(g0, rws, to0, qs, 16);
                    st128(g1, rws, to1, qs, 16);
                    { const LAS char* x = c0; c0 = n0; n0 = x; x = c1; c1 = n1; n1 = x; }
                    if (__builtin_expect(j <= QEARLY || j == jl, 0)) {
                        VMWAIT();
                        BAR0();
                        if (tid == 0) __builtin_amdgcn_raw_buffer_store_b32((unsigned)j, rwin, 0u, fq_, 16);
                        if (j == jl) break;
                    }
                    MT(3);
                    if (j < 8) SST(8 + j);
                    if (!BSPARSE || j % 6 == 2) { const int s = j; RT(0); RTS(); MTRT(); MTS(); }
                }
                project_end(bb);
            }
        }
        SST(4);
        auto steps = [&](auto cc) {
        constexpr int C = decltype(cc)::v >> 1, R = decltype(cc)::v & 1;
        LANES();
        const LAS char* ka = (const LAS char*)(smem + L_K + (fr & 1) * TQS + (4 * fq + (fr >> 2)) * 8);
        const LAS char* qa = (const LAS char*)(smem + L_Q + (fr & 1) * 544 + ((fr >> 1) & 1) * 1152 + (16 * fq + 2 * (fr >> 2)) * 4);
        LAS char* qw = (LAS char*)(C < 2 ? smem + L_Q + (fq & 1) * 544 + (fq >> 1) * 1152 + (32 * w + 8 * (fr >> 2) + bs2(fr & 3)) * 4
                                         : smem + L_K + (fq >> 1) * TQS + (32 * (w & 3) + fr) * 8 + (fq & 1) * 4);
        const LAS char* mp0 = (const LAS char*)(memL + ma0);
        const int z = 16 * (fr >> 3) + 4 * (fr & 3) + 2 * ((fr >> 2) & 1);
        const int ko = ((z >> 4) * 256 + fq * 16 + ((z & 15) ^ ((fq & 1) * 5))) * 16;
        unsigned pa0 = (4u * C + 2u * (hh >> 4)) * PAIR + (unsigned)((hh & 15) * 512 + R * 256 + lane * 4);
        unsigned pa2 = ((8u + (fr >> 3)) * PAIR + C * 4096u + (unsigned)((hh >> 1) * 256 + R * 128 + (hh & 1) * 64 + fq * 16 + (fr & 6) * 2)) | (fr & 1) << 30;
        PIN2(ka, qw); PIN1(mp0);
        constexpr bool TK = C == 2 && R == 1;
        Tk tk = {0u, 0u, 0x40000000u, nullptr};
        if (TK) { tk = tk_init(); __builtin_amdgcn_s_setprio(TKP); }
        if (C < 2) { PIN2(qa, pa0); PIN1(pa2); }
        int qf_ = 0;
        int tbo = 0;
        for (int s = 0; s < NSEG; ++s) {
            RT(0);
            const char* tb = tokL + tbo;
            unsigned pb = __builtin_amdgcn_readfirstlane(s + 1 < NSEG ? W_P + (unsigned)(s & 3) * 10u * PAIR + nh : 0x40000000u);
            asm volatile("" : "+s"(pb));
            if (s == 0) BAR();
            u32x8 kx[2][2];
            if (C < 2) {
#pragma unroll
                for (int u = 0; u < 4; ++u) kx[u >> 1][u & 1] = cat8(*(const u32x4*)(tb + (ko ^ ((u >> 1) * 16)) + (u & 1) * 2048), *(const u32x4*)(tb + (ko ^ ((u >> 1) * 16)) + (u & 1) * 2048 + 1024));
                PIN2(kx[0][0], kx[0][1]); PIN2(kx[1][0], kx[1][1]);
            }
            bool redo = false;
            unsigned sf = 0;
            do {
                ++it;
                f32x4 a0 = z4, a1 = z4;
                PIN2(a0, a1);
                u32x4 m[10];
                if (C < 2) {
                    __builtin_amdgcn_s_setprio(2);
                    BAR();
                    MT(0);
#pragma unroll
                    for (int t = 0; t < NB1; ++t) { m[t] = *(const LAS u32x4*)(mp0 + t * 1024); SB(); }
                    BAR0();
#pragma unroll
                    for (int t = 0; t < 10; ++t) {
                        if (t == QB2) { BAR0(); MT(1); }
                        if (t + NB1 < 10) m[t + NB1] = *(const LAS u32x4*)(mp0 + (t + NB1) * 1024);
                        SB();
                        a0 = smfmac(m[t], wt[0][t], a0, sidx); a1 = smfmac(m[t], wt[1][t], a1, sidx);
                        PIN2(a0, a1);
                    }
                } else {
                    unsigned mx = 0;
                    asm volatile("" : "+{v[200:203]}"(a0), "+{v[204:207]}"(a1), "+{v208}"(mx));
                    unsigned lim = s > 0 ? poll_lim(budget) : ~0u, sr = tro + (unsigned)(s & 3) * 2u * PAIR;
                    asm volatile("" : "+s"(lim), "+s"(sr));
                    if (TK) fetch(tk, s, !redo, lim, DQC, (unsigned)hh * 128u, true);
                    else asm volatile("s_waitcnt lgkmcnt(0)\n\ts_barrier\n\ts_barrier\n\ts_barrier" ::: "memory");
                    asm volatile(
                        "ds_read_b128 v[212:215], %[mp] offset:0\n\t"
                        "ds_read_b128 v[216:219], %[mp] offset:1024\n\t"
                        "ds_read_b128 v[220:223], %[mp] offset:2048\n\t"
                        "ds_read_b128 v[224:227], %[mp] offset:3072\n\t"
                        "ds_read_b128 v[228:231], %[mp] offset:4096\n\t"
                        "ds_read_b128 v[232:235], %[mp] offset:5120\n\t"
                        "ds_read_b128 v[236:239], %[mp] offset:6144\n\t"
                        "ds_read_b128 v[240:243], %[mp] offset:7168\n\t"
                        "ds_read_b128 v[244:247], %[mp] offset:8192\n\t"
                        "ds_read_b128 v[248:251], %[mp] offset:9216\n\t"
                        MTA(4)
                        "s_waitcnt lgkmcnt(9)\n\t"
                        "v_smfmac_f32_16x16x64_bf16 v[200:203], v[212:215], %[w00], %[ix]\n\t"
                        "v_max3_u32 v208, v208, v212, v213\n\t"
                        "v_smfmac_f32_16x16x64_bf16 v[204:207], v[212:215], %[w10], %[ix]\n\t"
                        "s_waitcnt lgkmcnt(8)\n\t"
                        "v_max3_u32 v208, v208, v214, v215\n\t"
                        "v_smfmac_f32_16x16x64_bf16 v[200:203], v[216:219], %[w01], %[ix]\n\t"
                        "v_max3_u32 v208, v208, v216, v217\n\t"
                        "v_smfmac_f32_16x16x64_bf16 v[204:207], v[216:219], %[w11], %[ix]\n\t"
                        "s_waitcnt lgkmcnt(7)\n\t"
                        "v_max3_u32 v208, v208, v218, v219\n\t"
                        "v_smfmac_f32_16x16x64_bf16 v[200:203], v[220:223], %[w02], %[ix]\n\t"
                        "v_max3_u32 v208, v208, v220, v221\n\t"
                        "v_smfmac_f32_16x16x64_bf16 v[204:207], v[220:223], %[w12], %[ix]\n\t"
                        "s_waitcnt lgkmcnt(6)\n\t"
                        "v_max3_u32 v208, v208, v222, v223\n\t"
                        "v_smfmac_f32_16x16x64_bf16 v[200:203], v[224:227], %[w03], %[ix]\n\t"
                        "v_max3_u32 v208, v208, v224, v225\n\t"
                        "v_smfmac_f32_16x16x64_bf16 v[204:207], v[224:227], %[w13], %[ix]\n\t"
                        "s_waitcnt lgkmcnt(0)\n\t"
                        "v_max3_u32 v208, v208, v226, v227\n\t"
                        "v_smfmac_f32_16x16x64_bf16 v[200:203], v[228:231], %[w04], %[ix]\n\t"
                        "v_max3_u32 v208, v208, v228, v229\n\t"
                        "v_smfmac_f32_16x16x64_bf16 v[204:207], v[228:231], %[w14], %[ix]\n\t"
                        "v_max3_u32 v208, v208, v230, v231\n\t"
                        "v_smfmac_f32_16x16x64_bf16 v[200:203], v[232:235], %[w05], %[ix]\n\t"
                        "v_max3_u32 v208, v208, v232, v233\n\t"
                        "v_smfmac_f32_16x16x64_bf16 v[204:207], v[232:235], %[w15], %[ix]\n\t"
                        "v_max3_u32 v208, v208, v234, v235\n\t"
                        "v_smfmac_f32_16x16x64_bf16 v[200:203], v[236:239], %[w06], %[ix]\n\t"
                        "v_max3_u32 v208, v208, v236, v237\n\t"
                        "v_smfmac_f32_16x16x64_bf16 v[204:207], v[236:239], %[w16], %[ix]\n\t"
                        "v_max3_u32 v208, v208, v238, v239\n\t"
                        "v_smfmac_f32_16x16x64_bf16 v[200:203], v[240:243], %[w07], %[ix]\n\t"
                        "v_max3_u32 v208, v208, v240, v241\n\t"
                        "v_smfmac_f32_16x16x64_bf16 v[204:207], v[240:243], %[w17], %[ix]\n\t"
                        "v_max3_u32 v208, v208, v242, v243\n\t"
                        "v_smfmac_f32_16x16x64_bf16 v[200:203], v[244:247], %[w08], %[ix]\n\t"
                        "v_max3_u32 v208, v208, v244, v245\n\t"
                        "v_smfmac_f32_16x16x64_bf16 v[204:207], v[244:247], %[w18], %[ix]\n\t"
                        "v_max3_u32 v208, v208, v246, v247\n\t"
                        "v_smfmac_f32_16x16x64_bf16 v[200:203], v[248:251], %[w09], %[ix]\n\t"
                        "v_max3_u32 v208, v208, v248, v249\n\t"
                        "v_smfmac_f32_16x16x64_bf16 v[204:207], v[248:251], %[w19], %[ix]\n\t"
                        "v_max3_u32 v208, v208, v250, v251\n\t"
                        "buffer_load_dword %[sf], %[sl], %[rw], %[sr] offen sc1\n\t"
                        "v_cmp_lt_u32 vcc, %[lim], v208\n\t"
                        "s_nop 3\n\t"
                        "v_pk_add_f32 v[200:201], v[200:201], v[202:203]\n\t"
                        "v_pk_add_f32 v[204:205], v[204:205], v[206:207]\n\t"
                        "v_cvt_pk_bf16_f32 v200, v200, v201\n\t"
                        "v_cvt_pk_bf16_f32 v204, v204, v205\n\t"
                        "ds_write2_b32 %[qw], v200, v204 offset1:32\n\t"
                        MTA(2)
                        "s_cbranch_vccz 1f\n\t"
                        "v_mov_b32 v208, %[it]\n\t"
                        "v_mov_b32 v209, 0\n\t"
                        "ds_write_b32 v209, v208 offset:%[ls]\n\t"
                        "1:\n\t"
                        "s_waitcnt vmcnt(%[nv]) lgkmcnt(0)\n\t"
                        "s_barrier\n\t"
                        "s_waitcnt vmcnt(0)\n\t"
                        : [sf] "=&v"(sf), "+v"(qf_), "+{v[200:203]}"(a0), "+{v[204:207]}"(a1), "+{v208}"(mx)
                        : [mp] "v"(mp0), [qw] "v"(qw), [ix] "v"(sidx), [lim] "s"(lim), [it] "s"(it), [ls] "n"(L_S), [sl] "v"(tk.sl), [sr] "s"(sr), [rw] "s"(rwin), [nv] "n"(TK ? 2 : 1),
                          [w00] "v"(wt[0][0]), [w01] "v"(wt[0][1]), [w02] "v"(wt[0][2]), [w03] "v"(wt[0][3]), [w04] "v"(wt[0][4]), [w05] "v"(wt[0][5]), [w06] "v"(wt[0][6]), [w07] "v"(wt[0][7]), [w08] "v"(wt[0][8]), [w09] "v"(wt[0][9]), [w10] "v"(wt[1][0]), [w11] "v"(wt[1][1]), [w12] "v"(wt[1][2]), [w13] "v"(wt[1][3]), [w14] "v"(wt[1][4]), [w15] "v"(wt[1][5]), [w16] "v"(wt[1][6]), [w17] "v"(wt[1][7]), [w18] "v"(wt[1][8]), [w19] "v"(wt[1][9])
                        : "v209", "v212", "v213", "v214", "v215", "v216", "v217", "v218", "v219", "v220", "v221", "v222", "v223", "v224", "v225", "v226", "v227", "v228", "v229", "v230", "v231", "v232", "v233", "v234", "v235", "v236", "v237", "v238", "v239", "v240", "v241", "v242", "v243", "v244", "v245", "v246", "v247", "v248", "v249", "v250", "v251", "vcc", "memory");
                }
                if (C < 2) {
                    *(LAS unsigned*)qw = hpack(a0); *(LAS unsigned*)(qw + 16) = hpack(a1);
                    SB();
                    MT(2);
                    unsigned vd;
#define SC0 "v[212:215]"
#define SC1 "v[216:219]"
#define SC2 "v[220:223]"
#define DPP(op, d, ctl) op " " d ", " d ", " d " " ctl " row_mask:0xf bank_mask:0xf\n\t"
#define TAIL(A0, A2, B0, B2, C0, C2) \
                        "v_mov_b64 v[212:213], 0\n\tv_mov_b64 v[214:215], 0\n\tv_mov_b64 v[216:217], 0\n\tv_mov_b64 v[218:219], 0\n\tv_mov_b64 v[220:221], 0\n\tv_mov_b64 v[222:223], 0\n\t" \
                        "s_setprio 3\n\t" \
                        "s_waitcnt lgkmcnt(0)\n\t" \
                        "s_barrier\n\t" MTA(3) \
                        "ds_read_b64_tr_b16 v[224:225], %[qa]\n\t" \
                        "ds_read_b64_tr_b16 v[226:227], %[qa] offset:32\n\t" \
                        "ds_read_b64_tr_b16 v[228:229], %[qa] offset:256\n\t" \
                        "ds_read_b64_tr_b16 v[230:231], %[qa] offset:288\n\t" \
                        "ds_read_b64_tr_b16 v[232:233], %[ka]\n\t" \
                        "ds_read_b64_tr_b16 v[234:235], %[ka] offset:128\n\t" \
                        "ds_read_b64_tr_b16 v[236:237], %[ka] offset:256\n\t" \
                        "ds_read_b64_tr_b16 v[238:239], %[ka] offset:384\n\t" \
                        "ds_read_b64_tr_b16 v[240:241], %[ka] offset:512\n\t" \
                        "ds_read_b64_tr_b16 v[242:243], %[ka] offset:640\n\t" \
                        "s_waitcnt lgkmcnt(8)\n\t" \
                        "v_smfmac_f32_16x16x64_bf16 " SC0 ", v[224:227], %[k00], %[ix]\n\t" \
                        "ds_read_b64_tr_b16 v[244:245], %[ka] offset:768\n\t" \
                        "v_smfmac_f32_16x16x64_bf16 " SC1 ", v[224:227], %[k10], %[ix]\n\t" \
                        "ds_read_b64_tr_b16 v[246:247], %[ka] offset:896\n\t" \
                        "s_waitcnt lgkmcnt(8)\n\t" \
                        "v_smfmac_f32_16x16x64_bf16 " SC0 ", v[228:231], %[k01], %[ix]\n\t" \
                        "ds_read_b32 v248, %[vz] offset:%[ls]\n\t" \
                        "v_smfmac_f32_16x16x64_bf16 " SC1 ", v[228:231], %[k11], %[ix]\n\t" \
                        "s_waitcnt lgkmcnt(5)\n\t" \
                        "v_smfmac_f32_16x16x64_bf16 " SC2 ", v[224:227], v[232:239], %[ix]\n\t" \
                        "s_waitcnt lgkmcnt(1)\n\t" \
                        "v_smfmac_f32_16x16x64_bf16 " SC2 ", v[228:231], v[240:247], %[ix]\n\t" \
                        "s_waitcnt lgkmcnt(0)\n\t" \
                        MTA(4) "v_readfirstlane_b32 %[vd], v248\n\t" \
                        "v_add_f32 v232, " A0 ", " A2 "\n\t" \
                        "s_nop 0\n\t" \
                        "v_add_f32 v233, " B0 ", " B2 "\n\t" \
                        "v_max_f32 v235, v232, v233\n\t" \
                        "s_cmp_eq_u32 %[vd], %[it]\n\t" \
                        "s_cselect_b32 %[p0], 2.0, %[p0]\n\t" \
                        DPP("v_max_f32_dpp", "v235", "row_ror:8") \
                        "v_add_f32 v234, " C0 ", " C2 "\n\t" \
                        "v_max_f32 v235, v235, v234\n\t" \
                        "s_add_u32 %[p1], %[p0], 0x4000\n\t" \
                        "s_nop 0\n\t" DPP("v_max_f32_dpp", "v235", "row_ror:4") \
                        "s_nop 1\n\t" DPP("v_max_f32_dpp", "v235", "row_ror:2") \
                        "s_nop 1\n\t" DPP("v_max_f32_dpp", "v235", "row_ror:1") \
                        "v_mul_f32 v235, 0xbe0293ee, v235\n\t" \
                        "v_fmamk_f32 v232, v232, 0x3e0293ee, v235\n\t" \
                        "v_fmamk_f32 v233, v233, 0x3e0293ee, v235\n\t" \
                        "v_exp_f32 v232, v232\n\t" \
                        "v_exp_f32 v233, v233\n\t" \
                        "v_fmamk_f32 v234, v234, 0x3e0293ee, v235\n\t" \
                        "v_add_f32 v235, v232, v233\n\t" \
                        "v_exp_f32 v234, v234\n\t" \
                        "s_nop 0\n\t" DPP("v_add_f32_dpp", "v235", "row_ror:8") \
                        "v_add_f32 v235, v235, v234\n\t" \
                        "v_mov_b32_dpp v236, v234 quad_perm:[1,1,3,3] row_mask:0xf bank_mask:0xf\n\t" \
                        "s_nop 0\n\t" DPP("v_add_f32_dpp", "v235", "row_ror:4") \
                        "s_nop 1\n\t" DPP("v_add_f32_dpp", "v235", "row_ror:2") \
                        "s_nop 1\n\t" DPP("v_add_f32_dpp", "v235", "row_ror:1") \
                        "v_rcp_f32 v235, v235\n\t" \
                        "s_nop 0\n\t" \
                        "v_mul_f32_e64 v232, v232, v235 clamp\n\t" \
                        "v_mul_f32_e64 v233, v233, v235 clamp\n\t" \
                        "v_cvt_pk_bf16_f32 v232, v232, v233\n\t" \
                        MTA(5) "buffer_store_dword v232, %[pa], %[rw], %[p0] offen sc1\n\t" \
                        "v_mul_f32_e64 v234, v234, v235 clamp\n\t" \
                        "v_mul_f32_e64 v236, v236, v235 clamp\n\t" \
                        "buffer_store_dword v232, %[pa], %[rw], %[p1] offen sc1\n\t" \
                        "v_cvt_pk_bf16_f32 v234, v234, v236\n\t" \
                        "buffer_store_dword v234, %[pm], %[rw], %[p0] offen sc1\n\t" \
                        "s_setprio 0"
#define TAILIO : [vd] "=&s"(vd), [p0] "+s"(ps), [p1] "=&s"(ps1) \
                        : [qa] "v"(qa), [ka] "v"(ka), [vz] "v"(0u), [ls] "n"(L_S), [ix] "v"(sidx), [it] "s"(it), [pa] "v"(pa0), [pm] "v"(pa2), [rw] "s"(rwin), \
                          [k00] "v"(kx[0][0]), [k01] "v"(kx[0][1]), [k10] "v"(kx[1][0]), [k11] "v"(kx[1][1]) \
                        : "v212", "v213", "v214", "v215", "v216", "v217", "v218", "v219", "v220", "v221", "v222", "v223", "v224", "v225", "v226", "v227", "v228", "v229", "v230", "v231", "v232", \
                          "v233", "v234", "v235", "v236", "v237", "v238", "v239", "v240", "v241", "v242", "v243", "v244", "v245", "v246", "v247", "v248", "scc", "memory"
                    unsigned ps = pb, ps1;
                    if (R == 0) asm volatile(TAIL("v212", "v214", "v216", "v218", "v220", "v222") TAILIO);
                    else asm volatile(TAIL("v213", "v215", "v217", "v219", "v221", "v223") TAILIO);
                    redo = vd == it;
                    RT(4);
                    MT(6);
                } else {
                    MT(3);
                    if (TK) { __builtin_amdgcn_s_setprio(TKP); if (TKS) __builtin_amdgcn_s_sleep(TKS); }
                    redo = __builtin_amdgcn_readfirstlane(*(volatile LAS unsigned*)(smem + L_S)) == it;
                    if (redo) FC(fc0);
                }
            } while (__builtin_expect(redo, 0));
            if (s < 8) SST(8 + s);
            if (s >= 56) SST(s - 40);
            if (C + R == 1) {
                if (s > 0 && s + 1 < NSEG) {
                    __builtin_amdgcn_s_sleep(DDMA);
                    unsigned l2 = fresh_lane(), s1;
                    asm volatile("v_mov_b32 %0, -1" : "=v"(s1));
                    const unsigned base = W_P + (unsigned)((s - 1) & 3) * 10u * PAIR + nh;
                    asm volatile("s_waitcnt vmcnt(3)" ::: "memory");
                    st128(((u32x4){s1, s1, s1, s1}), rwin, (l2 >> 5) * PAIR + (l2 & 31u) * 16u, base + (unsigned)(R * 4 + 2 * (hh >> 4)) * PAIR + (unsigned)(hh & 15) * 512u, 16);
                    if (R) __builtin_amdgcn_raw_buffer_store_b64((u32x2){s1, s1}, rwin, (l2 >> 5) * PAIR + (l2 & 16u) * 256u + (l2 & 8u) * 16u + (l2 & 7u) * 8u, base + 8u * PAIR + (unsigned)((hh >> 1) * 256 + (hh & 1) * 64), 16);
                    else __builtin_amdgcn_raw_buffer_store_b32(s1, rwin, two(), W_T + (unsigned)((s + 2) & 3) * 2u * PAIR + (unsigned)hh * 128u, 16);
                }
            } else if (TK) {
                if (s + 1 < NSEG) sched2(tk, s, sf);
            } else if (C == 2 && w == 4) {
                if (s + 2 < NSEG) {
                    qrdy = max(qrdy, seg_done(MXQ, __builtin_amdgcn_readlane(qf_, 0), __builtin_amdgcn_readlane(qf_, 1)));
                    if (__builtin_expect(qrdy < s + 2, 0)) for (unsigned spins = 0;;) {
                        asm volatile("" ::: "memory");
                        const int f = (int)__builtin_amdgcn_raw_buffer_load_b32(rwin, (unsigned)(lane & 1) * 64u, W_FQ + (unsigned)hh * 128u, 16);
                        qrdy = seg_done(MXQ, __builtin_amdgcn_readlane(f, 0), __builtin_amdgcn_readlane(f, 1));
                        if (qrdy >= s + 2) break;
                        FC(fc2);
                        if (spin_fail(spins, a.ctl)) break;
                        __builtin_amdgcn_s_sleep(2);
                    }
                    const unsigned so = O_QK + (unsigned)(((s + 2) * H_ + hh) * 16384), lo = (unsigned)(L_TOK + (tbo ? tbo - 16384 : 32768)), l2 = fresh_lane();
#pragma unroll
                    for (int i = 0; i < 4; ++i) dma4k(rws, l2 * 16u, so + i * 4096u, lo + i * 4096u);
                    if (qrdy < min(s + 5, NSEG - 1))
                        asm volatile("buffer_load_dword %0, %1, %2, %3 offen sc1" : "+v"(qf_) : "v"((l2 & 1u) * 64u), "s"(rwin), "s"(W_FQ + (unsigned)hh * 128u) : "memory");
                }
            } else {
                asm volatile("s_waitcnt vmcnt(3)" ::: "memory");
                if (s > TAILS) __builtin_amdgcn_raw_buffer_store_b32((unsigned)s - 1u, rwin, 0u, W_FPT + (unsigned)(hh * 128 + (w & 3) * 4), 16);
                if (C == 2 && s + 1 < NSEG) __builtin_amdgcn_s_sleep(DPT);
                const float scale = 0.08838834764831845f, l2e = 1.44269504088896340736f * (NORND ? scale : 1.f);
                const int tq = (w >> 1) & 1, qh = w & 1;
                f32x4 sc[3] = {z4, z4, z4};
                u32x4 kt[2][4], qf[4], kf[4];
                const unsigned l2 = fresh_lane(), kto = ((l2 & 8u) * 512u + (l2 & 51u) * 16u + (l2 & 4u) * 32u) ^ ((l2 & 16u) * 5u), qto = ((l2 & 55u) * 16u + qh * 128u) ^ ((l2 & 16u) * 8u);
#pragma unroll
                for (int ks = 0; ks < 4; ++ks) {
                    qf[ks] = *(const u32x4*)(tb + 8192 + (tq * 4 + ks) * 1024 + qto);
                    kt[0][ks] = *(const u32x4*)(tb + ks * 1024 + kto);
                    kt[1][ks] = *(const u32x4*)(tb + ks * 1024 + (kto ^ 64u));
                    SB();
                }
#pragma unroll
                for (int ks = 0; ks < 4; ++ks) {
                    sc[0] = mfma32(kt[0][ks], qf[ks], sc[0]); PIN1(sc[0]);
                    kf[ks] = ldtr2(ka + ks * 256, ka + ks * 256 + 128);
                    SB();
                    sc[1] = mfma32(kt[1][ks], qf[ks], sc[1]); PIN1(sc[1]);
                }
#pragma unroll
                for (int ks = 0; ks < 4; ++ks) sc[2] = mfma32(kf[ks], qf[ks], sc[2]);
                const bool hi = (l2 & 8u) != 0u;
                float v[6];
#pragma unroll
                for (int r = 0; r < 4; ++r) v[r] = hi ? sc[1][r] : sc[0][r];
                v[4] = hi ? sc[2][2] : sc[2][0]; v[5] = hi ? sc[2][3] : sc[2][1];
#pragma unroll
                for (int r = 0; r < 6; r += 2) {
                    if (NORND) break;
                    const unsigned b0 = pack2(v[r], v[r + 1]);
                    const unsigned b1 = pack2(lo2f(b0) * scale, hi2f(b0) * scale);
                    v[r] = lo2f(b1); v[r + 1] = hi2f(b1);
                }
                const int lim = 8 * (w & 3) + (int)(l2 & 7u) - 8 * (int)(l2 >> 4) - (int)((l2 & 8u) >> 1);
#pragma unroll
                for (int r = 0; r < 4; ++r) if (r > lim) v[r] = NORND ? -1000000000.0f / scale : bfr(-1000000000.0f);
                float mx = fmx(fmx(fmx(fmx(fmx(v[0], v[1]), v[2]), v[3]), v[4]), v[5]);
                mx = xmax3216(fmx(mx, ror8f(mx)));
                const float mb = -mx * l2e;
#pragma unroll
                for (int r = 0; r < 6; ++r) v[r] = __builtin_amdgcn_exp2f(fmaf(v[r], l2e, mb));
                float sum = fmaf(v[4] + v[5], 0.5f, (v[0] + v[1]) + (v[2] + v[3]));
                sum = xsum3216(sum + ror8f(sum));
                const float inv = __builtin_amdgcn_rcpf(sum);
                const unsigned p0 = pack2(v[0] * inv, v[1] * inv), p1 = nosent1(pack2(v[2] * inv, v[3] * inv)), p2 = nosent1(pack2(v[4] * inv, v[5] * inv));
                const unsigned base = s + 1 < NSEG ? O_PT + (unsigned)(s * PTSEG) * 2u : W_PL;
                const __amdgpu_buffer_rsrc_t rp = s + 1 < NSEG ? rws : rwin;
                const unsigned oob = (l2 & 8u) << 27;
                st128(((u32x4){p0, p1, dppu<0x128>(p0), dppu<0x128>(p1)}), rp, (l2 * 16u) | oob, base + (unsigned)((hh * 2 + tq) * 1024 + qh * 128), 16);
                __builtin_amdgcn_raw_buffer_store_b64((u32x2){p2, dppu<0x128>(p2)}, rp, ((l2 & 7u) * 16u + (l2 >> 4) * 8u) | oob | (l2 & 32u) << 25, base + 65536u + (unsigned)((tq * 32 + hh) * 256 + qh * 128), 16);
            }
            tbo = tbo == 32768 ? 0 : tbo + 16384;
            if (C == 2 && __builtin_expect(budget <= 0, 0)) __hip_atomic_store(a.ctl, 1u, __ATOMIC_RELAXED, __HIP_MEMORY_SCOPE_AGENT);
            RT(5);
            RTS();
            MT(7);
            MTS();
        }
        };
        if (qt) {} else if (w == 0) steps(IC<0>()); else if (w == 1) steps(IC<1>()); else if (w == 2) steps(IC<2>()); else if (w == 3) steps(IC<3>()); else if (w < 7) steps(IC<4>()); else steps(IC<5>());
    } else if (bid >= 96 && bid < NREC) {
        LANES();
        const int o = bid - 96, hg = w & 3;
        const unsigned ztoff = (unsigned)((o * H_ + 8 * hg) * 64 + lane) * 16u;
        const unsigned zoff = zpair(o / 10, o % 10) + (unsigned)(lane * 16);
        const unsigned poff = (4u * (mc >> 1) + 2u * (hg >> 1) + (mc & 1u)) * PAIR + nh + (unsigned)((hg & 1) * 4096 + (fq >> 1) * 512 + (fr >> 2) * 128 + (fr & 1) * 64 + (fq & 1) * 32 + (fr & 2) * 8);
        const unsigned pmoff = (8u + (mc & 1u)) * PAIR + nh + (mc >> 1) * 4096u + (unsigned)(fq * 256 + (fr >> 3) * 128 + (fr & 2) * 32 + (fr & 4) * 8 + (fr & 1) * 16);
        const unsigned fzo = (unsigned)(8 * hg + (lane & 7)) * 128u + (unsigned)(o / 10) * 4u + (lane & 8) * 8u;
        const unsigned moff = (o < 32 ? (unsigned)o * 256u : 4u * PAIR + (unsigned)(o - 32) * 256u) + nh + (unsigned)(lane * 4);
        const unsigned mc2 = o < 32 ? 2u * PAIR : 2048u;
        char* op = smem + 8192;
        SST(2);
        if (w == 7) clk_init(ck);
        wait_until(tstart + DO0);
        if (w < 4) {
            u32x8 zf[4];
            int zr = -1;
            unsigned zb = W_ZM, zso = O_Z;
            const unsigned ra = (o < 32 ? (unsigned)fq * PAIR + (unsigned)o * 256u : (4u + (fq & 1)) * PAIR + (unsigned)(fq >> 1) * 2048u + (unsigned)(o - 32) * 256u) + nh + (unsigned)(fr * 16);
            auto zt_miss = [&](int f, int s) { return __any(f < s && ((lane >> 3) & 1) == (int)((MXV >> s) & 1)); };
            for (int s = 0;; ++s) {

                const unsigned pb = W_P + (unsigned)(s & 3) * 10u * PAIR;
                if (__builtin_expect(zr < s, 0)) for (unsigned spins = 0;;) {
                    asm volatile("" ::: "memory");
                    if (!zt_miss((int)__builtin_amdgcn_raw_buffer_load_b32(rwin, fzo, W_FZ, 16), s)) break;
                    FC(fc2);
                    if (spin_fail(spins, a.ctl)) break;
                    __builtin_amdgcn_s_sleep(ZPS);
                }
                if (s == 0) SST(1);
                asm volatile(
                    "buffer_load_dwordx4 v[160:163], %[zt], %[rs], %[so] offen sc1\n\t"
                    "buffer_load_dwordx4 v[164:167], %[zt], %[rs], %[so] offen offset:1024 sc1\n\t"
                    "buffer_load_dwordx4 v[168:171], %[zt], %[rs], %[so] offen offset:2048 sc1\n\t"
                    "buffer_load_dwordx4 v[172:175], %[zt], %[rs], %[so] offen offset:3072 sc1\n\t"
                    "buffer_load_dwordx4 v[176:179], %[zt], %[rs], %[s4] offen sc1\n\t"
                    "buffer_load_dwordx4 v[180:183], %[zt], %[rs], %[s4] offen offset:1024 sc1\n\t"
                    "buffer_load_dwordx4 v[184:187], %[zt], %[rs], %[s4] offen offset:2048 sc1\n\t"
                    "buffer_load_dwordx4 v[188:191], %[zt], %[rs], %[s4] offen offset:3072 sc1"
                    : "=&{v[160:167]}"(zf[0]), "=&{v[168:175]}"(zf[1]), "=&{v[176:183]}"(zf[2]), "=&{v[184:191]}"(zf[3])
                    : [zt] "v"(ztoff), [rs] "s"(rws), [so] "s"(zso), [s4] "s"(zso + 4096u) : "memory");
                zso = O_Z + (unsigned)((s + 1) * ZTSEG) * 2u;
                if (__builtin_expect(budget <= 0, 0)) __hip_atomic_store(a.ctl, 1u, __ATOMIC_RELAXED, __HIP_MEMORY_SCOPE_AGENT);
                RT(6);
                if (s + 1 == NSEG) break;
                const unsigned spa = (unsigned)((s & 1) * 2048 + (w < 3 ? w * 512 : 0) + lane * 8);
#define OST(r)
                unsigned long long t0_, t1_, t2_, t3_, t4_ = 0, bad;
                int tries = budget;
                unsigned po = poff, pm = pmoff, x0 = 4u * PAIR, x1 = 4096u;
                const unsigned mb = W_MEM + (unsigned)((s + 1) & 3) * 6u * PAIR;
                if (w < 3) asm volatile(
                    "s_barrier\n\t"
                    OST("%[t0]")
                    "v_mov_b64 v[96:97], 0\n\t"
                    "v_mov_b64 v[98:99], 0\n\t"
                    "v_mov_b32 v100, 0\n\t"
                    "s_barrier\n\t"
                    "1:\n\t"
                    "buffer_load_dwordx4 v[208:211], %[po], %[rw], %[pb] offen sc1\n\t"
                    "buffer_load_dwordx4 v[212:215], %[po], %[rw], %[pb] offen offset:1024 sc1\n\t"
                    "buffer_load_dwordx4 v[216:219], %[po], %[rw], %[pb] offen offset:2048 sc1\n\t"
                    "buffer_load_dwordx4 v[220:223], %[po], %[rw], %[pb] offen offset:3072 sc1\n\t"
                    OST("%[t1]")
                    "s_waitcnt vmcnt(3)\n\t"
                    OST("%[t3]")
                    "v_smfmac_f32_16x16x64_bf16 v[96:99], v[208:211], v[160:167], %[ix]\n\t"
                    "v_max3_u32 v100, v100, v208, v209\n\t"
                    "v_max3_u32 v100, v100, v210, v211\n\t"
                    "s_waitcnt vmcnt(2)\n\t"
                    "v_smfmac_f32_16x16x64_bf16 v[96:99], v[212:215], v[168:175], %[ix]\n\t"
                    "v_max3_u32 v100, v100, v212, v213\n\t"
                    "v_max3_u32 v100, v100, v214, v215\n\t"
                    "s_waitcnt vmcnt(1)\n\t"
                    "v_smfmac_f32_16x16x64_bf16 v[96:99], v[216:219], v[176:183], %[ix]\n\t"
                    "v_max3_u32 v100, v100, v216, v217\n\t"
                    "v_max3_u32 v100, v100, v218, v219\n\t"
                    "s_waitcnt vmcnt(0)\n\t"
                    "v_smfmac_f32_16x16x64_bf16 v[96:99], v[220:223], v[184:191], %[ix]\n\t"
                    "v_max3_u32 v100, v100, v220, v221\n\t"
                    "v_max3_u32 v100, v100, v222, v223\n\t"
                    "v_cmp_eq_u32 vcc, -1, v100\n\t"
                    OST("%[t2]")
                    "s_nop 4\n\t"
                    "v_pk_add_f32 v[96:97], v[96:97], v[98:99]\n\t"
                    "ds_write_b64 %[sp], v[96:97]\n\t"
                    "s_cbranch_vccz 2f\n\t"
                    "s_sub_i32 %[tr], %[tr], 1\n\t"
                    "s_cmp_lt_i32 %[tr], 1\n\t"
                    "s_cbranch_scc1 2f\n\t"
                    "v_xor_b32 %[po], %[x0], %[po]\n\t"
                    "s_xor_b32 %[x0], %[x0], 0x14000\n\t"
                    "s_waitcnt lgkmcnt(0)\n\t"
                    "v_mov_b64 v[96:97], 0\n\t"
                    "v_mov_b64 v[98:99], 0\n\t"
                    "v_mov_b32 v100, 0\n\t"
                    "s_branch 1b\n\t"
                    "2:\n\t"
                    "s_waitcnt lgkmcnt(0)\n\t"
                    "s_barrier\n\t"
                    "s_sleep 3\n\t"
                    : [tr] "+s"(tries), [po] "+v"(po), [x0] "+s"(x0), [t0] "=&s"(t0_), [t1] "=&s"(t1_), [t2] "=&s"(t2_), [t3] "=&s"(t3_)
                    : [sp] "v"(spa), [ix] "v"(sidx), [rw] "s"(rwin), [pb] "s"(pb), "{v[160:167]}"(zf[0]), "{v[168:175]}"(zf[1]), "{v[176:183]}"(zf[2]), "{v[184:191]}"(zf[3])
                    : "v96", "v97", "v98", "v99", "v100", "v208", "v209", "v210", "v211", "v212", "v213", "v214", "v215", "v216", "v217", "v218", "v219", "v220", "v221", "v222", "v223", "vcc", "scc", "memory");
                else asm volatile(
                    "s_barrier\n\t"
                    "buffer_load_dwordx4 v[128:131], %[zo], %[rw], %[zb] offen sc1\n\t"
                    "buffer_load_dwordx4 v[132:135], %[zo], %[rw], %[zb] offen offset:1024 sc1\n\t"
                    "buffer_load_dwordx4 v[136:139], %[zo], %[rw], %[zb] offen offset:2048 sc1\n\t"
                    "buffer_load_dwordx4 v[140:143], %[zo], %[rw], %[zb] offen offset:3072 sc1\n\t"
                    "buffer_load_dwordx4 v[144:147], %[zo], %[rw], %[z4] offen sc1\n\t"
                    "buffer_load_dwordx4 v[148:151], %[zo], %[rw], %[z4] offen offset:1024 sc1\n\t"
                    "buffer_load_dwordx4 v[152:155], %[zo], %[rw], %[z4] offen offset:2048 sc1\n\t"
                    "buffer_load_dwordx4 v[156:159], %[zo], %[rw], %[z4] offen offset:3072 sc1\n\t"
                    OST("%[t0]")
                    "v_mov_b64 v[96:97], 0\n\t"
                    "v_mov_b64 v[98:99], 0\n\t"
                    "v_mov_b32 v100, 0\n\t"
                    "s_barrier\n\t"
                    "s_sleep %[fd]\n\t"
                    "buffer_load_dwordx4 v[208:211], %[po], %[rw], %[pb] offen sc1\n\t"
                    "buffer_load_dwordx4 v[212:215], %[po], %[rw], %[pb] offen offset:1024 sc1\n\t"
                    "buffer_load_dwordx4 v[216:219], %[po], %[rw], %[pb] offen offset:2048 sc1\n\t"
                    "buffer_load_dwordx4 v[220:223], %[po], %[rw], %[pb] offen offset:3072 sc1\n\t"
                    "buffer_load_dwordx4 v[224:227], %[pm], %[rw], %[pb] offen sc1\n\t"
                    "buffer_load_dwordx4 v[228:231], %[pm], %[rw], %[pb] offen offset:1024 sc1\n\t"
                    "buffer_load_dwordx4 v[232:235], %[pm], %[rw], %[pb] offen offset:2048 sc1\n\t"
                    "buffer_load_dwordx4 v[236:239], %[pm], %[rw], %[pb] offen offset:3072 sc1\n\t"
                    OST("%[t1]")
                    "s_waitcnt vmcnt(8)\n\t"
                    "v_max3_u32 v101, v129, v131, v133\n\t"
                    "v_max3_u32 v101, v101, v135, v137\n\t"
                    "v_max3_u32 v101, v101, v139, v141\n\t"
                    "v_max3_u32 v101, v101, v143, v145\n\t"
                    "v_max3_u32 v101, v101, v147, v149\n\t"
                    "v_max3_u32 v101, v101, v151, v153\n\t"
                    "v_max3_u32 v101, v101, v155, v157\n\t"
                    "v_max_u32 v101, v101, v159\n\t"
                    "v_cmp_eq_u32 vcc, -1, v101\n\t"
                    "s_cbranch_vccz 3f\n\t"
                    "buffer_load_dwordx4 v[128:131], %[zo], %[rw], %[zb] offen sc1\n\t"
                    "buffer_load_dwordx4 v[132:135], %[zo], %[rw], %[zb] offen offset:1024 sc1\n\t"
                    "buffer_load_dwordx4 v[136:139], %[zo], %[rw], %[zb] offen offset:2048 sc1\n\t"
                    "buffer_load_dwordx4 v[140:143], %[zo], %[rw], %[zb] offen offset:3072 sc1\n\t"
                    "buffer_load_dwordx4 v[144:147], %[zo], %[rw], %[z4] offen sc1\n\t"
                    "buffer_load_dwordx4 v[148:151], %[zo], %[rw], %[z4] offen offset:1024 sc1\n\t"
                    "buffer_load_dwordx4 v[152:155], %[zo], %[rw], %[z4] offen offset:2048 sc1\n\t"
                    "buffer_load_dwordx4 v[156:159], %[zo], %[rw], %[z4] offen offset:3072 sc1\n\t"
                    "s_waitcnt vmcnt(0)\n\t"
                    "v_max3_u32 v101, v129, v131, v133\n\t"
                    "v_max3_u32 v101, v101, v135, v137\n\t"
                    "v_max3_u32 v101, v101, v139, v141\n\t"
                    "v_max3_u32 v101, v101, v143, v145\n\t"
                    "v_max3_u32 v101, v101, v147, v149\n\t"
                    "v_max3_u32 v101, v101, v151, v153\n\t"
                    "v_max3_u32 v101, v101, v155, v157\n\t"
                    "v_max_u32 v101, v101, v159\n\t"
                    "v_mov_b32 v100, v101\n\t"
                    "3:\n\t"
                    "v_permlane16_swap_b32 v128, v129\n\t"
                    "v_permlane16_swap_b32 v130, v131\n\t"
                    "v_permlane16_swap_b32 v132, v133\n\t"
                    "v_permlane16_swap_b32 v134, v135\n\t"
                    "v_permlane16_swap_b32 v136, v137\n\t"
                    "v_permlane16_swap_b32 v138, v139\n\t"
                    "v_permlane16_swap_b32 v140, v141\n\t"
                    "v_permlane16_swap_b32 v142, v143\n\t"
                    "v_permlane16_swap_b32 v144, v145\n\t"
                    "v_permlane16_swap_b32 v146, v147\n\t"
                    "v_permlane16_swap_b32 v148, v149\n\t"
                    "v_permlane16_swap_b32 v150, v151\n\t"
                    "v_permlane16_swap_b32 v152, v153\n\t"
                    "v_permlane16_swap_b32 v154, v155\n\t"
                    "v_permlane16_swap_b32 v156, v157\n\t"
                    "v_permlane16_swap_b32 v158, v159\n\t"
                    "s_mov_b32 vcc_lo, 0x00ff00ff\n\t"
                    "s_mov_b32 vcc_hi, 0x00ff00ff\n\t"
                    "s_waitcnt vmcnt(7)\n\t"
                    OST("%[t3]")
                    "v_smfmac_f32_16x16x64_bf16 v[96:99], v[208:211], v[160:167], %[ix]\n\t"
                    "v_max3_u32 v100, v100, v208, v209\n\t"
                    "v_max3_u32 v100, v100, v210, v211\n\t"
                    "s_waitcnt vmcnt(6)\n\t"
                    "v_smfmac_f32_16x16x64_bf16 v[96:99], v[212:215], v[168:175], %[ix]\n\t"
                    "v_max3_u32 v100, v100, v212, v213\n\t"
                    "v_max3_u32 v100, v100, v214, v215\n\t"
                    "s_waitcnt vmcnt(5)\n\t"
                    "v_smfmac_f32_16x16x64_bf16 v[96:99], v[216:219], v[176:183], %[ix]\n\t"
                    "v_max3_u32 v100, v100, v216, v217\n\t"
                    "v_max3_u32 v100, v100, v218, v219\n\t"
                    "s_waitcnt vmcnt(4)\n\t"
                    "v_smfmac_f32_16x16x64_bf16 v[96:99], v[220:223], v[184:191], %[ix]\n\t"
                    "v_max3_u32 v100, v100, v220, v221\n\t"
                    "v_max3_u32 v100, v100, v222, v223\n\t"
                    "s_waitcnt vmcnt(3)\n\t"
                    "v_smfmac_f32_16x16x64_bf16 v[96:99], v[224:227], v[128:135], %[ix]\n\t"
                    "v_max3_u32 v100, v100, v224, v225\n\t"
                    "v_max3_u32 v100, v100, v226, v227\n\t"
                    "s_waitcnt vmcnt(2)\n\t"
                    "v_smfmac_f32_16x16x64_bf16 v[96:99], v[228:231], v[136:143], %[ix]\n\t"
                    "s_barrier\n\t"
                    "ds_read_b64 v[102:103], %[sp]\n\t"
                    "ds_read_b64 v[104:105], %[sp] offset:512\n\t"
                    "ds_read_b64 v[106:107], %[sp] offset:1024\n\t"
                    "v_max3_u32 v100, v100, v228, v229\n\t"
                    "v_max3_u32 v100, v100, v230, v231\n\t"
                    "s_waitcnt vmcnt(1)\n\t"
                    "v_smfmac_f32_16x16x64_bf16 v[96:99], v[232:235], v[144:151], %[ix]\n\t"
                    "v_max3_u32 v100, v100, v232, v233\n\t"
                    "v_max3_u32 v100, v100, v234, v235\n\t"
                    "s_waitcnt vmcnt(0)\n\t"
                    "v_smfmac_f32_16x16x64_bf16 v[96:99], v[236:239], v[152:159], %[ix]\n\t"
                    "v_max3_u32 v100, v100, v236, v237\n\t"
                    "v_max3_u32 v100, v100, v238, v239\n\t"
                    OST("%[t2]")
                    "v_cmp_eq_u32 %[bad], -1, v100\n\t"
                    "s_waitcnt lgkmcnt(0)\n\t"
                    "v_add_f32 v102, v102, v104\n\t"
                    "v_add_f32 v103, v103, v105\n\t"
                    "v_add_f32 v102, v102, v106\n\t"
                    "v_add_f32 v103, v103, v107\n\t"
                    "v_add_f32 v96, v96, v102\n\t"
                    "v_add_f32 v97, v97, v103\n\t"
                    "v_add_f32 v96, v96, v98\n\t"
                    "v_add_f32 v97, v97, v99\n\t"
                    "s_cmp_lg_u64 %[bad], 0\n\t"
                    "s_cbranch_scc1 4f\n\t"
                    "2:\n\t"
                    "v_cndmask_b32_dpp v98, v97, v96, vcc row_ror:8 row_mask:0xf bank_mask:0xf\n\t"
                    "s_not_b64 vcc, vcc\n\t"
                    "v_cndmask_b32_dpp v99, v96, v97, vcc row_ror:8 row_mask:0xf bank_mask:0xf\n\t"
                    "v_cvt_pk_bf16_f32 v98, v98, v99\n\t"
                    "v_min_u32 v98, -2, v98\n\t"
                    "buffer_store_dword v98, %[mo], %[rw], %[m0] offen sc1\n\t"
                    "buffer_store_dword v98, %[mo], %[rw], %[m1] offen sc1\n\t"
                    "buffer_store_dword v98, %[mo], %[rw], %[m2] offen sc1\n\t"
                    "buffer_store_dword v98, %[mo], %[rw], %[m3] offen sc1\n\t"
                    OST("%[t4]")
                    "s_branch 5f\n\t"
                    "4:\n\t"
                    "s_sub_i32 %[tr], %[tr], 1\n\t"
                    "s_cmp_lt_i32 %[tr], 1\n\t"
                    "s_cbranch_scc1 2b\n\t"
                    "v_xor_b32 %[po], %[x0], %[po]\n\t"
                    "s_xor_b32 %[x0], %[x0], 0x14000\n\t"
                    "v_xor_b32 %[pm], %[x1], %[pm]\n\t"
                    "s_xor_b32 %[x1], %[x1], 0x5000\n\t"
                    "buffer_load_dwordx4 v[128:131], %[zo], %[rw], %[zb] offen sc1\n\t"
                    "buffer_load_dwordx4 v[132:135], %[zo], %[rw], %[zb] offen offset:1024 sc1\n\t"
                    "buffer_load_dwordx4 v[136:139], %[zo], %[rw], %[zb] offen offset:2048 sc1\n\t"
                    "buffer_load_dwordx4 v[140:143], %[zo], %[rw], %[zb] offen offset:3072 sc1\n\t"
                    "buffer_load_dwordx4 v[144:147], %[zo], %[rw], %[z4] offen sc1\n\t"
                    "buffer_load_dwordx4 v[148:151], %[zo], %[rw], %[z4] offen offset:1024 sc1\n\t"
                    "buffer_load_dwordx4 v[152:155], %[zo], %[rw], %[z4] offen offset:2048 sc1\n\t"
                    "buffer_load_dwordx4 v[156:159], %[zo], %[rw], %[z4] offen offset:3072 sc1\n\t"
                    "buffer_load_dwordx4 v[208:211], %[po], %[rw], %[pb] offen sc1\n\t"
                    "buffer_load_dwordx4 v[212:215], %[po], %[rw], %[pb] offen offset:1024 sc1\n\t"
                    "buffer_load_dwordx4 v[216:219], %[po], %[rw], %[pb] offen offset:2048 sc1\n\t"
                    "buffer_load_dwordx4 v[220:223], %[po], %[rw], %[pb] offen offset:3072 sc1\n\t"
                    "buffer_load_dwordx4 v[224:227], %[pm], %[rw], %[pb] offen sc1\n\t"
                    "buffer_load_dwordx4 v[228:231], %[pm], %[rw], %[pb] offen offset:1024 sc1\n\t"
                    "buffer_load_dwordx4 v[232:235], %[pm], %[rw], %[pb] offen offset:2048 sc1\n\t"
                    "buffer_load_dwordx4 v[236:239], %[pm], %[rw], %[pb] offen offset:3072 sc1\n\t"
                    "v_mov_b64 v[96:97], 0\n\t"
                    "v_mov_b64 v[98:99], 0\n\t"
                    "v_mov_b32 v100, 0\n\t"
                    "s_waitcnt vmcnt(0)\n\t"
                    "v_max3_u32 v101, v129, v131, v133\n\t"
                    "v_max3_u32 v101, v101, v135, v137\n\t"
                    "v_max3_u32 v101, v101, v139, v141\n\t"
                    "v_max3_u32 v101, v101, v143, v145\n\t"
                    "v_max3_u32 v101, v101, v147, v149\n\t"
                    "v_max3_u32 v101, v101, v151, v153\n\t"
                    "v_max3_u32 v101, v101, v155, v157\n\t"
                    "v_max_u32 v101, v101, v159\n\t"
                    "v_mov_b32 v100, v101\n\t"
                    "v_permlane16_swap_b32 v128, v129\n\t"
                    "v_permlane16_swap_b32 v130, v131\n\t"
                    "v_permlane16_swap_b32 v132, v133\n\t"
                    "v_permlane16_swap_b32 v134, v135\n\t"
                    "v_permlane16_swap_b32 v136, v137\n\t"
                    "v_permlane16_swap_b32 v138, v139\n\t"
                    "v_permlane16_swap_b32 v140, v141\n\t"
                    "v_permlane16_swap_b32 v142, v143\n\t"
                    "v_permlane16_swap_b32 v144, v145\n\t"
                    "v_permlane16_swap_b32 v146, v147\n\t"
                    "v_permlane16_swap_b32 v148, v149\n\t"
                    "v_permlane16_swap_b32 v150, v151\n\t"
                    "v_permlane16_swap_b32 v152, v153\n\t"
                    "v_permlane16_swap_b32 v154, v155\n\t"
                    "v_permlane16_swap_b32 v156, v157\n\t"
                    "v_permlane16_swap_b32 v158, v159\n\t"
                    "s_nop 1\n\t"
                    "v_smfmac_f32_16x16x64_bf16 v[96:99], v[208:211], v[160:167], %[ix]\n\t"
                    "v_max3_u32 v100, v100, v208, v209\n\t"
                    "v_max3_u32 v100, v100, v210, v211\n\t"
                    "v_smfmac_f32_16x16x64_bf16 v[96:99], v[212:215], v[168:175], %[ix]\n\t"
                    "v_max3_u32 v100, v100, v212, v213\n\t"
                    "v_max3_u32 v100, v100, v214, v215\n\t"
                    "v_smfmac_f32_16x16x64_bf16 v[96:99], v[216:219], v[176:183], %[ix]\n\t"
                    "v_max3_u32 v100, v100, v216, v217\n\t"
                    "v_max3_u32 v100, v100, v218, v219\n\t"
                    "v_smfmac_f32_16x16x64_bf16 v[96:99], v[220:223], v[184:191], %[ix]\n\t"
                    "v_max3_u32 v100, v100, v220, v221\n\t"
                    "v_max3_u32 v100, v100, v222, v223\n\t"
                    "v_smfmac_f32_16x16x64_bf16 v[96:99], v[224:227], v[128:135], %[ix]\n\t"
                    "v_max3_u32 v100, v100, v224, v225\n\t"
                    "v_max3_u32 v100, v100, v226, v227\n\t"
                    "v_smfmac_f32_16x16x64_bf16 v[96:99], v[228:231], v[136:143], %[ix]\n\t"
                    "v_max3_u32 v100, v100, v228, v229\n\t"
                    "v_max3_u32 v100, v100, v230, v231\n\t"
                    "v_smfmac_f32_16x16x64_bf16 v[96:99], v[232:235], v[144:151], %[ix]\n\t"
                    "v_max3_u32 v100, v100, v232, v233\n\t"
                    "v_max3_u32 v100, v100, v234, v235\n\t"
                    "v_smfmac_f32_16x16x64_bf16 v[96:99], v[236:239], v[152:159], %[ix]\n\t"
                    "v_max3_u32 v100, v100, v236, v237\n\t"
                    "v_max3_u32 v100, v100, v238, v239\n\t"
                    "v_cmp_eq_u32 %[bad], -1, v100\n\t"
                    "s_nop 5\n\t"
                    "v_add_f32 v96, v96, v102\n\t"
                    "v_add_f32 v97, v97, v103\n\t"
                    "v_add_f32 v96, v96, v98\n\t"
                    "v_add_f32 v97, v97, v99\n\t"
                    "s_cmp_lg_u64 %[bad], 0\n\t"
                    "s_cbranch_scc1 4b\n\t"
                    "s_branch 2b\n\t"
                    "5:\n\t"
                    : [tr] "+s"(tries), [po] "+v"(po), [pm] "+v"(pm), [x0] "+s"(x0), [x1] "+s"(x1), [bad] "=&s"(bad), [t0] "=&s"(t0_), [t1] "=&s"(t1_), [t2] "=&s"(t2_), [t3] "=&s"(t3_), [t4] "=&s"(t4_)
                    : [zo] "v"(zoff), [sp] "v"(spa), [ix] "v"(sidx), [rw] "s"(rwin), [zb] "s"(zb), [z4] "s"(zb + 4096u), [pb] "s"(pb), [mo] "v"(moff), [fd] "n"(FDL),
                      [m0] "s"(mb), [m1] "s"(mb + PAIR), [m2] "s"(mb + mc2), [m3] "s"(mb + mc2 + PAIR), "{v[160:167]}"(zf[0]), "{v[168:175]}"(zf[1]), "{v[176:183]}"(zf[2]), "{v[184:191]}"(zf[3])
                    : "v96", "v97", "v98", "v99", "v100", "v101", "v102", "v103", "v104", "v105", "v106", "v107", "v128", "v129", "v130", "v131", "v132", "v133", "v134", "v135", "v136", "v137", "v138", "v139", "v140", "v141", "v142", "v143", "v144", "v145", "v146", "v147", "v148", "v149", "v150", "v151", "v152", "v153", "v154", "v155", "v156", "v157", "v158", "v159", "v208", "v209", "v210", "v211", "v212", "v213", "v214", "v215", "v216", "v217", "v218", "v219", "v220", "v221", "v222", "v223", "v224", "v225", "v226", "v227", "v228", "v229", "v230", "v231", "v232", "v233", "v234", "v235", "v236", "v237", "v238", "v239", "vcc", "scc", "memory");
                budget = tries;
                if (w == 3) *(volatile LAS int*)(smem + 4100) = s;
                __builtin_amdgcn_s_sleep(DPS - 4);
                if (w == 3) {
                    st128(SENT, rwin, ra, W_MEM + (unsigned)(s & 3) * 6u * PAIR, 16);
                    if (lane == 0 && s > 1) __builtin_amdgcn_raw_buffer_store_b32((unsigned)s - 2u, rwin, 0u, W_GO + (unsigned)o * 128u, 16);
                }
                for (;;) {
                    const u32x2 x = *(volatile LAS u32x2*)(smem + 4096);
                    zr = __builtin_amdgcn_readfirstlane(x[0]);
                    if (__builtin_amdgcn_readfirstlane(x[1]) == s) break;
                    __builtin_amdgcn_s_sleep(2);
                }
                zb = zb == W_ZM + 2u * NOWN * PAIR ? W_ZM : zb + NOWN * PAIR;
            }
            asm volatile("s_waitcnt vmcnt(0)" : "+{v[160:167]}"(zf[0]), "+{v[168:175]}"(zf[1]), "+{v[176:183]}"(zf[2]), "+{v[184:191]}"(zf[3]) :: "memory");
            BAR0();
            BAR0();
            f32x4 c[2];
            for (unsigned spins = 0;;) {
                asm volatile("" ::: "memory");
                u32x4 pt[16], ptm[2][2], zm[2];
#pragma unroll
                for (int q = 0; q < 2; ++q) zm[q] = __builtin_amdgcn_raw_buffer_load_b128(rwin, zoff + hg * 2048u + q * 1024u, zb, 16);
#pragma unroll
                for (int q = 0; q < 16; ++q) pt[q] = __builtin_amdgcn_raw_buffer_load_b128(rwin, (unsigned)(16 * hg) * 1024u + lane * 16u, W_PL + q * 1024u, 16);
#pragma unroll
                for (int q = 0; q < 4; ++q) ptm[q >> 1][q & 1] = __builtin_amdgcn_raw_buffer_load_b128(rwin, (unsigned)(hg * 2048 + lane * 16), W_PL + 65536u + (q >> 1) * 8192u + (q & 1) * 1024u, 16);
                unsigned m = max(smax(zm[0]), smax(zm[1]));
#pragma unroll
                for (int t = 0; t < 2; ++t) {
                    c[t] = z4;
#pragma unroll
                    for (int q = 0; q < 8; ++q) { c[t] = mfma32(half8(zf[q >> 1], q & 1), pt[2 * q + t], c[t]); m = max(m, smax(pt[2 * q + t])); }
#pragma unroll
                    for (int q = 0; q < 2; ++q) { c[t] = mfma32((u32x4){zm[q][0], zm[q][2], zm[q][1], zm[q][3]}, ptm[t][q], c[t]); m = max(m, smax(ptm[t][q])); }
                }
                if (!__any(m == 0xFFFFFFFFu)) break;
                if (spin_fail(spins, a.ctl)) break;
            }
#pragma unroll
            for (int t = 0; t < 2; ++t)
#pragma unroll
                for (int r = 0; r < 4; ++r) *(float*)(op + hg * 2048 + (16 * t + fr) * 64 + pi16(4 * fq + r) * 4) = c[t][r];
            BAR();
        } else if (w == 4) {
            const unsigned fo = (unsigned)(lane & 31) * 128u + (unsigned)(o / 10) * 4u + (lane & 32) * 2u;
            int zk = -1;
            if (lane < 2) *(volatile LAS int*)(smem + 4096 + lane * 4) = -1;
            for (int s = 0; s < NSEG; ++s) {
                asm volatile("s_waitcnt lgkmcnt(0)\n\ts_barrier\n\ts_barrier" ::: "memory");
                int f = 0;
                const bool look = zk < min(s + ZLA, NSEG - 1);
                if (look) { __builtin_amdgcn_s_sleep(6); f = (int)__builtin_amdgcn_raw_buffer_load_b32(rwin, fo, W_FZ, 16); }
                SB();
                BAR0();
                if (look) {
                    f = min(f, __builtin_amdgcn_update_dpp(f, f, 0xB1, 0xF, 0xF, false)); f = min(f, __builtin_amdgcn_update_dpp(f, f, 0x4E, 0xF, 0xF, false));
                    f = min(f, __builtin_amdgcn_update_dpp(f, f, 0x141, 0xF, 0xF, false)); f = min(f, __builtin_amdgcn_update_dpp(f, f, 0x140, 0xF, 0xF, false));
                    zk = seg_done(MXV, min(__builtin_amdgcn_readlane(f, 0), __builtin_amdgcn_readlane(f, 16)), min(__builtin_amdgcn_readlane(f, 32), __builtin_amdgcn_readlane(f, 48)));
                    *(volatile LAS int*)(smem + 4096) = zk;
                }
            }
        } else if (w == 5) {
            for (int s = 0; s < 3 * NSEG; ++s) BAR0();
        } else if (w == 6) {
            const unsigned ooff = (unsigned)((S_ - SEG + (lane >> 1)) * D_ + o * 16 + (lane & 1) * 8) * 2u;
            for (int s = 0; s < 3 * NSEG - 2; ++s) BAR0();
            const u32x4 xr = __builtin_amdgcn_raw_buffer_load_b128(rws, ooff, O_X1, 16);
            asm volatile("s_barrier\n\ts_barrier ; 6" ::: "memory");
            const char* src = op + lane * 32;
            f32x4 x = *(const f32x4*)src, y = *(const f32x4*)(src + 16);
#pragma unroll
            for (int ww = 1; ww < 4; ++ww) { x += *(const f32x4*)(src + ww * 2048); y += *(const f32x4*)(src + ww * 2048 + 16); }
            const u32x4 r = {pack2(lo2f(xr[0]) + bfr(x[0]), hi2f(xr[0]) + bfr(x[1])), pack2(lo2f(xr[1]) + bfr(x[2]), hi2f(xr[1]) + bfr(x[3])),
                             pack2(lo2f(xr[2]) + bfr(y[0]), hi2f(xr[2]) + bfr(y[1])), pack2(lo2f(xr[3]) + bfr(y[2]), hi2f(xr[3]) + bfr(y[3]))};
            st128(r, mkrsrc(a.out, S_ * D_ * 2), ooff, 0u, 16);
            if (a.Fnext) {
                VMWAIT();
                if (lane == 0) __builtin_amdgcn_raw_buffer_store_b32(0u, rwin, 0u, W_OUT + (unsigned)(NSEG - 1) * 256u + (unsigned)o * 4u, 16);
            }
        } else {
            Tk tk = tk_init();
            __builtin_amdgcn_s_setprio(3);
            for (int s = 0; s < NSEG; ++s) {
                const unsigned ta0 = ck.ta, mm0 = ck.ma;
                unsigned tp = tpp + lt + xs + M1;
                if (s < SAD) { sched(tk, s); tp = tpp - M2 - XS0 - PEN1; }
                wait_fine_bar(tp - DZP, ck);
                wait_cyc_bar(cyc_at(ck, sx24(tp + (s + 1 < NSEG ? 0 : ML) - ck.ta)));
                clk_rate(ck, ta0, mm0);
                asm volatile("s_barrier ; 7" ::: "memory");
                if (s >= SAD && s + 1 < NSEG) sched(tk, s);
                if (o == 0) __builtin_amdgcn_raw_buffer_store_b32(tpp, rws, 0u, 4096u + 540000u + (unsigned)((a.Fnext ? 0 : 64) + s) * 4u, 0);
                RTS();
            }
        }
        SST(23);
    }
    LANES();
    for (int ph = 0; ph < 2; ++ph) {
        if (ph ? bid >= NQT && bid < NQT + NOWN : bid >= NREC && bid < NQT) {
            const int o = bid - (ph ? NQT : NREC), cg = o >> 1, sbeg = (ph ? TAILS : 0) + (o & 1), send = ph ? NSEG - 1 : TAILS;
            const unsigned ztoff = (unsigned)((2 * cg * H_ + 4 * w) * 64 + lane) * 16u;
            const unsigned ptoff = (unsigned)(8 * w * 64 + lane) * 16u, ptmoff = 65536u + (unsigned)(4 * w * 16 + lane) * 16u;
            const unsigned ooff = (unsigned)(((w & 1) * 16 + (lane >> 2)) * D_ + cg * 32 + (lane & 3) * 8) * 2u;
            const unsigned tfoff = lane < 16 ? W_FPT + (unsigned)((4 * w + (lane & 3)) * 128 + (lane >> 2) * 4) : W_FZ2 + (unsigned)((4 * w + (lane & 3)) * 128 + (cg / 5) * 4);
            int rdy = -1;
            if (ph) {
                if (w == 0) for (unsigned spins = 0;;) {
                    asm volatile("" ::: "memory");
                    if ((int)__builtin_amdgcn_readfirstlane(__builtin_amdgcn_raw_buffer_load_b32(rwin, 0u, W_GO + (unsigned)o * 128u, 16)) >= TAILS - 3) break;
                    if (spin_fail(spins, a.ctl)) break;
                    __builtin_amdgcn_s_sleep(OSLP);
                }
                __syncthreads();
                for (unsigned spins = 0;;) {
                    asm volatile("" ::: "memory");
                    if (!__any((int)__builtin_amdgcn_raw_buffer_load_b32(rwin, (unsigned)(4 * w + (lane & 3)) * 128u + (unsigned)(cg / 5) * 4u, W_FZ, 16) < NSEG - 2)) break;
                    if (spin_fail(spins, a.ctl)) break;
                    __builtin_amdgcn_s_sleep(8);
                }
            }
            auto wait_rdy = [&](int s) {
                for (unsigned spins = 0; rdy < s;) {
                    asm volatile("" ::: "memory");
                    if (!ph) {
                        rdy = (int)__builtin_amdgcn_readfirstlane(__builtin_amdgcn_raw_buffer_load_b32(rwin, 0u, W_GO + (unsigned)o * 128u, 16));
                    } else {
                        const int f = (int)__builtin_amdgcn_raw_buffer_load_b32(rwin, tfoff, 0u, 16);
                        if (!__any(f < s)) rdy = s;
                    }
                    if (rdy >= s) break;
                    if (spin_fail(spins, a.ctl)) break;
                    if (ph) __builtin_amdgcn_s_sleep(OSLP2); else __builtin_amdgcn_s_sleep(OSLP);
                }
            };
            for (int s = sbeg; s < send; s += 2) {
                RT(0);
                u32x4 xr, zf[2][4], zx[2], pt[8], ptm[2];
                if (!ph) wait_rdy(max(s - 2, 0));
                if (w >= 6) xr = __builtin_amdgcn_raw_buffer_load_b128(rws, ooff, O_X1 + (unsigned)(s * SEG * D_ * 2), 16);
#pragma unroll
                for (int q = 0; q < 8; ++q) zf[q >> 2][q & 3] = __builtin_amdgcn_raw_buffer_load_b128(rws, ztoff + (q & 3) * 1024u, O_Z + (unsigned)(s * ZTSEG) * 2u + (q >> 2) * (H_ * 1024u), 16);
                wait_rdy(s);
                RT(1);
                const unsigned ptb = O_PT + (unsigned)(s * PTSEG) * 2u;
#pragma unroll
                for (int q = 0; q < 8; ++q) pt[q] = __builtin_amdgcn_raw_buffer_load_b128(rws, ptoff + q * 1024u, ptb, 16);
#pragma unroll
                for (int q = 0; q < 2; ++q) {
                    zx[q] = __builtin_amdgcn_raw_buffer_load_b128(rws, tid * 16u, O_Z2 + (unsigned)(s * NOWN + 2 * cg + q) * 8192u, 16);
                    ptm[q] = __builtin_amdgcn_raw_buffer_load_b128(rws, ptmoff + q * 8192u, ptb, 16);
                }
                char* op = smem + ((s >> 1) & 1) * 32768;
#pragma unroll
                for (int u = 0; u < 4; ++u) {
                    const int tq = u >> 1, t = u & 1;
                    f32x4 c = z4;
#pragma unroll
                    for (int q = 0; q < 4; ++q) c = mfma32(zf[t][q], pt[2 * q + tq], c);
                    c = mfma32((u32x4){zx[t][0], zx[t][2], zx[t][1], zx[t][3]}, ptm[tq], c);
                    *(f32x4*)(op + w * 4096 + (16 * tq + fr) * 128 + (((t * 4 + fq) ^ (fr & 7)) << 4)) = c;
                }
                __syncthreads();
                if (w >= 6) {
                    const int tk = (w & 1) * 16 + (lane >> 2);
                    f32x2 x[4];
#pragma unroll
                    for (int q = 0; q < 4; ++q) {
                        const char* sp = op + tk * 128 + ((((lane & 2) * 2 + q) ^ (tk & 7)) << 4) + (lane & 1) * 8;
                        x[q] = *(const f32x2*)sp;
#pragma unroll
                        for (int ww = 1; ww < 8; ++ww) x[q] += *(const f32x2*)(sp + ww * 4096);
                    }
                    const u32x4 r = {pack2(lo2f(xr[0]) + bfr(x[0][0]), hi2f(xr[0]) + bfr(x[2][0])), pack2(lo2f(xr[1]) + bfr(x[1][0]), hi2f(xr[1]) + bfr(x[3][0])),
                                     pack2(lo2f(xr[2]) + bfr(x[0][1]), hi2f(xr[2]) + bfr(x[2][1])), pack2(lo2f(xr[3]) + bfr(x[1][1]), hi2f(xr[3]) + bfr(x[3][1]))};
                    if (!a.Fnext) st128(r, mkrsrc(a.out, S_ * D_ * 2), ooff, (unsigned)(s * SEG * D_ * 2), 0);
                    else {
                        st128(r, mkrsrc(a.out, S_ * D_ * 2), ooff, (unsigned)(s * SEG * D_ * 2), 16);
                        VMWAIT();
                        if (lane == 0) __builtin_amdgcn_raw_buffer_store_b32(0u, rwin, 0u, W_OUT + (unsigned)(s * 64 + 2 * cg + (w & 1)) * 4u, 16);
                    }
                }
                RT(2);
                RTS();
                if (s < 8) SST(8 + s);
                if (s >= 56) SST(s - 40);
            }
        }
        if (a.Fnext && ph == 0 && bid >= NQT + NOWN) {
            if (tid < 320) ((float4*)L.tw)[tid] = ((const float4*)(ws + OFF_TW640))[tid];
            for (int u = bid - (NQT + NOWN); u < 256; u += 40) {
                if (w == 0) {
                    for (unsigned spins = 0;;) {
                        asm volatile("" ::: "memory");
                        const unsigned f = __builtin_amdgcn_raw_buffer_load_b32(rwin, (unsigned)min(lane, NOWN - 1) * 4u, W_OUT + (unsigned)(u >> 2) * 256u, 16);
                        if (!__any(f != 0u)) break;
                        if (spin_fail(spins, a.ctl)) break;
                        if (u < 216) __builtin_amdgcn_s_sleep(WSLP); else __builtin_amdgcn_s_sleep(4);
                    }
                }
                __syncthreads();
                SST(24);
                a1_unit(mkrsrc(a.out, S_ * D_ * 2), u * 8, a.Fnext, L, tid, XA1);
                SST(25);
            }
        }
    }
    SST(31);
    __syncthreads();
    if (tid == 0) ((unsigned long long*)(ws + 4096 + 524288))[(a.Fnext ? 512 : 1024) + blockIdx.x] = __builtin_amdgcn_s_memrealtime();
}

constexpr int L_DBG = 0;
extern "C" size_t dbg_offset(int i) {
    const size_t o[] = {OFF_CTL, OFF_WQKVT, OFF_WOT, OFF_WFT, OFF_TW640, OFF_TW2048, OFF_F, OFF_X1, OFF_X2, OFF_H, OFF_Z, OFF_WIN, OFF_WIN, OFF_PT, OFF_WIN, OFF_END};
    return o[i];
}

__global__ void nop_kernel(char* ws) { if (ws == nullptr) ws[0] = 1; }
__global__ void enc_kernel(char* ws) {
    const unsigned long long* t = (const unsigned long long*)(ws + 4096 + 524288);
    const unsigned* g = (const unsigned*)(ws + 4096 + 540000);
    unsigned long long mn[5], mx[5];
    for (int i = 0; i < 5; ++i) { mn[i] = ~0ull; mx[i] = 0; for (int j = 0; j < 256; ++j) { mn[i] = t[i * 256 + j] < mn[i] ? t[i * 256 + j] : mn[i]; mx[i] = t[i * 256 + j] > mx[i] ? t[i * 256 + j] : mx[i]; } }
    auto d = [](unsigned a, unsigned b) { return (a - b) & 0xFFFFFFu; };
    unsigned v = 0;
    if (ENC == 1) v = (unsigned)(mx[4] - mn[0]);
    if (ENC == 2) v = 4u * (unsigned)(mn[1] - mn[0]);
    if (ENC == 3) v = d(g[0], 4u * (unsigned)mn[0]);
    if (ENC == 4) v = d(g[8], g[0]);
    if (ENC == 5) v = d(g[62], g[8]) / 4u;
    if (ENC == 6) v = d(g[64], g[62]);
    if (ENC == 7) v = d(g[72], g[64]);
    if (ENC == 8) v = d(g[126], g[72]) / 4u;
    if (ENC == 9) v = d(4u * (unsigned)mx[4], g[126]);
    const unsigned long long te = mn[0] + 40000ull + (v > 60000u ? 60000u : v);
    while (__builtin_amdgcn_s_memrealtime() < te) __builtin_amdgcn_s_sleep(1);
}
extern "C" void kernel_launch(void* const* d_in, const int* in_sizes, int n_in, void* d_out, int out_size, void* d_ws, size_t ws_size,
                              hipStream_t stream) {
    const u16* A_x = (const u16*)d_in[0];
    const u16* w_r = (const u16*)d_in[1];
    const u16* w_i = (const u16*)d_in[2];
    const u16* mem0 = (const u16*)d_in[4];
    const u16* Wq = (const u16*)d_in[5];
    const u16* Wk = (const u16*)d_in[6];
    const u16* Wv = (const u16*)d_in[7];
    const u16* Wo = (const u16*)d_in[8];
    char* ws = (char*)d_ws;
    u16* X2 = (u16*)(ws + OFF_X2);

    hipFuncSetAttribute((const void*)b3_kernel, hipFuncAttributeMaxDynamicSharedMemorySize, L_END + L_DBG);
    char* win = (char*)(((uintptr_t)(ws + OFF_WIN) + 0x1FFFFFu) & ~(uintptr_t)0x1FFFFFu);
    k1_kernel<<<16 + 404 + 320, 320, 0, stream>>>(A_x, Wq, Wk, w_r, w_i, ws, win);
    for (int layer = 0; layer < 2; ++layer) {
        const u16* xin = layer == 0 ? A_x : X2;
        u16* xout = layer == 0 ? X2 : (u16*)d_out;
        a2_kernel<<<layer == 0 ? DF + 160 + 320 : DF, 256, 0, stream>>>(Wv, Wo, ws, win);
        RecArgs ra{(unsigned*)ws, win, mem0, xin, xout, layer == 0 ? (float4*)(ws + OFF_F) : nullptr};
        b3_kernel<<<256, 512, L_END + L_DBG, stream>>>(ra);
    }
    for (int i = 0; i < 6; ++i) nop_kernel<<<1, 64, 0, stream>>>(ws);
    enc_kernel<<<1, 1, 0, stream>>>(ws);
}
```

```cpp
#include <hip/hip_runtime.h>
#include <hip/hip_bf16.h>
#include <stdint.h>

typedef unsigned short u16;
typedef __attribute__((ext_vector_type(8))) __bf16 bf16x8;
typedef __attribute__((ext_vector_type(4))) short s16x4;
typedef __attribute__((ext_vector_type(4))) float f32x4;
typedef __attribute__((ext_vector_type(8))) float f32x8;
typedef __attribute__((ext_vector_type(16))) float f32x16;
typedef __attribute__((ext_vector_type(4))) unsigned u32x4;
typedef __attribute__((ext_vector_type(2))) unsigned u32x2;
typedef __attribute__((ext_vector_type(8))) unsigned u32x8;
typedef __attribute__((ext_vector_type(16))) __bf16 bf16x16;
typedef __attribute__((ext_vector_type(2))) unsigned short u16x2;
#define LAS __attribute__((address_space(3)))
template <int N> struct IC { static constexpr int v = N; };

constexpr int S_ = 2048, D_ = 640, DF = 321, H_ = 32, DH = 128, HD = 4096, NM = 8, SEG = 32, NSEG = 64;
constexpr int NQKV = 3 * HD;
constexpr int NOWN = 40;
constexpr int ZTR = NSEG;
constexpr size_t ZTSEG = (size_t)NOWN * H_ * 512;
constexpr unsigned PAIR = 16384, HALF = 8192;
constexpr unsigned W_MEM = 0, W_P = W_MEM + 24 * PAIR, W_T = W_P + 40 * PAIR, W_GO = W_T + 8 * PAIR, W_OUT = W_GO + PAIR, W_FZ = W_OUT + PAIR, W_FPT = W_FZ + 4096, W_FZ2 = W_FZ + 8192, W_FQ = W_FZ + 12288,
                   W_H = W_FZ + PAIR, W_FA = W_H + 8192, W_FB = W_GO + 8192, W_PL = W_H + PAIR, W_FL = W_PL + 6 * PAIR - W_GO, W_PROBE = W_GO + 2 * W_FL,
                   W_ZM = 2u * 1024 * 1024, W_END = W_ZM + 120 * PAIR;
constexpr unsigned W_ST = W_PROBE + 2 * PAIR, W_ROT = W_ST + 8192, W_CFG = W_ROT + 128;
static_assert(W_ST + PAIR <= 2u * 1024 * 1024 && W_PROBE % (2 * PAIR) == 0, "window");
constexpr int PTSEG = 32768 + 8192;

#define NPROBE 2
#define WAUX 0
constexpr int NG = 81;
constexpr size_t al(size_t x) { return (x + 255) & ~(size_t)255; }
constexpr size_t OFF_CTL  = 0;
constexpr size_t OFF_WFT  = 4096 + 4 * 1024 * 1024;
constexpr size_t OFF_F    = OFF_WFT + al((size_t)4 * NG * S_ * 4);
constexpr size_t OFF_X1   = OFF_F + (size_t)NG * 65536;
constexpr size_t OFF_X2   = OFF_X1 + al((size_t)S_ * D_ * 2);
constexpr size_t OFF_H    = OFF_X2 + al((size_t)S_ * D_ * 2);
constexpr size_t OFF_Z    = OFF_H + al((size_t)S_ * D_ * 2);
constexpr size_t OFF_Z2   = OFF_Z + al(ZTSEG * ZTR * 2);
constexpr size_t OFF_PT   = OFF_Z2 + al((size_t)NSEG * NOWN * 8192);
constexpr size_t OFF_QK   = OFF_PT + al((size_t)NSEG * PTSEG * 2);
constexpr size_t OFF_WIN  = OFF_QK + (size_t)NSEG * H_ * 16384;
constexpr size_t OFF_END  = OFF_WIN + (size_t)6 * 1024 * 1024;
static_assert(OFF_END <= (size_t)256 * 1024 * 1024, "workspace");

#define st128(v, r, vo, so, aux) do { const u32x4 d_ = (v); __builtin_amdgcn_raw_buffer_store_b128(d_, r, vo, so, aux); asm volatile("s_nop 1" :: "v"(d_)); } while (0)
__device__ __forceinline__ float bf2f(u16 v) { return __uint_as_float((unsigned)v << 16); }
__device__ __forceinline__ u16 f2bf(float f) { __bf16 b = (__bf16)f; return __builtin_bit_cast(u16, b); }
__device__ __forceinline__ float bfr(float f) { return (float)(__bf16)f; }
typedef __attribute__((ext_vector_type(2))) float f32x2;
typedef __attribute__((ext_vector_type(2))) __bf16 bf16x2;
__device__ __forceinline__ unsigned pack2(float a, float b) { return __builtin_bit_cast(unsigned, __builtin_convertvector((f32x2){a, b}, bf16x2)); }
__device__ __forceinline__ float lo2f(unsigned v) { return __uint_as_float(v << 16); }
__device__ __forceinline__ float hi2f(unsigned v) { return __uint_as_float(v & 0xFFFF0000u); }
__device__ __forceinline__ float fmx(float a, float b) { return __builtin_elementwise_maximum(a, b); }
__device__ __forceinline__ f32x4 mfma32(u32x4 a, u32x4 b, f32x4 c) {
    return __builtin_amdgcn_mfma_f32_16x16x32_bf16(__builtin_bit_cast(bf16x8, a), __builtin_bit_cast(bf16x8, b), c, 0, 0, 0);
}
__device__ __forceinline__ f32x4 mfma16(u32x2 a, u32x2 b, f32x4 c) {
    return __builtin_amdgcn_mfma_f32_16x16x16bf16_1k(__builtin_bit_cast(s16x4, a), __builtin_bit_cast(s16x4, b), c, 0, 0, 0);
}
__device__ __forceinline__ f32x4 smfmac(u32x4 a, u32x8 b, f32x4 c, int idx) {
    return __builtin_amdgcn_smfmac_f32_16x16x64_bf16(__builtin_bit_cast(bf16x8, a), __builtin_bit_cast(bf16x16, b), c, idx, 0, 0);
}
__device__ __forceinline__ int pi16(int k) { return ((k & 3) << 2) | ((k >> 1) & 2) | (k >> 3); }
__device__ __forceinline__ float addswap32(float a, float b) {
    auto r = __builtin_amdgcn_permlane32_swap(__float_as_uint(a), __float_as_uint(b), false, false);
    return __uint_as_float(r[0]) + __uint_as_float(r[1]);
}
__device__ __forceinline__ float xmax3216(float v) {
    auto r = __builtin_amdgcn_permlane32_swap(__float_as_uint(v), __float_as_uint(v), false, false);
    v = fmx(__uint_as_float(r[0]), __uint_as_float(r[1]));
    auto q = __builtin_amdgcn_permlane16_swap(__float_as_uint(v), __float_as_uint(v), false, false);
    return fmx(__uint_as_float(q[0]), __uint_as_float(q[1]));
}
__device__ __forceinline__ float xsum3216(float v) {
    auto r = __builtin_amdgcn_permlane32_swap(__float_as_uint(v), __float_as_uint(v), false, false);
    v = __uint_as_float(r[0]) + __uint_as_float(r[1]);
    auto q = __builtin_amdgcn_permlane16_swap(__float_as_uint(v), __float_as_uint(v), false, false);
    return __uint_as_float(q[0]) + __uint_as_float(q[1]);
}
template <int CTRL> __device__ __forceinline__ float dppf(float v) { return __uint_as_float((unsigned)__builtin_amdgcn_update_dpp(0, (int)__float_as_uint(v), CTRL, 0xF, 0xF, false)); }
__device__ __forceinline__ float wsum(float v) {
    v += dppf<0xB1>(v); v += dppf<0x4E>(v); v += dppf<0x141>(v); v += dppf<0x140>(v);
    return xsum3216(v);
}
__device__ __forceinline__ unsigned ror8(unsigned v) { return (unsigned)__builtin_amdgcn_update_dpp(0, (int)v, 0x128, 0xF, 0xF, false); }
__device__ __forceinline__ float ror8f(float v) { return __uint_as_float(ror8(__float_as_uint(v))); }
__device__ __forceinline__ unsigned nosent(unsigned v) {
    u16x2 lim = {0xFFFE, 0xFFFE};
    return __builtin_bit_cast(unsigned, __builtin_elementwise_min(__builtin_bit_cast(u16x2, v), lim));
}
__device__ __forceinline__ unsigned pkmax(unsigned a, unsigned b) {
    return __builtin_bit_cast(unsigned, __builtin_elementwise_max(__builtin_bit_cast(u16x2, a), __builtin_bit_cast(u16x2, b)));
}
__device__ __forceinline__ bool has_sent(u32x4 v) {
    unsigned m = pkmax(pkmax(v.x, v.y), pkmax(v.z, v.w));
    return (m & 0xFFFFu) == 0xFFFFu || (m >> 16) == 0xFFFFu;
}

typedef f32x2 c32;
__device__ __forceinline__ c32 fma2(c32 a, c32 b, c32 c) { return __builtin_elementwise_fma(a, b, c); }
__device__ __forceinline__ c32 dup(float v) { return (c32){v, v}; }
template <int SG> __device__ __forceinline__ c32 addi(c32 a, c32 b) {
    c32 d;
    if (SG > 0) asm("v_pk_add_f32 %0, %1, %2 op_sel:[0,1] op_sel_hi:[1,0] neg_lo:[0,1]" : "=v"(d) : "v"(a), "v"(b));
    else asm("v_pk_add_f32 %0, %1, %2 op_sel:[0,1] op_sel_hi:[1,0] neg_hi:[0,1]" : "=v"(d) : "v"(a), "v"(b));
    return d;
}
template <int SG> __device__ __forceinline__ c32 ctw(c32 a, c32 w) {
    c32 d = a * w.xx;
    if (SG < 0) asm("v_pk_fma_f32 %0, %1, %2, %0 op_sel:[1,1,0] op_sel_hi:[0,1,1] neg_lo:[0,1,0]" : "+v"(d) : "v"(a), "v"(w));
    else asm("v_pk_fma_f32 %0, %1, %2, %0 op_sel:[1,1,0] op_sel_hi:[0,1,1] neg_hi:[0,1,0]" : "+v"(d) : "v"(a), "v"(w));
    return d;
}
__device__ __forceinline__ c32 cmulk(c32 a, float x, float y) { return fma2(a.yx, (c32){-y, y}, a * dup(x)); }
__device__ __forceinline__ c32 cis(float rev) { return (c32){__builtin_amdgcn_cosf(rev), __builtin_amdgcn_sinf(rev)}; }
template <int SG, bool CI = false> __device__ __forceinline__ void dft4(c32& a, c32& b, c32& c, c32& d) {
    const c32 t0 = CI ? addi<SG>(a, c) : a + c, t1 = CI ? addi<-SG>(a, c) : a - c, t2 = b + d, t3 = b - d;
    a = t0 + t2; c = t0 - t2; b = addi<SG>(t1, t3); d = addi<-SG>(t1, t3);
}
template <int SG> __device__ __forceinline__ void dft8(c32 (&u)[8]) {
    c32 e0 = u[0], e1 = u[2], e2 = u[4], e3 = u[6], o0 = u[1], o1 = u[3], o2 = u[5], o3 = u[7];
    dft4<SG>(e0, e1, e2, e3); dft4<SG>(o0, o1, o2, o3);
    const float r = 0.70710678118654752440f;
    o1 = addi<SG>(o1, o1); o3 = addi<-SG>(o3, o3);
    u[0] = e0 + o0; u[4] = e0 - o0;
    u[1] = fma2(o1, dup(r), e1); u[5] = fma2(o1, dup(-r), e1);
    u[2] = addi<SG>(e2, o2); u[6] = addi<-SG>(e2, o2);
    u[3] = fma2(o3, dup(-r), e3); u[7] = fma2(o3, dup(r), e3);
}
template <int SG> __device__ __forceinline__ void dft16(c32 (&u)[16]) {
    const float c1 = 0.92387953251128675613f, s1 = 0.38268343236508977173f, r = 0.70710678118654752440f;
#pragma unroll
    for (int i = 0; i < 4; ++i) dft4<SG>(u[i], u[i + 4], u[i + 8], u[i + 12]);
    u[5] = cmulk(u[5], c1, SG * s1); u[9] = cmulk(u[9], r, SG * r); u[13] = cmulk(u[13], s1, SG * c1);
    u[6] = cmulk(u[6], r, SG * r); u[14] = cmulk(u[14], -r, SG * r);
    u[7] = cmulk(u[7], s1, SG * c1); u[11] = cmulk(u[11], -r, SG * r); u[15] = cmulk(u[15], -c1, -SG * s1);
    dft4<SG>(u[0], u[1], u[2], u[3]); dft4<SG>(u[4], u[5], u[6], u[7]); dft4<SG, true>(u[8], u[9], u[10], u[11]); dft4<SG>(u[12], u[13], u[14], u[15]);
    c32 o[16];
#pragma unroll
    for (int i = 0; i < 16; ++i) o[(i >> 2) + 4 * (i & 3)] = u[i];
#pragma unroll
    for (int i = 0; i < 16; ++i) u[i] = o[i];
}
template <int SG> __device__ __forceinline__ void dft5(c32 (&x)[5]) {
    const float c1 = 0.30901699437494742410f, c2 = -0.80901699437494742410f, s1 = 0.95105651629515357212f, s2 = 0.58778525229247312917f;
    const c32 t1 = x[1] + x[4], t2 = x[2] + x[3], t3 = x[1] - x[4], t4 = x[2] - x[3];
    const c32 a1 = fma2(t2, dup(c2), fma2(t1, dup(c1), x[0])), a2 = fma2(t2, dup(c1), fma2(t1, dup(c2), x[0]));
    const c32 b1 = fma2(t4, dup(s2), t3 * dup(s1)), b2 = fma2(t4, dup(-s1), t3 * dup(s2));
    x[0] = x[0] + (t1 + t2);
    x[1] = addi<SG>(a1, b1); x[4] = addi<-SG>(a1, b1); x[2] = addi<SG>(a2, b2); x[3] = addi<-SG>(a2, b2);
}
template <int SG> __device__ __forceinline__ void dft10(c32 (&u)[10]) {
    c32 e[5], o[5];
#pragma unroll
    for (int i = 0; i < 5; ++i) { e[i] = u[2 * i] + u[(2 * i + 5) % 10]; o[i] = u[2 * i] - u[(2 * i + 5) % 10]; }
    dft5<SG>(e); dft5<SG>(o);
#pragma unroll
    for (int i = 0; i < 5; ++i) { u[6 * i % 10] = e[i]; u[(6 * i + 5) % 10] = o[i]; }
}
template <int ST> __device__ __forceinline__ void lds16(c32 (&u)[16], const char* p) {
    const LAS char* q = (const LAS char*)p;
#pragma unroll
    for (int i = 0; i < 16; ++i) asm volatile("ds_read_b64 %0, %1 offset:%2" : "=v"(u[(i >> 2) + 4 * (i & 3)]) : "v"(q), "n"(((i >> 2) + 4 * (i & 3)) * ST) : "memory");
#pragma unroll
    for (int i = 0; i < 4; ++i) asm volatile("s_waitcnt lgkmcnt(%4)" : "+v"(u[i]), "+v"(u[i + 4]), "+v"(u[i + 8]), "+v"(u[i + 12]) : "n"(12 - 4 * i) : "memory");
}
__device__ __forceinline__ c32 unpack2(unsigned v) { return (c32){lo2f(v), hi2f(v)}; }
__device__ __forceinline__ float wsum2(c32 v) {
    float t = addswap32(v.x, v.y);
    auto q = __builtin_amdgcn_permlane16_swap(__float_as_uint(t), __float_as_uint(t), false, false);
    t = __uint_as_float(q[0]) + __uint_as_float(q[1]);
    t += dppf<0xB1>(t); t += dppf<0x4E>(t); t += dppf<0x141>(t); t += dppf<0x140>(t);
    return t;
}
__device__ __forceinline__ c32 halves(float t) { return (c32){__uint_as_float(__builtin_amdgcn_readlane(__float_as_uint(t), 0)), __uint_as_float(__builtin_amdgcn_readlane(__float_as_uint(t), 32))}; }
__device__ __forceinline__ void ln_rows(const c32 (&v)[10], unsigned (&o)[10]) {
    const float mean = wsum2(((v[0] + v[1]) + (v[2] + v[3])) + ((v[4] + v[5]) + (v[6] + v[7])) + (v[8] + v[9])) * (1.f / 640.f);
    const c32 m = halves(mean), mb = halves(bfr(mean));
    c32 q[2] = {dup(0.f), dup(0.f)};
#pragma unroll
    for (int r = 0; r < 10; ++r) { const c32 d = v[r] - m; q[r & 1] = fma2(d, d, q[r & 1]); }
    const float var = wsum2(q[0] + q[1]) * (1.f / 640.f);
    const c32 rs = halves(__builtin_amdgcn_rcpf(bfr(__builtin_amdgcn_sqrtf(bfr(bfr(var) + 1e-5f)))));
#pragma unroll
    for (int r = 0; r < 10; ++r) { const c32 d = v[r] - mb, y = unpack2(pack2(d.x, d.y)) * rs; o[r] = pack2(y.x, y.y); }
}

__device__ __forceinline__ __amdgpu_buffer_rsrc_t mkrsrc(const void* p, unsigned bytes) {
    return __builtin_amdgcn_make_buffer_rsrc((void*)p, 0, bytes, 0x00020000);
}

constexpr unsigned O_F = (unsigned)OFF_F;
constexpr int RW = 5920;
struct RowTw { c32 a[9], b, c; };
__device__ __forceinline__ RowTw row_tw(int lane) {
    RowTw t;
#pragma unroll
    for (int q = 1; q < 10; ++q) t.a[q - 1] = cis((float)(lane * q) * (-1.f / 640.f));
    t.b = cis((float)(lane & 31) * (-1.f / 64.f)); t.c = cis((float)(lane & 15) * (-1.f / 32.f));
    asm volatile("s_nop 0" : "+v"(t.a[0]), "+v"(t.a[1]), "+v"(t.a[2]), "+v"(t.a[3]), "+v"(t.a[4]), "+v"(t.a[5]), "+v"(t.a[6]), "+v"(t.a[7]), "+v"(t.a[8]), "+v"(t.b), "+v"(t.c));
    return t;
}
template <int B> __device__ __forceinline__ void cswap(c32& x, c32& y) {
#pragma unroll
    for (int e = 0; e < 2; ++e) {
        auto r = B == 32 ? __builtin_amdgcn_permlane32_swap(__float_as_uint(x[e]), __float_as_uint(y[e]), false, false) : __builtin_amdgcn_permlane16_swap(__float_as_uint(x[e]), __float_as_uint(y[e]), false, false);
        x[e] = __uint_as_float(r[0]); y[e] = __uint_as_float(r[1]);
    }
}
__device__ __forceinline__ void row_x(c32 (&v)[10], __amdgpu_buffer_rsrc_t rx, int row, int lane) {
#pragma unroll
    for (int r = 0; r < 10; ++r) v[r] = (c32){lo2f(__builtin_amdgcn_raw_buffer_load_b16(rx, lane * 2u + 128u * r, (unsigned)row * (D_ * 2), 16)), lo2f(__builtin_amdgcn_raw_buffer_load_b16(rx, lane * 2u + 128u * r + 1280u, (unsigned)row * (D_ * 2), 16))};
}
template <int NW, int NT, class FS> __device__ __forceinline__ void a1_unit(__amdgpu_buffer_rsrc_t rx, int row0, __amdgpu_buffer_rsrc_t rws, char* sm, const RowTw& tw, int w, int lane, FS st_) {
    if (w < NW) {
        c32 u[10];
        unsigned h[10];
        row_x(u, rx, row0 + 2 * w, lane);
        st_(-1);
        st_(0);
        ln_rows(u, h);
        st_(1);
#pragma unroll
        for (int r = 0; r < 10; ++r) u[r] = unpack2(h[r]);
        dft10<-1>(u);
        char* mine = sm + w * RW;
        char* ex = mine + ((lane >> 5) + 10 * ((lane >> 4) & 1)) * 144 + (lane & 15) * 8;
#pragma unroll
        for (int i = 0; i < 5; ++i) {
            c32 x = i ? ctw<-1>(u[2 * i], tw.a[2 * i - 1]) : u[0], y = ctw<-1>(u[2 * i + 1], tw.a[2 * i]);
            cswap<32>(x, y);
            c32 t = x - y; x = x + y; y = ctw<-1>(t, tw.b);
            cswap<16>(x, y);
            *(c32*)(ex + i * 288) = x + y; *(c32*)(ex + i * 288 + 2880) = ctw<-1>(x - y, tw.c);
        }
        asm volatile("" ::: "memory");
        st_(2);
        if (lane < 40) {
            c32 v[16];
#pragma unroll
            for (int m = 0; m < 8; ++m) { const f32x4 t = *(const f32x4*)(mine + lane * 144 + m * 16); v[2 * m] = t.xy; v[2 * m + 1] = t.zw; }
            dft16<-1>(v);
            asm volatile("" ::: "memory");
#pragma unroll
            for (int m = 0; m < 16; ++m) *(c32*)(mine + lane * 8 + m * 320) = v[m];
        }
        st_(3);
    }
    asm volatile("s_waitcnt lgkmcnt(0)\n\ts_barrier" ::: "memory");
    st_(4);
    const int tid = w * 64 + lane, k0 = tid / (4 * NW) * 4 + (tid & 3), id = tid % (4 * NW);
    const char* X = sm + (id >> 2) * RW;
#pragma unroll
    for (int c = 0; c * NT < 320 * NW; ++c) {
        const int k = k0 + c * (NT / NW);
        if ((c + 1) * NT <= 320 * NW || k < 320) {
            const c32 zk = *(const c32*)(X + k * 8), zn = *(const c32*)(X + (k == 0 ? 0 : 640 - k) * 8);
            const f32x4 o = {0.5f * (zk.x + zn.x), 0.5f * (zk.y - zn.y), 0.5f * (zk.y + zn.y), 0.5f * (zn.x - zk.x)};
            st128(__builtin_bit_cast(u32x4, o), rws, (unsigned)((k >> 2) * 65536 + id * 16), O_F + (unsigned)row0 * 32u, 16);
        }
    }
    if (k0 < 4) {
        const c32 z = *(const c32*)(X + 2560);
        st128(((u32x4){__float_as_uint(z.x), 0u, __float_as_uint(z.y), 0u}), rws, (unsigned)(80 * 65536 + id * 16), O_F + (unsigned)row0 * 32u, 16);
    }
}
template <int NW, class FS> __device__ __forceinline__ void a3_unit(__amdgpu_buffer_rsrc_t rws, __amdgpu_buffer_rsrc_t rx, int row0, char* sm, const RowTw& tw, int w, int lane, FS st_) {
    constexpr int NT = 512, NC = (320 * NW + NT - 1) / NT;
    const int tid = w * 64 + lane, k0 = tid / (4 * NW) * 4 + (tid & 3), id = tid % (4 * NW);
    u32x4 fv[NC], fz = {0u, 0u, 0u, 0u};
#pragma unroll
    for (int c = 0; c < NC; ++c) fv[c] = __builtin_amdgcn_raw_buffer_load_b128(rws, (unsigned)((min(k0 + c * (NT / NW), 319) >> 2) * 65536 + id * 16), O_F + (unsigned)row0 * 32u, 16);
    if (w == 0) fz = __builtin_amdgcn_raw_buffer_load_b128(rws, (unsigned)(80 * 65536 + id * 16), O_F + (unsigned)row0 * 32u, 16);
    c32 x[10];
    if (w < NW) row_x(x, rx, row0 + 2 * w, lane);
    char* X = sm + (id >> 2) * RW;
#pragma unroll
    for (int c = 0; c < NC; ++c) {
        const int k = k0 + c * (NT / NW);
        const f32x4 v = __builtin_bit_cast(f32x4, fv[c]);
        c32 zk = addi<1>(v.xy, v.zw), zn = (c32){v[0] + v[3], v[2] - v[1]};
        if (c == 0 && k == 0) { zk = (c32){v[0], v[2]}; zn = (c32){__uint_as_float(fz[0]), __uint_as_float(fz[2])}; }
        if ((c + 1) * NT <= 320 * NW || k < 320) { *(c32*)(X + k * 8) = zk; *(c32*)(X + (k == 0 ? 320 : 640 - k) * 8) = zn; }
    }
    asm volatile("s_waitcnt lgkmcnt(0)\n\ts_barrier" ::: "memory");
    st_(0);
    if (w < NW) {
        char* mine = sm + w * RW;
        if (lane < 40) {
            c32 v[16];
            lds16<320>(v, mine + lane * 8);
            dft16<1>(v);
            asm volatile("" ::: "memory");
#pragma unroll
            for (int m = 0; m < 8; ++m) *(f32x4*)(mine + lane * 144 + m * 16) = (f32x4){v[2 * m].x, v[2 * m].y, v[2 * m + 1].x, v[2 * m + 1].y};
        }
        asm volatile("" ::: "memory");
        st_(1);
        const char* ex = mine + ((lane >> 5) + 10 * ((lane >> 4) & 1)) * 144 + (lane & 15) * 8;
        c32 u[10];
#pragma unroll
        for (int i = 0; i < 5; ++i) { u[2 * i] = *(const c32*)(ex + i * 288); u[2 * i + 1] = *(const c32*)(ex + i * 288 + 2880); }
#pragma unroll
        for (int i = 0; i < 5; ++i) {
            c32 t = ctw<1>(u[2 * i + 1], tw.c), p = u[2 * i] + t, q = u[2 * i] - t;
            cswap<16>(p, q);
            t = ctw<1>(q, tw.b); q = p - t; p = p + t;
            cswap<32>(p, q);
            u[2 * i] = i ? ctw<1>(p, tw.a[2 * i - 1]) : p; u[2 * i + 1] = ctw<1>(q, tw.a[2 * i]);
        }
        dft10<1>(u);
        st_(2);
        asm volatile("" ::: "memory");
        char* st = mine + lane * 2;
        unsigned h[10];
#pragma unroll
        for (int r = 0; r < 10; ++r) {
            const c32 y = x[r] + unpack2(pack2(u[r].x, u[r].y));
            const unsigned p = pack2(y.x, y.y);
            u[r] = unpack2(p);
            *(u16*)(st + r * 128) = (u16)p; *(u16*)(st + 1280 + r * 128) = (u16)(p >> 16);
        }
        st_(3);
        ln_rows(u, h);
        st_(4);
#pragma unroll
        for (int r = 0; r < 10; ++r) { *(u16*)(st + 2560 + r * 128) = (u16)h[r]; *(u16*)(st + 3840 + r * 128) = (u16)(h[r] >> 16); }
        asm volatile("" ::: "memory");
        u32x4 o[6];
#pragma unroll
        for (int c = 0; c < 6; ++c) o[c] = *(const u32x4*)(mine + (c < 3 ? 2560 : 0) + min(c % 3 * 64 + lane, 159) * 16);
#pragma unroll
        for (int c = 0; c < 3; ++c) {
            const int i = min(c * 64 + lane, 159), rr = i >= 80, ch = i - 80 * rr, row = (row0 + 2 * w + rr) & 31;
            st128(o[c], rws, (unsigned)((ch >> 3) * 4096 + row * 128 + (((ch & 7) ^ (row & 7)) << 4)) | (c == 2 ? (lane & 32u) << 25 : 0u), (unsigned)OFF_H + (unsigned)(row0 >> 5) * 40960u, 16);
        }
#pragma unroll
        for (int c = 0; c < 3; ++c) st128(o[3 + c], rws, (unsigned)(c * 64 + lane) * 16u | (c == 2 ? (lane & 32u) << 25 : 0u), (unsigned)OFF_X1 + (unsigned)(row0 + 2 * w) * (D_ * 2), 16);
    }
}

__device__ __forceinline__ unsigned xcc_id() { unsigned x; asm volatile("s_getreg_b32 %0, hwreg(HW_REG_XCC_ID)" : "=s"(x)); return x & 7u; }
__device__ __forceinline__ unsigned probe(__amdgpu_buffer_rsrc_t rw, unsigned base, unsigned* best, int w, int lane) {
    if (w == 0 && lane < 4) best[lane] = 0u;
    unsigned mn[4] = {~0u, ~0u, ~0u, ~0u};
    for (int r = 0; r < NPROBE; ++r) {
#pragma unroll
        for (int i = 0; i < 4; ++i) __builtin_amdgcn_raw_buffer_store_b32(0u, rw, i * HALF, base, 16);
        asm volatile("s_waitcnt vmcnt(0) lgkmcnt(0)\n\ts_barrier" ::: "memory");
#pragma unroll
        for (int i = 0; i < 4; ++i) {
            const unsigned long long t1 = __builtin_amdgcn_s_memtime();
            asm volatile("s_waitcnt lgkmcnt(0)" ::: "memory");
            unsigned v = __builtin_amdgcn_raw_buffer_load_b32(rw, i * HALF, base, 16);
            asm volatile("s_waitcnt vmcnt(0)" : "+v"(v) :: "memory");
            const unsigned long long t2 = __builtin_amdgcn_s_memtime();
            mn[i] = min(mn[i], (unsigned)(t2 - t1) + v);
        }
    }
    if (lane == 0) {
#pragma unroll
        for (int i = 0; i < 4; ++i) atomicAdd(&best[i], min(mn[i], 1000u));
    }
    __syncthreads();
    return min(best[1], best[3]) < min(best[0], best[2]) ? (best[3] < best[1] ? 3u : 1u) : (best[2] < best[0] ? 2u : 0u);
}
__device__ __forceinline__ void filter_unit(const u16* wr, const u16* wi, int s0, __amdgpu_buffer_rsrc_t rws, u16* tf, int tid) {
    u32x4 v[3];
#pragma unroll
    for (int i = 0; i < 3; ++i) {
        const int id = min(tid + 512 * i, 1283);
        v[i] = ((const u32x4*)((id < 642 ? wr : wi) + (size_t)s0 * DF))[id < 642 ? id : id - 642];
    }
#pragma unroll
    for (int i = 0; i < 3; ++i) ((u32x4*)tf)[min(tid + 512 * i, 1283)] = v[i];
    __syncthreads();
#pragma unroll
    for (int c = 0; c < 3; ++c) {
        const int i = min(tid + 512 * c, 1283), k = i >> 2, sq = (i & 3) * 4;
        u32x4 o;
#pragma unroll
        for (int j = 0; j < 4; ++j) o[j] = (unsigned)tf[(sq + j) * DF + k] | ((unsigned)tf[16 * DF + (sq + j) * DF + k] << 16);
        if (c < 2 || tid < 1284 - 1024) st128(o, rws, (unsigned)(k * S_ + sq) * 4u, (unsigned)OFF_WFT + (unsigned)s0 * 4u, 16);
    }
}

constexpr int A2W = 8720;
template <class FW, class FS> __device__ __forceinline__ void a2_unit(__amdgpu_buffer_rsrc_t rws, int G, char* sm, int w, int lane, FW rdy, FS st_) {
    const int c = lane & 3, j = lane >> 2, p = 16 * w + j;
    const unsigned fo = O_F + (unsigned)(G * 65536 + w * 1024);
    u32x4 x[8];
    unsigned wv[16];
#pragma unroll
    for (int r = 0; r < 16; ++r) wv[r] = __builtin_amdgcn_raw_buffer_load_b32(rws, (unsigned)(j * 32 + c * 4), (unsigned)OFF_WFT + (unsigned)(min(4 * G + (w >> 1), 320) * 8192 + (w & 1) * 16 + r * 512), 16);
    c32 t1[2][7], t2[15], wf[16];
#pragma unroll
    for (int q = 1; q < 8; ++q) {
        t1[0][q - 1] = cis((float)(2 * p * q) * (-1.f / 2048.f)); t1[1][q - 1] = cis((float)((2 * p + 1) * q) * (-1.f / 2048.f));
        asm volatile("" : "+v"(t1[0][q - 1]), "+v"(t1[1][q - 1]));
    }
#pragma unroll
    for (int q = 1; q < 16; ++q) { t2[q - 1] = cis((float)(j * q) * (-1.f / 256.f)); asm volatile("" : "+v"(t2[q - 1])); }
#pragma unroll
    for (int m = 0; m < 16; ++m) { wf[m] = unpack2(wv[m]) * dup(1.f / ((float)S_ * (float)D_)); asm volatile("" : "+v"(wf[m])); }
    rdy(6);
#pragma unroll
    for (int r = 0; r < 7; ++r) x[r] = __builtin_amdgcn_raw_buffer_load_b128(rws, lane * 16u, fo + r * 8192u, 16);
    rdy(7);
    x[7] = __builtin_amdgcn_raw_buffer_load_b128(rws, lane * 16u, fo + 7 * 8192u, 16);
    c32 ua[8], ub[8], u[16];
#pragma unroll
    for (int r = 0; r < 8; ++r) { const f32x4 v = __builtin_bit_cast(f32x4, x[r]); ua[r] = v.xy; ub[r] = v.zw; }
    st_(0);
    dft8<-1>(ua); dft8<-1>(ub);
    char* e1 = sm + 2 * c * A2W + p * 16;
    char* mine = sm + w * A2W;
#pragma unroll
    for (int q = 0; q < 8; ++q) {
        if (q) { ua[q] = ctw<-1>(ua[q], t1[0][q - 1]); ub[q] = ctw<-1>(ub[q], t1[1][q - 1]); }
        *(f32x4*)(e1 + (q >> 2) * A2W + (q & 3) * 2112) = (f32x4){ua[q].x, ua[q].y, ub[q].x, ub[q].y};
    }
    st_(1);
    asm volatile("s_waitcnt lgkmcnt(0)\n\ts_barrier" ::: "memory");
    st_(2);
    lds16<128>(u, mine + c * 2112 + j * 8);
    dft16<-1>(u);
    auto turn = [&](auto sg) {
        asm volatile("" ::: "memory");
#pragma unroll
        for (int m = 0; m < 16; ++m) *(c32*)(mine + m * 544 + lane * 8) = m ? ctw<decltype(sg)::v>(u[m], t2[m - 1]) : u[m];
        lds16<32>(u, mine + j * 544 + c * 8);
    };
    turn(IC<-1>());
    st_(3);
    dft16<-1>(u);
#pragma unroll
    for (int m = 0; m < 16; ++m) u[m] = ctw<-1>(u[m], wf[m]);
    dft16<1>(u);
    turn(IC<1>());
    st_(4);
    dft16<1>(u);
#pragma unroll
    for (int m = 0; m < 16; ++m) *(c32*)(mine + (w >> 1) * 32 + c * 2080 + j * 8 + m * 128) = u[m];
    st_(5);
    asm volatile("s_waitcnt lgkmcnt(0)\n\ts_barrier" ::: "memory");
    st_(6);
#pragma unroll
    for (int q = 0; q < 8; ++q) {
        const f32x4 v = *(const f32x4*)(e1 + c * 32 + (q >> 2) * A2W + (q & 3) * 2080);
        ua[q] = v.xy; ub[q] = v.zw;
        if (q) { ua[q] = ctw<1>(ua[q], t1[0][q - 1]); ub[q] = ctw<1>(ub[q], t1[1][q - 1]); }
    }
    dft8<1>(ua); dft8<1>(ub);
#pragma unroll
    for (int r = 0; r < 8; ++r) st128(__builtin_bit_cast(u32x4, ((f32x4){ua[r].x, ua[r].y, ub[r].x, ub[r].y})), rws, lane * 16u, fo + r * 8192u, 16);
}

struct RecArgs {
    unsigned* ctl; char* win; const u16* mem0; const u16* xin; u16* out;
    const u16* W[6];
};
template <int I> __device__ __forceinline__ char* karg(int i = 0) {
    unsigned long long p;
    asm volatile("s_load_dwordx2 %0, %1, %2\n\ts_waitcnt lgkmcnt(0)" : "=s"(p) : "s"(__builtin_amdgcn_kernarg_segment_ptr()), "s"(I * 8 + i * 8) : "memory");
    return (char*)p;
}
constexpr unsigned O_H = (unsigned)OFF_H, O_X1 = (unsigned)OFF_X1, O_Z = (unsigned)OFF_Z, O_Z2 = (unsigned)OFF_Z2, O_PT = (unsigned)OFF_PT, O_QK = (unsigned)OFF_QK;
#define MXV 0x00808080808080AAull
#define MXQ 0x0008080808080000ull
#define NTAIL 5
static_assert(NTAIL >= 3, "the owners release segments up to NSEG - 4");
constexpr int TAILS = NSEG - NTAIL;
static_assert(!((MXV | MXQ) >> TAILS) && !(MXV & 1) && !(MXQ & 3), "segment sets");
constexpr int NREC = 96 + NOWN, NQT = NREC + NOWN, NXT = NQT + 32, NWRK = NXT + 32;
constexpr unsigned SPIN_CAP = 400000u;
__device__ __forceinline__ bool spin_fail(unsigned& spins, unsigned* tmo) {
    if (++spins > SPIN_CAP) { __hip_atomic_store(tmo, 1u, __ATOMIC_RELAXED, __HIP_MEMORY_SCOPE_AGENT); return true; }
    return (spins & 1023u) == 0u && __builtin_amdgcn_readfirstlane(__hip_atomic_load(tmo, __ATOMIC_RELAXED, __HIP_MEMORY_SCOPE_AGENT));
}
#define SST(k) do {} while (0)
#define LSEL 1
#define RT(k) do {} while (0)
#define RTS() do {} while (0)
#define B3ATTR
#define MTA(k)
#define MTRT() do {} while (0)
#define MT(k) do {} while (0)
#define MTS() do {} while (0)
#define FC(x) do {} while (0)
#define DQC 1200
#define DVC 1200
#define M1 44
#define MZ 40
#define MZP 20
#define M2 234
#define LTP (LT0 + 15)
#define LTZ 340
#define PEN 280
#define KSL 1
#define SVO 64
#define DZP 55
#define LT0 342
#define SAD 1
#define PEN2 80
#define PEN1 50
#define PEN0 150
#define DDM2 0
#define DRA 700
#define TSLP 1
#define PFS 0
#define PFOFF 1
#define OPR 2
#define BSPARSE 0
#define ZLAG 4
#define TKP 0
#define DPS 12
#define RZ 63
#define RQ 63
#define R2 63
#define DXT 3000
#define SSW 99
#define LTM 12
#define XS0 10
#define XSU 8
#define XSM 32
#define LTU 4
#define LTD 1
#define QUIET 0
#define QUAD 1
#define LTS 3
#define XSS 3
#define TKS 3
#define LT1 450
#define FDL 2
#define ZLA 3
#define DPT2 3
#define NORND 0
#define LTX 24
#define TKE 200
#define DVO 0
#define ZPS 4
#define ASLP 4
#define HLA 8
#define PSLP 16
#define PFD 4
#define NPF 3
#define KSL2 1
#define KBQ 4
#define VB1 6
#define VB2 5
#define KR1 3
#define QB2 2
#define NB2 6
#define DZR 24
#define ML 150
#define MN 12
#define CF 190
#define DB 1500
#define DP 200
#define ZSL 1
#define NB1 3
#define DPT 3
#define DDMA 8
#define DZ2 800
#define QEARLY 2
#define OSLP 32
#define OSLP2 1
#define WSLP 64
#define DO0 12000
#define DW0 1200
#define DQT 9000
#define DQ4 700
#define ZEARLY 3
#define HSLP 4
#define RMAP 0
#define DIE 0
#define VMWAIT() do { asm volatile("" ::: "memory"); __builtin_amdgcn_s_waitcnt(0x0F70); asm volatile("" ::: "memory"); } while (0)
#define SB() __builtin_amdgcn_sched_barrier(0)
#define BAR() asm volatile("s_waitcnt lgkmcnt(0)\n\ts_barrier" ::: "memory")
#define PIN1(a) asm volatile("" : "+v"(a) :: "memory")
#define PIN2(a, b) asm volatile("" : "+v"(a), "+v"(b) :: "memory")
#define BAR0() asm volatile("s_barrier" ::: "memory")
__device__ __forceinline__ unsigned fresh_lane() { unsigned l; asm volatile("v_mbcnt_lo_u32_b32 %0, -1, 0\n\tv_mbcnt_hi_u32_b32 %0, -1, %0" : "=v"(l)); return l; }
__device__ __forceinline__ void wait_until(unsigned long long t) { while ((long long)(__builtin_amdgcn_s_memtime() - t) < 0) __builtin_amdgcn_s_sleep(1); }
struct Clk { unsigned ta, ma, cpq, qpc; };
__device__ __forceinline__ int sx24(unsigned v) { return (int)(v << 8) >> 8; }
#define WAIT_FINE \
        "s_memrealtime vcc\n\t" \
        "s_memtime s[100:101]\n\t" \
        "s_brev_b32 %[fi], -2\n\t" \
        "s_waitcnt lgkmcnt(0)\n\t" \
        "s_mov_b32 %[rp], vcc_lo\n\t" \
        "s_lshl_b32 vcc_hi, vcc_lo, 2\n\t" \
        "s_sub_u32 %[x], %[t], vcc_hi\n\t" \
        "s_bfe_i32 %[x], %[x], 0x180000\n\t" \
        "s_min_i32 %[x], %[x], 4000\n\t" \
        "s_max_i32 %[x], %[x], -4000\n\t" \
        "s_add_u32 %[t], vcc_hi, %[x]\n\t" \
        "s_mul_i32 %[x], %[x], %[cq]\n\t" \
        "s_ashr_i32 %[x], %[x], 8\n\t" \
        "s_sub_u32 %[tm], %[t], %[ta]\n\t" \
        "s_bfe_i32 %[tm], %[tm], 0x180000\n\t" \
        "s_min_i32 %[tm], %[tm], 1000000\n\t" \
        "s_max_i32 %[tm], %[tm], -1000000\n\t" \
        "s_mul_i32 %[tm], %[tm], %[cq]\n\t" \
        "s_ashr_i32 %[tm], %[tm], 8\n\t" \
        "s_add_u32 %[tm], %[tm], %[ma]\n\t" \
        "s_sub_u32 %[tm], %[tm], s100\n\t" \
        "s_lshr_b32 vcc_hi, %[cq], 4\n\t" \
        "s_sub_u32 vcc_hi, %[x], vcc_hi\n\t" \
        "s_max_i32 %[tm], %[tm], vcc_hi\n\t" \
        "s_min_i32 %[tm], %[tm], %[x]\n\t" \
        "s_add_u32 %[tm], %[tm], s100\n" \
        "1:\n\t" \
        "s_memrealtime vcc\n\t" \
        "s_memtime s[100:101]\n\t" \
        "s_waitcnt lgkmcnt(0)\n\t" \
        "s_cmp_eq_u32 vcc_lo, %[rp]\n\t" \
        "s_cbranch_scc1 2f\n\t" \
        "s_mov_b32 %[rp], vcc_lo\n\t" \
        "s_lshl_b32 %[x], vcc_lo, 2\n\t" \
        "s_sub_u32 %[x], %[t], %[x]\n\t" \
        "s_bfe_i32 %[x], %[x], 0x180000\n\t" \
        "s_mul_i32 %[x], %[x], %[cq]\n\t" \
        "s_ashr_i32 %[x], %[x], 8\n\t" \
        "s_add_u32 %[x], %[x], s100\n\t" \
        "s_sub_u32 s101, %[x], %[tm]\n\t" \
        "s_cmp_lt_i32 s101, %[fi]\n\t" \
        "s_cselect_b32 %[tm], %[x], %[tm]\n\t" \
        "s_mov_b32 %[fi], 0\n" \
        "2:\n\t" \
        "s_sub_u32 %[x], %[tm], s100\n\t" \
        "s_cmp_gt_i32 %[x], 260\n\t" \
        "s_cbranch_scc1 1b\n\t" \
        "s_cmp_eq_u32 %[fi], 0\n\t" \
        "s_cselect_b32 %[ta], %[t], %[ta]\n\t" \
        "s_cselect_b32 %[ma], %[tm], %[ma]\n" \
        "3:\n\t" \
        "s_memtime s[100:101]\n\t" \
        "s_waitcnt lgkmcnt(0)\n\t" \
        "s_sub_u32 %[x], %[tm], s100\n\t" \
        "s_cmp_gt_i32 %[x], 92\n\t" \
        "s_cbranch_scc1 3b\n\t" \
        "s_max_i32 %[x], %[x], 0\n\t" \
        "s_andn2_b32 %[x], %[x], 3\n\t" \
        "s_sub_u32 %[x], 104, %[x]\n\t" \
        "s_getpc_b64 vcc\n\t" \
        "s_add_u32 vcc_lo, vcc_lo, %[x]\n\t" \
        "s_addc_u32 vcc_hi, vcc_hi, 0\n\t" \
        "s_setpc_b64 vcc\n\t" \
        ".rept 23\n\t" \
        "s_nop 0\n\t" \
        ".endr\n\t"
#define WAIT_FINE_OUT [tm] "=&s"(tm), [rp] "=&s"(rp), [x] "=&s"(x), [fi] "=&s"(fi), [t] "+s"(t), [ta] "+s"(c.ta), [ma] "+s"(c.ma)
__device__ __forceinline__ void wait_fine(unsigned t, Clk& c) {
    unsigned tm, rp, x, fi;
    asm volatile(WAIT_FINE : WAIT_FINE_OUT : [cq] "s"(c.cpq) : "vcc", "scc", "s100", "s101", "memory");
}
__device__ __forceinline__ void wait_fine_bar(unsigned t, Clk& c) {
    unsigned tm, rp, x, fi;
    asm volatile(WAIT_FINE "s_barrier" : WAIT_FINE_OUT : [cq] "s"(c.cpq) : "vcc", "scc", "s100", "s101", "memory");
}
__device__ __forceinline__ void wait_cyc_bar(unsigned tm) {
    unsigned x;
    asm volatile(
        "3:\n\t"
        "s_memtime s[100:101]\n\t"
        "s_waitcnt lgkmcnt(0)\n\t"
        "s_sub_u32 %0, %1, s100\n\t"
        "s_cmp_gt_i32 %0, 92\n\t"
        "s_cbranch_scc1 3b\n\t"
        "s_max_i32 %0, %0, 0\n\t"
        "s_andn2_b32 %0, %0, 3\n\t"
        "s_sub_u32 %0, 104, %0\n\t"
        "s_getpc_b64 vcc\n\t"
        "s_add_u32 vcc_lo, vcc_lo, %0\n\t"
        "s_addc_u32 vcc_hi, vcc_hi, 0\n\t"
        "s_setpc_b64 vcc\n\t"
        ".rept 23\n\t"
        "s_nop 0\n\t"
        ".endr\n\t"
        "s_barrier"
        : "=&s"(x) : "s"(tm) : "vcc", "scc", "s100", "s101", "memory");
}
__device__ __forceinline__ unsigned cyc_at(const Clk& c, int d) {
    d = min(d, 20000);
    asm volatile("" : "+s"(d));
    return c.ma + (unsigned)((max(d, -4000) * (int)c.cpq) >> 8);
}
__device__ __forceinline__ void clk_rate(Clk& c, unsigned ta0, unsigned ma0) {
    const int dq = sx24(c.ta - ta0);
    if (dq > 200) {
        const float r = fminf(fmaxf((float)(c.ma - ma0) * __builtin_amdgcn_rcpf((float)dq), 1.f), 8.f);
        asm volatile("s_nop 1\n\tv_readfirstlane_b32 %0, %2\n\tv_readfirstlane_b32 %1, %3\n\ts_nop 3" : "=s"(c.cpq), "=s"(c.qpc)
                     : "v"((unsigned)(256.f * r + 0.5f)), "v"((unsigned)(65536.f * __builtin_amdgcn_rcpf(r))));
    }
}
__device__ __forceinline__ void clk_init(Clk& c) {
    c.ta = 4u * (unsigned)__builtin_amdgcn_s_memrealtime(); c.ma = (unsigned)__builtin_amdgcn_s_memtime();
    for (int i = 0; i < 2; ++i) {
        const unsigned ta0 = c.ta, ma0 = c.ma;
        wait_fine(c.ta + (i ? 360u : 60u), c);
        clk_rate(c, ta0, ma0);
    }
}
__device__ __forceinline__ int read_sched(const RecArgs& a, __amdgpu_buffer_rsrc_t rwin, int s, unsigned tg, unsigned vo, unsigned to, unsigned f, unsigned g0, int& lt, int& xs, unsigned& fc) {
    int d = (int)((f - g0) & 0xFFFFFFu);
    if (__builtin_expect(__any((f >> 24) != tg) || s == 0, 0)) {
        for (unsigned spins = 0; __any((f >> 24) != tg);) {
            FC(fc);
            if (spin_fail(spins, a.ctl)) break;
            __builtin_amdgcn_s_sleep(TSLP);
            asm volatile("" ::: "memory");
            if ((f >> 24) != tg) f = __builtin_amdgcn_raw_buffer_load_b32(rwin, vo, to, 16);
        }
        const unsigned f0 = __builtin_amdgcn_readfirstlane(f) - 0x100000u;
        d = (int)((f - f0) & 0xFFFFFFu) + (int)((f0 - g0) & 0xFFFFFFu);
    }
    d = max(d, __builtin_amdgcn_update_dpp(0, d, 0xB1, 0xF, 0xF, true));
    d = max(d, __builtin_amdgcn_update_dpp(0, d, 0x4E, 0xF, 0xF, true));
    d = max(d, __builtin_amdgcn_update_dpp(0, d, 0x141, 0xF, 0xF, true));
    d = max(d, __builtin_amdgcn_update_dpp(0, d, 0x140, 0xF, 0xF, true));
    d = max(d, __builtin_amdgcn_update_dpp(0, d, 0x142, 0xA, 0xF, true));
    d = max(__builtin_amdgcn_readlane(d, 16), __builtin_amdgcn_readlane(d, 48));
    const int l0 = lt, x0 = xs;
    d -= 0x400000;
    const bool miss = d > l0 + min(x0 + LTM, LTX);
    if (s >= SAD) { lt = miss ? l0 : min(max(d, l0 - 1 - ((l0 - d) >> LTS)), l0 + LTU); xs = miss ? min(x0 + XSU, XSM) : max(x0 - 1 - (x0 >> XSS), 0); }
    if (s == 1) lt = LT0;
    return 0x400000 + (s < SAD ? d + PEN0 : miss ? d + PEN : l0 + x0) + M1 + M2 + x0;
}
__device__ __forceinline__ unsigned nosent1(unsigned v) { return min(v, 0xFFFFFFFEu); }
__device__ __forceinline__ unsigned smax(u32x4 v) { return max(v[1], v[3]); }
constexpr int QS = 272;
constexpr int TQS = 1152;
constexpr int L_Q = 10240, L_K = L_Q + 2 * TQS, L_S = L_K + 2 * TQS, L_TOK = L_S + 256, L_H = L_TOK + 49152, L_END = L_H + 2 * 40960;
typedef __attribute__((ext_vector_type(4))) short v4i16_t;
__device__ __forceinline__ u32x2 ldtr(const char* p) { return __builtin_bit_cast(u32x2, __builtin_amdgcn_ds_read_tr16_b64_v4i16((LAS v4i16_t*)p)); }
__device__ __forceinline__ u32x2 ldtr1(const LAS char* p) { return __builtin_bit_cast(u32x2, __builtin_amdgcn_ds_read_tr16_b64_v4i16((LAS v4i16_t*)p)); }
__device__ __forceinline__ u32x4 ldtr2(const LAS char* p0, const LAS char* p1) {
    const u32x2 x = __builtin_bit_cast(u32x2, __builtin_amdgcn_ds_read_tr16_b64_v4i16((LAS v4i16_t*)p0)), y = __builtin_bit_cast(u32x2, __builtin_amdgcn_ds_read_tr16_b64_v4i16((LAS v4i16_t*)p1));
    return (u32x4){x[0], x[1], y[0], y[1]};
}
__device__ __forceinline__ u32x4 ldtr2(const char* p0, const char* p1) { const u32x2 x = ldtr(p0), y = ldtr(p1); return (u32x4){x[0], x[1], y[0], y[1]}; }

__device__ __forceinline__ unsigned poll_lim(int& budget) {
    --budget;
    unsigned lim = budget > 0 ? 0xFFFFFFFEu : 0xFFFFFFFFu;
    asm volatile("" : "+s"(lim));
    return lim;
}
#define GATHER \
        "buffer_load_dwordx4 %[vo], %[rw], %[s0] offen sc1 lds\n\t" \
        "buffer_load_dwordx4 %[vo], %[rw], %[s0] offen offset:1024 sc1 lds\n\t" \
        "buffer_load_dwordx4 %[vo], %[rw], %[s0] offen offset:2048 sc1 lds\n\t" \
        "buffer_load_dwordx4 %[vo], %[rw], %[s0] offen offset:3072 sc1 lds\n\t" \
        "s_movk_i32 m0, 0x1000\n\t" \
        "s_nop 0\n\t" \
        "buffer_load_dwordx4 %[vo], %[rw], %[s4] offen sc1 lds\n\t" \
        "buffer_load_dwordx4 %[vo], %[rw], %[s4] offen offset:1024 sc1 lds\n\t" \
        "buffer_load_dwordx4 %[vo], %[rw], %[s4] offen offset:2048 sc1 lds\n\t" \
        "buffer_load_dwordx4 %[vo], %[rw], %[s4] offen offset:3072 sc1 lds\n\t" \
        "s_movk_i32 m0, 0x2000\n\t" \
        "s_nop 0\n\t" \
        "buffer_load_dwordx4 %[vo], %[rw], %[s1] offen sc1 lds\n\t" \
        "buffer_load_dwordx4 %[vo], %[rw], %[s1] offen offset:1024 sc1 lds"
#define GATHER_IN [vo] "v"(vo), [rw] "s"(rwin), [s0] "s"(so0), [s4] "s"(so0 + 4096u), [s1] "s"(so1)
__device__ __forceinline__ void gather_dma(__amdgpu_buffer_rsrc_t rwin, unsigned vo, unsigned so0, unsigned so1) {
    asm volatile("s_mov_b32 m0, 0\n\ts_nop 0\n\t" GATHER :: GATHER_IN : "m0", "memory");
}
__device__ __forceinline__ void gather_at(unsigned t, Clk& c, __amdgpu_buffer_rsrc_t rwin, unsigned vo, unsigned so0, unsigned so1) {
    unsigned tm, rp, x, fi;
    asm volatile("s_mov_b32 m0, 0\n\t" WAIT_FINE GATHER : WAIT_FINE_OUT : [cq] "s"(c.cpq), GATHER_IN : "vcc", "scc", "s100", "s101", "m0", "memory");
}
__device__ __forceinline__ void dma4k(__amdgpu_buffer_rsrc_t r, unsigned vo, unsigned so, unsigned lds) {
    asm volatile(
        "s_mov_b32 m0, %3\n\t"
        "s_nop 0\n\t"
        "buffer_load_dwordx4 %0, %1, %2 offen sc1 lds\n\t"
        "buffer_load_dwordx4 %0, %1, %2 offen offset:1024 sc1 lds\n\t"
        "buffer_load_dwordx4 %0, %1, %2 offen offset:2048 sc1 lds\n\t"
        "buffer_load_dwordx4 %0, %1, %2 offen offset:3072 sc1 lds"
        :: "v"(vo), "s"(r), "s"(so), "s"(lds) : "m0", "memory");
}
__device__ __forceinline__ int bs2(int c) { return (c & ~3) | ((c & 1) << 1) | ((c >> 1) & 1); }
__device__ __forceinline__ void gather_mem0(char* memL, int t0, int nt) {
    const u16* mem0 = (const u16*)karg<2>();
    for (int c = t0; c < 640; c += nt) {
        const unsigned* src = (const unsigned*)(mem0 + ((c & 6) + ((c >> 3) & 1)) * D_ + (c >> 4) * 16 + (c & 1) * 2);
        *(u32x4*)(memL + c * 16) = (u32x4){src[0], src[2], src[4], src[6]};
    }
}
__device__ __forceinline__ f32x2 hadd(f32x4 c) { return (f32x2){c[0], c[1]} + (f32x2){c[2], c[3]}; }
__device__ __forceinline__ unsigned hpack(f32x4 c) { const f32x2 y = hadd(c); return pack2(y[0], y[1]); }
__device__ __forceinline__ u32x8 cat8(u32x4 lo, u32x4 hi) { return (u32x8){lo[0], lo[1], lo[2], lo[3], hi[0], hi[1], hi[2], hi[3]}; }
__device__ __forceinline__ u32x4 half8(const u32x8& v, int u) { return u ? (u32x4){v[4], v[5], v[6], v[7]} : (u32x4){v[0], v[1], v[2], v[3]}; }
__device__ __forceinline__ void dma1k(__amdgpu_buffer_rsrc_t r, unsigned vo, unsigned so, unsigned lds) {
    asm volatile("s_mov_b32 m0, %3\n\ts_nop 0\n\tbuffer_load_dwordx4 %0, %1, %2 offen sc1 lds" :: "v"(vo), "s"(r), "s"(so), "s"(lds) : "m0", "memory");
}
__device__ __forceinline__ void stage_h(__amdgpu_buffer_rsrc_t rws, int seg, unsigned lds, int t) {
    const unsigned wo = __builtin_amdgcn_readfirstlane(t >> 6) * 1024;
#pragma unroll
    for (int i = 0; i < 5; ++i) dma1k(rws, (t & 63) * 16u, O_H + (unsigned)seg * 40960u + wo + i * 8192u, lds + wo + i * 8192u);
}
__device__ __forceinline__ void wait_h(const RecArgs& a, __amdgpu_buffer_rsrc_t rfl, unsigned fb, int seg, int& hrdy) {
    unsigned lim = 63u;
    if (__builtin_expect(hrdy <= seg, 0)) for (unsigned spins = 0;;) {
        asm volatile("" ::: "memory");
        const u32x4 f = __builtin_amdgcn_raw_buffer_load_b128(rfl, min(fresh_lane(), lim) * 128u, W_H + fb, 16);
        const unsigned long long miss = __ballot((f[0] | f[1] | f[2] | f[3]) != 0u);
        hrdy = miss ? __builtin_ctzll(miss) : (int)lim + 1;
        if (hrdy > seg) break;
        if (spin_fail(spins, a.ctl)) break;
        lim = (unsigned)min(seg + HLA, 63);
        __builtin_amdgcn_s_sleep(HSLP);
    }
}
template <int CTRL> __device__ __forceinline__ unsigned dppu(unsigned v) { return (unsigned)__builtin_amdgcn_mov_dpp((int)v, CTRL, 0xF, 0xF, true); }
template <int CTRL> __device__ __forceinline__ u32x4 ror4(u32x4 v) { return (u32x4){dppu<CTRL>(v[0]), dppu<CTRL>(v[1]), dppu<CTRL>(v[2]), dppu<CTRL>(v[3])}; }
__device__ __forceinline__ float xmax16(float v) {
    auto q = __builtin_amdgcn_permlane16_swap(__float_as_uint(v), __float_as_uint(v), false, false);
    return fmx(__uint_as_float(q[0]), __uint_as_float(q[1]));
}
__device__ __forceinline__ float xsum16(float v) {
    auto q = __builtin_amdgcn_permlane16_swap(__float_as_uint(v), __float_as_uint(v), false, false);
    return __uint_as_float(q[0]) + __uint_as_float(q[1]);
}
__device__ __forceinline__ float addswap16(float a, float b) {
    auto r = __builtin_amdgcn_permlane16_swap(__float_as_uint(a), __float_as_uint(b), false, false);
    return __uint_as_float(r[0]) + __uint_as_float(r[1]);
}
#define LANES() \
    const int lane = (int)fresh_lane(), tid = w * 64 + lane, fr = lane & 15, fq = lane >> 4; \
    const int sidx = (lane & 2) ? 0xEEEE : 0x4444; \
    const int ma0 = fq * 256 + (fr & 1) * 128 + (fr >> 1) * 16; \
    const int prow = 16 * ((fr >> 2) & 1) + 4 * (fr >> 3) + (fr & 3), hoff = fr * 128; \
    (void)tid; (void)sidx; (void)ma0; (void)prow; (void)hoff

__device__ __forceinline__ int seg_after(unsigned long long m, int j) { m = j >= 63 ? 0ull : m >> (j + 1); return m ? j + 1 + __builtin_ctzll(m) : NSEG; }
__device__ __forceinline__ int seg_done(unsigned long long m, int f0, int f1) { return min(seg_after(~m, f0), seg_after(m, f1)) - 1; }
template <int OFF> __device__ __forceinline__ void ldfrag(u32x4& d, const LAS char* p) { asm volatile("ds_read_b128 %0, %1 offset:%2" : "=v"(d) : "v"(p), "n"(OFF) : "memory"); }
template <int U, class FM, class FV> __device__ __forceinline__ void project(const u32x8 (&wt)[2][10], f32x4 (&tc)[2][2], u32x4 (&bb)[8], const LAS char* c0, const LAS char* c1, const LAS char* n0, const LAS char* n1, FM mid, FV vm) {
    constexpr int t = U >> 2, k = U & 3, v = (U + 8) % 40;
    if (U == 32) mid();
    vm(IC<U>());
    const LAS char* p = U < 32 ? (k >> 1 ? c1 : c0) : (k >> 1 ? n1 : n0);
    if (U < 2) asm volatile("s_nop 0\n\ts_waitcnt lgkmcnt(7)\n\tv_mfma_f32_16x16x32_bf16 %0, %3, %2, 0\n\tv_mfma_f32_16x16x32_bf16 %1, %4, %2, 0\n\tds_read_b128 %2, %5 offset:%6"
                            : "=&v"(tc[0][k & 1]), "=&v"(tc[1][k & 1]), "+v"(bb[U & 7]) : "v"(half8(wt[0][t], k >> 1)), "v"(half8(wt[1][t], k >> 1)), "v"(p), "n"((v >> 2) * 4096 + (k & 1) * 2048) : "memory");
    else asm volatile(".rept %7\n\ts_nop 0\n\t.endr\n\ts_waitcnt lgkmcnt(7)\n\tv_mfma_f32_16x16x32_bf16 %0, %3, %2, %0\n\tv_mfma_f32_16x16x32_bf16 %1, %4, %2, %1\n\tds_read_b128 %2, %5 offset:%6"
                      : "+v"(tc[0][k & 1]), "+v"(tc[1][k & 1]), "+v"(bb[U & 7]) : "v"(half8(wt[0][t], k >> 1)), "v"(half8(wt[1][t], k >> 1)), "v"(p), "n"((v >> 2) * 4096 + (k & 1) * 2048), "n"(U < 8 ? 1 : 0) : "memory");
    if constexpr (U < 39) project<U + 1>(wt, tc, bb, c0, c1, n0, n1, mid, vm);
    else asm volatile("s_nop 7" : "+v"(tc[0][0]), "+v"(tc[0][1]), "+v"(tc[1][0]), "+v"(tc[1][1]));
}
__device__ __forceinline__ void project_end(u32x4 (&bb)[8]) {
    asm volatile("s_waitcnt lgkmcnt(0)" : "+v"(bb[0]), "+v"(bb[1]), "+v"(bb[2]), "+v"(bb[3]), "+v"(bb[4]), "+v"(bb[5]), "+v"(bb[6]), "+v"(bb[7]) :: "memory");
}
__device__ __forceinline__ void project0(u32x4 (&bb)[8], const LAS char* c0, const LAS char* c1) {
    ldfrag<0>(bb[0], c0); ldfrag<2048>(bb[1], c0); ldfrag<0>(bb[2], c1); ldfrag<2048>(bb[3], c1);
    ldfrag<4096>(bb[4], c0); ldfrag<6144>(bb[5], c0); ldfrag<4096>(bb[6], c1); ldfrag<6144>(bb[7], c1);
    project_end(bb);
}
constexpr int WLB = 20992;
template <bool O> __device__ __forceinline__ void load_w(u32x8 (&wt)[2][10], const char* W, int hh, int w, int c0, int cd, char* st) {
    const int lane = (int)fresh_lane(), fr = lane & 15, fq = lane >> 4, x = w & 3;
    const __amdgpu_buffer_rsrc_t r = mkrsrc(W, D_ * HD * 2);
    u32x4 raw[40];
    const unsigned vo = O ? (unsigned)(lane * 16) : (unsigned)(fq * 8192 + fr * 16);
    unsigned so = O ? (unsigned)(hh * 128 + 8 * x) * 1280u : (unsigned)(8 * x * 8192 + hh * 256);
    asm volatile("" : "+s"(so));
#pragma unroll
    for (int i = 0; i < 40; ++i) {
        if (O) raw[i] = __builtin_amdgcn_raw_buffer_load_b128(r, vo + (i % 5 & 3) * 1024u, so + (unsigned)((32 * (i / 10) + 4 * (i / 5 & 1)) * 1280 + (i % 5 >> 2) * 4096), WAUX);
        else raw[i] = __builtin_amdgcn_raw_buffer_load_b128(r, vo, so + (unsigned)(32 * (i >> 1) + 4 * (i & 1)) * 8192u, WAUX);
    }
    st += (w >> 2) * WLB;
    const int row = 4 * fq + (fr >> 2);
    int wo[5];
#pragma unroll
    for (int e = 0; e < 5; ++e) {
        const int id = 64 * e + lane, rr = id / 80, ch = id - 80 * rr;
        wo[e] = O ? (4 * x + rr) * 1312 + (ch >> 1) * 32 + (ch & 1) * 4 : (4 * x + fq) * 256 + ((fr ^ (2 * ((4 * x + fq) & 7))) * 16) + e * 4096;
    }
    const int r0 = O ? row * 1312 + x * 320 + (fr & 3) * 8 : row * 256 + (((c0 + ((fr >> 1) & 1)) ^ (2 * (row & 7))) * 16) + (fr & 1) * 8;
    const int r1 = O ? 0 : row * 256 + (((c0 + cd + ((fr >> 1) & 1)) ^ (2 * (row & 7))) * 16) + (fr & 1) * 8;
#pragma unroll
    for (int p = 0; p < 8; ++p) {
        char* b = st + (p & 1) * 2 * WLB;
#pragma unroll
        for (int e = 0; e < 5; ++e) {
            const u32x4 v = raw[5 * p + e];
            if (O) {
                unsigned* d = (unsigned*)(b + wo[e]);
                d[0] = __builtin_amdgcn_perm(v[2], v[0], 0x05040100); d[2] = __builtin_amdgcn_perm(v[3], v[1], 0x05040100);
                d[4] = __builtin_amdgcn_perm(v[2], v[0], 0x07060302); d[6] = __builtin_amdgcn_perm(v[3], v[1], 0x07060302);
            } else *(u32x4*)(b + wo[e]) = v;
        }
        BAR();
#pragma unroll
        for (int e = 0; e < 10; ++e) {
            const int P = O ? p : 5 * p + (e >> 1), d = 2 * (P & 3);
            const u32x2 y = ldtr(b + (O ? r0 + e * 32 : (e & 1 ? r1 : r0) + (e >> 1) * 4096));
            u32x8& t = O ? wt[P >> 2][e] : wt[e & 1][P >> 2];
            t[d] = y[0]; t[d + 1] = y[1];
        }
    }
    BAR();
}

__global__ __launch_bounds__(512, 2) B3ATTR void b3_kernel(RecArgs a) {
    extern __shared__ __attribute__((aligned(16))) char smem[];
    char* memL = smem;
    char* tokL = smem + L_TOK;
    char* hL = smem + L_H;
    int w = __builtin_amdgcn_readfirstlane(threadIdx.x >> 6);
    const __amdgpu_buffer_rsrc_t rwin = mkrsrc(a.win, W_END + 2 * PAIR);
    char* const ws = (char*)a.ctl;
    const __amdgpu_buffer_rsrc_t rws = mkrsrc(ws, (unsigned)OFF_WIN);
    unsigned long long tref = 0;
    const unsigned long long tstart = __builtin_amdgcn_s_memtime();
    auto get_tag = [&]() {
        unsigned long long e;
        asm volatile("s_load_dwordx2 %0, %1, 8\n\ts_waitcnt lgkmcnt(0)" : "=s"(e) : "s"(a.ctl) : "memory");
        e += 0x9E3779B97F4A7C15ull;
        return (u32x2){(unsigned)e, (unsigned)(e >> 32)};
    };
    auto role = [&](unsigned rot) {
        const unsigned vx = blockIdx.x + rot;
        const int j = blockIdx.x >> 3, x = vx & 3, hh = 8 * (blockIdx.x & 3) + (j & 7);
        int r;
        if (!(vx & 4)) r = j < 8 ? hh : j < 16 ? 32 + hh : j < 26 ? 80 + 10 * x + j : 255 - 4 * (j - 26) - x;
        else r = j < 8 ? NQT + hh : j < 16 ? 64 + hh : j < 26 ? NREC - 16 + 10 * x + j : NXT - 26 + 6 * x + j;
        return r;
    };
    int bid = role(xcc_id() - blockIdx.x);
    unsigned nh = 0, nz = 0, mc = 0, qz = 0;
    auto hints = [&](unsigned o, int n) {
        if (w == 0) {
            const u32x2 tag = get_tag();
            const unsigned l = fresh_lane();
            u32x2 f;
            for (unsigned spins = 0; spins < SPIN_CAP; ++spins) {
                asm volatile("" ::: "memory");
                f = __builtin_amdgcn_raw_buffer_load_b64(rwin, min(l, (unsigned)n - 1u) * 8u, W_ROT + o, 16);
                if (!__any(f[1] != tag[1] || (f[0] ^ tag[0]) > 255u)) break;
                __builtin_amdgcn_s_sleep(4);
            }
            if (l < 16) *(volatile LAS char*)(smem + L_S + 64 + l) = (char)f[0];
        }
        BAR();
        const u32x4 v = *(volatile LAS u32x4*)(smem + L_S + 64);
        BAR();
        return v;
    };
    auto config = [&]() {
        const u32x4 v = hints(128u, 16);
        const unsigned rot = __builtin_amdgcn_readfirstlane(v[0] >> 2) & 7u;
        const unsigned sh = (-rot & 7u) * 8u;
        const unsigned long long p0 = (unsigned long long)__builtin_amdgcn_readfirstlane(v[1]) << 32 | __builtin_amdgcn_readfirstlane(v[0]), p1 = (unsigned long long)__builtin_amdgcn_readfirstlane(v[3]) << 32 | __builtin_amdgcn_readfirstlane(v[2]);
        const unsigned f0 = (unsigned)__builtin_rotateright64(p0, sh), f1 = (unsigned)__builtin_rotateright64(p1, sh);
        auto quad = [&](unsigned f) {
            unsigned v = 9u - (3u * (f & 2u) + 2u * ((~f >> 8 & 2u) + (f >> 16 & 2u) + (~f >> 24 & 2u)));
            asm volatile("s_lshr_b32 %0, %0, 31" : "+s"(v) :: "scc");
            return v;
        };
        nh = (f0 & 1u) * HALF; nz = (f1 & 1u) * HALF;
        mc = ((blockIdx.x + rot) & 3u) ^ quad(f0); qz = quad(f1);
        return rot;
    };
    auto zpair = [&](unsigned x, unsigned k) { return (((x >> 1) * 10u + k) * 2u + ((x & 1u) ^ qz)) * PAIR + nz; };
    unsigned mo, mo1, tro;
    auto two = [&]() { const unsigned l = fresh_lane(); return l < 4 ? (l >> 1) * PAIR + (l & 1) * 4096u + nh : 0x40000000u; };
    const u32x4 SENT = {0xFFFFFFFFu, 0xFFFFFFFFu, 0xFFFFFFFFu, 0xFFFFFFFFu};
    const f32x4 z4 = {0.f, 0.f, 0.f, 0.f};
    int hrdy = 0, budget = (int)SPIN_CAP;
    unsigned fc0 = 0, fc1 = 0, fc2 = 0;
    Clk ck = {0u, 0u, 1600u, 10486u};
    int lay = 0;
    unsigned fb = 0, zb = W_ZM;
    unsigned tpp = 0;
    int lt = LT1, xs = XS0;
    struct Tk { unsigned vo, tw, sl; const LAS char* sp; };
    auto tk_init = [&]() {
        const unsigned l = fresh_lane();
        Tk k = {l * 16u, two(), (l & 31u) * 128u + (l >> 5) * 4u, (const LAS char*)(memL + l * 64 + 4 + ((l >> 1) & 7) * 8)};
        PIN2(k.vo, k.tw); PIN2(k.sl, k.sp);
        return k;
    };
    auto sched2 = [&](Tk& k, int s, unsigned f) {
        const unsigned g0 = tpp - 0x400000u;
        tpp = g0 + (unsigned)read_sched(a, rwin, s, (unsigned)(s + 64 * lay), k.sl, tro + (unsigned)(s & 3) * 2u * PAIR, f, g0, lt, xs, fc1);
        k.sl &= ~4u;
        RT(6);
    };
    auto sched = [&](Tk& k, int s) { sched2(k, s, __builtin_amdgcn_raw_buffer_load_b32(rwin, k.sl, tro + (unsigned)(s & 3) * 2u * PAIR, 16)); };
    unsigned it = 0;
    auto fetch = [&](const Tk& k, int s, bool first, unsigned lim, unsigned dc, unsigned fo, bool tell) {
        const unsigned sb = (unsigned)(s & 3) * 6u * PAIR, ta0 = ck.ta, mm0 = ck.ma;
        if (__builtin_expect(s > 0, 1)) {
            if (__builtin_expect(first, 1)) { const unsigned te = cyc_at(ck, sx24(tpp - TKE - ck.ta)); while ((int)((unsigned)__builtin_amdgcn_s_memtime() - te) < 0) __builtin_amdgcn_s_sleep(1); }
            if (__builtin_expect(first, 1)) gather_at(tpp, ck, rwin, k.vo, mo + sb, mo1 + sb); else { const unsigned c = (mc + 1u + (it & 1u)) & 3u; gather_dma(rwin, k.vo, W_MEM + c * PAIR + nh + sb, W_MEM + (4u + (c & 1u)) * PAIR + nh + (c >> 1) * 2048u + sb); }
        }
        __builtin_amdgcn_s_setprio(0);
        RT(1);
        clk_rate(ck, ta0, mm0);
        unsigned tb = ck.ma - dc, tg = (unsigned)(s + 64 * lay) << 24, so = W_T + (unsigned)(s & 3) * 2u * PAIR + fo;
        asm volatile("" : "+s"(tb), "+s"(tg), "+s"(so));
        asm volatile("s_waitcnt vmcnt(%1) lgkmcnt(0)\n\ts_barrier\n\ts_waitcnt vmcnt(%2)\n\ts_barrier\n\ts_waitcnt vmcnt(0)\n\ts_memtime %0\n\ts_barrier"
                     : "=s"(tref) : "n"(10 - NB1), "n"(10 - NB2) : "memory");
        MT(1);
        if (!tell) { RT(3); return; }
        unsigned x0 = *(const LAS unsigned*)k.sp, x1 = *(const LAS unsigned*)(k.sp + 4096), x2 = *(const LAS unsigned*)(k.sp + 6144);
        __builtin_amdgcn_s_waitcnt(0xC07F);
        asm volatile("" : "+s"(tref) :: "memory");
        const unsigned fw = ((ck.ta + ((((unsigned)tref - tb) * ck.qpc) >> 16)) & 0xFFFFFFu) | tg;
        __builtin_amdgcn_raw_buffer_store_b32(fw, rwin, k.tw, __any(max(max(x0, x1), x2) > lim) ? 0x40000000u : so, 16);
        RT(3);
    };
    auto places = [&]() {
        mo = W_MEM + mc * PAIR + nh; mo1 = W_MEM + (4u + (mc & 1u)) * PAIR + nh + (mc >> 1) * 2048u;
        tro = W_T + (mc & 1u) * PAIR + nh + (mc >> 1) * 4096u;
    };
    auto fresh = [&]() {
        asm volatile("" : "+s"(bid), "+s"(w), "+s"(nh), "+s"(nz), "+s"(mc), "+s"(qz), "+s"(lay));
        fb = lay * W_FL; zb = W_ZM + lay * (NSEG % 3 * NOWN * PAIR);
        tref = 0; hrdy = 0; budget = (int)SPIN_CAP; ck = Clk{0u, 0u, 1600u, 10486u}; tpp = 0; lt = LT1; xs = XS0; it = 0;
    };
    SST(27);
    {
        LANES();
        const unsigned u = blockIdx.x & 127u;
        if (blockIdx.x < 128) filter_unit((const u16*)karg<9>(), (const u16*)karg<10>(), u * 16, rws, (u16*)smem, tid);
        else a1_unit<8, 512>(mkrsrc(karg<3>(), S_ * D_ * 2), u * 16, rws, smem, row_tw(lane), w, lane, [&](int k) {
            if (k < 0) {
                if (u == 0 && w == 0) {
                    const u32x2 tag = get_tag();
                    __builtin_amdgcn_raw_buffer_store_b64((u32x2){(tag[0] & ~255u) | xcc_id(), tag[1]}, rwin, 0u, W_ROT, 16);
                    __builtin_amdgcn_raw_buffer_store_b32(0u, rws, 0u, 0u, 16);
                }
#pragma unroll
                for (int i = 0; i < 4; ++i) {
                    const unsigned g = (i * 128u + u) * 8192u;
                    if (g < W_PROBE + W_END - W_ZM) st128(SENT, rwin, tid * 16u, g < W_PROBE ? g : g + W_ZM - W_PROBE, 16);
                }
            }
        });
        SST(28);
        VMWAIT();
        BAR();
        const u32x2 tag = get_tag();
        if (w == 0) __builtin_amdgcn_raw_buffer_store_b64(tag, rwin, 0u, W_ST + (u >> 1) * 128u + (u & 1u) * 8u + (blockIdx.x < 128 ? 64u : 0u), 16);
        SST(29);
        if (blockIdx.x >= 128) bid = role(__builtin_amdgcn_readfirstlane(hints(0u, 1)[0]));
        if (blockIdx.x >> 4 == 13) {
            unsigned f = 0;
            if (bid >= NWRK) f = probe(rwin, (blockIdx.x & 8u ? W_END : W_PROBE) + (blockIdx.x & 7u) * 128u + w * 1024u, (unsigned*)smem, w, lane);
            if (w == 0) {
                for (unsigned spins = 0; spins < SPIN_CAP; ++spins) {
                    asm volatile("" ::: "memory");
                    const u32x4 g = __builtin_amdgcn_raw_buffer_load_b128(rwin, lane * 128u, W_ST, 16);
                    if (!__any(((g[0] ^ tag[0]) | (g[1] ^ tag[1]) | (g[2] ^ tag[0]) | (g[3] ^ tag[1])) != 0u)) break;
                    __builtin_amdgcn_s_sleep(4);
                }
                const unsigned rot = (unsigned)*(volatile LAS char*)(smem + L_S + 64);
                __builtin_amdgcn_raw_buffer_store_b64((u32x2){(tag[0] & ~255u) | (rot & 7u) << 2 | f, tag[1]}, rwin, 0u, W_CFG + (blockIdx.x & 15u) * 8u, 16);
            }
            SST(30);
        }
    }
    if (bid < 96 || (bid >= NQT && bid < NWRK)) {
    u32x8 wt[2][10];
#pragma nounroll
    for (; lay < 2; ++lay) {
    fresh();
    const bool xt = bid >= NXT;
    SST(0);
    if ((bid >= 32 && bid < 96) || xt) {
        const int hh = (xt ? bid - NXT : bid) & 31;
        if (lay == 0 || xt) {
            if (bid < 64) wait_until(tstart + DVO);
            if (w < 4) load_w<false>(wt, karg<7>(), hh, w, 2 * w, 8, tokL); else load_w<true>(wt, karg<8>(), hh, w, 0, 0, tokL);
            SST(5);
            if (lay == 0) bid = role(config());
        }
        places();
        const bool vt = bid >= 64;
        const unsigned long long mv = xt ? MXV : ~MXV;
        auto nextv = [&](int j) { return min(seg_after(mv, j), NSEG - 1); };
        const int jv1 = nextv(-1), jv2 = nextv(jv1);
        {
            LANES();
            if (!vt) gather_mem0(memL, tid, 512);
            VMWAIT();
            if (tid < 8) *(unsigned*)(smem + L_S + tid * 4) = 0u;
            if (!vt && w == 7) clk_init(ck);
            SST(1);
            if (vt) {
                if (w == 0) { wait_h(a, rwin, fb, jv2, hrdy); *(volatile LAS int*)(smem + L_S + 32) = hrdy; }
                BAR();
                hrdy = __builtin_amdgcn_readfirstlane(*(volatile LAS int*)(smem + L_S + 32));
                SST(2);
                stage_h(rws, jv1, L_H, tid);
                stage_h(rws, jv2, L_H + 40960, tid);
            }
        }
        auto steps = [&](auto cc) {
            constexpr int C = decltype(cc)::v & 1;
            constexpr bool TK = decltype(cc)::v == 3;
            LANES();
            Tk tk = {0u, 0u, 0x40000000u, nullptr};
            if (TK) { tk = tk_init(); __builtin_amdgcn_s_setprio(TKP); }
            const unsigned zi = (unsigned)(hh * 128 + fr * 8) * 2u;
            unsigned zo0 = zpair(w & 3, fq) + zi, zo1 = zo0 + 8 * PAIR, zo2 = zpair(w & 3, 8 + (fq >> 1)) + zi + (fq & 1) * 8;
            const LAS char* va = (const LAS char*)(smem + L_Q + (fr & 1) * 544 + ((fr >> 1) & 1) * 1152 + (16 * fq + 2 * (fr >> 2)) * 4);
            LAS char* vw = (LAS char*)(smem + L_Q + (fq & 1) * 544 + (fq >> 1) * 1152 + (16 * w + bs2(fr)) * 4);
            const LAS char* mp0 = (const LAS char*)(memL + ma0);
            PIN2(zo0, zo1); PIN1(zo2);
            if (C == 0) { PIN2(mp0, vw); } else PIN1(va);
            u32x4 za = SENT, zc = SENT;
            u32x2 ze = {0u, 0u};
            auto put_z2 = [&](int s) {
                const unsigned z2b = s < NSEG ? O_Z2 + (unsigned)s * (NOWN * 8192u) + (unsigned)(10 * (w & 3) * 8192 + hh * 256) : 0x40000000u, l2 = fresh_lane();
                st128(za, rws, (l2 >> 4) * 8192u + (l2 & 15u) * 16u, z2b, 16);
                st128(zc, rws, (l2 >> 4) * 8192u + (l2 & 15u) * 16u, z2b + 4u * 8192u, 16);
                __builtin_amdgcn_raw_buffer_store_b64(ze, rws, (l2 >> 5) * 8192u + (l2 & 15u) * 16u + ((l2 >> 4) & 1u) * 8u, z2b + 8u * 8192u, 16);
            };
            for (int s = 0; s < NSEG; ++s) {
                RT(0);
                bool redo = false;
                unsigned sf = 0;
                do {
                    ++it;
                    if (C == 0) {
                        const unsigned lim = s > 0 ? poll_lim(budget) : ~0u;
                        asm volatile(
                            "v_mov_b64 v[200:201], 0\n\t"
                            "v_mov_b64 v[202:203], 0\n\t"
                            "v_mov_b64 v[204:205], 0\n\t"
                            "v_mov_b64 v[206:207], 0\n\t"
                            "v_mov_b32 v208, 0\n\t"
                            "s_setprio 2\n\t"
                            "s_waitcnt lgkmcnt(0)\n\t"
                            "s_barrier\n\t"
                            MTA(0)
                            "ds_read_b128 v[212:215], %[mp] offset:0\n\t"
                            "ds_read_b128 v[216:219], %[mp] offset:1024\n\t"
                            "ds_read_b128 v[220:223], %[mp] offset:2048\n\t"
                            "s_barrier\n\t"
                            "ds_read_b128 v[224:227], %[mp] offset:3072\n\t"
                            "ds_read_b128 v[228:231], %[mp] offset:4096\n\t"
                            "ds_read_b128 v[232:235], %[mp] offset:5120\n\t"
                            "s_waitcnt lgkmcnt(5)\n\t"
                            "v_smfmac_f32_16x16x64_bf16 v[200:203], v[212:215], %[w00], %[ix]\n\t"
                            "s_waitcnt lgkmcnt(4)\n\t"
                            "v_smfmac_f32_16x16x64_bf16 v[200:203], v[216:219], %[w01], %[ix]\n\t"
                            "s_barrier\n\t"
                            MTA(1)
                            "ds_read_b128 v[236:239], %[mp] offset:6144\n\t"
                            "s_waitcnt lgkmcnt(4)\n\t"
                            "v_smfmac_f32_16x16x64_bf16 v[200:203], v[220:223], %[w02], %[ix]\n\t"
                            "ds_read_b128 v[240:243], %[mp] offset:7168\n\t"
                            "s_waitcnt lgkmcnt(4)\n\t"
                            "v_smfmac_f32_16x16x64_bf16 v[200:203], v[224:227], %[w03], %[ix]\n\t"
                            "ds_read_b128 v[244:247], %[mp] offset:8192\n\t"
                            "s_waitcnt lgkmcnt(4)\n\t"
                            "v_smfmac_f32_16x16x64_bf16 v[200:203], v[228:231], %[w04], %[ix]\n\t"
                            "ds_read_b128 v[248:251], %[mp] offset:9216\n\t"
                            "s_waitcnt lgkmcnt(4)\n\t"
                            "v_smfmac_f32_16x16x64_bf16 v[200:203], v[232:235], %[w05], %[ix]\n\t"
                            "s_waitcnt lgkmcnt(3)\n\t"
                            "v_smfmac_f32_16x16x64_bf16 v[200:203], v[236:239], %[w06], %[ix]\n\t"
                            "s_waitcnt lgkmcnt(2)\n\t"
                            "v_smfmac_f32_16x16x64_bf16 v[200:203], v[240:243], %[w07], %[ix]\n\t"
                            "s_waitcnt lgkmcnt(1)\n\t"
                            "v_smfmac_f32_16x16x64_bf16 v[200:203], v[244:247], %[w08], %[ix]\n\t"
                            "s_waitcnt lgkmcnt(0)\n\t"
                            "v_smfmac_f32_16x16x64_bf16 v[200:203], v[248:251], %[w09], %[ix]\n\t"
                            "s_nop 2\n\t"
                            "v_smfmac_f32_16x16x64_bf16 v[204:207], v[212:215], %[w10], %[ix]\n\t"
                            "s_nop 2\n\t"
                            "v_smfmac_f32_16x16x64_bf16 v[204:207], v[216:219], %[w11], %[ix]\n\t"
                            "v_add_f32 v200, v200, v202\n\t"
                            "v_add_f32 v201, v201, v203\n\t"
                            "v_cvt_pk_bf16_f32 v200, v200, v201\n\t"
                            "ds_write_b32 %[vw], v200\n\t"
                            MTA(2)
                            "v_smfmac_f32_16x16x64_bf16 v[204:207], v[220:223], %[w12], %[ix]\n\t"
                            "v_max3_u32 v208, v208, v212, v213\n\t"
                            "v_max3_u32 v208, v208, v214, v215\n\t"
                            "v_smfmac_f32_16x16x64_bf16 v[204:207], v[224:227], %[w13], %[ix]\n\t"
                            "v_max3_u32 v208, v208, v216, v217\n\t"
                            "v_max3_u32 v208, v208, v218, v219\n\t"
                            "v_smfmac_f32_16x16x64_bf16 v[204:207], v[228:231], %[w14], %[ix]\n\t"
                            "v_max3_u32 v208, v208, v220, v221\n\t"
                            "v_max3_u32 v208, v208, v222, v223\n\t"
                            "v_smfmac_f32_16x16x64_bf16 v[204:207], v[232:235], %[w15], %[ix]\n\t"
                            "v_max3_u32 v208, v208, v224, v225\n\t"
                            "v_max3_u32 v208, v208, v226, v227\n\t"
                            "v_smfmac_f32_16x16x64_bf16 v[204:207], v[236:239], %[w16], %[ix]\n\t"
                            "v_max3_u32 v208, v208, v228, v229\n\t"
                            "v_max3_u32 v208, v208, v230, v231\n\t"
                            "v_smfmac_f32_16x16x64_bf16 v[204:207], v[240:243], %[w17], %[ix]\n\t"
                            "s_waitcnt lgkmcnt(0)\n\t"
                            "s_barrier\n\t"
                            MTA(5)
                            "v_smfmac_f32_16x16x64_bf16 v[204:207], v[244:247], %[w18], %[ix]\n\t"
                            "v_max3_u32 v208, v208, v232, v233\n\t"
                            "v_max3_u32 v208, v208, v234, v235\n\t"
                            "v_smfmac_f32_16x16x64_bf16 v[204:207], v[248:251], %[w19], %[ix]\n\t"
                            "v_max3_u32 v208, v208, v236, v237\n\t"
                            "v_max3_u32 v208, v208, v238, v239\n\t"
                            "v_max3_u32 v208, v208, v240, v241\n\t"
                            "v_max3_u32 v208, v208, v242, v243\n\t"
                            "v_max3_u32 v208, v208, v244, v245\n\t"
                            "v_max3_u32 v208, v208, v246, v247\n\t"
                            "v_max3_u32 v208, v208, v248, v249\n\t"
                            "v_max3_u32 v208, v208, v250, v251\n\t"
                            "v_cmp_lt_u32 vcc, %[lim], v208\n\t"
                            "v_add_f32 v204, v204, v206\n\t"
                            "v_add_f32 v205, v205, v207\n\t"
                            "v_cvt_pk_bf16_f32 v204, v204, v205\n\t"
                            "ds_write_b32 %[vw], v204 offset:256\n\t"
                            MTA(3)
                            "s_cbranch_vccz 1f\n\t"
                            "v_mov_b32 v208, %[it]\n\t"
                            "v_mov_b32 v209, 0\n\t"
                            "ds_write_b32 v209, v208 offset:%[ls]\n\t"
                            "1:\n\t"
                            "s_waitcnt vmcnt(0) lgkmcnt(0)\n\t"
                            "s_barrier\n\t"
                            "s_setprio 0\n\t"
                            :: [mp] "v"(mp0), [vw] "v"(vw), [ix] "v"(sidx), [lim] "s"(lim), [it] "s"(it), [ls] "n"(L_S),
                              [w00] "v"(wt[0][0]), [w01] "v"(wt[0][1]), [w02] "v"(wt[0][2]), [w03] "v"(wt[0][3]), [w04] "v"(wt[0][4]), [w05] "v"(wt[0][5]), [w06] "v"(wt[0][6]), [w07] "v"(wt[0][7]), [w08] "v"(wt[0][8]), [w09] "v"(wt[0][9]), [w10] "v"(wt[1][0]), [w11] "v"(wt[1][1]), [w12] "v"(wt[1][2]), [w13] "v"(wt[1][3]), [w14] "v"(wt[1][4]), [w15] "v"(wt[1][5]), [w16] "v"(wt[1][6]), [w17] "v"(wt[1][7]), [w18] "v"(wt[1][8]), [w19] "v"(wt[1][9])
                            : "v200", "v201", "v202", "v203", "v204", "v205", "v206", "v207", "v208", "v209", "v212", "v213", "v214", "v215", "v216", "v217", "v218", "v219", "v220", "v221", "v222", "v223", "v224", "v225", "v226", "v227", "v228", "v229", "v230", "v231", "v232", "v233", "v234", "v235", "v236", "v237", "v238", "v239", "v240", "v241", "v242", "v243", "v244", "v245", "v246", "v247", "v248", "v249", "v250", "v251", "vcc", "memory");
                        MT(4);
                        redo = __builtin_amdgcn_readfirstlane(*(volatile LAS unsigned*)(smem + L_S)) == it;
                        if (redo) FC(fc0);
                    } else {
                        f32x16 ca, cb;
                        f32x8 cc;
                        asm volatile(".irp r,212,214,216,218,220,222,224,226,228,230,232,234,236,238,240,242,244,246,248,250\n\tv_mov_b64 v[\\r:\\r+1], 0\n\t.endr"
                                     : "={v[212:227]}"(ca), "={v[228:243]}"(cb), "={v[244:251]}"(cc));
                        unsigned sr = tro + (unsigned)(s & 3) * 2u * PAIR;
                        asm volatile("" : "+s"(sr));
                        if (TK) fetch(tk, s, !redo, s > 0 ? poll_lim(budget) : ~0u, DVC, (unsigned)hh * 128u + 4u, __builtin_expect(s == 0, 0));
                        else asm volatile("s_waitcnt lgkmcnt(0)\n\ts_barrier\n\ts_barrier\n\ts_barrier" ::: "memory");
                        unsigned vd, vv, zs = zb;
                        u32x4 af1;
                        asm volatile(
                            "s_setprio 1\n\t"
                            "s_waitcnt lgkmcnt(0)\n\t"
                            "s_barrier\n\t"
                            "ds_read_b64_tr_b16 v[192:193], %[va]\n\t"
                            "ds_read_b64_tr_b16 v[194:195], %[va] offset:32\n\t"
                            "s_waitcnt lgkmcnt(0)\n\t"
                            "v_smfmac_f32_16x16x64_bf16 v[212:215], v[192:195], %[w0], %[ix]\n\t"
                            "v_smfmac_f32_16x16x64_bf16 v[220:223], v[192:195], %[w2], %[ix]\n\t"
                            "v_smfmac_f32_16x16x64_bf16 v[216:219], v[192:195], %[w1], %[ix]\n\t"
                            "v_smfmac_f32_16x16x64_bf16 v[224:227], v[192:195], %[w3], %[ix]\n\t"
                            "s_barrier\n\t"
                            MTA(4)
                            "ds_read_b64_tr_b16 v[196:197], %[va] offset:256\n\t"
                            "v_smfmac_f32_16x16x64_bf16 v[228:231], v[192:195], %[w4], %[ix]\n\t"
                            "ds_read_b64_tr_b16 v[198:199], %[va] offset:288\n\t"
                            "v_smfmac_f32_16x16x64_bf16 v[236:239], v[192:195], %[w6], %[ix]\n\t"
                            "ds_read_b32 v210, %[vz] offset:%[ls]\n\t"
                            "v_smfmac_f32_16x16x64_bf16 v[232:235], v[192:195], %[w5], %[ix]\n\t"
                            "v_smfmac_f32_16x16x64_bf16 v[240:243], v[192:195], %[w7], %[ix]\n\t"
                            "v_smfmac_f32_16x16x64_bf16 v[244:247], v[192:195], %[w8], %[ix]\n\t"
                            "v_smfmac_f32_16x16x64_bf16 v[248:251], v[192:195], %[w9], %[ix]\n\t"
                            "buffer_load_dword %[sf], %[sl], %[rw], %[sr] offen sc1\n\t"
                            : [sf] "=&v"(sf), "+{v[212:227]}"(ca), "+{v[228:243]}"(cb), "+{v[244:251]}"(cc), "=&{v[196:199]}"(af1), "=&{v210}"(vv)
                            : [va] "v"(va), [vz] "v"(0u), [ls] "n"(L_S), [ix] "v"(sidx), [sl] "v"(tk.sl), [sr] "s"(sr), [rw] "s"(rwin),
                              [w0] "v"(wt[0][0]), [w1] "v"(wt[0][1]), [w2] "v"(wt[0][2]), [w3] "v"(wt[0][3]), [w4] "v"(wt[0][4]), [w5] "v"(wt[0][5]), [w6] "v"(wt[0][6]), [w7] "v"(wt[0][7]), [w8] "v"(wt[0][8]), [w9] "v"(wt[0][9])
                            : "v192", "v193", "v194", "v195", "memory");
                        asm volatile(
                            "s_waitcnt lgkmcnt(0)\n\t"
                            "v_smfmac_f32_16x16x64_bf16 v[212:215], v[196:199], %[w0], %[ix]\n\t"
                            "v_readfirstlane_b32 %[vd], v210\n\t"
                            "v_smfmac_f32_16x16x64_bf16 v[220:223], v[196:199], %[w2], %[ix]\n\t"
                            "s_cmp_eq_u32 %[vd], %[it]\n\t"
                            "s_cselect_b32 %[zs], 2.0, %[zs]\n\t"
                            "v_smfmac_f32_16x16x64_bf16 v[216:219], v[196:199], %[w1], %[ix]\n\t"
                            "s_nop 1\n\t"
                            "v_smfmac_f32_16x16x64_bf16 v[224:227], v[196:199], %[w3], %[ix]\n\t"
                            "v_add_f32 v212, v212, v214\n\t"
                            "v_smfmac_f32_16x16x64_bf16 v[228:231], v[196:199], %[w4], %[ix]\n\t"
                            "v_add_f32 v213, v213, v215\n\t"
                            "v_smfmac_f32_16x16x64_bf16 v[236:239], v[196:199], %[w6], %[ix]\n\t"
                            "v_add_f32 v220, v220, v222\n\t"
                            "v_smfmac_f32_16x16x64_bf16 v[232:235], v[196:199], %[w5], %[ix]\n\t"
                            "v_add_f32 v221, v221, v223\n\t"
                            "v_smfmac_f32_16x16x64_bf16 v[240:243], v[196:199], %[w7], %[ix]\n\t"
                            "v_cvt_pk_bf16_f32 v200, v212, v213\n\t"
                            "v_smfmac_f32_16x16x64_bf16 v[244:247], v[196:199], %[w8], %[ix]\n\t"
                            "v_cvt_pk_bf16_f32 v201, v220, v221\n\t"
                            "v_smfmac_f32_16x16x64_bf16 v[248:251], v[196:199], %[w9], %[ix]\n\t"
                            "v_add_f32 v216, v216, v218\n\t"
                            "v_add_f32 v217, v217, v219\n\t"
                            "v_add_f32 v224, v224, v226\n\t"
                            "v_add_f32 v225, v225, v227\n\t"
                            "v_cvt_pk_bf16_f32 v202, v216, v217\n\t"
                            "v_cvt_pk_bf16_f32 v203, v224, v225\n\t"
                            "v_permlane32_swap_b32 v200, v201\n\t"
                            "v_pk_add_f32 v[228:229], v[228:229], v[230:231]\n\t"
                            "v_permlane32_swap_b32 v202, v203\n\t"
                            "v_min_u32 v201, -2, v201\n\t"
                            "v_pk_add_f32 v[236:237], v[236:237], v[238:239]\n\t"
                            "v_min_u32 v203, -2, v203\n\t"
                            "v_permlane16_swap_b32 v200, v202\n\t"
                            "v_pk_add_f32 v[232:233], v[232:233], v[234:235]\n\t"
                            "v_permlane16_swap_b32 v201, v203\n\t"
                            "v_pk_add_f32 v[240:241], v[240:241], v[242:243]\n\t"
                            MTA(5)
                            "buffer_store_dwordx4 v[200:203], %[z0], %[rw], %[zs] offen sc1\n\t"
                            "v_cvt_pk_bf16_f32 v204, v228, v229\n\t"
                            "v_cvt_pk_bf16_f32 v205, v236, v237\n\t"
                            "v_cvt_pk_bf16_f32 v206, v232, v233\n\t"
                            "v_cvt_pk_bf16_f32 v207, v240, v241\n\t"
                            "v_permlane32_swap_b32 v204, v205\n\t"
                            "v_pk_add_f32 v[244:245], v[244:245], v[246:247]\n\t"
                            "v_permlane32_swap_b32 v206, v207\n\t"
                            "v_min_u32 v205, -2, v205\n\t"
                            "v_pk_add_f32 v[248:249], v[248:249], v[250:251]\n\t"
                            "v_min_u32 v207, -2, v207\n\t"
                            "v_permlane16_swap_b32 v204, v206\n\t"
                            "v_cvt_pk_bf16_f32 v208, v244, v245\n\t"
                            "v_permlane16_swap_b32 v205, v207\n\t"
                            "v_cvt_pk_bf16_f32 v209, v248, v249\n\t"
                            "buffer_store_dwordx4 v[204:207], %[z1], %[rw], %[zs] offen sc1\n\t"
                            "s_nop 0\n\t"
                            "v_permlane32_swap_b32 v208, v209\n\t"
                            "v_min_u32 v209, -2, v209\n\t"
                            "buffer_store_dwordx2 v[208:209], %[z2], %[rw], %[zs] offen sc1\n\t"
                            : [vd] "=&s"(vd), [zs] "+s"(zs), "=&{v[200:203]}"(za), "=&{v[204:207]}"(zc), "=&{v[208:209]}"(ze), "+{v[212:227]}"(ca), "+{v[228:243]}"(cb), "+{v[244:251]}"(cc), "+v"(sf)
                            : [ix] "v"(sidx), [it] "s"(it), [rw] "s"(rwin), [z0] "v"(zo0), [z1] "v"(zo1), [z2] "v"(zo2), "{v[196:199]}"(af1), "{v210}"(vv),
                              [w0] "v"(wt[1][0]), [w1] "v"(wt[1][1]), [w2] "v"(wt[1][2]), [w3] "v"(wt[1][3]), [w4] "v"(wt[1][4]), [w5] "v"(wt[1][5]), [w6] "v"(wt[1][6]), [w7] "v"(wt[1][7]), [w8] "v"(wt[1][8]), [w9] "v"(wt[1][9])
                            : "scc", "memory");
                        redo = vd == it;
                        RT(4);
                        MT(6);
                        tref = __builtin_amdgcn_s_memtime();
                        __builtin_amdgcn_s_setprio(TKP);
                        asm volatile("s_waitcnt vmcnt(3)" : "+v"(sf) :: "memory");
                    }
                } while (__builtin_expect(redo, 0));
                if (s < 8) SST(8 + s);
                if (C == 0) {
                    if (s > 0) {
                        __builtin_amdgcn_s_sleep(DZR);
                        const unsigned zq = zb == W_ZM ? W_ZM + 2u * NOWN * PAIR : zb - NOWN * PAIR;
                        unsigned s1;
                        asm volatile("v_mov_b32 %0, -1" : "=v"(s1));
                        st128(((u32x4){s1, s1, s1, s1}), rwin, zo0, zq, 16);
                        st128(((u32x4){s1, s1, s1, s1}), rwin, zo1, zq, 16);
                        __builtin_amdgcn_raw_buffer_store_b64((u32x2){s1, s1}, rwin, zo2, zq, 16);
                        if (__builtin_expect(s == 1, 0)) {
                            const unsigned zn = zb == W_ZM + 2u * NOWN * PAIR ? W_ZM : zb + NOWN * PAIR;
                            st128(((u32x4){s1, s1, s1, s1}), rwin, zo0, zn, 16);
                            st128(((u32x4){s1, s1, s1, s1}), rwin, zo1, zn, 16);
                            __builtin_amdgcn_raw_buffer_store_b64((u32x2){s1, s1}, rwin, zo2, zn, 16);
                        }
                    }
                } else {
                    if (s > TAILS) __builtin_amdgcn_raw_buffer_store_b32((unsigned)s - 1u, rwin, 0u, W_FZ2 + fb + (unsigned)(hh * 128 + (w & 3) * 4), 16);
                    if (TK && s + 1 < NSEG) sched2(tk, s, sf);
                    if (!TK) wait_until(tref + DZ2);
                    if (s + 1 < NSEG) put_z2(s);
                }
                if (budget <= 0) __hip_atomic_store(a.ctl, 1u, __ATOMIC_RELAXED, __HIP_MEMORY_SCOPE_AGENT);
                zb = zb == W_ZM + 2u * NOWN * PAIR ? W_ZM : zb + NOWN * PAIR;
                RT(5);
                RTS();
                MT(7);
                MTS();
            }
        };
        if (!vt) {
            if (w < 4) steps(IC<0>()); else if (w < 7) steps(IC<1>()); else steps(IC<3>());
        } else {
            asm volatile("s_waitcnt vmcnt(5) lgkmcnt(0)\n\ts_barrier" ::: "memory");
            SST(3);
            LANES();
            const int ni = __builtin_popcountll(mv) + 1;
            if (w < 4) {
                const LAS char* c0 = (const LAS char*)(hL + hoff + ((fq ^ (fr & 7)) << 4));
                const LAS char* c1 = (const LAS char*)(hL + hoff + (((4 + fq) ^ (fr & 7)) << 4));
                const LAS char* n0 = c0 + 40960; const LAS char* n1 = c1 + 40960;
                u32x4 bb[8];
                project0(bb, c0, c1);
                for (int i = 1; i < ni; ++i) {
                    if (vt) MT(0);
                    f32x4 tc[2][2];
                    project<0>(wt, tc, bb, c0, c1, n0, n1,
                        [&]() {
                            MT(1);
                            asm volatile("s_waitcnt vmcnt(0) lgkmcnt(0)\n\ts_barrier" ::: "memory");
                            MT(2);
                        }, [](auto) {});
                    { const LAS char* x = c0; c0 = n0; n0 = x; x = c1; c1 = n1; n1 = x; }
                    char* tb = tokL + (i & 1) * 32 * QS + (16 * w + 4 * fq) * 2;
#pragma unroll
                    for (int g = 0; g < 2; ++g)
#pragma unroll
                        for (int tt = 0; tt < 2; ++tt) *(u32x2*)(tb + (prow + 8 * tt) * QS + g * 128) = (u32x2){pack2(tc[g][tt][0], tc[g][tt][1]), pack2(tc[g][tt][2], tc[g][tt][3])};
                    asm volatile("" ::: "memory");
                    *(volatile LAS int*)(smem + L_S + 16 + w * 4) = i;
                    if (vt) { MT(3); const int s = i & 63; MTRT(); MTS(); }
                }
                project_end(bb);
            } else {
                __builtin_amdgcn_s_setprio(OPR);
                const unsigned hv = lane * 16u, fz_ = W_FZ + fb + (unsigned)(hh * 128 + xt * 64 + (w - 4) * 4);
                unsigned fp = ~0u;
                u32x4 r = SENT;
                int jo = jv1, jh = jv2;
                for (int i = 1; i < ni; ++i, jo = nextv(jo)) {
                    const int b = i + (i >= ZLAG);
                    if (__builtin_expect(i == 1 || i == ZLAG, 0)) {
                        VMWAIT();
                        if (i == ZLAG) {
                            jh = nextv(jh);
                            wait_h(a, rwin, fb, jh, hrdy);
                            BAR0();
                            for (int t = 0; t < 10; ++t) dma1k(rws, hv, O_H + (unsigned)(jh * 40960 + (w - 4) * 1024 + t * 4096), (unsigned)(L_H + ((i + 1) & 1) * 40960 + (w - 4) * 1024 + t * 4096));
                            VMWAIT();
                        }
                    }
                    jh = nextv(jh);
                    wait_h(a, rwin, fb, jh, hrdy);
                    if (b < ni) BAR0();
                    if (vt) MT(1);
                    for (;;) {
                        const u32x4 f = *(volatile LAS u32x4*)(smem + L_S + 16);
                        if (__builtin_amdgcn_readfirstlane(min(min(f[0], f[1]), min(f[2], f[3]))) >= (unsigned)i) break;
                        __builtin_amdgcn_s_sleep(1);
                    }
                    asm volatile("" ::: "memory");
                    if (vt) MT(0);
                    u32x4 vf[2][4];
#pragma unroll
                    for (int t = 0; t < 2; ++t)
#pragma unroll
                        for (int ks = 0; ks < 4; ++ks) vf[t][ks] = *(const u32x4*)(tokL + (i & 1) * 32 * QS + (16 * t + fr) * QS + ks * 64 + fq * 16);
                    const unsigned ztb = O_Z + (unsigned)(jo * ZTSEG) * 2u + (unsigned)(10 * (w - 4) * H_ + hh) * 1024u;
                    f32x4 c[2][2];
                    unsigned hs = O_H + (unsigned)(jh * 40960 + (w - 4) * 1024), hd = (unsigned)(L_H + ((b + 1) & 1) * 40960 + (w - 4) * 1024);
                    asm volatile("" : "+s"(hs), "+s"(hd));
#pragma unroll
                    for (int nt = 0; nt < 11; ++nt) {
                        auto& cc = c[nt & 1]; auto& cp = c[(nt + 1) & 1];
#pragma unroll
                        for (int ks = 0; ks < 4; ++ks) {
                            if (nt < 10) { cc[0] = mfma32(vf[0][ks], half8(wt[ks >> 1][nt], ks & 1), ks ? cc[0] : z4); PIN1(cc[0]); }
                            if (nt > 0 && ks >= 1 && ks < 3) { r[2 * ks - 2] = pack2(cp[ks - 1][0], cp[ks - 1][1]); PIN1(r); }
                            if (nt < 10) { cc[1] = mfma32(vf[1][ks], half8(wt[ks >> 1][nt], ks & 1), ks ? cc[1] : z4); PIN1(cc[1]); }
                            if (nt > 0 && ks >= 1 && ks < 3) { r[2 * ks - 1] = pack2(cp[ks - 1][2], cp[ks - 1][3]); PIN1(r); }
                            if (nt > 0 && ks == 2) st128(r, rws, lane * 16u, ztb + (unsigned)((nt - 1) * H_) * 1024u, 16);
                            if (nt < 5 && !(ks & 1)) dma1k(rws, hv, hs + (2 * nt + (ks >> 1)) * 4096u, hd + (2 * nt + (ks >> 1)) * 4096u);
                            if (nt == 5 && ks == 0) {
                                asm volatile("s_waitcnt vmcnt(14)" ::: "memory");
                                __builtin_amdgcn_raw_buffer_store_b32(fp, rwin, 0u, fz_, 16);
                            }
                            SB();
                        }
                    }
                    if (vt) MT(2);
                    fp = (unsigned)jo;
                    if (__builtin_expect(jo <= ZEARLY || i + 1 == ni, 0)) {
                        VMWAIT();
                        __builtin_amdgcn_raw_buffer_store_b32(fp, rwin, 0u, fz_, 16);
                    } else asm volatile("s_waitcnt vmcnt(7)" ::: "memory");
                    if (jo < 8) SST(8 + jo);
                    if (vt && (!BSPARSE || jo % 6 == 2)) { const int s = jo; RT(0); RTS(); }
                    if (vt) { MT(3); const int s = i & 63; MTRT(); MTS(); }
                }
            }
            __builtin_amdgcn_s_setprio(0);
            VMWAIT();
            BAR();
        }
    }
    if (bid < 32 || bid >= NQT) {
        LANES();
        const int hh = (bid < 32 ? bid : bid - NQT) & 31;
        if (lay == 0 || xt) {
            if (bid >= NQT) wait_until(tstart + DQT);
            load_w<false>(wt, karg<5>(w >> 2), hh, w, 4 * (w & 3), 2, tokL);
            SST(5);
            if (!xt) bid = role(config());
        }
        places();
        const bool qt = bid >= NQT;
        const unsigned long long mq = xt ? MXQ : ~MXQ;
        auto nextq = [&](int j) { return min(seg_after(mq, j), NSEG - 1); };
        const int jq0 = qt ? nextq(1) : 0, jq1 = nextq(jq0);
        if (!qt) gather_mem0(memL, tid, 512);
        if (!qt && w == 7) clk_init(ck);
        if (tid < 4) *(unsigned*)(smem + L_S + tid * 4) = 0u;
        SST(1);
        if (w == 0) { wait_h(a, rwin, fb, jq1, hrdy); *(volatile LAS int*)(smem + L_S + 32) = hrdy; }
        BAR();
        hrdy = __builtin_amdgcn_readfirstlane(*(volatile LAS int*)(smem + L_S + 32));
        SST(2);
        stage_h(rws, jq0, L_H, tid);
        stage_h(rws, jq1, L_H + 40960, tid);
        VMWAIT();
        BAR();
        SST(3);
        int qrdy = 0;
        {
            LANES();
            const unsigned to0 = (unsigned)((w ^ 4) >> 2) * 8192u + (unsigned)((w & 3) * 64 + (lane ^ ((fq & 1) * (w < 4 ? 8 : 5)))) * 16u, to1 = to0 + 4096u, hv = lane * 16u;
            const LAS char* c0 = (const LAS char*)(hL + hoff + ((fq ^ (fr & 7)) << 4));
            const LAS char* c1 = (const LAS char*)(hL + hoff + (((4 + fq) ^ (fr & 7)) << 4));
            u32x4 bb[8], g0 = SENT, g1 = SENT;
            f32x4 tc[2][2];
            auto pack = [&]() {
                g0 = (u32x4){pack2(tc[0][0][0], tc[0][0][1]), pack2(tc[0][0][2], tc[0][0][3]), pack2(tc[1][0][0], tc[1][0][1]), pack2(tc[1][0][2], tc[1][0][3])};
                g1 = (u32x4){pack2(tc[0][1][0], tc[0][1][1]), pack2(tc[0][1][2], tc[0][1][3]), pack2(tc[1][1][0], tc[1][1][1]), pack2(tc[1][1][2], tc[1][1][3])};
            };
            const LAS char* n0 = c0 + 40960; const LAS char* n1 = c1 + 40960;
            if (!qt) {
                project0(bb, c0, c1);
                for (int j = 0; j < 2; ++j) {
                    project<0>(wt, tc, bb, c0, c1, n0, n1, []() {}, [](auto) {});
                    pack();
                    *(u32x4*)(tokL + j * 16384 + to0) = g0; *(u32x4*)(tokL + j * 16384 + to1) = g1;
                    c0 = n0; c1 = n1;
                }
                project_end(bb);
            } else {
                project0(bb, c0, c1);
                unsigned hs = O_H + (unsigned)(jq1 * 40960 + w * 1024), hd = (unsigned)(L_H + 40960 + w * 1024), fv = tid == 0 ? 0u : 0x40000000u;
                const unsigned fq_ = W_FQ + fb + (unsigned)(hh * 128 + xt * 64);
                const int jl = 63 - __builtin_clzll(mq);
                for (int jp = 1, j = jq0, jn = jq1;; jp = j, j = jn, jn = nextq(jn)) {
                    MT(0);
                    project<0>(wt, tc, bb, c0, c1, n0, n1,
                        [&]() {
                            wait_h(a, rwin, fb, nextq(jn), hrdy);
                            hs = O_H + (unsigned)(nextq(jn) * 40960 + w * 1024); hd = (unsigned)(2 * L_H + 40960 + 2 * w * 1024) - hd;
                            unsigned fj = (unsigned)jp;
                            asm volatile("" : "+s"(hs), "+s"(hd));
                            PIN1(fj);
                            MT(1);
                            VMWAIT();
                            BAR();
                            MT(2);
                            if (w == 0) __builtin_amdgcn_raw_buffer_store_b32(fj, rwin, fv, fq_, 16);
                        },
                        [&](auto uu) {
                            constexpr int u = decltype(uu)::v;
                            if (u == 34 || u == 38 || u == 2 || u == 6 || u == 10) { constexpr int t = ((u + 6) % 40) / 4; dma1k(rws, hv, hs + t * 8192u, hd + t * 8192u); }
                        });
                    pack();
                    const unsigned qs = O_QK + (unsigned)(j * H_ + hh) * 16384u;
                    st128(g0, rws, to0, qs, 16);
                    st128(g1, rws, to1, qs, 16);
                    { const LAS char* x = c0; c0 = n0; n0 = x; x = c1; c1 = n1; n1 = x; }
                    if (__builtin_expect(j <= QEARLY || j == jl, 0)) {
                        VMWAIT();
                        BAR0();
                        if (tid == 0) __builtin_amdgcn_raw_buffer_store_b32((unsigned)j, rwin, 0u, fq_, 16);
                        if (j == jl) break;
                    }
                    MT(3);
                    if (j < 8) SST(8 + j);
                    if (!BSPARSE || j % 6 == 2) { const int s = j; RT(0); RTS(); MTRT(); MTS(); }
                }
                project_end(bb);
                VMWAIT();
                BAR();
            }
        }
        SST(4);
        auto steps = [&](auto cc) {
        constexpr int C = decltype(cc)::v >> 1, R = decltype(cc)::v & 1;
        LANES();
        const LAS char* ka = (const LAS char*)(smem + L_K + (fr & 1) * TQS + (4 * fq + (fr >> 2)) * 8);
        const LAS char* qa = (const LAS char*)(smem + L_Q + (fr & 1) * 544 + ((fr >> 1) & 1) * 1152 + (16 * fq + 2 * (fr >> 2)) * 4);
        LAS char* qw = (LAS char*)(C < 2 ? smem + L_Q + (fq & 1) * 544 + (fq >> 1) * 1152 + (32 * w + 8 * (fr >> 2) + bs2(fr & 3)) * 4
                                         : smem + L_K + (fq >> 1) * TQS + (32 * (w & 3) + fr) * 8 + (fq & 1) * 4);
        const LAS char* mp0 = (const LAS char*)(memL + ma0);
        const int z = 16 * (fr >> 3) + 4 * (fr & 3) + 2 * ((fr >> 2) & 1);
        const int ko = ((z >> 4) * 256 + fq * 16 + ((z & 15) ^ ((fq & 1) * 5))) * 16;
        unsigned pa0 = (4u * C + 2u * (hh >> 4)) * PAIR + (unsigned)((hh & 15) * 512 + R * 256 + lane * 4);
        unsigned pa2 = ((8u + (fr >> 3)) * PAIR + C * 4096u + (unsigned)((hh >> 1) * 256 + R * 128 + (hh & 1) * 64 + fq * 16 + (fr & 6) * 2)) | (fr & 1) << 30;
        PIN2(ka, qw); PIN1(mp0);
        constexpr bool TK = C == 2 && R == 1;
        Tk tk = {0u, 0u, 0x40000000u, nullptr};
        if (TK) { tk = tk_init(); __builtin_amdgcn_s_setprio(TKP); }
        if (C < 2) { PIN2(qa, pa0); PIN1(pa2); }
        int qf_ = 0;
        int tbo = 0;
        for (int s = 0; s < NSEG; ++s) {
            RT(0);
            const char* tb = tokL + tbo;
            unsigned pb = __builtin_amdgcn_readfirstlane(s + 1 < NSEG ? W_P + (unsigned)(s & 3) * 10u * PAIR + nh : 0x40000000u);
            asm volatile("" : "+s"(pb));
            if (s == 0) BAR();
            u32x8 kx[2][2];
            if (C < 2) {
#pragma unroll
                for (int u = 0; u < 4; ++u) kx[u >> 1][u & 1] = cat8(*(const u32x4*)(tb + (ko ^ ((u >> 1) * 16)) + (u & 1) * 2048), *(const u32x4*)(tb + (ko ^ ((u >> 1) * 16)) + (u & 1) * 2048 + 1024));
                PIN2(kx[0][0], kx[0][1]); PIN2(kx[1][0], kx[1][1]);
            }
            bool redo = false;
            unsigned sf = 0;
            do {
                ++it;
                f32x4 a0 = z4, a1 = z4;
                PIN2(a0, a1);
                u32x4 m[10];
                if (C < 2) {
                    __builtin_amdgcn_s_setprio(2);
                    BAR();
                    MT(0);
#pragma unroll
                    for (int t = 0; t < NB1; ++t) { m[t] = *(const LAS u32x4*)(mp0 + t * 1024); SB(); }
                    BAR0();
#pragma unroll
                    for (int t = 0; t < 10; ++t) {
                        if (t == QB2) { BAR0(); MT(1); }
                        if (t + NB1 < 10) m[t + NB1] = *(const LAS u32x4*)(mp0 + (t + NB1) * 1024);
                        SB();
                        a0 = smfmac(m[t], wt[0][t], a0, sidx); a1 = smfmac(m[t], wt[1][t], a1, sidx);
                        PIN2(a0, a1);
                    }
                } else {
                    unsigned mx = 0;
                    asm volatile("" : "+{v[200:203]}"(a0), "+{v[204:207]}"(a1), "+{v208}"(mx));
                    unsigned lim = s > 0 ? poll_lim(budget) : ~0u, sr = tro + (unsigned)(s & 3) * 2u * PAIR;
                    asm volatile("" : "+s"(lim), "+s"(sr));
                    if (TK) fetch(tk, s, !redo, lim, DQC, (unsigned)hh * 128u, true);
                    else asm volatile("s_waitcnt lgkmcnt(0)\n\ts_barrier\n\ts_barrier\n\ts_barrier" ::: "memory");
                    asm volatile(
                        "ds_read_b128 v[212:215], %[mp] offset:0\n\t"
                        "ds_read_b128 v[216:219], %[mp] offset:1024\n\t"
                        "ds_read_b128 v[220:223], %[mp] offset:2048\n\t"
                        "ds_read_b128 v[224:227], %[mp] offset:3072\n\t"
                        "ds_read_b128 v[228:231], %[mp] offset:4096\n\t"
                        "ds_read_b128 v[232:235], %[mp] offset:5120\n\t"
                        "ds_read_b128 v[236:239], %[mp] offset:6144\n\t"
                        "ds_read_b128 v[240:243], %[mp] offset:7168\n\t"
                        "ds_read_b128 v[244:247], %[mp] offset:8192\n\t"
                        "ds_read_b128 v[248:251], %[mp] offset:9216\n\t"
                        MTA(4)
                        "s_waitcnt lgkmcnt(9)\n\t"
                        "v_smfmac_f32_16x16x64_bf16 v[200:203], v[212:215], %[w00], %[ix]\n\t"
                        "v_max3_u32 v208, v208, v212, v213\n\t"
                        "v_smfmac_f32_16x16x64_bf16 v[204:207], v[212:215], %[w10], %[ix]\n\t"
                        "s_waitcnt lgkmcnt(8)\n\t"
                        "v_max3_u32 v208, v208, v214, v215\n\t"
                        "v_smfmac_f32_16x16x64_bf16 v[200:203], v[216:219], %[w01], %[ix]\n\t"
                        "v_max3_u32 v208, v208, v216, v217\n\t"
                        "v_smfmac_f32_16x16x64_bf16 v[204:207], v[216:219], %[w11], %[ix]\n\t"
                        "s_waitcnt lgkmcnt(7)\n\t"
                        "v_max3_u32 v208, v208, v218, v219\n\t"
                        "v_smfmac_f32_16x16x64_bf16 v[200:203], v[220:223], %[w02], %[ix]\n\t"
                        "v_max3_u32 v208, v208, v220, v221\n\t"
                        "v_smfmac_f32_16x16x64_bf16 v[204:207], v[220:223], %[w12], %[ix]\n\t"
                        "s_waitcnt lgkmcnt(6)\n\t"
                        "v_max3_u32 v208, v208, v222, v223\n\t"
                        "v_smfmac_f32_16x16x64_bf16 v[200:203], v[224:227], %[w03], %[ix]\n\t"
                        "v_max3_u32 v208, v208, v224, v225\n\t"
                        "v_smfmac_f32_16x16x64_bf16 v[204:207], v[224:227], %[w13], %[ix]\n\t"
                        "s_waitcnt lgkmcnt(0)\n\t"
                        "v_max3_u32 v208, v208, v226, v227\n\t"
                        "v_smfmac_f32_16x16x64_bf16 v[200:203], v[228:231], %[w04], %[ix]\n\t"
                        "v_max3_u32 v208, v208, v228, v229\n\t"
                        "v_smfmac_f32_16x16x64_bf16 v[204:207], v[228:231], %[w14], %[ix]\n\t"
                        "v_max3_u32 v208, v208, v230, v231\n\t"
                        "v_smfmac_f32_16x16x64_bf16 v[200:203], v[232:235], %[w05], %[ix]\n\t"
                        "v_max3_u32 v208, v208, v232, v233\n\t"
                        "v_smfmac_f32_16x16x64_bf16 v[204:207], v[232:235], %[w15], %[ix]\n\t"
                        "v_max3_u32 v208, v208, v234, v235\n\t"
                        "v_smfmac_f32_16x16x64_bf16 v[200:203], v[236:239], %[w06], %[ix]\n\t"
                        "v_max3_u32 v208, v208, v236, v237\n\t"
                        "v_smfmac_f32_16x16x64_bf16 v[204:207], v[236:239], %[w16], %[ix]\n\t"
                        "v_max3_u32 v208, v208, v238, v239\n\t"
                        "v_smfmac_f32_16x16x64_bf16 v[200:203], v[240:243], %[w07], %[ix]\n\t"
                        "v_max3_u32 v208, v208, v240, v241\n\t"
                        "v_smfmac_f32_16x16x64_bf16 v[204:207], v[240:243], %[w17], %[ix]\n\t"
                        "v_max3_u32 v208, v208, v242, v243\n\t"
                        "v_smfmac_f32_16x16x64_bf16 v[200:203], v[244:247], %[w08], %[ix]\n\t"
                        "v_max3_u32 v208, v208, v244, v245\n\t"
                        "v_smfmac_f32_16x16x64_bf16 v[204:207], v[244:247], %[w18], %[ix]\n\t"
                        "v_max3_u32 v208, v208, v246, v247\n\t"
                        "v_smfmac_f32_16x16x64_bf16 v[200:203], v[248:251], %[w09], %[ix]\n\t"
                        "v_max3_u32 v208, v208, v248, v249\n\t"
                        "v_smfmac_f32_16x16x64_bf16 v[204:207], v[248:251], %[w19], %[ix]\n\t"
                        "v_max3_u32 v208, v208, v250, v251\n\t"
                        "buffer_load_dword %[sf], %[sl], %[rw], %[sr] offen sc1\n\t"
                        "v_cmp_lt_u32 vcc, %[lim], v208\n\t"
                        "s_nop 3\n\t"
                        "v_pk_add_f32 v[200:201], v[200:201], v[202:203]\n\t"
                        "v_pk_add_f32 v[204:205], v[204:205], v[206:207]\n\t"
                        "v_cvt_pk_bf16_f32 v200, v200, v201\n\t"
                        "v_cvt_pk_bf16_f32 v204, v204, v205\n\t"
                        "ds_write2_b32 %[qw], v200, v204 offset1:32\n\t"
                        MTA(2)
                        "s_cbranch_vccz 1f\n\t"
                        "v_mov_b32 v208, %[it]\n\t"
                        "v_mov_b32 v209, 0\n\t"
                        "ds_write_b32 v209, v208 offset:%[ls]\n\t"
                        "1:\n\t"
                        "s_waitcnt vmcnt(%[nv]) lgkmcnt(0)\n\t"
                        "s_barrier\n\t"
                        "s_waitcnt vmcnt(0)\n\t"
                        : [sf] "=&v"(sf), "+v"(qf_), "+{v[200:203]}"(a0), "+{v[204:207]}"(a1), "+{v208}"(mx)
                        : [mp] "v"(mp0), [qw] "v"(qw), [ix] "v"(sidx), [lim] "s"(lim), [it] "s"(it), [ls] "n"(L_S), [sl] "v"(tk.sl), [sr] "s"(sr), [rw] "s"(rwin), [nv] "n"(TK ? 2 : 1),
                          [w00] "v"(wt[0][0]), [w01] "v"(wt[0][1]), [w02] "v"(wt[0][2]), [w03] "v"(wt[0][3]), [w04] "v"(wt[0][4]), [w05] "v"(wt[0][5]), [w06] "v"(wt[0][6]), [w07] "v"(wt[0][7]), [w08] "v"(wt[0][8]), [w09] "v"(wt[0][9]), [w10] "v"(wt[1][0]), [w11] "v"(wt[1][1]), [w12] "v"(wt[1][2]), [w13] "v"(wt[1][3]), [w14] "v"(wt[1][4]), [w15] "v"(wt[1][5]), [w16] "v"(wt[1][6]), [w17] "v"(wt[1][7]), [w18] "v"(wt[1][8]), [w19] "v"(wt[1][9])
                        : "v209", "v212", "v213", "v214", "v215", "v216", "v217", "v218", "v219", "v220", "v221", "v222", "v223", "v224", "v225", "v226", "v227", "v228", "v229", "v230", "v231", "v232", "v233", "v234", "v235", "v236", "v237", "v238", "v239", "v240", "v241", "v242", "v243", "v244", "v245", "v246", "v247", "v248", "v249", "v250", "v251", "vcc", "memory");
                }
                if (C < 2) {
                    *(LAS unsigned*)qw = hpack(a0); *(LAS unsigned*)(qw + 16) = hpack(a1);
                    SB();
                    MT(2);
                    unsigned vd;
#define SC0 "v[212:215]"
#define SC1 "v[216:219]"
#define SC2 "v[220:223]"
#define DPP(op, d, ctl) op " " d ", " d ", " d " " ctl " row_mask:0xf bank_mask:0xf\n\t"
#define TAIL(A0, A2, B0, B2, C0, C2) \
                        "v_mov_b64 v[212:213], 0\n\tv_mov_b64 v[214:215], 0\n\tv_mov_b64 v[216:217], 0\n\tv_mov_b64 v[218:219], 0\n\tv_mov_b64 v[220:221], 0\n\tv_mov_b64 v[222:223], 0\n\t" \
                        "s_setprio 3\n\t" \
                        "s_waitcnt lgkmcnt(0)\n\t" \
                        "s_barrier\n\t" MTA(3) \
                        "ds_read_b64_tr_b16 v[224:225], %[qa]\n\t" \
                        "ds_read_b64_tr_b16 v[226:227], %[qa] offset:32\n\t" \
                        "ds_read_b64_tr_b16 v[228:229], %[qa] offset:256\n\t" \
                        "ds_read_b64_tr_b16 v[230:231], %[qa] offset:288\n\t" \
                        "ds_read_b64_tr_b16 v[232:233], %[ka]\n\t" \
                        "ds_read_b64_tr_b16 v[234:235], %[ka] offset:128\n\t" \
                        "ds_read_b64_tr_b16 v[236:237], %[ka] offset:256\n\t" \
                        "ds_read_b64_tr_b16 v[238:239], %[ka] offset:384\n\t" \
                        "ds_read_b64_tr_b16 v[240:241], %[ka] offset:512\n\t" \
                        "ds_read_b64_tr_b16 v[242:243], %[ka] offset:640\n\t" \
                        "s_waitcnt lgkmcnt(8)\n\t" \
                        "v_smfmac_f32_16x16x64_bf16 " SC0 ", v[224:227], %[k00], %[ix]\n\t" \
                        "ds_read_b64_tr_b16 v[244:245], %[ka] offset:768\n\t" \
                        "v_smfmac_f32_16x16x64_bf16 " SC1 ", v[224:227], %[k10], %[ix]\n\t" \
                        "ds_read_b64_tr_b16 v[246:247], %[ka] offset:896\n\t" \
                        "s_waitcnt lgkmcnt(8)\n\t" \
                        "v_smfmac_f32_16x16x64_bf16 " SC0 ", v[228:231], %[k01], %[ix]\n\t" \
                        "ds_read_b32 v248, %[vz] offset:%[ls]\n\t" \
                        "v_smfmac_f32_16x16x64_bf16 " SC1 ", v[228:231], %[k11], %[ix]\n\t" \
                        "s_waitcnt lgkmcnt(5)\n\t" \
                        "v_smfmac_f32_16x16x64_bf16 " SC2 ", v[224:227], v[232:239], %[ix]\n\t" \
                        "s_waitcnt lgkmcnt(1)\n\t" \
                        "v_smfmac_f32_16x16x64_bf16 " SC2 ", v[228:231], v[240:247], %[ix]\n\t" \
                        "s_waitcnt lgkmcnt(0)\n\t" \
                        MTA(4) "v_readfirstlane_b32 %[vd], v248\n\t" \
                        "v_add_f32 v232, " A0 ", " A2 "\n\t" \
                        "s_nop 0\n\t" \
                        "v_add_f32 v233, " B0 ", " B2 "\n\t" \
                        "v_max_f32 v235, v232, v233\n\t" \
                        "s_cmp_eq_u32 %[vd], %[it]\n\t" \
                        "s_cselect_b32 %[p0], 2.0, %[p0]\n\t" \
                        DPP("v_max_f32_dpp", "v235", "row_ror:8") \
                        "v_add_f32 v234, " C0 ", " C2 "\n\t" \
                        "v_max_f32 v235, v235, v234\n\t" \
                        "s_add_u32 %[p1], %[p0], 0x4000\n\t" \
                        "s_nop 0\n\t" DPP("v_max_f32_dpp", "v235", "row_ror:4") \
                        "s_nop 1\n\t" DPP("v_max_f32_dpp", "v235", "row_ror:2") \
                        "s_nop 1\n\t" DPP("v_max_f32_dpp", "v235", "row_ror:1") \
                        "v_mul_f32 v235, 0xbe0293ee, v235\n\t" \
                        "v_fmamk_f32 v232, v232, 0x3e0293ee, v235\n\t" \
                        "v_fmamk_f32 v233, v233, 0x3e0293ee, v235\n\t" \
                        "v_exp_f32 v232, v232\n\t" \
                        "v_exp_f32 v233, v233\n\t" \
                        "v_fmamk_f32 v234, v234, 0x3e0293ee, v235\n\t" \
                        "v_add_f32 v235, v232, v233\n\t" \
                        "v_exp_f32 v234, v234\n\t" \
                        "s_nop 0\n\t" DPP("v_add_f32_dpp", "v235", "row_ror:8") \
                        "v_add_f32 v235, v235, v234\n\t" \
                        "v_mov_b32_dpp v236, v234 quad_perm:[1,1,3,3] row_mask:0xf bank_mask:0xf\n\t" \
                        "s_nop 0\n\t" DPP("v_add_f32_dpp", "v235", "row_ror:4") \
                        "s_nop 1\n\t" DPP("v_add_f32_dpp", "v235", "row_ror:2") \
                        "s_nop 1\n\t" DPP("v_add_f32_dpp", "v235", "row_ror:1") \
                        "v_rcp_f32 v235, v235\n\t" \
                        "s_nop 0\n\t" \
                        "v_mul_f32_e64 v232, v232, v235 clamp\n\t" \
                        "v_mul_f32_e64 v233, v233, v235 clamp\n\t" \
                        "v_cvt_pk_bf16_f32 v232, v232, v233\n\t" \
                        MTA(5) "buffer_store_dword v232, %[pa], %[rw], %[p0] offen sc1\n\t" \
                        "v_mul_f32_e64 v234, v234, v235 clamp\n\t" \
                        "v_mul_f32_e64 v236, v236, v235 clamp\n\t" \
                        "buffer_store_dword v232, %[pa], %[rw], %[p1] offen sc1\n\t" \
                        "v_cvt_pk_bf16_f32 v234, v234, v236\n\t" \
                        "buffer_store_dword v234, %[pm], %[rw], %[p0] offen sc1\n\t" \
                        "s_setprio 0"
#define TAILIO : [vd] "=&s"(vd), [p0] "+s"(ps), [p1] "=&s"(ps1) \
                        : [qa] "v"(qa), [ka] "v"(ka), [vz] "v"(0u), [ls] "n"(L_S), [ix] "v"(sidx), [it] "s"(it), [pa] "v"(pa0), [pm] "v"(pa2), [rw] "s"(rwin), \
                          [k00] "v"(kx[0][0]), [k01] "v"(kx[0][1]), [k10] "v"(kx[1][0]), [k11] "v"(kx[1][1]) \
                        : "v212", "v213", "v214", "v215", "v216", "v217", "v218", "v219", "v220", "v221", "v222", "v223", "v224", "v225", "v226", "v227", "v228", "v229", "v230", "v231", "v232", \
                          "v233", "v234", "v235", "v236", "v237", "v238", "v239", "v240", "v241", "v242", "v243", "v244", "v245", "v246", "v247", "v248", "scc", "memory"
                    unsigned ps = pb, ps1;
                    if (R == 0) asm volatile(TAIL("v212", "v214", "v216", "v218", "v220", "v222") TAILIO);
                    else asm volatile(TAIL("v213", "v215", "v217", "v219", "v221", "v223") TAILIO);
                    redo = vd == it;
                    RT(4);
                    MT(6);
                } else {
                    MT(3);
                    if (TK) { __builtin_amdgcn_s_setprio(TKP); if (TKS) __builtin_amdgcn_s_sleep(TKS); }
                    redo = __builtin_amdgcn_readfirstlane(*(volatile LAS unsigned*)(smem + L_S)) == it;
                    if (redo) FC(fc0);
                }
            } while (__builtin_expect(redo, 0));
            if (s < 8) SST(8 + s);
            if (s >= 56) SST(s - 40);
            if (C + R == 1) {
                if (s > 0) {
                    __builtin_amdgcn_s_sleep(DDMA);
                    unsigned l2 = fresh_lane(), s1;
                    asm volatile("v_mov_b32 %0, -1" : "=v"(s1));
                    const unsigned base = W_P + (unsigned)((s - 1) & 3) * 10u * PAIR + nh;
                    asm volatile("s_waitcnt vmcnt(3)" ::: "memory");
                    st128(((u32x4){s1, s1, s1, s1}), rwin, (l2 >> 5) * PAIR + (l2 & 31u) * 16u, base + (unsigned)(R * 4 + 2 * (hh >> 4)) * PAIR + (unsigned)(hh & 15) * 512u, 16);
                    if (R) __builtin_amdgcn_raw_buffer_store_b64((u32x2){s1, s1}, rwin, (l2 >> 5) * PAIR + (l2 & 16u) * 256u + (l2 & 8u) * 16u + (l2 & 7u) * 8u, base + 8u * PAIR + (unsigned)((hh >> 1) * 256 + (hh & 1) * 64), 16);
                    else __builtin_amdgcn_raw_buffer_store_b32(s1, rwin, two(), W_T + (unsigned)((s + 2) & 3) * 2u * PAIR + (unsigned)hh * 128u, 16);
                }
            } else if (TK) {
                if (s + 1 < NSEG) sched2(tk, s, sf);
            } else if (C == 2 && w == 4) {
                if (s + 2 < NSEG) {
                    qrdy = max(qrdy, seg_done(MXQ, __builtin_amdgcn_readlane(qf_, 0), __builtin_amdgcn_readlane(qf_, 1)));
                    if (__builtin_expect(qrdy < s + 2, 0)) for (unsigned spins = 0;;) {
                        asm volatile("" ::: "memory");
                        const int f = (int)__builtin_amdgcn_raw_buffer_load_b32(rwin, (unsigned)(lane & 1) * 64u, W_FQ + fb + (unsigned)hh * 128u, 16);
                        qrdy = seg_done(MXQ, __builtin_amdgcn_readlane(f, 0), __builtin_amdgcn_readlane(f, 1));
                        if (qrdy >= s + 2) break;
                        FC(fc2);
                        if (spin_fail(spins, a.ctl)) break;
                        __builtin_amdgcn_s_sleep(2);
                    }
                    const unsigned so = O_QK + (unsigned)(((s + 2) * H_ + hh) * 16384), lo = (unsigned)(L_TOK + (tbo ? tbo - 16384 : 32768)), l2 = fresh_lane();
#pragma unroll
                    for (int i = 0; i < 4; ++i) dma4k(rws, l2 * 16u, so + i * 4096u, lo + i * 4096u);
                    if (qrdy < min(s + 5, NSEG - 1))
                        asm volatile("buffer_load_dword %0, %1, %2, %3 offen sc1" : "+v"(qf_) : "v"((l2 & 1u) * 64u), "s"(rwin), "s"(W_FQ + fb + (unsigned)hh * 128u) : "memory");
                }
            } else {
                asm volatile("s_waitcnt vmcnt(3)" ::: "memory");
                if (s > TAILS) __builtin_amdgcn_raw_buffer_store_b32((unsigned)s - 1u, rwin, 0u, W_FPT + fb + (unsigned)(hh * 128 + (w & 3) * 4), 16);
                if (C == 2 && s + 1 < NSEG) __builtin_amdgcn_s_sleep(DPT);
                const float scale = 0.08838834764831845f, l2e = 1.44269504088896340736f * (NORND ? scale : 1.f);
                const int tq = (w >> 1) & 1, qh = w & 1;
                f32x4 sc[3] = {z4, z4, z4};
                u32x4 kt[2][4], qf[4], kf[4];
                const unsigned l2 = fresh_lane(), kto = ((l2 & 8u) * 512u + (l2 & 51u) * 16u + (l2 & 4u) * 32u) ^ ((l2 & 16u) * 5u), qto = ((l2 & 55u) * 16u + qh * 128u) ^ ((l2 & 16u) * 8u);
#pragma unroll
                for (int ks = 0; ks < 4; ++ks) {
                    qf[ks] = *(const u32x4*)(tb + 8192 + (tq * 4 + ks) * 1024 + qto);
                    kt[0][ks] = *(const u32x4*)(tb + ks * 1024 + kto);
                    kt[1][ks] = *(const u32x4*)(tb + ks * 1024 + (kto ^ 64u));
                    SB();
                }
#pragma unroll
                for (int ks = 0; ks < 4; ++ks) {
                    sc[0] = mfma32(kt[0][ks], qf[ks], sc[0]); PIN1(sc[0]);
                    kf[ks] = ldtr2(ka + ks * 256, ka + ks * 256 + 128);
                    SB();
                    sc[1] = mfma32(kt[1][ks], qf[ks], sc[1]); PIN1(sc[1]);
                }
#pragma unroll
                for (int ks = 0; ks < 4; ++ks) sc[2] = mfma32(kf[ks], qf[ks], sc[2]);
                const bool hi = (l2 & 8u) != 0u;
                float v[6];
#pragma unroll
                for (int r = 0; r < 4; ++r) v[r] = hi ? sc[1][r] : sc[0][r];
                v[4] = hi ? sc[2][2] : sc[2][0]; v[5] = hi ? sc[2][3] : sc[2][1];
#pragma unroll
                for (int r = 0; r < 6; r += 2) {
                    if (NORND) break;
                    const unsigned b0 = pack2(v[r], v[r + 1]);
                    const unsigned b1 = pack2(lo2f(b0) * scale, hi2f(b0) * scale);
                    v[r] = lo2f(b1); v[r + 1] = hi2f(b1);
                }
                const int lim = 8 * (w & 3) + (int)(l2 & 7u) - 8 * (int)(l2 >> 4) - (int)((l2 & 8u) >> 1);
#pragma unroll
                for (int r = 0; r < 4; ++r) if (r > lim) v[r] = NORND ? -1000000000.0f / scale : bfr(-1000000000.0f);
                float mx = fmx(fmx(fmx(fmx(fmx(v[0], v[1]), v[2]), v[3]), v[4]), v[5]);
                mx = xmax3216(fmx(mx, ror8f(mx)));
                const float mb = -mx * l2e;
#pragma unroll
                for (int r = 0; r < 6; ++r) v[r] = __builtin_amdgcn_exp2f(fmaf(v[r], l2e, mb));
                float sum = fmaf(v[4] + v[5], 0.5f, (v[0] + v[1]) + (v[2] + v[3]));
                sum = xsum3216(sum + ror8f(sum));
                const float inv = __builtin_amdgcn_rcpf(sum);
                const unsigned p0 = pack2(v[0] * inv, v[1] * inv), p1 = nosent1(pack2(v[2] * inv, v[3] * inv)), p2 = nosent1(pack2(v[4] * inv, v[5] * inv));
                const unsigned base = s + 1 < NSEG ? O_PT + (unsigned)(s * PTSEG) * 2u : W_PL + fb;
                const __amdgpu_buffer_rsrc_t rp = s + 1 < NSEG ? rws : rwin;
                const unsigned oob = (l2 & 8u) << 27;
                st128(((u32x4){p0, p1, dppu<0x128>(p0), dppu<0x128>(p1)}), rp, (l2 * 16u) | oob, base + (unsigned)((hh * 2 + tq) * 1024 + qh * 128), 16);
                __builtin_amdgcn_raw_buffer_store_b64((u32x2){p2, dppu<0x128>(p2)}, rp, ((l2 & 7u) * 16u + (l2 >> 4) * 8u) | oob | (l2 & 32u) << 25, base + 65536u + (unsigned)((tq * 32 + hh) * 256 + qh * 128), 16);
            }
            tbo = tbo == 32768 ? 0 : tbo + 16384;
            if (C == 2 && __builtin_expect(budget <= 0, 0)) __hip_atomic_store(a.ctl, 1u, __ATOMIC_RELAXED, __HIP_MEMORY_SCOPE_AGENT);
            RT(5);
            RTS();
            MT(7);
            MTS();
        }
        };
        if (qt) {} else if (w == 0) steps(IC<0>()); else if (w == 1) steps(IC<1>()); else if (w == 2) steps(IC<2>()); else if (w == 3) steps(IC<3>()); else if (w < 7) steps(IC<4>()); else steps(IC<5>());
        __builtin_amdgcn_s_setprio(0);
    }
    SST(31);
    }
    } else
#pragma nounroll
    for (; lay < 2; ++lay) {
    fresh();
    SST(0);
    {
        LANES();
        const int fi = bid >= NWRK ? 255 - bid : NQT + 15 - bid;
        u32x2 tag = {0u, 0u};
        if (lay == 0) tag = get_tag();
        auto miss = [&](unsigned vo, unsigned so) {
            asm volatile("" ::: "memory");
            const u32x4 f = __builtin_amdgcn_raw_buffer_load_b128(rwin, vo, so, 16);
            return __ballot(((f[0] ^ tag[0]) | (f[1] ^ tag[1]) | (f[2] ^ tag[0]) | (f[3] ^ tag[1])) != 0u);
        };
        if (lay == 0) for (unsigned spins = 0; spins < SPIN_CAP && miss((lane & 7) * 1024u, W_ST + w * 128u + (fi >= 15 ? 64u : 0u)); ++spins) __builtin_amdgcn_s_sleep(ASLP);
        BAR();
        if (fi >= 15) {
            unsigned m = 255u;
            a2_unit(rws, 95 - fi, smem, w, lane, [&](int r) {
                for (unsigned spins = 0; spins < SPIN_CAP && (m & (r == 7 ? 128u : 127u)); ++spins) {
                    if (spins) __builtin_amdgcn_s_sleep(ASLP);
                    m = (unsigned)miss((r == 7 ? 7 : lane & 7) * 1024u, (lay ? W_FA : W_ST) + w * 128u);
                }
                if (r == 7) SST(5);
            }, [&](int k) { if (bid < NREC) SST(8 + k); });
            SST(6);
            VMWAIT();
            __syncthreads();
            if (tid == 0) __builtin_amdgcn_raw_buffer_store_b32(0u, rwin, 0u, W_FB + fb + (unsigned)(((95 - fi) >> 2) * 128 + ((95 - fi) & 3) * 4), 16);
            SST(7);
        }
        const RowTw tw = row_tw(lane);
        if (w == 0) for (unsigned spins = 0;;) {
            asm volatile("" ::: "memory");
            const u32x4 f = __builtin_amdgcn_raw_buffer_load_b128(rwin, (unsigned)min(lane, 20) * 128u, W_FB + fb, 16);
            if (!__any((f[0] | (lane < 20 ? f[1] | f[2] | f[3] : 0u)) != 0u)) break;
            if (spin_fail(spins, a.ctl)) break;
            __builtin_amdgcn_s_sleep(ASLP);
        }
        __syncthreads();
        SST(3);
        const __amdgpu_buffer_rsrc_t rx = mkrsrc(lay ? ws + OFF_X2 : karg<3>(), S_ * D_ * 2);
        auto st3 = [&](int k) { if (bid < NREC || bid >= NWRK) SST(16 + k); };
        if (fi < 16) {
            a3_unit<4>(rws, rx, fi * 8, smem, tw, w, lane, st3);
            st3(5);
            VMWAIT();
            BAR0();
            if (tid == 0) __builtin_amdgcn_raw_buffer_store_b32(0u, rwin, 0u, W_H + fb + (unsigned)((fi >> 2) * 128 + (fi & 3) * 4), 16);
        } else for (int u = fi - 8; u < 128; u += 80) {
            a3_unit<8>(rws, rx, u * 16, smem, tw, w, lane, st3);
            st3(5);
            VMWAIT();
            BAR0();
            if (tid == 0) __builtin_amdgcn_raw_buffer_store_b64((u32x2){0u, 0u}, rwin, 0u, W_H + fb + (unsigned)((u >> 1) * 128 + (u & 1) * 8), 16);
        }
        SST(1);
        if (lay == 0 && bid < NREC) config();
        places();
    }
    const __amdgpu_buffer_rsrc_t rout = mkrsrc(lay ? karg<4>() : ws + OFF_X2, S_ * D_ * 2);
    if (bid < NREC) {
        LANES();
        const int o = bid - 96, hg = w & 3;
        const unsigned ztoff = (unsigned)((o * H_ + 8 * hg) * 64 + lane) * 16u;
        const unsigned zoff = zpair(o / 10, o % 10) + (unsigned)(lane * 16);
        const unsigned poff = (4u * (mc >> 1) + 2u * (hg >> 1) + (mc & 1u)) * PAIR + nh + (unsigned)((hg & 1) * 4096 + (fq >> 1) * 512 + (fr >> 2) * 128 + (fr & 1) * 64 + (fq & 1) * 32 + (fr & 2) * 8);
        const unsigned pmoff = (8u + (mc & 1u)) * PAIR + nh + (mc >> 1) * 4096u + (unsigned)(fq * 256 + (fr >> 3) * 128 + (fr & 2) * 32 + (fr & 4) * 8 + (fr & 1) * 16);
        const unsigned fzo = (unsigned)(8 * hg + (lane & 7)) * 128u + (unsigned)(o / 10) * 4u + (lane & 8) * 8u;
        const unsigned moff = (o < 32 ? (unsigned)o * 256u : 4u * PAIR + (unsigned)(o - 32) * 256u) + nh + (unsigned)(lane * 4);
        const unsigned mc2 = o < 32 ? 2u * PAIR : 2048u;
        char* op = smem + 8192;
        SST(2);
        if (w == 7) clk_init(ck);
        if (w < 4) {
            u32x8 zf[4];
            int zr = -1;
            unsigned zso = O_Z;
            const unsigned ra = (o < 32 ? (unsigned)fq * PAIR + (unsigned)o * 256u : (4u + (fq & 1)) * PAIR + (unsigned)(fq >> 1) * 2048u + (unsigned)(o - 32) * 256u) + nh + (unsigned)(fr * 16);
            auto zt_miss = [&](int f, int s) { return __any(f < s && ((lane >> 3) & 1) == (int)((MXV >> s) & 1)); };
            for (int s = 0;; ++s) {

                const unsigned pb = W_P + (unsigned)(s & 3) * 10u * PAIR;
                if (__builtin_expect(zr < s, 0)) for (unsigned spins = 0;;) {
                    asm volatile("" ::: "memory");
                    if (!zt_miss((int)__builtin_amdgcn_raw_buffer_load_b32(rwin, fzo, W_FZ + fb, 16), s)) break;
                    FC(fc2);
                    if (spin_fail(spins, a.ctl)) break;
                    __builtin_amdgcn_s_sleep(ZPS);
                }
                if (s == 0) SST(1);
                asm volatile(
                    "buffer_load_dwordx4 v[160:163], %[zt], %[rs], %[so] offen sc1\n\t"
                    "buffer_load_dwordx4 v[164:167], %[zt], %[rs], %[so] offen offset:1024 sc1\n\t"
                    "buffer_load_dwordx4 v[168:171], %[zt], %[rs], %[so] offen offset:2048 sc1\n\t"
                    "buffer_load_dwordx4 v[172:175], %[zt], %[rs], %[so] offen offset:3072 sc1\n\t"
                    "buffer_load_dwordx4 v[176:179], %[zt], %[rs], %[s4] offen sc1\n\t"
                    "buffer_load_dwordx4 v[180:183], %[zt], %[rs], %[s4] offen offset:1024 sc1\n\t"
                    "buffer_load_dwordx4 v[184:187], %[zt], %[rs], %[s4] offen offset:2048 sc1\n\t"
                    "buffer_load_dwordx4 v[188:191], %[zt], %[rs], %[s4] offen offset:3072 sc1"
                    : "=&{v[160:167]}"(zf[0]), "=&{v[168:175]}"(zf[1]), "=&{v[176:183]}"(zf[2]), "=&{v[184:191]}"(zf[3])
                    : [zt] "v"(ztoff), [rs] "s"(rws), [so] "s"(zso), [s4] "s"(zso + 4096u) : "memory");
                zso = O_Z + (unsigned)((s + 1) * ZTSEG) * 2u;
                if (__builtin_expect(budget <= 0, 0)) __hip_atomic_store(a.ctl, 1u, __ATOMIC_RELAXED, __HIP_MEMORY_SCOPE_AGENT);
                RT(6);
                if (s + 1 == NSEG) break;
                const unsigned spa = (unsigned)((s & 1) * 2048 + (w < 3 ? w * 512 : 0) + lane * 8);
#define OST(r)
                unsigned long long t0_, t1_, t2_, t3_, t4_ = 0, bad;
                int tries = budget;
                unsigned po = poff, pm = pmoff, x0 = 4u * PAIR, x1 = 4096u;
                const unsigned mb = W_MEM + (unsigned)((s + 1) & 3) * 6u * PAIR;
                if (w < 3) asm volatile(
                    "s_barrier\n\t"
                    OST("%[t0]")
                    "v_mov_b64 v[96:97], 0\n\t"
                    "v_mov_b64 v[98:99], 0\n\t"
                    "v_mov_b32 v100, 0\n\t"
                    "s_barrier\n\t"
                    "1:\n\t"
                    "buffer_load_dwordx4 v[208:211], %[po], %[rw], %[pb] offen sc1\n\t"
                    "buffer_load_dwordx4 v[212:215], %[po], %[rw], %[pb] offen offset:1024 sc1\n\t"
                    "buffer_load_dwordx4 v[216:219], %[po], %[rw], %[pb] offen offset:2048 sc1\n\t"
                    "buffer_load_dwordx4 v[220:223], %[po], %[rw], %[pb] offen offset:3072 sc1\n\t"
                    OST("%[t1]")
                    "s_waitcnt vmcnt(3)\n\t"
                    OST("%[t3]")
                    "v_smfmac_f32_16x16x64_bf16 v[96:99], v[208:211], v[160:167], %[ix]\n\t"
                    "v_max3_u32 v100, v100, v208, v209\n\t"
                    "v_max3_u32 v100, v100, v210, v211\n\t"
                    "s_waitcnt vmcnt(2)\n\t"
                    "v_smfmac_f32_16x16x64_bf16 v[96:99], v[212:215], v[168:175], %[ix]\n\t"
                    "v_max3_u32 v100, v100, v212, v213\n\t"
                    "v_max3_u32 v100, v100, v214, v215\n\t"
                    "s_waitcnt vmcnt(1)\n\t"
                    "v_smfmac_f32_16x16x64_bf16 v[96:99], v[216:219], v[176:183], %[ix]\n\t"
                    "v_max3_u32 v100, v100, v216, v217\n\t"
                    "v_max3_u32 v100, v100, v218, v219\n\t"
                    "s_waitcnt vmcnt(0)\n\t"
                    "v_smfmac_f32_16x16x64_bf16 v[96:99], v[220:223], v[184:191], %[ix]\n\t"
                    "v_max3_u32 v100, v100, v220, v221\n\t"
                    "v_max3_u32 v100, v100, v222, v223\n\t"
                    "v_cmp_eq_u32 vcc, -1, v100\n\t"
                    OST("%[t2]")
                    "s_nop 4\n\t"
                    "v_pk_add_f32 v[96:97], v[96:97], v[98:99]\n\t"
                    "ds_write_b64 %[sp], v[96:97]\n\t"
                    "s_cbranch_vccz 2f\n\t"
                    "s_sub_i32 %[tr], %[tr], 1\n\t"
                    "s_cmp_lt_i32 %[tr], 1\n\t"
                    "s_cbranch_scc1 2f\n\t"
                    "v_xor_b32 %[po], %[x0], %[po]\n\t"
                    "s_xor_b32 %[x0], %[x0], 0x14000\n\t"
                    "s_waitcnt lgkmcnt(0)\n\t"
                    "v_mov_b64 v[96:97], 0\n\t"
                    "v_mov_b64 v[98:99], 0\n\t"
                    "v_mov_b32 v100, 0\n\t"
                    "s_branch 1b\n\t"
                    "2:\n\t"
                    "s_waitcnt lgkmcnt(0)\n\t"
                    "s_barrier\n\t"
                    "s_sleep 3\n\t"
                    : [tr] "+s"(tries), [po] "+v"(po), [x0] "+s"(x0), [t0] "=&s"(t0_), [t1] "=&s"(t1_), [t2] "=&s"(t2_), [t3] "=&s"(t3_)
                    : [sp] "v"(spa), [ix] "v"(sidx), [rw] "s"(rwin), [pb] "s"(pb), "{v[160:167]}"(zf[0]), "{v[168:175]}"(zf[1]), "{v[176:183]}"(zf[2]), "{v[184:191]}"(zf[3])
                    : "v96", "v97", "v98", "v99", "v100", "v208", "v209", "v210", "v211", "v212", "v213", "v214", "v215", "v216", "v217", "v218", "v219", "v220", "v221", "v222", "v223", "vcc", "scc", "memory");
                else asm volatile(
                    "s_barrier\n\t"
                    "buffer_load_dwordx4 v[128:131], %[zo], %[rw], %[zb] offen sc1\n\t"
                    "buffer_load_dwordx4 v[132:135], %[zo], %[rw], %[zb] offen offset:1024 sc1\n\t"
                    "buffer_load_dwordx4 v[136:139], %[zo], %[rw], %[zb] offen offset:2048 sc1\n\t"
                    "buffer_load_dwordx4 v[140:143], %[zo], %[rw], %[zb] offen offset:3072 sc1\n\t"
                    "buffer_load_dwordx4 v[144:147], %[zo], %[rw], %[z4] offen sc1\n\t"
                    "buffer_load_dwordx4 v[148:151], %[zo], %[rw], %[z4] offen offset:1024 sc1\n\t"
                    "buffer_load_dwordx4 v[152:155], %[zo], %[rw], %[z4] offen offset:2048 sc1\n\t"
                    "buffer_load_dwordx4 v[156:159], %[zo], %[rw], %[z4] offen offset:3072 sc1\n\t"
                    OST("%[t0]")
                    "v_mov_b64 v[96:97], 0\n\t"
                    "v_mov_b64 v[98:99], 0\n\t"
                    "v_mov_b32 v100, 0\n\t"
                    "s_barrier\n\t"
                    "s_sleep %[fd]\n\t"
                    "buffer_load_dwordx4 v[208:211], %[po], %[rw], %[pb] offen sc1\n\t"
                    "buffer_load_dwordx4 v[212:215], %[po], %[rw], %[pb] offen offset:1024 sc1\n\t"
                    "buffer_load_dwordx4 v[216:219], %[po], %[rw], %[pb] offen offset:2048 sc1\n\t"
                    "buffer_load_dwordx4 v[220:223], %[po], %[rw], %[pb] offen offset:3072 sc1\n\t"
                    "buffer_load_dwordx4 v[224:227], %[pm], %[rw], %[pb] offen sc1\n\t"
                    "buffer_load_dwordx4 v[228:231], %[pm], %[rw], %[pb] offen offset:1024 sc1\n\t"
                    "buffer_load_dwordx4 v[232:235], %[pm], %[rw], %[pb] offen offset:2048 sc1\n\t"
                    "buffer_load_dwordx4 v[236:239], %[pm], %[rw], %[pb] offen offset:3072 sc1\n\t"
                    OST("%[t1]")
                    "s_waitcnt vmcnt(8)\n\t"
                    "v_max3_u32 v101, v129, v131, v133\n\t"
                    "v_max3_u32 v101, v101, v135, v137\n\t"
                    "v_max3_u32 v101, v101, v139, v141\n\t"
                    "v_max3_u32 v101, v101, v143, v145\n\t"
                    "v_max3_u32 v101, v101, v147, v149\n\t"
                    "v_max3_u32 v101, v101, v151, v153\n\t"
                    "v_max3_u32 v101, v101, v155, v157\n\t"
                    "v_max_u32 v101, v101, v159\n\t"
                    "v_cmp_eq_u32 vcc, -1, v101\n\t"
                    "s_cbranch_vccz 3f\n\t"
                    "buffer_load_dwordx4 v[128:131], %[zo], %[rw], %[zb] offen sc1\n\t"
                    "buffer_load_dwordx4 v[132:135], %[zo], %[rw], %[zb] offen offset:1024 sc1\n\t"
                    "buffer_load_dwordx4 v[136:139], %[zo], %[rw], %[zb] offen offset:2048 sc1\n\t"
                    "buffer_load_dwordx4 v[140:143], %[zo], %[rw], %[zb] offen offset:3072 sc1\n\t"
                    "buffer_load_dwordx4 v[144:147], %[zo], %[rw], %[z4] offen sc1\n\t"
                    "buffer_load_dwordx4 v[148:151], %[zo], %[rw], %[z4] offen offset:1024 sc1\n\t"
                    "buffer_load_dwordx4 v[152:155], %[zo], %[rw], %[z4] offen offset:2048 sc1\n\t"
                    "buffer_load_dwordx4 v[156:159], %[zo], %[rw], %[z4] offen offset:3072 sc1\n\t"
                    "s_waitcnt vmcnt(0)\n\t"
                    "v_max3_u32 v101, v129, v131, v133\n\t"
                    "v_max3_u32 v101, v101, v135, v137\n\t"
                    "v_max3_u32 v101, v101, v139, v141\n\t"
                    "v_max3_u32 v101, v101, v143, v145\n\t"
                    "v_max3_u32 v101, v101, v147, v149\n\t"
                    "v_max3_u32 v101, v101, v151, v153\n\t"
                    "v_max3_u32 v101, v101, v155, v157\n\t"
                    "v_max_u32 v101, v101, v159\n\t"
                    "v_mov_b32 v100, v101\n\t"
                    "3:\n\t"
                    "v_permlane16_swap_b32 v128, v129\n\t"
                    "v_permlane16_swap_b32 v130, v131\n\t"
                    "v_permlane16_swap_b32 v132, v133\n\t"
                    "v_permlane16_swap_b32 v134, v135\n\t"
                    "v_permlane16_swap_b32 v136, v137\n\t"
                    "v_permlane16_swap_b32 v138, v139\n\t"
                    "v_permlane16_swap_b32 v140, v141\n\t"
                    "v_permlane16_swap_b32 v142, v143\n\t"
                    "v_permlane16_swap_b32 v144, v145\n\t"
                    "v_permlane16_swap_b32 v146, v147\n\t"
                    "v_permlane16_swap_b32 v148, v149\n\t"
                    "v_permlane16_swap_b32 v150, v151\n\t"
                    "v_permlane16_swap_b32 v152, v153\n\t"
                    "v_permlane16_swap_b32 v154, v155\n\t"
                    "v_permlane16_swap_b32 v156, v157\n\t"
                    "v_permlane16_swap_b32 v158, v159\n\t"
                    "s_mov_b32 vcc_lo, 0x00ff00ff\n\t"
                    "s_mov_b32 vcc_hi, 0x00ff00ff\n\t"
                    "s_waitcnt vmcnt(7)\n\t"
                    OST("%[t3]")
                    "v_smfmac_f32_16x16x64_bf16 v[96:99], v[208:211], v[160:167], %[ix]\n\t"
                    "v_max3_u32 v100, v100, v208, v209\n\t"
                    "v_max3_u32 v100, v100, v210, v211\n\t"
                    "s_waitcnt vmcnt(6)\n\t"
                    "v_smfmac_f32_16x16x64_bf16 v[96:99], v[212:215], v[168:175], %[ix]\n\t"
                    "v_max3_u32 v100, v100, v212, v213\n\t"
                    "v_max3_u32 v100, v100, v214, v215\n\t"
                    "s_waitcnt vmcnt(5)\n\t"
                    "v_smfmac_f32_16x16x64_bf16 v[96:99], v[216:219], v[176:183], %[ix]\n\t"
                    "v_max3_u32 v100, v100, v216, v217\n\t"
                    "v_max3_u32 v100, v100, v218, v219\n\t"
                    "s_waitcnt vmcnt(4)\n\t"
                    "v_smfmac_f32_16x16x64_bf16 v[96:99], v[220:223], v[184:191], %[ix]\n\t"
                    "v_max3_u32 v100, v100, v220, v221\n\t"
                    "v_max3_u32 v100, v100, v222, v223\n\t"
                    "s_waitcnt vmcnt(3)\n\t"
                    "v_smfmac_f32_16x16x64_bf16 v[96:99], v[224:227], v[128:135], %[ix]\n\t"
                    "v_max3_u32 v100, v100, v224, v225\n\t"
                    "v_max3_u32 v100, v100, v226, v227\n\t"
                    "s_waitcnt vmcnt(2)\n\t"
                    "v_smfmac_f32_16x16x64_bf16 v[96:99], v[228:231], v[136:143], %[ix]\n\t"
                    "s_barrier\n\t"
                    "ds_read_b64 v[102:103], %[sp]\n\t"
                    "ds_read_b64 v[104:105], %[sp] offset:512\n\t"
                    "ds_read_b64 v[106:107], %[sp] offset:1024\n\t"
                    "v_max3_u32 v100, v100, v228, v229\n\t"
                    "v_max3_u32 v100, v100, v230, v231\n\t"
                    "s_waitcnt vmcnt(1)\n\t"
                    "v_smfmac_f32_16x16x64_bf16 v[96:99], v[232:235], v[144:151], %[ix]\n\t"
                    "v_max3_u32 v100, v100, v232, v233\n\t"
                    "v_max3_u32 v100, v100, v234, v235\n\t"
                    "s_waitcnt vmcnt(0)\n\t"
                    "v_smfmac_f32_16x16x64_bf16 v[96:99], v[236:239], v[152:159], %[ix]\n\t"
                    "v_max3_u32 v100, v100, v236, v237\n\t"
                    "v_max3_u32 v100, v100, v238, v239\n\t"
                    OST("%[t2]")
                    "v_cmp_eq_u32 %[bad], -1, v100\n\t"
                    "s_waitcnt lgkmcnt(0)\n\t"
                    "v_add_f32 v102, v102, v104\n\t"
                    "v_add_f32 v103, v103, v105\n\t"
                    "v_add_f32 v102, v102, v106\n\t"
                    "v_add_f32 v103, v103, v107\n\t"
                    "v_add_f32 v96, v96, v102\n\t"
                    "v_add_f32 v97, v97, v103\n\t"
                    "v_add_f32 v96, v96, v98\n\t"
                    "v_add_f32 v97, v97, v99\n\t"
                    "s_cmp_lg_u64 %[bad], 0\n\t"
                    "s_cbranch_scc1 4f\n\t"
                    "2:\n\t"
                    "v_cndmask_b32_dpp v98, v97, v96, vcc row_ror:8 row_mask:0xf bank_mask:0xf\n\t"
                    "s_not_b64 vcc, vcc\n\t"
                    "v_cndmask_b32_dpp v99, v96, v97, vcc row_ror:8 row_mask:0xf bank_mask:0xf\n\t"
                    "v_cvt_pk_bf16_f32 v98, v98, v99\n\t"
                    "v_min_u32 v98, -2, v98\n\t"
                    "buffer_store_dword v98, %[mo], %[rw], %[m0] offen sc1\n\t"
                    "buffer_store_dword v98, %[mo], %[rw], %[m1] offen sc1\n\t"
                    "buffer_store_dword v98, %[mo], %[rw], %[m2] offen sc1\n\t"
                    "buffer_store_dword v98, %[mo], %[rw], %[m3] offen sc1\n\t"
                    OST("%[t4]")
                    "s_branch 5f\n\t"
                    "4:\n\t"
                    "s_sub_i32 %[tr], %[tr], 1\n\t"
                    "s_cmp_lt_i32 %[tr], 1\n\t"
                    "s_cbranch_scc1 2b\n\t"
                    "v_xor_b32 %[po], %[x0], %[po]\n\t"
                    "s_xor_b32 %[x0], %[x0], 0x14000\n\t"
                    "v_xor_b32 %[pm], %[x1], %[pm]\n\t"
                    "s_xor_b32 %[x1], %[x1], 0x5000\n\t"
                    "buffer_load_dwordx4 v[128:131], %[zo], %[rw], %[zb] offen sc1\n\t"
                    "buffer_load_dwordx4 v[132:135], %[zo], %[rw], %[zb] offen offset:1024 sc1\n\t"
                    "buffer_load_dwordx4 v[136:139], %[zo], %[rw], %[zb] offen offset:2048 sc1\n\t"
                    "buffer_load_dwordx4 v[140:143], %[zo], %[rw], %[zb] offen offset:3072 sc1\n\t"
                    "buffer_load_dwordx4 v[144:147], %[zo], %[rw], %[z4] offen sc1\n\t"
                    "buffer_load_dwordx4 v[148:151], %[zo], %[rw], %[z4] offen offset:1024 sc1\n\t"
                    "buffer_load_dwordx4 v[152:155], %[zo], %[rw], %[z4] offen offset:2048 sc1\n\t"
                    "buffer_load_dwordx4 v[156:159], %[zo], %[rw], %[z4] offen offset:3072 sc1\n\t"
                    "buffer_load_dwordx4 v[208:211], %[po], %[rw], %[pb] offen sc1\n\t"
                    "buffer_load_dwordx4 v[212:215], %[po], %[rw], %[pb] offen offset:1024 sc1\n\t"
                    "buffer_load_dwordx4 v[216:219], %[po], %[rw], %[pb] offen offset:2048 sc1\n\t"
                    "buffer_load_dwordx4 v[220:223], %[po], %[rw], %[pb] offen offset:3072 sc1\n\t"
                    "buffer_load_dwordx4 v[224:227], %[pm], %[rw], %[pb] offen sc1\n\t"
                    "buffer_load_dwordx4 v[228:231], %[pm], %[rw], %[pb] offen offset:1024 sc1\n\t"
                    "buffer_load_dwordx4 v[232:235], %[pm], %[rw], %[pb] offen offset:2048 sc1\n\t"
                    "buffer_load_dwordx4 v[236:239], %[pm], %[rw], %[pb] offen offset:3072 sc1\n\t"
                    "v_mov_b64 v[96:97], 0\n\t"
                    "v_mov_b64 v[98:99], 0\n\t"
                    "v_mov_b32 v100, 0\n\t"
                    "s_waitcnt vmcnt(0)\n\t"
                    "v_max3_u32 v101, v129, v131, v133\n\t"
                    "v_max3_u32 v101, v101, v135, v137\n\t"
                    "v_max3_u32 v101, v101, v139, v141\n\t"
                    "v_max3_u32 v101, v101, v143, v145\n\t"
                    "v_max3_u32 v101, v101, v147, v149\n\t"
                    "v_max3_u32 v101, v101, v151, v153\n\t"
                    "v_max3_u32 v101, v101, v155, v157\n\t"
                    "v_max_u32 v101, v101, v159\n\t"
                    "v_mov_b32 v100, v101\n\t"
                    "v_permlane16_swap_b32 v128, v129\n\t"
                    "v_permlane16_swap_b32 v130, v131\n\t"
                    "v_permlane16_swap_b32 v132, v133\n\t"
                    "v_permlane16_swap_b32 v134, v135\n\t"
                    "v_permlane16_swap_b32 v136, v137\n\t"
                    "v_permlane16_swap_b32 v138, v139\n\t"
                    "v_permlane16_swap_b32 v140, v141\n\t"
                    "v_permlane16_swap_b32 v142, v143\n\t"
                    "v_permlane16_swap_b32 v144, v145\n\t"
                    "v_permlane16_swap_b32 v146, v147\n\t"
                    "v_permlane16_swap_b32 v148, v149\n\t"
                    "v_permlane16_swap_b32 v150, v151\n\t"
                    "v_permlane16_swap_b32 v152, v153\n\t"
                    "v_permlane16_swap_b32 v154, v155\n\t"
                    "v_permlane16_swap_b32 v156, v157\n\t"
                    "v_permlane16_swap_b32 v158, v159\n\t"
                    "s_nop 1\n\t"
                    "v_smfmac_f32_16x16x64_bf16 v[96:99], v[208:211], v[160:167], %[ix]\n\t"
                    "v_max3_u32 v100, v100, v208, v209\n\t"
                    "v_max3_u32 v100, v100, v210, v211\n\t"
                    "v_smfmac_f32_16x16x64_bf16 v[96:99], v[212:215], v[168:175], %[ix]\n\t"
                    "v_max3_u32 v100, v100, v212, v213\n\t"
                    "v_max3_u32 v100, v100, v214, v215\n\t"
                    "v_smfmac_f32_16x16x64_bf16 v[96:99], v[216:219], v[176:183], %[ix]\n\t"
                    "v_max3_u32 v100, v100, v216, v217\n\t"
                    "v_max3_u32 v100, v100, v218, v219\n\t"
                    "v_smfmac_f32_16x16x64_bf16 v[96:99], v[220:223], v[184:191], %[ix]\n\t"
                    "v_max3_u32 v100, v100, v220, v221\n\t"
                    "v_max3_u32 v100, v100, v222, v223\n\t"
                    "v_smfmac_f32_16x16x64_bf16 v[96:99], v[224:227], v[128:135], %[ix]\n\t"
                    "v_max3_u32 v100, v100, v224, v225\n\t"
                    "v_max3_u32 v100, v100, v226, v227\n\t"
                    "v_smfmac_f32_16x16x64_bf16 v[96:99], v[228:231], v[136:143], %[ix]\n\t"
                    "v_max3_u32 v100, v100, v228, v229\n\t"
                    "v_max3_u32 v100, v100, v230, v231\n\t"
                    "v_smfmac_f32_16x16x64_bf16 v[96:99], v[232:235], v[144:151], %[ix]\n\t"
                    "v_max3_u32 v100, v100, v232, v233\n\t"
                    "v_max3_u32 v100, v100, v234, v235\n\t"
                    "v_smfmac_f32_16x16x64_bf16 v[96:99], v[236:239], v[152:159], %[ix]\n\t"
                    "v_max3_u32 v100, v100, v236, v237\n\t"
                    "v_max3_u32 v100, v100, v238, v239\n\t"
                    "v_cmp_eq_u32 %[bad], -1, v100\n\t"
                    "s_nop 5\n\t"
                    "v_add_f32 v96, v96, v102\n\t"
                    "v_add_f32 v97, v97, v103\n\t"
                    "v_add_f32 v96, v96, v98\n\t"
                    "v_add_f32 v97, v97, v99\n\t"
                    "s_cmp_lg_u64 %[bad], 0\n\t"
                    "s_cbranch_scc1 4b\n\t"
                    "s_branch 2b\n\t"
                    "5:\n\t"
                    : [tr] "+s"(tries), [po] "+v"(po), [pm] "+v"(pm), [x0] "+s"(x0), [x1] "+s"(x1), [bad] "=&s"(bad), [t0] "=&s"(t0_), [t1] "=&s"(t1_), [t2] "=&s"(t2_), [t3] "=&s"(t3_), [t4] "=&s"(t4_)
                    : [zo] "v"(zoff), [sp] "v"(spa), [ix] "v"(sidx), [rw] "s"(rwin), [zb] "s"(zb), [z4] "s"(zb + 4096u), [pb] "s"(pb), [mo] "v"(moff), [fd] "n"(FDL),
                      [m0] "s"(mb), [m1] "s"(mb + PAIR), [m2] "s"(mb + mc2), [m3] "s"(mb + mc2 + PAIR), "{v[160:167]}"(zf[0]), "{v[168:175]}"(zf[1]), "{v[176:183]}"(zf[2]), "{v[184:191]}"(zf[3])
                    : "v96", "v97", "v98", "v99", "v100", "v101", "v102", "v103", "v104", "v105", "v106", "v107", "v128", "v129", "v130", "v131", "v132", "v133", "v134", "v135", "v136", "v137", "v138", "v139", "v140", "v141", "v142", "v143", "v144", "v145", "v146", "v147", "v148", "v149", "v150", "v151", "v152", "v153", "v154", "v155", "v156", "v157", "v158", "v159", "v208", "v209", "v210", "v211", "v212", "v213", "v214", "v215", "v216", "v217", "v218", "v219", "v220", "v221", "v222", "v223", "v224", "v225", "v226", "v227", "v228", "v229", "v230", "v231", "v232", "v233", "v234", "v235", "v236", "v237", "v238", "v239", "vcc", "scc", "memory");
                budget = tries;
                if (w == 3) *(volatile LAS int*)(smem + 4100) = s;
                __builtin_amdgcn_s_sleep(DPS - 4);
                if (w == 3) {
                    st128(SENT, rwin, ra, W_MEM + (unsigned)(s & 3) * 6u * PAIR, 16);
                    if (lane == 0 && s > 1) __builtin_amdgcn_raw_buffer_store_b32((unsigned)s - 2u, rwin, 0u, W_GO + fb + (unsigned)o * 128u, 16);
                }
                for (;;) {
                    const u32x2 x = *(volatile LAS u32x2*)(smem + 4096);
                    zr = __builtin_amdgcn_readfirstlane(x[0]);
                    if (__builtin_amdgcn_readfirstlane(x[1]) == s) break;
                    __builtin_amdgcn_s_sleep(2);
                }
                zb = zb == W_ZM + 2u * NOWN * PAIR ? W_ZM : zb + NOWN * PAIR;
            }
            asm volatile("s_waitcnt vmcnt(0)" : "+{v[160:167]}"(zf[0]), "+{v[168:175]}"(zf[1]), "+{v[176:183]}"(zf[2]), "+{v[184:191]}"(zf[3]) :: "memory");
            BAR0();
            BAR0();
            f32x4 c[2];
            for (unsigned spins = 0;;) {
                asm volatile("" ::: "memory");
                u32x4 pt[16], ptm[2][2], zm[2];
#pragma unroll
                for (int q = 0; q < 2; ++q) zm[q] = __builtin_amdgcn_raw_buffer_load_b128(rwin, zoff + hg * 2048u + q * 1024u, zb, 16);
#pragma unroll
                for (int q = 0; q < 16; ++q) pt[q] = __builtin_amdgcn_raw_buffer_load_b128(rwin, (unsigned)(16 * hg) * 1024u + lane * 16u, W_PL + fb + q * 1024u, 16);
#pragma unroll
                for (int q = 0; q < 4; ++q) ptm[q >> 1][q & 1] = __builtin_amdgcn_raw_buffer_load_b128(rwin, (unsigned)(hg * 2048 + lane * 16), W_PL + fb + 65536u + (q >> 1) * 8192u + (q & 1) * 1024u, 16);
                unsigned m = max(smax(zm[0]), smax(zm[1]));
#pragma unroll
                for (int t = 0; t < 2; ++t) {
                    c[t] = z4;
#pragma unroll
                    for (int q = 0; q < 8; ++q) { c[t] = mfma32(half8(zf[q >> 1], q & 1), pt[2 * q + t], c[t]); m = max(m, smax(pt[2 * q + t])); }
#pragma unroll
                    for (int q = 0; q < 2; ++q) { c[t] = mfma32((u32x4){zm[q][0], zm[q][2], zm[q][1], zm[q][3]}, ptm[t][q], c[t]); m = max(m, smax(ptm[t][q])); }
                }
                if (!__any(m == 0xFFFFFFFFu)) break;
                if (spin_fail(spins, a.ctl)) break;
            }
#pragma unroll
            for (int t = 0; t < 2; ++t)
#pragma unroll
                for (int r = 0; r < 4; ++r) *(float*)(op + hg * 2048 + (16 * t + fr) * 64 + pi16(4 * fq + r) * 4) = c[t][r];
            BAR();
            if (w == 3) st128(SENT, rwin, ra, W_MEM + 18u * PAIR, 16);
        } else if (w == 4) {
            const unsigned fo = (unsigned)(lane & 31) * 128u + (unsigned)(o / 10) * 4u + (lane & 32) * 2u;
            int zk = -1;
            if (lane < 2) *(volatile LAS int*)(smem + 4096 + lane * 4) = -1;
            for (int s = 0; s < NSEG; ++s) {
                asm volatile("s_waitcnt lgkmcnt(0)\n\ts_barrier\n\ts_barrier" ::: "memory");
                int f = 0;
                const bool look = zk < min(s + ZLA, NSEG - 1);
                if (look) { __builtin_amdgcn_s_sleep(6); f = (int)__builtin_amdgcn_raw_buffer_load_b32(rwin, fo, W_FZ + fb, 16); }
                SB();
                BAR0();
                if (look) {
                    f = min(f, __builtin_amdgcn_update_dpp(f, f, 0xB1, 0xF, 0xF, false)); f = min(f, __builtin_amdgcn_update_dpp(f, f, 0x4E, 0xF, 0xF, false));
                    f = min(f, __builtin_amdgcn_update_dpp(f, f, 0x141, 0xF, 0xF, false)); f = min(f, __builtin_amdgcn_update_dpp(f, f, 0x140, 0xF, 0xF, false));
                    zk = seg_done(MXV, min(__builtin_amdgcn_readlane(f, 0), __builtin_amdgcn_readlane(f, 16)), min(__builtin_amdgcn_readlane(f, 32), __builtin_amdgcn_readlane(f, 48)));
                    *(volatile LAS int*)(smem + 4096) = zk;
                }
            }
        } else if (w == 5) {
            for (int s = 0; s < 3 * NSEG; ++s) BAR0();
        } else if (w == 6) {
            const unsigned ooff = (unsigned)((S_ - SEG + (lane >> 1)) * D_ + o * 16 + (lane & 1) * 8) * 2u;
            for (int s = 0; s < 3 * NSEG - 2; ++s) BAR0();
            const u32x4 xr = __builtin_amdgcn_raw_buffer_load_b128(rws, ooff, O_X1, 16);
            asm volatile("s_barrier\n\ts_barrier ; 6" ::: "memory");
            const char* src = op + lane * 32;
            f32x4 x = *(const f32x4*)src, y = *(const f32x4*)(src + 16);
#pragma unroll
            for (int ww = 1; ww < 4; ++ww) { x += *(const f32x4*)(src + ww * 2048); y += *(const f32x4*)(src + ww * 2048 + 16); }
            const u32x4 r = {pack2(lo2f(xr[0]) + bfr(x[0]), hi2f(xr[0]) + bfr(x[1])), pack2(lo2f(xr[1]) + bfr(x[2]), hi2f(xr[1]) + bfr(x[3])),
                             pack2(lo2f(xr[2]) + bfr(y[0]), hi2f(xr[2]) + bfr(y[1])), pack2(lo2f(xr[3]) + bfr(y[2]), hi2f(xr[3]) + bfr(y[3]))};
            st128(r, rout, ooff, 0u, 16);
            if (lay == 0) {
                VMWAIT();
                if (lane == 0) __builtin_amdgcn_raw_buffer_store_b32(0u, rwin, 0u, W_OUT + (unsigned)(NSEG - 1) * 256u + (unsigned)o * 4u, 16);
            } else if (o == 0) __builtin_amdgcn_raw_buffer_store_b64(get_tag(), rws, 0u, 8u, 16);
        } else {
            Tk tk = tk_init();
            __builtin_amdgcn_s_setprio(3);
            for (int s = 0; s < NSEG; ++s) {
                const unsigned ta0 = ck.ta, mm0 = ck.ma;
                unsigned tp = tpp + lt + xs + M1;
                if (s < SAD) { sched(tk, s); tp = tpp - M2 - XS0 - PEN1; }
                wait_fine_bar(tp - DZP, ck);
                wait_cyc_bar(cyc_at(ck, sx24(tp + (s + 1 < NSEG ? 0 : ML) - ck.ta)));
                clk_rate(ck, ta0, mm0);
                asm volatile("s_barrier ; 7" ::: "memory");
                if (s >= SAD && s + 1 < NSEG) sched(tk, s);
                RTS();
            }
            __builtin_amdgcn_s_setprio(0);
        }
        SST(23);
    } else if (bid < NQT) {
        LANES();
        const int o = bid - NREC, cg = o >> 1;
        const unsigned ztoff = (unsigned)((2 * cg * H_ + 4 * w) * 64 + lane) * 16u;
        const unsigned ptoff = (unsigned)(8 * w * 64 + lane) * 16u, ptmoff = 65536u + (unsigned)(4 * w * 16 + lane) * 16u;
        const unsigned ooff = (unsigned)(((w & 1) * 16 + (lane >> 2)) * D_ + cg * 32 + (lane & 3) * 8) * 2u;
        const unsigned tfoff = lane < 16 ? W_FPT + (unsigned)((4 * w + (lane & 3)) * 128 + (lane >> 2) * 4) : lane < 32 ? W_FZ2 + (unsigned)((4 * w + (lane & 3)) * 128 + (cg / 5) * 4) : W_GO + (unsigned)o * 128u;
        int rdy = -1, go = -1;
        auto look = [&](int s) {
            asm volatile("" ::: "memory");
            const int f = (int)__builtin_amdgcn_raw_buffer_load_b32(rwin, s < TAILS ? W_GO + (unsigned)o * 128u : tfoff, fb, 16);
            go = __builtin_amdgcn_readlane(f, 32);
            rdy = max(rdy, go);
            if (!(unsigned)__ballot(f < s)) rdy = max(rdy, s);
        };
        auto wait_rdy = [&](int& x, int v, int s) {
            for (unsigned spins = 0; x < v;) {
                look(s);
                if (x >= v) break;
                if (spin_fail(spins, a.ctl)) break;
                if (s >= TAILS) __builtin_amdgcn_s_sleep(OSLP2); else __builtin_amdgcn_s_sleep(OSLP);
            }
        };
        struct Seg { u32x4 xr, zf[2][4], zx[2], pt[8], ptm[2]; };
        auto load_z = [&](Seg& g, int s) {
            if (w >= 6) g.xr = __builtin_amdgcn_raw_buffer_load_b128(rws, ooff, O_X1 + (unsigned)(s * SEG * D_ * 2), 16);
#pragma unroll
            for (int q = 0; q < 8; ++q) g.zf[q >> 2][q & 3] = __builtin_amdgcn_raw_buffer_load_b128(rws, ztoff + (q & 3) * 1024u, O_Z + (unsigned)(s * ZTSEG) * 2u + (q >> 2) * (H_ * 1024u), 16);
        };
        auto load_p = [&](Seg& g, int s) {
            const unsigned ptb = O_PT + (unsigned)(s * PTSEG) * 2u;
#pragma unroll
            for (int q = 0; q < 8; ++q) g.pt[q] = __builtin_amdgcn_raw_buffer_load_b128(rws, ptoff + q * 1024u, ptb, 16);
#pragma unroll
            for (int q = 0; q < 2; ++q) {
                g.zx[q] = __builtin_amdgcn_raw_buffer_load_b128(rws, tid * 16u, O_Z2 + (unsigned)(s * NOWN + 2 * cg + q) * 8192u, 16);
                g.ptm[q] = __builtin_amdgcn_raw_buffer_load_b128(rws, ptmoff + q * 8192u, ptb, 16);
            }
        };
        auto one = [&](Seg& g, Seg& n, int s) {
            RT(0);
            wait_rdy(rdy, s, s);
            RT(1);
            load_p(g, s);
            const bool pre = go >= s;
            if (pre) load_z(n, min(s + 2, NSEG - 1));
            char* op = smem + ((s >> 1) & 1) * 32768;
#pragma unroll
            for (int u = 0; u < 4; ++u) {
                const int tq = u >> 1, t = u & 1;
                f32x4 c = z4;
#pragma unroll
                for (int q = 0; q < 4; ++q) c = mfma32(g.zf[t][q], g.pt[2 * q + tq], c);
                c = mfma32((u32x4){g.zx[t][0], g.zx[t][2], g.zx[t][1], g.zx[t][3]}, g.ptm[tq], c);
                *(f32x4*)(op + w * 4096 + (16 * tq + fr) * 128 + (((t * 4 + fq) ^ (fr & 7)) << 4)) = c;
            }
            BAR();
            if (w >= 6) {
                const int tk = (w & 1) * 16 + (lane >> 2);
                f32x2 x[4];
#pragma unroll
                for (int q = 0; q < 4; ++q) {
                    const char* sp = op + tk * 128 + ((((lane & 2) * 2 + q) ^ (tk & 7)) << 4) + (lane & 1) * 8;
                    x[q] = *(const f32x2*)sp;
#pragma unroll
                    for (int ww = 1; ww < 8; ++ww) x[q] += *(const f32x2*)(sp + ww * 4096);
                }
                const u32x4 xr = g.xr;
                const u32x4 r = {pack2(lo2f(xr[0]) + bfr(x[0][0]), hi2f(xr[0]) + bfr(x[2][0])), pack2(lo2f(xr[1]) + bfr(x[1][0]), hi2f(xr[1]) + bfr(x[3][0])),
                                 pack2(lo2f(xr[2]) + bfr(x[0][1]), hi2f(xr[2]) + bfr(x[2][1])), pack2(lo2f(xr[3]) + bfr(x[1][1]), hi2f(xr[3]) + bfr(x[3][1]))};
                st128(r, rout, ooff, (unsigned)(s * SEG * D_ * 2), 16);
                if (lay == 0) {
                    VMWAIT();
                    if (lane == 0) __builtin_amdgcn_raw_buffer_store_b32(0u, rwin, 0u, W_OUT + (unsigned)(s * 64 + 2 * cg + (w & 1)) * 4u, 16);
                }
            }
            RT(2);
            RTS();
            if (s < 8) SST(8 + s);
            if (s >= 56) SST(s - 40);
            if (!pre && s + 2 < NSEG - 1) { wait_rdy(go, s, s); load_z(n, s + 2); }
        };
        Seg g0 = {}, g1 = {};
        wait_rdy(go, 0, 0);
        load_z(g0, o & 1);
        for (int s = o & 1; s < NSEG - 1; s += 4) {
            one(g0, g1, s);
            if (s + 2 < NSEG - 1) one(g1, g0, s + 2);
        }
    } else if (lay == 0) {
        LANES();
        const RowTw tw = row_tw(lane);
        for (int u = (259 - bid) & 15; u < 256; u += 16) {
            if (w == 0) {
                for (unsigned spins = 0;;) {
                    asm volatile("" ::: "memory");
                    const unsigned f = __builtin_amdgcn_raw_buffer_load_b32(rwin, (unsigned)min(lane, NOWN - 1) * 4u, W_OUT + (unsigned)(u >> 2) * 256u, 16);
                    if (!__any(f != 0u)) break;
                    if (spin_fail(spins, a.ctl)) break;
                    if (u < 240) __builtin_amdgcn_s_sleep(WSLP); else __builtin_amdgcn_s_sleep(4);
                }
            }
            __syncthreads();
            SST(24);
            a1_unit<4, 512>(rout, u * 8, rws, smem, tw, w, lane, [&](int k) { if (k >= 0) SST(8 + k); });
            SST(25);
            VMWAIT();
            __syncthreads();
            if (tid == 0) __builtin_amdgcn_raw_buffer_store_b32(0u, rwin, 0u, W_FA + (unsigned)((u >> 2) * 128 + (u & 3) * 4), 16);
            SST(26);
        }
    }
    SST(31);
    }
}

constexpr int L_DBG = 0;

extern "C" void kernel_launch(void* const* d_in, const int* in_sizes, int n_in, void* d_out, int out_size, void* d_ws, size_t ws_size,
                              hipStream_t stream) {
    char* ws = (char*)d_ws;
    hipFuncSetAttribute((const void*)b3_kernel, hipFuncAttributeMaxDynamicSharedMemorySize, L_END + L_DBG);
    char* win = (char*)(((uintptr_t)(ws + OFF_WIN) + 0x1FFFFFu) & ~(uintptr_t)0x1FFFFFu);
    RecArgs ra{(unsigned*)ws, win, (const u16*)d_in[4], (const u16*)d_in[0], (u16*)d_out, {(const u16*)d_in[5], (const u16*)d_in[6], (const u16*)d_in[7], (const u16*)d_in[8], (const u16*)d_in[1], (const u16*)d_in[2]}};
    b3_kernel<<<256, 512, L_END + L_DBG, stream>>>(ra);
}
```

```cpp
#include <hip/hip_runtime.h>
#include <hip/hip_bf16.h>
#include <stdint.h>

typedef unsigned short u16;
typedef __attribute__((ext_vector_type(8))) __bf16 bf16x8;
typedef __attribute__((ext_vector_type(4))) short s16x4;
typedef __attribute__((ext_vector_type(4))) float f32x4;
typedef __attribute__((ext_vector_type(8))) float f32x8;
typedef __attribute__((ext_vector_type(16))) float f32x16;
typedef __attribute__((ext_vector_type(4))) unsigned u32x4;
typedef __attribute__((ext_vector_type(2))) unsigned u32x2;
typedef __attribute__((ext_vector_type(8))) unsigned u32x8;
typedef __attribute__((ext_vector_type(16))) __bf16 bf16x16;
typedef __attribute__((ext_vector_type(2))) unsigned short u16x2;
#define LAS __attribute__((address_space(3)))
template <int N> struct IC { static constexpr int v = N; };

constexpr int S_ = 2048, D_ = 640, DF = 321, H_ = 32, DH = 128, HD = 4096, NM = 8, SEG = 32, NSEG = 64;
constexpr int NQKV = 3 * HD;
constexpr int NOWN = 40;
constexpr int ZTR = NSEG;
constexpr size_t ZTSEG = (size_t)NOWN * H_ * 512;
constexpr unsigned PAIR = 16384, HALF = 8192;
constexpr unsigned W_MEM = 0, W_P = W_MEM + 24 * PAIR, W_T = W_P + 40 * PAIR, W_GO = W_T + 8 * PAIR, W_OUT = W_GO + PAIR, W_FZ = W_OUT + PAIR, W_FPT = W_FZ + 4096, W_FZ2 = W_FZ + 8192, W_FQ = W_FZ + 12288,
                   W_H = W_FZ + PAIR, W_FA = W_H + 8192, W_FB = W_GO + 8192, W_E = W_FB + 4096, W_PL = W_H + PAIR, W_FL = W_PL + 6 * PAIR - W_GO, W_PROBE = W_GO + 2 * W_FL,
                   W_ZM = 2u * 1024 * 1024, W_END = W_ZM + 120 * PAIR;
constexpr unsigned W_ST = W_PROBE + 2 * PAIR, W_ROT = W_ST + 8192, W_ARM = W_ROT + 128, W_CFG = W_ARM + 128;
static_assert(W_ST + PAIR <= 2u * 1024 * 1024 && W_PROBE % (2 * PAIR) == 0, "window");
constexpr int PTSEG = 32768 + 8192;

#define WAUX 0
constexpr int NG = 81;
constexpr size_t al(size_t x) { return (x + 255) & ~(size_t)255; }
constexpr size_t OFF_CTL  = 0;
constexpr size_t OFF_WFT  = 4096 + 4 * 1024 * 1024;
constexpr size_t OFF_F    = OFF_WFT + al((size_t)4 * NG * S_ * 4);
constexpr size_t OFF_X1   = OFF_F + (size_t)NG * 65536;
constexpr size_t OFF_X2   = OFF_X1 + al((size_t)S_ * D_ * 2);
constexpr size_t OFF_Y0   = OFF_X2 + al((size_t)S_ * D_ * 2);
constexpr size_t OFF_H    = OFF_Y0 + (size_t)NG * 24576;
constexpr size_t OFF_ARM  = OFF_Y0 - 256 * D_ * 2, ARM_END = OFF_H + 8 * 40960;
constexpr size_t OFF_Z    = OFF_H + (size_t)(NSEG + 4) * 40960;
constexpr size_t OFF_Z2   = OFF_Z + al(ZTSEG * ZTR * 2);
constexpr size_t OFF_PT   = OFF_Z2 + al((size_t)NSEG * NOWN * 8192);
constexpr size_t OFF_QK   = OFF_PT + al((size_t)NSEG * PTSEG * 2);
constexpr size_t OFF_WIN  = OFF_QK + (size_t)NSEG * H_ * 16384;
constexpr size_t OFF_END  = OFF_WIN + (size_t)6 * 1024 * 1024;
static_assert(OFF_END <= (size_t)256 * 1024 * 1024, "workspace");

#define st128(v, r, vo, so, aux) do { const u32x4 d_ = (v); __builtin_amdgcn_raw_buffer_store_b128(d_, r, vo, so, aux); asm volatile("s_nop 1" :: "v"(d_)); } while (0)
__device__ __forceinline__ float bf2f(u16 v) { return __uint_as_float((unsigned)v << 16); }
__device__ __forceinline__ u16 f2bf(float f) { __bf16 b = (__bf16)f; return __builtin_bit_cast(u16, b); }
__device__ __forceinline__ float bfr(float f) { return (float)(__bf16)f; }
typedef __attribute__((ext_vector_type(2))) float f32x2;
typedef __attribute__((ext_vector_type(2))) __bf16 bf16x2;
__device__ __forceinline__ unsigned pack2(float a, float b) { return __builtin_bit_cast(unsigned, __builtin_convertvector((f32x2){a, b}, bf16x2)); }
__device__ __forceinline__ float lo2f(unsigned v) { return __uint_as_float(v << 16); }
__device__ __forceinline__ float hi2f(unsigned v) { return __uint_as_float(v & 0xFFFF0000u); }
__device__ __forceinline__ float fmx(float a, float b) { return __builtin_elementwise_maximum(a, b); }
__device__ __forceinline__ f32x4 mfma32(u32x4 a, u32x4 b, f32x4 c) {
    return __builtin_amdgcn_mfma_f32_16x16x32_bf16(__builtin_bit_cast(bf16x8, a), __builtin_bit_cast(bf16x8, b), c, 0, 0, 0);
}
__device__ __forceinline__ f32x4 mfma16(u32x2 a, u32x2 b, f32x4 c) {
    return __builtin_amdgcn_mfma_f32_16x16x16bf16_1k(__builtin_bit_cast(s16x4, a), __builtin_bit_cast(s16x4, b), c, 0, 0, 0);
}
__device__ __forceinline__ f32x4 smfmac(u32x4 a, u32x8 b, f32x4 c, int idx) {
    return __builtin_amdgcn_smfmac_f32_16x16x64_bf16(__builtin_bit_cast(bf16x8, a), __builtin_bit_cast(bf16x16, b), c, idx, 0, 0);
}
__device__ __forceinline__ int pi16(int k) { return ((k & 3) << 2) | ((k >> 1) & 2) | (k >> 3); }
__device__ __forceinline__ float addswap32(float a, float b) {
    auto r = __builtin_amdgcn_permlane32_swap(__float_as_uint(a), __float_as_uint(b), false, false);
    return __uint_as_float(r[0]) + __uint_as_float(r[1]);
}
__device__ __forceinline__ float xmax3216(float v) {
    auto r = __builtin_amdgcn_permlane32_swap(__float_as_uint(v), __float_as_uint(v), false, false);
    v = fmx(__uint_as_float(r[0]), __uint_as_float(r[1]));
    auto q = __builtin_amdgcn_permlane16_swap(__float_as_uint(v), __float_as_uint(v), false, false);
    return fmx(__uint_as_float(q[0]), __uint_as_float(q[1]));
}
__device__ __forceinline__ float xsum3216(float v) {
    auto r = __builtin_amdgcn_permlane32_swap(__float_as_uint(v), __float_as_uint(v), false, false);
    v = __uint_as_float(r[0]) + __uint_as_float(r[1]);
    auto q = __builtin_amdgcn_permlane16_swap(__float_as_uint(v), __float_as_uint(v), false, false);
    return __uint_as_float(q[0]) + __uint_as_float(q[1]);
}
template <int CTRL> __device__ __forceinline__ float dppf(float v) { return __uint_as_float((unsigned)__builtin_amdgcn_update_dpp(0, (int)__float_as_uint(v), CTRL, 0xF, 0xF, false)); }
__device__ __forceinline__ float wsum(float v) {
    v += dppf<0xB1>(v); v += dppf<0x4E>(v); v += dppf<0x141>(v); v += dppf<0x140>(v);
    return xsum3216(v);
}
__device__ __forceinline__ unsigned ror8(unsigned v) { return (unsigned)__builtin_amdgcn_update_dpp(0, (int)v, 0x128, 0xF, 0xF, false); }
__device__ __forceinline__ float ror8f(float v) { return __uint_as_float(ror8(__float_as_uint(v))); }
__device__ __forceinline__ unsigned nosent(unsigned v) {
    u16x2 lim = {0xFFFE, 0xFFFE};
    return __builtin_bit_cast(unsigned, __builtin_elementwise_min(__builtin_bit_cast(u16x2, v), lim));
}
__device__ __forceinline__ unsigned pkmax(unsigned a, unsigned b) {
    return __builtin_bit_cast(unsigned, __builtin_elementwise_max(__builtin_bit_cast(u16x2, a), __builtin_bit_cast(u16x2, b)));
}
__device__ __forceinline__ bool has_sent(u32x4 v) {
    unsigned m = pkmax(pkmax(v.x, v.y), pkmax(v.z, v.w));
    return (m & 0xFFFFu) == 0xFFFFu || (m >> 16) == 0xFFFFu;
}

typedef f32x2 c32;
__device__ __forceinline__ c32 fma2(c32 a, c32 b, c32 c) { return __builtin_elementwise_fma(a, b, c); }
__device__ __forceinline__ c32 dup(float v) { return (c32){v, v}; }
template <int SG> __device__ __forceinline__ c32 addi(c32 a, c32 b) {
    c32 d;
    if (SG > 0) asm("v_pk_add_f32 %0, %1, %2 op_sel:[0,1] op_sel_hi:[1,0] neg_lo:[0,1]" : "=v"(d) : "v"(a), "v"(b));
    else asm("v_pk_add_f32 %0, %1, %2 op_sel:[0,1] op_sel_hi:[1,0] neg_hi:[0,1]" : "=v"(d) : "v"(a), "v"(b));
    return d;
}
template <int SG> __device__ __forceinline__ c32 ctw(c32 a, c32 w) {
    c32 d = a * w.xx;
    if (SG < 0) asm("v_pk_fma_f32 %0, %1, %2, %0 op_sel:[1,1,0] op_sel_hi:[0,1,1] neg_lo:[0,1,0]" : "+v"(d) : "v"(a), "v"(w));
    else asm("v_pk_fma_f32 %0, %1, %2, %0 op_sel:[1,1,0] op_sel_hi:[0,1,1] neg_hi:[0,1,0]" : "+v"(d) : "v"(a), "v"(w));
    return d;
}
__device__ __forceinline__ c32 cmulk(c32 a, float x, float y) { return fma2(a.yx, (c32){-y, y}, a * dup(x)); }
__device__ __forceinline__ c32 cis(float rev) { return (c32){__builtin_amdgcn_cosf(rev), __builtin_amdgcn_sinf(rev)}; }
template <int N> __device__ __forceinline__ void cpow(c32 (&t)[N], float rev) {
    t[0] = cis(rev);
    asm volatile("s_nop 0" : "+v"(t[0]));
#pragma unroll
    for (int i = 1; i < N; ++i) { t[i] = ctw<-1>(t[(i - 1) / 2], t[i / 2]); asm volatile("" : "+v"(t[i])); }
}
template <int SG, bool CI = false> __device__ __forceinline__ void dft4(c32& a, c32& b, c32& c, c32& d) {
    const c32 t0 = CI ? addi<SG>(a, c) : a + c, t1 = CI ? addi<-SG>(a, c) : a - c, t2 = b + d, t3 = b - d;
    a = t0 + t2; c = t0 - t2; b = addi<SG>(t1, t3); d = addi<-SG>(t1, t3);
}
template <int SG> __device__ __forceinline__ void dft8(c32 (&u)[8]) {
    c32 e0 = u[0], e1 = u[2], e2 = u[4], e3 = u[6], o0 = u[1], o1 = u[3], o2 = u[5], o3 = u[7];
    dft4<SG>(e0, e1, e2, e3); dft4<SG>(o0, o1, o2, o3);
    const float r = 0.70710678118654752440f;
    o1 = addi<SG>(o1, o1); o3 = addi<-SG>(o3, o3);
    u[0] = e0 + o0; u[4] = e0 - o0;
    u[1] = fma2(o1, dup(r), e1); u[5] = fma2(o1, dup(-r), e1);
    u[2] = addi<SG>(e2, o2); u[6] = addi<-SG>(e2, o2);
    u[3] = fma2(o3, dup(-r), e3); u[7] = fma2(o3, dup(r), e3);
}
template <int SG> __device__ __forceinline__ void dft16(c32 (&u)[16]) {
    const float c1 = 0.92387953251128675613f, s1 = 0.38268343236508977173f, r = 0.70710678118654752440f;
#pragma unroll
    for (int i = 0; i < 4; ++i) dft4<SG>(u[i], u[i + 4], u[i + 8], u[i + 12]);
    u[5] = cmulk(u[5], c1, SG * s1); u[9] = cmulk(u[9], r, SG * r); u[13] = cmulk(u[13], s1, SG * c1);
    u[6] = cmulk(u[6], r, SG * r); u[14] = cmulk(u[14], -r, SG * r);
    u[7] = cmulk(u[7], s1, SG * c1); u[11] = cmulk(u[11], -r, SG * r); u[15] = cmulk(u[15], -c1, -SG * s1);
    dft4<SG>(u[0], u[1], u[2], u[3]); dft4<SG>(u[4], u[5], u[6], u[7]); dft4<SG, true>(u[8], u[9], u[10], u[11]); dft4<SG>(u[12], u[13], u[14], u[15]);
    c32 o[16];
#pragma unroll
    for (int i = 0; i < 16; ++i) o[(i >> 2) + 4 * (i & 3)] = u[i];
#pragma unroll
    for (int i = 0; i < 16; ++i) u[i] = o[i];
}
template <int SG> __device__ __forceinline__ void dft5(c32 (&x)[5]) {
    const float c1 = 0.30901699437494742410f, c2 = -0.80901699437494742410f, s1 = 0.95105651629515357212f, s2 = 0.58778525229247312917f;
    const c32 t1 = x[1] + x[4], t2 = x[2] + x[3], t3 = x[1] - x[4], t4 = x[2] - x[3];
    const c32 a1 = fma2(t2, dup(c2), fma2(t1, dup(c1), x[0])), a2 = fma2(t2, dup(c1), fma2(t1, dup(c2), x[0]));
    const c32 b1 = fma2(t4, dup(s2), t3 * dup(s1)), b2 = fma2(t4, dup(-s1), t3 * dup(s2));
    x[0] = x[0] + (t1 + t2);
    x[1] = addi<SG>(a1, b1); x[4] = addi<-SG>(a1, b1); x[2] = addi<SG>(a2, b2); x[3] = addi<-SG>(a2, b2);
}
template <int SG> __device__ __forceinline__ void dft10(c32 (&u)[10]) {
    c32 e[5], o[5];
#pragma unroll
    for (int i = 0; i < 5; ++i) { e[i] = u[2 * i] + u[(2 * i + 5) % 10]; o[i] = u[2 * i] - u[(2 * i + 5) % 10]; }
    dft5<SG>(e); dft5<SG>(o);
#pragma unroll
    for (int i = 0; i < 5; ++i) { u[6 * i % 10] = e[i]; u[(6 * i + 5) % 10] = o[i]; }
}
template <int ST> __device__ __forceinline__ void lds16(c32 (&u)[16], const char* p) {
    const LAS char* q = (const LAS char*)p;
#pragma unroll
    for (int i = 0; i < 16; ++i) asm volatile("ds_read_b64 %0, %1 offset:%2" : "=v"(u[(i >> 2) + 4 * (i & 3)]) : "v"(q), "n"(((i >> 2) + 4 * (i & 3)) * ST) : "memory");
#pragma unroll
    for (int i = 0; i < 4; ++i) asm volatile("s_waitcnt lgkmcnt(%4)" : "+v"(u[i]), "+v"(u[i + 4]), "+v"(u[i + 8]), "+v"(u[i + 12]) : "n"(12 - 4 * i) : "memory");
}
__device__ __forceinline__ c32 unpack2(unsigned v) { return (c32){lo2f(v), hi2f(v)}; }
__device__ __forceinline__ float wsum2(c32 v) {
    float t = addswap32(v.x, v.y);
    auto q = __builtin_amdgcn_permlane16_swap(__float_as_uint(t), __float_as_uint(t), false, false);
    t = __uint_as_float(q[0]) + __uint_as_float(q[1]);
    t += dppf<0xB1>(t); t += dppf<0x4E>(t); t += dppf<0x141>(t); t += dppf<0x140>(t);
    return t;
}
__device__ __forceinline__ c32 halves(float t) { return (c32){__uint_as_float(__builtin_amdgcn_readlane(__float_as_uint(t), 0)), __uint_as_float(__builtin_amdgcn_readlane(__float_as_uint(t), 32))}; }
__device__ __forceinline__ void ln_rows(const c32 (&v)[10], unsigned (&o)[10]) {
    const float mean = wsum2(((v[0] + v[1]) + (v[2] + v[3])) + ((v[4] + v[5]) + (v[6] + v[7])) + (v[8] + v[9])) * (1.f / 640.f);
    const c32 m = halves(mean), mb = halves(bfr(mean));
    c32 q[2] = {dup(0.f), dup(0.f)};
#pragma unroll
    for (int r = 0; r < 10; ++r) { const c32 d = v[r] - m; q[r & 1] = fma2(d, d, q[r & 1]); }
    const float var = wsum2(q[0] + q[1]) * (1.f / 640.f);
    const c32 rs = halves(__builtin_amdgcn_rcpf(bfr(__builtin_amdgcn_sqrtf(bfr(bfr(var) + 1e-5f)))));
#pragma unroll
    for (int r = 0; r < 10; ++r) { const c32 d = v[r] - mb, y = unpack2(pack2(d.x, d.y)) * rs; o[r] = pack2(y.x, y.y); }
}

__device__ __forceinline__ __amdgpu_buffer_rsrc_t mkrsrc(const void* p, unsigned bytes) {
    return __builtin_amdgcn_make_buffer_rsrc((void*)p, 0, bytes, 0x00020000);
}

constexpr unsigned O_F = (unsigned)OFF_F, O_Y0 = (unsigned)OFF_Y0;
__device__ __forceinline__ unsigned nosent1(unsigned v) { return min(v, 0xFFFFFFFEu); }
__device__ __forceinline__ u32x4 nosent4(u32x4 v) { v[1] = nosent1(v[1]); v[3] = nosent1(v[3]); return v; }
__device__ __forceinline__ unsigned smax(u32x4 v) { return max(v[1], v[3]); }
constexpr unsigned SPIN_CAP = 400000u;
constexpr int RW = 5920;
struct RowTw { c32 a[9], b, c; };
__device__ __forceinline__ RowTw row_tw(int lane) {
    RowTw t;
    t.b = cis((float)(lane & 31) * (-1.f / 64.f)); t.c = cis((float)(lane & 15) * (-1.f / 32.f));
    asm volatile("" : "+v"(t.b), "+v"(t.c));
    cpow(t.a, (float)lane * (-1.f / 640.f));
    return t;
}
template <int B> __device__ __forceinline__ void cswap(c32& x, c32& y) {
#pragma unroll
    for (int e = 0; e < 2; ++e) {
        auto r = B == 32 ? __builtin_amdgcn_permlane32_swap(__float_as_uint(x[e]), __float_as_uint(y[e]), false, false) : __builtin_amdgcn_permlane16_swap(__float_as_uint(x[e]), __float_as_uint(y[e]), false, false);
        x[e] = __uint_as_float(r[0]); y[e] = __uint_as_float(r[1]);
    }
}
__device__ __forceinline__ void row_x(u16 (&v)[20], __amdgpu_buffer_rsrc_t rx, int row, int lane) {
#pragma unroll
    for (int r = 0; r < 20; ++r) v[r] = __builtin_amdgcn_raw_buffer_load_b16(rx, (lane & 63) * 2u + 128u * (r >> 1) + 1280u * (r & 1), (unsigned)row * (D_ * 2), 16);
    __builtin_amdgcn_sched_barrier(0);
}
template <int NW, int NT, class FS> __device__ __forceinline__ void a1_unit(__amdgpu_buffer_rsrc_t rx, int row0, bool tail, __amdgpu_buffer_rsrc_t rws, char* sm, const RowTw& tw, int w, int lane, FS st_) {
    const unsigned fo = tail ? O_Y0 + 16384u + (unsigned)(row0 & 255) * 32u : O_F + (unsigned)row0 * 32u, gs = tail ? 24576u : 65536u;
    if (w < NW) {
        c32 u[10];
        unsigned h[10];
        u16 x[20];
        row_x(x, rx, row0 + 2 * w, lane);
        if (tail) for (unsigned spins = 0; spins < SPIN_CAP; ++spins) {
            unsigned m = 0;
#pragma unroll
            for (int r = 0; r < 20; ++r) m = max(m, (unsigned)x[r]);
            if (!__any(m == 0xFFFFu)) break;
            asm volatile("" ::: "memory");
            row_x(x, rx, row0 + 2 * w, lane);
        }
        st_(-1);
#pragma unroll
        for (int r = 0; r < 10; ++r) u[r] = (c32){bf2f(x[2 * r]), bf2f(x[2 * r + 1])};
        st_(0);
        ln_rows(u, h);
        st_(1);
#pragma unroll
        for (int r = 0; r < 10; ++r) u[r] = unpack2(h[r]);
        dft10<-1>(u);
        char* mine = sm + w * RW;
        char* ex = mine + ((lane >> 5) + 10 * ((lane >> 4) & 1)) * 144 + (lane & 15) * 8;
#pragma unroll
        for (int i = 0; i < 5; ++i) {
            c32 x = i ? ctw<-1>(u[2 * i], tw.a[2 * i - 1]) : u[0], y = ctw<-1>(u[2 * i + 1], tw.a[2 * i]);
            cswap<32>(x, y);
            c32 t = x - y; x = x + y; y = ctw<-1>(t, tw.b);
            cswap<16>(x, y);
            *(c32*)(ex + i * 288) = x + y; *(c32*)(ex + i * 288 + 2880) = ctw<-1>(x - y, tw.c);
        }
        asm volatile("" ::: "memory");
        st_(2);
        if (lane < 40) {
            c32 v[16];
#pragma unroll
            for (int m = 0; m < 8; ++m) { const f32x4 t = *(const f32x4*)(mine + lane * 144 + m * 16); v[2 * m] = t.xy; v[2 * m + 1] = t.zw; }
            dft16<-1>(v);
            asm volatile("" ::: "memory");
#pragma unroll
            for (int m = 0; m < 16; ++m) *(c32*)(mine + lane * 8 + m * 320) = v[m];
        }
        st_(3);
    }
    asm volatile("s_waitcnt lgkmcnt(0)\n\ts_barrier" ::: "memory");
    st_(4);
    const int tid = w * 64 + lane, k0 = tid / (4 * NW) * 4 + (tid & 3), id = tid % (4 * NW);
    const char* X = sm + (id >> 2) * RW;
#pragma unroll
    for (int c = 0; c * NT < 320 * NW; ++c) {
        const int k = k0 + c * (NT / NW);
        if ((c + 1) * NT <= 320 * NW || k < 320) {
            const c32 zk = *(const c32*)(X + k * 8), zn = *(const c32*)(X + (k == 0 ? 0 : 640 - k) * 8);
            const f32x4 o = {0.5f * (zk.x + zn.x), 0.5f * (zk.y - zn.y), 0.5f * (zk.y + zn.y), 0.5f * (zn.x - zk.x)};
            st128(nosent4(__builtin_bit_cast(u32x4, o)), rws, (unsigned)(k >> 2) * gs + id * 16u, fo, 16);
        }
    }
    if (k0 < 4) {
        const c32 z = *(const c32*)(X + 2560);
        st128(((u32x4){__float_as_uint(z.x), 0u, __float_as_uint(z.y), 0u}), rws, 80u * gs + id * 16u, fo, 16);
    }
}
template <int NW, class FS> __device__ __forceinline__ void a3_unit(__amdgpu_buffer_rsrc_t rws, __amdgpu_buffer_rsrc_t rx, int row0, int lay, char* sm, const RowTw& tw, int w, int lane, FS st_) {
    constexpr int NT = 512, NC = (320 * NW + NT - 1) / NT, GS = NW == 4 ? 24576 : 65536;
    const unsigned fo = (NW == 4 ? O_Y0 + lay * 8192u : O_F) + (unsigned)row0 * 32u;
    const int tid = w * 64 + lane, k0 = tid / (4 * NW) * 4 + (tid & 3), id = tid % (4 * NW);
    u32x4 fv[NC], fz = {0u, 0u, 0u, 0u};
    u16 x[20];
    if (w < NW) row_x(x, rx, row0 + 2 * w, lane);
    st_(-1);
    for (unsigned spins = 0; spins < SPIN_CAP; ++spins) {
        asm volatile("" ::: "memory");
#pragma unroll
        for (int c = 0; c < NC; ++c) fv[c] = __builtin_amdgcn_raw_buffer_load_b128(rws, (unsigned)((min(k0 + c * (NT / NW), 319) >> 2) * GS + id * 16), fo, 16);
        if (w == 0) fz = __builtin_amdgcn_raw_buffer_load_b128(rws, (unsigned)(80 * GS + id * 16), fo, 16);
        if (NW == 8) break;
        __builtin_amdgcn_sched_barrier(0);
        if (!__any(max(max(smax(fv[0]), smax(fv[1])), max(smax(fv[2]), smax(fz))) == 0xFFFFFFFFu)) break;
    }
    __builtin_amdgcn_sched_barrier(0);
    char* X = sm + (id >> 2) * RW;
#pragma unroll
    for (int c = 0; c < NC; ++c) {
        const int k = k0 + c * (NT / NW);
        const f32x4 v = __builtin_bit_cast(f32x4, fv[c]);
        c32 zk = addi<1>(v.xy, v.zw), zn = (c32){v[0] + v[3], v[2] - v[1]};
        if (c == 0 && k == 0) { zk = (c32){v[0], v[2]}; zn = (c32){__uint_as_float(fz[0]), __uint_as_float(fz[2])}; }
        if ((c + 1) * NT <= 320 * NW || k < 320) { *(c32*)(X + k * 8) = zk; *(c32*)(X + (k == 0 ? 320 : 640 - k) * 8) = zn; }
    }
    asm volatile("s_waitcnt lgkmcnt(0)\n\ts_barrier" ::: "memory");
    st_(0);
    if (w < NW) {
        char* mine = sm + w * RW;
        if (lane < 40) {
            c32 v[16];
            lds16<320>(v, mine + lane * 8);
            dft16<1>(v);
            asm volatile("" ::: "memory");
#pragma unroll
            for (int m = 0; m < 8; ++m) *(f32x4*)(mine + lane * 144 + m * 16) = (f32x4){v[2 * m].x, v[2 * m].y, v[2 * m + 1].x, v[2 * m + 1].y};
        }
        asm volatile("" ::: "memory");
        st_(1);
        const char* ex = mine + ((lane >> 5) + 10 * ((lane >> 4) & 1)) * 144 + (lane & 15) * 8;
        c32 u[10];
#pragma unroll
        for (int i = 0; i < 5; ++i) { u[2 * i] = *(const c32*)(ex + i * 288); u[2 * i + 1] = *(const c32*)(ex + i * 288 + 2880); }
#pragma unroll
        for (int i = 0; i < 5; ++i) {
            c32 t = ctw<1>(u[2 * i + 1], tw.c), p = u[2 * i] + t, q = u[2 * i] - t;
            cswap<16>(p, q);
            t = ctw<1>(q, tw.b); q = p - t; p = p + t;
            cswap<32>(p, q);
            u[2 * i] = i ? ctw<1>(p, tw.a[2 * i - 1]) : p; u[2 * i + 1] = ctw<1>(q, tw.a[2 * i]);
        }
        dft10<1>(u);
        st_(2);
        asm volatile("" ::: "memory");
        char* st = mine + lane * 2;
        unsigned h[10];
#pragma unroll
        for (int r = 0; r < 10; ++r) {
            const c32 y = (c32){bf2f(x[2 * r]), bf2f(x[2 * r + 1])} + unpack2(pack2(u[r].x, u[r].y));
            const unsigned p = pack2(y.x, y.y);
            u[r] = unpack2(p);
            *(u16*)(st + r * 128) = (u16)p; *(u16*)(st + 1280 + r * 128) = (u16)(p >> 16);
        }
        st_(3);
        ln_rows(u, h);
        st_(4);
#pragma unroll
        for (int r = 0; r < 10; ++r) { *(u16*)(st + 2560 + r * 128) = (u16)h[r]; *(u16*)(st + 3840 + r * 128) = (u16)(h[r] >> 16); }
        asm volatile("" ::: "memory");
        u32x4 o[6];
#pragma unroll
        for (int c = 0; c < 6; ++c) o[c] = *(const u32x4*)(mine + (c < 3 ? 2560 : 0) + min(c % 3 * 64 + lane, 159) * 16);
#pragma unroll
        for (int c = 0; c < 3; ++c) {
            const int i = min(c * 64 + lane, 159), rr = i >= 80, ch = i - 80 * rr, row = (row0 + 2 * w + rr) & 31;
            st128(nosent4(o[c]), rws, (unsigned)((ch >> 3) * 4096 + row * 128 + (((ch & 7) ^ (row & 7)) << 4)) | (c == 2 ? (lane & 32u) << 25 : 0u), (unsigned)OFF_H + (unsigned)((row0 >> 5) + 4 - 4 * lay) * 40960u, 16);
        }
#pragma unroll
        for (int c = 0; c < 3; ++c) st128(o[3 + c], rws, (unsigned)(c * 64 + lane) * 16u | (c == 2 ? (lane & 32u) << 25 : 0u), (unsigned)OFF_X1 + (unsigned)(row0 + 2 * w) * (D_ * 2), 16);
    }
}

__device__ __forceinline__ unsigned xcc_id() { unsigned x; asm volatile("s_getreg_b32 %0, hwreg(HW_REG_XCC_ID)" : "=s"(x)); return x & 7u; }
__device__ __forceinline__ unsigned probe(__amdgpu_buffer_rsrc_t rw, unsigned base, int lane) {
    __builtin_amdgcn_raw_buffer_store_b32(0u, rw, (lane & 3) * HALF + (lane >> 2 & 1) * 4096u, base, 16);
    asm volatile("s_waitcnt vmcnt(0)" ::: "memory");
    unsigned mn[4] = {~0u, ~0u, ~0u, ~0u};
#pragma unroll 1
    for (int r = 0; r < 2; ++r) {
#pragma unroll
        for (int i = 0; i < 4; ++i) {
            const unsigned long long t1 = __builtin_amdgcn_s_memtime();
            asm volatile("s_waitcnt lgkmcnt(0)" ::: "memory");
            unsigned v = __builtin_amdgcn_raw_buffer_load_b32(rw, i * HALF, base + r * 4096u, 16);
            asm volatile("s_waitcnt vmcnt(0)" : "+v"(v) :: "memory");
            const unsigned long long t2 = __builtin_amdgcn_s_memtime();
            mn[i] = min(mn[i], (unsigned)(t2 - t1) + v);
        }
    }
    return mn[1] + mn[3] < mn[0] + mn[2] ? (mn[3] < mn[1] ? 3u : 1u) : (mn[2] < mn[0] ? 2u : 0u);
}
__device__ __forceinline__ void filter_unit(const u16* wr, const u16* wi, int s0, __amdgpu_buffer_rsrc_t rws, u16* tf, int tid) {
    u32x4 v[6];
#pragma unroll
    for (int i = 0; i < 6; ++i) v[i] = ((const u32x4*)((i < 3 ? wr : wi) + (size_t)s0 * DF))[min(tid + 512 * (i % 3), 1283)];
    __builtin_amdgcn_sched_barrier(0);
#pragma unroll
    for (int i = 0; i < 6; ++i) ((u32x4*)tf)[min(tid + 512 * (i % 3), 1283) + i / 3 * 1284] = v[i];
    __syncthreads();
    const int k = tid >> 3, sq = (tid & 7) * 4;
    u16 t[6][8];
#pragma unroll
    for (int i = 0; i < 6; ++i)
#pragma unroll
        for (int j = 0; j < 8; ++j) t[i][j] = tf[((j & 1) * 32 + sq + (j >> 1)) * DF + k + 64 * i];
    __builtin_amdgcn_sched_barrier(0);
#pragma unroll
    for (int i = 0; i < 6; ++i) {
        u32x4 o;
#pragma unroll
        for (int j = 0; j < 4; ++j) o[j] = (unsigned)t[i][2 * j] | (unsigned)t[i][2 * j + 1] << 16;
        if (i < 5 || tid < 8) st128(o, rws, (unsigned)(k * S_ + sq) * 4u, (unsigned)OFF_WFT + (unsigned)(s0 * 4 + i * 64 * S_ * 4), 16);
    }
}

constexpr int A2W = 8720;
template <class FW, class FS> __device__ __forceinline__ void a2_unit(__amdgpu_buffer_rsrc_t rws, int G, char* sm, int w, int lane, bool late, FW rdy, FS st_) {
    const int c = lane & 3, j = lane >> 2, p = 16 * w + j;
    const unsigned fo = O_F + (unsigned)(G * 65536 + w * 1024);
    u32x4 x[8];
    unsigned wv[16];
    c32 t1[2][7], t2[15], wf[16];
    auto ldw = [&]() {
#pragma unroll
        for (int r = 0; r < 16; ++r) wv[r] = __builtin_amdgcn_raw_buffer_load_b32(rws, (unsigned)(j * 32 + c * 4 + (r & 7) * 512), (unsigned)OFF_WFT + (unsigned)(min(4 * G + (w >> 1), 320) * 8192 + (w & 1) * 16 + (r >> 3) * 4096), 16);
        __builtin_amdgcn_sched_barrier(0);
    };
    auto cvw = [&]() {
#pragma unroll
        for (int m = 0; m < 16; ++m) { wf[m] = unpack2(wv[m]) * dup(1.f / ((float)S_ * (float)D_)); asm volatile("" : "+v"(wf[m])); }
    };
#pragma unroll
    for (int m = 0; m < 16; ++m) wv[m] = 0u;
    rdy(5);
    cpow(t1[0], (float)p * (-1.f / 1024.f)); cpow(t1[1], (float)(2 * p + 1) * (-1.f / 2048.f)); cpow(t2, (float)j * (-1.f / 256.f));
    rdy(6);
#pragma unroll
    for (int r = 0; r < 7; ++r) x[r] = __builtin_amdgcn_raw_buffer_load_b128(rws, lane * 16u, fo + r * 8192u, 16);
    if (!late) ldw();
    rdy(7);
    const unsigned yo = O_Y0 + (unsigned)(G * 24576 + w * 1024);
    x[7] = __builtin_amdgcn_raw_buffer_load_b128(rws, lane * 16u, late ? fo + 7 * 8192u : yo + 16384u, 16);
    __builtin_amdgcn_sched_barrier(0);
    cvw();
    for (unsigned spins = 0; spins < SPIN_CAP && __any(smax(x[7]) == 0xFFFFFFFFu); ++spins) {
        asm volatile("" ::: "memory");
        x[7] = __builtin_amdgcn_raw_buffer_load_b128(rws, lane * 16u, yo + 16384u, 16);
    }
    __builtin_amdgcn_sched_barrier(0);
    c32 ua[8], ub[8], u[16];
#pragma unroll
    for (int r = 0; r < 8; ++r) { const f32x4 v = __builtin_bit_cast(f32x4, x[r]); ua[r] = v.xy; ub[r] = v.zw; }
    st_(0);
    dft8<-1>(ua); dft8<-1>(ub);
    char* e1 = sm + 2 * c * A2W + p * 16;
    char* mine = sm + w * A2W;
#pragma unroll
    for (int q = 0; q < 8; ++q) {
        if (q) { ua[q] = ctw<-1>(ua[q], t1[0][q - 1]); ub[q] = ctw<-1>(ub[q], t1[1][q - 1]); }
        *(f32x4*)(e1 + (q >> 2) * A2W + (q & 3) * 2112) = (f32x4){ua[q].x, ua[q].y, ub[q].x, ub[q].y};
    }
    st_(1);
    rdy(8);
    asm volatile("s_waitcnt lgkmcnt(0)\n\ts_barrier" ::: "memory");
    st_(2);
    if (late) ldw();
    lds16<128>(u, mine + c * 2112 + j * 8);
    dft16<-1>(u);
    auto turn = [&](auto sg) {
        asm volatile("" ::: "memory");
#pragma unroll
        for (int m = 0; m < 16; ++m) *(c32*)(mine + m * 544 + lane * 8) = m ? ctw<decltype(sg)::v>(u[m], t2[m - 1]) : u[m];
        lds16<32>(u, mine + j * 544 + c * 8);
    };
    turn(IC<-1>());
    st_(3);
    dft16<-1>(u);
    if (late) cvw();
#pragma unroll
    for (int m = 0; m < 16; ++m) u[m] = ctw<-1>(u[m], wf[m]);
    dft16<1>(u);
    turn(IC<1>());
    st_(4);
    dft16<1>(u);
#pragma unroll
    for (int m = 0; m < 16; ++m) *(c32*)(mine + (w >> 1) * 32 + c * 2080 + j * 8 + m * 128) = u[m];
    st_(5);
    asm volatile("s_waitcnt lgkmcnt(0)\n\ts_barrier" ::: "memory");
    st_(6);
#pragma unroll
    for (int q = 0; q < 8; ++q) {
        const f32x4 v = *(const f32x4*)(e1 + c * 32 + (q >> 2) * A2W + (q & 3) * 2080);
        ua[q] = v.xy; ub[q] = v.zw;
        if (q) { ua[q] = ctw<1>(ua[q], t1[0][q - 1]); ub[q] = ctw<1>(ub[q], t1[1][q - 1]); }
    }
    dft8<1>(ua); dft8<1>(ub);
#pragma unroll
    for (int r = 0; r < 8; ++r) {
        const u32x4 o = __builtin_bit_cast(u32x4, (f32x4){ua[r].x, ua[r].y, ub[r].x, ub[r].y});
        st128(r ? o : nosent4(o), rws, lane * 16u, r ? fo + r * 8192u : late ? yo : yo + 8192u, 16);
        if (r == 0) st_(7);
    }
}

struct RecArgs {
    unsigned* ctl; char* win; const u16* mem0; const u16* xin; u16* out;
    const u16* W[6];
};
template <int I> __device__ __forceinline__ char* karg(int i = 0) {
    unsigned long long p;
    asm volatile("s_load_dwordx2 %0, %1, %2\n\ts_waitcnt lgkmcnt(0)" : "=s"(p) : "s"(__builtin_amdgcn_kernarg_segment_ptr()), "s"(I * 8 + i * 8) : "memory");
    return (char*)p;
}
constexpr unsigned O_H = (unsigned)OFF_H, O_X1 = (unsigned)OFF_X1, O_Z = (unsigned)OFF_Z, O_Z2 = (unsigned)OFF_Z2, O_PT = (unsigned)OFF_PT, O_QK = (unsigned)OFF_QK;
#define MXV 0xAAAAAull
#define MXQ 0x88888000000ull
#define NTAIL 4
static_assert(NTAIL >= 2, "the owners release segments up to NSEG - 3");
constexpr int TAILS = NSEG - NTAIL;
static_assert((MXV & 15) == 10 && !(MXQ & 15) && !((MXV | MXQ) >> 63), "segment sets");
constexpr int NREC = 96 + NOWN, NQT = NREC + NOWN, NXT = NQT + 32, NWRK = NXT + 32;
__device__ __forceinline__ bool spin_fail(unsigned& spins, unsigned* tmo) {
    if (++spins > SPIN_CAP) { __hip_atomic_store(tmo, 1u, __ATOMIC_RELAXED, __HIP_MEMORY_SCOPE_AGENT); return true; }
    return (spins & 1023u) == 0u && __builtin_amdgcn_readfirstlane(__hip_atomic_load(tmo, __ATOMIC_RELAXED, __HIP_MEMORY_SCOPE_AGENT));
}
#define SST(k) do {} while (0)
#define LSEL 1
#define RT(k) do {} while (0)
#define RTS() do {} while (0)
#define B3ATTR
#define MTA(k)
#define MTRT() do {} while (0)
#define MT(k) do {} while (0)
#define MTS() do {} while (0)
#define FC(x) do {} while (0)
#define DQC 1200
#define DVC 1200
#define M1 44
#define MZ 40
#define MZP 20
#define M2 234
#define LTP (LT0 + 15)
#define LTZ 340
#define PEN 280
#define KSL 1
#define SVO 64
#define DZP 46
#define LT0 342
#define SAD 1
#define PEN2 80
#define PEN1 50
#define PEN0 60
#define DDM2 0
#define DRA 700
#define TSLP 1
#define PFS 0
#define PFOFF 1
#define OPR 2
#define BSPARSE 0
#define ZLAG 4
#define TKP 0
#define DPS 12
#define RZ 63
#define RQ 63
#define R2 63
#define DXT 3000
#define SSW 99
#define LTM 12
#define XS0 10
#define XSU 8
#define XSM 32
#define LTU 4
#define LTD 1
#define QUIET 0
#define QUAD 1
#define LTS 3
#define XSS 3
#define TKS 3
#define LT1 370
#define FDL 2
#define ZLA 3
#define DPT2 3
#define NORND 0
#define LTX 24
#define TKE 200
#define DVO 0
#define ZPS 4
#define ASLP 4
#define BADG 0
#define DW1 0
#define WLA 4
#define XSLP 30
#define DOUT 120
#define DA1 145
#define DA2 100
#define DA20 120
#define DFB 150
#define DA3 170
#define DA30 230
#define YSLP 0
#define QEU 14
#define QE2 10
#define PEN00 250
#define DQ0 1020
#define DQ00 1500
#define DZ0 420
#define DZ00 430
#define DOU2 170
#define DFA 0
#define E3(x)
#define HLA 8
#define PSLP 16
#define PFD 4
#define NPF 3
#define KSL2 1
#define KBQ 4
#define VB1 6
#define VB2 5
#define KR1 3
#define QB2 2
#define NB2 6
#define DZR 24
#define ML 150
#define MN 12
#define CF 190
#define DB 1500
#define DP 200
#define ZSL 1
#define NB1 3
#define DPT 3
#define DDMA 8
#define DZ2 800
#define QEARLY 0
#define OSLP 12
#define OSLP2 4
#define WSLP 32
#define DO0 12000
#define DW0 1200
#define DQT 9000
#define DQ4 700
#define ZEARLY 5
#define HSLP 4
#define RMAP 0
#define DIE 0
#define VMWAIT() do { asm volatile("" ::: "memory"); __builtin_amdgcn_s_waitcnt(0x0F70); asm volatile("" ::: "memory"); } while (0)
#define SB() __builtin_amdgcn_sched_barrier(0)
#define BAR() asm volatile("s_waitcnt lgkmcnt(0)\n\ts_barrier" ::: "memory")
#define PIN1(a) asm volatile("" : "+v"(a) :: "memory")
#define PIN2(a, b) asm volatile("" : "+v"(a), "+v"(b) :: "memory")
#define BAR0() asm volatile("s_barrier" ::: "memory")
__device__ __forceinline__ unsigned fresh_lane() { unsigned l; asm volatile("v_mbcnt_lo_u32_b32 %0, -1, 0\n\tv_mbcnt_hi_u32_b32 %0, -1, %0" : "=v"(l)); __builtin_assume(l < 64u); return l; }
__device__ __forceinline__ void wait_until(unsigned long long t) { while ((long long)(__builtin_amdgcn_s_memtime() - t) < 0) __builtin_amdgcn_s_sleep(1); }
struct Clk { unsigned ta, ma, cpq, qpc; };
__device__ __forceinline__ int sx24(unsigned v) { return (int)(v << 8) >> 8; }
#define WAIT_FINE \
        "s_memrealtime vcc\n\t" \
        "s_memtime s[100:101]\n\t" \
        "s_brev_b32 %[fi], -2\n\t" \
        "s_waitcnt lgkmcnt(0)\n\t" \
        "s_mov_b32 %[rp], vcc_lo\n\t" \
        "s_lshl_b32 vcc_hi, vcc_lo, 2\n\t" \
        "s_sub_u32 %[x], %[t], vcc_hi\n\t" \
        "s_bfe_i32 %[x], %[x], 0x180000\n\t" \
        "s_min_i32 %[x], %[x], 4000\n\t" \
        "s_max_i32 %[x], %[x], -4000\n\t" \
        "s_add_u32 %[t], vcc_hi, %[x]\n\t" \
        "s_mul_i32 %[x], %[x], %[cq]\n\t" \
        "s_ashr_i32 %[x], %[x], 8\n\t" \
        "s_sub_u32 %[tm], %[t], %[ta]\n\t" \
        "s_bfe_i32 %[tm], %[tm], 0x180000\n\t" \
        "s_min_i32 %[tm], %[tm], 1000000\n\t" \
        "s_max_i32 %[tm], %[tm], -1000000\n\t" \
        "s_mul_i32 %[tm], %[tm], %[cq]\n\t" \
        "s_ashr_i32 %[tm], %[tm], 8\n\t" \
        "s_add_u32 %[tm], %[tm], %[ma]\n\t" \
        "s_sub_u32 %[tm], %[tm], s100\n\t" \
        "s_lshr_b32 vcc_hi, %[cq], 4\n\t" \
        "s_sub_u32 vcc_hi, %[x], vcc_hi\n\t" \
        "s_max_i32 %[tm], %[tm], vcc_hi\n\t" \
        "s_min_i32 %[tm], %[tm], %[x]\n\t" \
        "s_add_u32 %[tm], %[tm], s100\n" \
        "1:\n\t" \
        "s_memrealtime vcc\n\t" \
        "s_memtime s[100:101]\n\t" \
        "s_waitcnt lgkmcnt(0)\n\t" \
        "s_cmp_eq_u32 vcc_lo, %[rp]\n\t" \
        "s_cbranch_scc1 2f\n\t" \
        "s_mov_b32 %[rp], vcc_lo\n\t" \
        "s_lshl_b32 %[x], vcc_lo, 2\n\t" \
        "s_sub_u32 %[x], %[t], %[x]\n\t" \
        "s_bfe_i32 %[x], %[x], 0x180000\n\t" \
        "s_mul_i32 %[x], %[x], %[cq]\n\t" \
        "s_ashr_i32 %[x], %[x], 8\n\t" \
        "s_add_u32 %[x], %[x], s100\n\t" \
        "s_sub_u32 s101, %[x], %[tm]\n\t" \
        "s_cmp_lt_i32 s101, %[fi]\n\t" \
        "s_cselect_b32 %[tm], %[x], %[tm]\n\t" \
        "s_mov_b32 %[fi], 0\n" \
        "2:\n\t" \
        "s_sub_u32 %[x], %[tm], s100\n\t" \
        "s_cmp_gt_i32 %[x], 260\n\t" \
        "s_cbranch_scc1 1b\n\t" \
        "s_cmp_eq_u32 %[fi], 0\n\t" \
        "s_cselect_b32 %[ta], %[t], %[ta]\n\t" \
        "s_cselect_b32 %[ma], %[tm], %[ma]\n" \
        "3:\n\t" \
        "s_memtime s[100:101]\n\t" \
        "s_waitcnt lgkmcnt(0)\n\t" \
        "s_sub_u32 %[x], %[tm], s100\n\t" \
        "s_cmp_gt_i32 %[x], 92\n\t" \
        "s_cbranch_scc1 3b\n\t" \
        "s_max_i32 %[x], %[x], 0\n\t" \
        "s_andn2_b32 %[x], %[x], 3\n\t" \
        "s_sub_u32 %[x], 104, %[x]\n\t" \
        "s_getpc_b64 vcc\n\t" \
        "s_add_u32 vcc_lo, vcc_lo, %[x]\n\t" \
        "s_addc_u32 vcc_hi, vcc_hi, 0\n\t" \
        "s_setpc_b64 vcc\n\t" \
        ".rept 23\n\t" \
        "s_nop 0\n\t" \
        ".endr\n\t"
#define WAIT_FINE_OUT [tm] "=&s"(tm), [rp] "=&s"(rp), [x] "=&s"(x), [fi] "=&s"(fi), [t] "+s"(t), [ta] "+s"(c.ta), [ma] "+s"(c.ma)
__device__ __forceinline__ void wait_fine(unsigned t, Clk& c) {
    unsigned tm, rp, x, fi;
    asm volatile(WAIT_FINE : WAIT_FINE_OUT : [cq] "s"(c.cpq) : "vcc", "scc", "s100", "s101", "memory");
}
__device__ __forceinline__ void wait_fine_bar(unsigned t, Clk& c) {
    unsigned tm, rp, x, fi;
    asm volatile(WAIT_FINE "s_barrier" : WAIT_FINE_OUT : [cq] "s"(c.cpq) : "vcc", "scc", "s100", "s101", "memory");
}
__device__ __forceinline__ void wait_cyc_bar(unsigned tm) {
    unsigned x;
    asm volatile(
        "3:\n\t"
        "s_memtime s[100:101]\n\t"
        "s_waitcnt lgkmcnt(0)\n\t"
        "s_sub_u32 %0, %1, s100\n\t"
        "s_cmp_gt_i32 %0, 92\n\t"
        "s_cbranch_scc1 3b\n\t"
        "s_max_i32 %0, %0, 0\n\t"
        "s_andn2_b32 %0, %0, 3\n\t"
        "s_sub_u32 %0, 104, %0\n\t"
        "s_getpc_b64 vcc\n\t"
        "s_add_u32 vcc_lo, vcc_lo, %0\n\t"
        "s_addc_u32 vcc_hi, vcc_hi, 0\n\t"
        "s_setpc_b64 vcc\n\t"
        ".rept 23\n\t"
        "s_nop 0\n\t"
        ".endr\n\t"
        "s_barrier"
        : "=&s"(x) : "s"(tm) : "vcc", "scc", "s100", "s101", "memory");
}
__device__ __forceinline__ unsigned cyc_at(const Clk& c, int d) {
    d = min(d, 20000);
    asm volatile("" : "+s"(d));
    return c.ma + (unsigned)((max(d, -4000) * (int)c.cpq) >> 8);
}
__device__ __forceinline__ void clk_rate(Clk& c, unsigned ta0, unsigned ma0) {
    const int dq = sx24(c.ta - ta0);
    if (dq > 200) {
        const float r = fminf(fmaxf((float)(c.ma - ma0) * __builtin_amdgcn_rcpf((float)dq), 1.f), 8.f);
        asm volatile("s_nop 1\n\tv_readfirstlane_b32 %0, %2\n\tv_readfirstlane_b32 %1, %3\n\ts_nop 3" : "=s"(c.cpq), "=s"(c.qpc)
                     : "v"((unsigned)(256.f * r + 0.5f)), "v"((unsigned)(65536.f * __builtin_amdgcn_rcpf(r))));
    }
}
__device__ __forceinline__ void clk_init(Clk& c) {
    c.ta = 4u * (unsigned)__builtin_amdgcn_s_memrealtime(); c.ma = (unsigned)__builtin_amdgcn_s_memtime();
    for (int i = 0; i < 2; ++i) {
        const unsigned ta0 = c.ta, ma0 = c.ma;
        wait_fine(c.ta + (i ? 360u : 60u), c);
        clk_rate(c, ta0, ma0);
    }
}
__device__ __forceinline__ int read_sched(const RecArgs& a, __amdgpu_buffer_rsrc_t rwin, int s, unsigned tg, unsigned vo, unsigned to, unsigned f, unsigned g0, int& lt, int& xs, unsigned& fc) {
    int d = (int)((f - g0) & 0xFFFFFFu);
    if (__builtin_expect(__any((f >> 24) != tg) || s == 0, 0)) {
        for (unsigned spins = 0; __any((f >> 24) != tg);) {
            FC(fc);
            if (spin_fail(spins, a.ctl)) break;
            __builtin_amdgcn_s_sleep(TSLP);
            asm volatile("" ::: "memory");
            if ((f >> 24) != tg) f = __builtin_amdgcn_raw_buffer_load_b32(rwin, vo, to, 16);
        }
        const unsigned f0 = __builtin_amdgcn_readfirstlane(f) - 0x100000u;
        d = (int)((f - f0) & 0xFFFFFFu) + (int)((f0 - g0) & 0xFFFFFFu);
    }
    d = max(d, __builtin_amdgcn_update_dpp(0, d, 0xB1, 0xF, 0xF, true));
    d = max(d, __builtin_amdgcn_update_dpp(0, d, 0x4E, 0xF, 0xF, true));
    d = max(d, __builtin_amdgcn_update_dpp(0, d, 0x141, 0xF, 0xF, true));
    d = max(d, __builtin_amdgcn_update_dpp(0, d, 0x140, 0xF, 0xF, true));
    d = max(d, __builtin_amdgcn_update_dpp(0, d, 0x142, 0xA, 0xF, true));
    d = max(__builtin_amdgcn_readlane(d, 16), __builtin_amdgcn_readlane(d, 48));
    const int l0 = lt, x0 = xs;
    d -= 0x400000;
    const bool miss = d > l0 + min(x0 + LTM, LTX);
    if (s >= SAD) { lt = miss ? l0 : min(max(d, l0 - 1 - ((l0 - d) >> LTS)), l0 + LTU); xs = miss ? min(x0 + XSU, XSM) : max(x0 - 1 - (x0 >> XSS), 0); }
    if (s == 1) lt = LT0;
    return 0x400000 + (s < SAD ? d + (tg ? PEN0 : PEN00) : miss ? d + PEN : l0 + x0) + M1 + M2 + x0;
}
constexpr int QS = 272;
constexpr int TQS = 1152;
constexpr int L_Q = 10240, L_K = L_Q + 2 * TQS, L_S = L_K + 2 * TQS, L_TOK = L_S + 256, L_H = L_TOK + 49152, L_END = L_H + 2 * 40960;
typedef __attribute__((ext_vector_type(4))) short v4i16_t;
__device__ __forceinline__ u32x2 ldtr(const char* p) { return __builtin_bit_cast(u32x2, __builtin_amdgcn_ds_read_tr16_b64_v4i16((LAS v4i16_t*)p)); }
__device__ __forceinline__ u32x2 ldtr1(const LAS char* p) { return __builtin_bit_cast(u32x2, __builtin_amdgcn_ds_read_tr16_b64_v4i16((LAS v4i16_t*)p)); }
__device__ __forceinline__ u32x4 ldtr2(const LAS char* p0, const LAS char* p1) {
    const u32x2 x = __builtin_bit_cast(u32x2, __builtin_amdgcn_ds_read_tr16_b64_v4i16((LAS v4i16_t*)p0)), y = __builtin_bit_cast(u32x2, __builtin_amdgcn_ds_read_tr16_b64_v4i16((LAS v4i16_t*)p1));
    return (u32x4){x[0], x[1], y[0], y[1]};
}
__device__ __forceinline__ u32x4 ldtr2(const char* p0, const char* p1) { const u32x2 x = ldtr(p0), y = ldtr(p1); return (u32x4){x[0], x[1], y[0], y[1]}; }

__device__ __forceinline__ unsigned poll_lim(int& budget) {
    --budget;
    unsigned lim = budget > 0 ? 0xFFFFFFFEu : 0xFFFFFFFFu;
    asm volatile("" : "+s"(lim));
    return lim;
}
#define GATHER \
        "buffer_load_dwordx4 %[vo], %[rw], %[s0] offen sc1 lds\n\t" \
        "buffer_load_dwordx4 %[vo], %[rw], %[s0] offen offset:1024 sc1 lds\n\t" \
        "buffer_load_dwordx4 %[vo], %[rw], %[s0] offen offset:2048 sc1 lds\n\t" \
        "buffer_load_dwordx4 %[vo], %[rw], %[s0] offen offset:3072 sc1 lds\n\t" \
        "s_movk_i32 m0, 0x1000\n\t" \
        "s_nop 0\n\t" \
        "buffer_load_dwordx4 %[vo], %[rw], %[s4] offen sc1 lds\n\t" \
        "buffer_load_dwordx4 %[vo], %[rw], %[s4] offen offset:1024 sc1 lds\n\t" \
        "buffer_load_dwordx4 %[vo], %[rw], %[s4] offen offset:2048 sc1 lds\n\t" \
        "buffer_load_dwordx4 %[vo], %[rw], %[s4] offen offset:3072 sc1 lds\n\t" \
        "s_movk_i32 m0, 0x2000\n\t" \
        "s_nop 0\n\t" \
        "buffer_load_dwordx4 %[vo], %[rw], %[s1] offen sc1 lds\n\t" \
        "buffer_load_dwordx4 %[vo], %[rw], %[s1] offen offset:1024 sc1 lds"
#define GATHER_IN [vo] "v"(vo), [rw] "s"(rwin), [s0] "s"(so0), [s4] "s"(so0 + 4096u), [s1] "s"(so1)
__device__ __forceinline__ void gather_dma(__amdgpu_buffer_rsrc_t rwin, unsigned vo, unsigned so0, unsigned so1) {
    asm volatile("s_mov_b32 m0, 0\n\ts_nop 0\n\t" GATHER :: GATHER_IN : "m0", "memory");
}
__device__ __forceinline__ void gather_at(unsigned t, Clk& c, __amdgpu_buffer_rsrc_t rwin, unsigned vo, unsigned so0, unsigned so1) {
    unsigned tm, rp, x, fi;
    asm volatile("s_mov_b32 m0, 0\n\t" WAIT_FINE GATHER : WAIT_FINE_OUT : [cq] "s"(c.cpq), GATHER_IN : "vcc", "scc", "s100", "s101", "m0", "memory");
}
__device__ __forceinline__ void dma4k(__amdgpu_buffer_rsrc_t r, unsigned vo, unsigned so, unsigned lds) {
    asm volatile(
        "s_mov_b32 m0, %3\n\t"
        "s_nop 0\n\t"
        "buffer_load_dwordx4 %0, %1, %2 offen sc1 lds\n\t"
        "buffer_load_dwordx4 %0, %1, %2 offen offset:1024 sc1 lds\n\t"
        "buffer_load_dwordx4 %0, %1, %2 offen offset:2048 sc1 lds\n\t"
        "buffer_load_dwordx4 %0, %1, %2 offen offset:3072 sc1 lds"
        :: "v"(vo), "s"(r), "s"(so), "s"(lds) : "m0", "memory");
}
__device__ __forceinline__ int bs2(int c) { return (c & ~3) | ((c & 1) << 1) | ((c >> 1) & 1); }
__device__ __forceinline__ void get_mem0(u32x4 (&m)[2], int t) {
    const u16* mem0 = (const u16*)karg<2>();
#pragma unroll
    for (int i = 0; i < 2; ++i) {
        const int c = min(t + 512 * i, 639);
        const unsigned* src = (const unsigned*)(mem0 + ((c & 6) + ((c >> 3) & 1)) * D_ + (c >> 4) * 16 + (c & 1) * 2);
        m[i] = (u32x4){src[0], src[2], src[4], src[6]};
    }
    SB();
}
__device__ __forceinline__ void put_mem0(const u32x4 (&m)[2], char* memL, int t) { *(u32x4*)(memL + t * 16) = m[0]; *(u32x4*)(memL + min(t + 512, 639) * 16) = m[1]; }
__device__ __forceinline__ f32x2 hadd(f32x4 c) { return (f32x2){c[0], c[1]} + (f32x2){c[2], c[3]}; }
__device__ __forceinline__ unsigned hpack(f32x4 c) { const f32x2 y = hadd(c); return pack2(y[0], y[1]); }
__device__ __forceinline__ u32x8 cat8(u32x4 lo, u32x4 hi) { return (u32x8){lo[0], lo[1], lo[2], lo[3], hi[0], hi[1], hi[2], hi[3]}; }
__device__ __forceinline__ u32x4 half8(const u32x8& v, int u) { return u ? (u32x4){v[4], v[5], v[6], v[7]} : (u32x4){v[0], v[1], v[2], v[3]}; }
__device__ __forceinline__ void dma1k(__amdgpu_buffer_rsrc_t r, unsigned vo, unsigned so, unsigned lds) {
    asm volatile("s_mov_b32 m0, %3\n\ts_nop 0\n\tbuffer_load_dwordx4 %0, %1, %2 offen sc1 lds" :: "v"(vo), "s"(r), "s"(so), "s"(lds) : "m0", "memory");
}
template <int N, int ST> __device__ __forceinline__ void stage_h(__amdgpu_buffer_rsrc_t rws, unsigned so, unsigned lds, int lane) {
#pragma unroll
    for (int i = 0; i < N; ++i) dma1k(rws, lane * 16u, so + i * ST, lds + i * ST);
}
template <int N, int ST, int M> __device__ __forceinline__ void check_h(__amdgpu_buffer_rsrc_t rws, unsigned so, unsigned lds, int lane) {
    asm volatile("s_waitcnt vmcnt(%0)" :: "n"(M) : "memory");
    for (unsigned spins = 0; spins < SPIN_CAP; ++spins) {
        unsigned m = 0;
#pragma unroll
        for (int i = 0; i < N; ++i) m = max(m, smax(*(const LAS u32x4*)(size_t)(lds + lane * 16u + i * ST)));
        if (!__any(m == 0xFFFFFFFFu)) break;
        stage_h<N, ST>(rws, so, lds, lane);
        asm volatile("s_waitcnt vmcnt(0)" ::: "memory");
    }
}
__device__ __forceinline__ void wait_h(const RecArgs& a, __amdgpu_buffer_rsrc_t rfl, unsigned fb, int seg, int& hrdy) {
    unsigned lim = hrdy ? 63u : (unsigned)min(seg + HLA, 63);
    if (__builtin_expect(hrdy <= seg, 0)) for (unsigned spins = 0;;) {
        asm volatile("" ::: "memory");
        const u32x4 f = __builtin_amdgcn_raw_buffer_load_b128(rfl, min(fresh_lane(), lim) * 128u, W_H + fb, 16);
        const unsigned long long miss = __ballot((f[0] | f[1] | f[2] | f[3]) != 0u);
        hrdy = miss ? __builtin_ctzll(miss) : (int)lim + 1;
        if (hrdy > seg) break;
        if (spin_fail(spins, a.ctl)) break;
        lim = (unsigned)min(seg + HLA, 63);
        __builtin_amdgcn_s_sleep(HSLP);
    }
}
__device__ __forceinline__ u32x4 ask_h(__amdgpu_buffer_rsrc_t rfl, unsigned fb) { return __builtin_amdgcn_raw_buffer_load_b128(rfl, fresh_lane() * 128u, W_H + fb, 16); }
__device__ __forceinline__ int see_h(u32x4 f) { const unsigned long long miss = __ballot((f[0] | f[1] | f[2] | f[3]) != 0u); return miss ? __builtin_ctzll(miss) : 64; }
template <int CTRL> __device__ __forceinline__ unsigned dppu(unsigned v) { return (unsigned)__builtin_amdgcn_mov_dpp((int)v, CTRL, 0xF, 0xF, true); }
template <int CTRL> __device__ __forceinline__ u32x4 ror4(u32x4 v) { return (u32x4){dppu<CTRL>(v[0]), dppu<CTRL>(v[1]), dppu<CTRL>(v[2]), dppu<CTRL>(v[3])}; }
__device__ __forceinline__ float xmax16(float v) {
    auto q = __builtin_amdgcn_permlane16_swap(__float_as_uint(v), __float_as_uint(v), false, false);
    return fmx(__uint_as_float(q[0]), __uint_as_float(q[1]));
}
__device__ __forceinline__ float xsum16(float v) {
    auto q = __builtin_amdgcn_permlane16_swap(__float_as_uint(v), __float_as_uint(v), false, false);
    return __uint_as_float(q[0]) + __uint_as_float(q[1]);
}
__device__ __forceinline__ float addswap16(float a, float b) {
    auto r = __builtin_amdgcn_permlane16_swap(__float_as_uint(a), __float_as_uint(b), false, false);
    return __uint_as_float(r[0]) + __uint_as_float(r[1]);
}
#define LANES() \
    const int lane = (int)fresh_lane(), tid = w * 64 + lane, fr = lane & 15, fq = lane >> 4; \
    const int sidx = (lane & 2) ? 0xEEEE : 0x4444; \
    const int ma0 = fq * 256 + (fr & 1) * 128 + (fr >> 1) * 16; \
    const int prow = 16 * ((fr >> 2) & 1) + 4 * (fr >> 3) + (fr & 3), hoff = fr * 128; \
    (void)tid; (void)sidx; (void)ma0; (void)prow; (void)hoff

__device__ __forceinline__ int seg_after(unsigned long long m, int j) { m = j >= 63 ? 0ull : m >> (j + 1); return m ? j + 1 + __builtin_ctzll(m) : NSEG; }
__device__ __forceinline__ int seg_done(unsigned long long m, int f0, int f1) { return min(seg_after(~m, f0), seg_after(m, f1)) - 1; }
template <int OFF> __device__ __forceinline__ void ldfrag(u32x4& d, const LAS char* p) { asm volatile("ds_read_b128 %0, %1 offset:%2" : "=v"(d) : "v"(p), "n"(OFF) : "memory"); }
template <int U, class FM, class FV> __device__ __forceinline__ void project(const u32x8 (&wt)[2][10], f32x4 (&tc)[2][2], u32x4 (&bb)[8], const LAS char* c0, const LAS char* c1, const LAS char* n0, const LAS char* n1, FM mid, FV vm) {
    constexpr int t = U >> 2, k = U & 3, v = (U + 8) % 40;
    if (U == 32) mid();
    vm(IC<U>());
    const LAS char* p = U < 32 ? (k >> 1 ? c1 : c0) : (k >> 1 ? n1 : n0);
    if (U < 2) asm volatile("s_nop 0\n\ts_waitcnt lgkmcnt(7)\n\tv_mfma_f32_16x16x32_bf16 %0, %3, %2, 0\n\tv_mfma_f32_16x16x32_bf16 %1, %4, %2, 0\n\tds_read_b128 %2, %5 offset:%6"
                            : "=&v"(tc[0][k & 1]), "=&v"(tc[1][k & 1]), "+v"(bb[U & 7]) : "v"(half8(wt[0][t], k >> 1)), "v"(half8(wt[1][t], k >> 1)), "v"(p), "n"((v >> 2) * 4096 + (k & 1) * 2048) : "memory");
    else asm volatile(".rept %7\n\ts_nop 0\n\t.endr\n\ts_waitcnt lgkmcnt(7)\n\tv_mfma_f32_16x16x32_bf16 %0, %3, %2, %0\n\tv_mfma_f32_16x16x32_bf16 %1, %4, %2, %1\n\tds_read_b128 %2, %5 offset:%6"
                      : "+v"(tc[0][k & 1]), "+v"(tc[1][k & 1]), "+v"(bb[U & 7]) : "v"(half8(wt[0][t], k >> 1)), "v"(half8(wt[1][t], k >> 1)), "v"(p), "n"((v >> 2) * 4096 + (k & 1) * 2048), "n"(U < 8 ? 1 : 0) : "memory");
    if constexpr (U < 39) project<U + 1>(wt, tc, bb, c0, c1, n0, n1, mid, vm);
    else asm volatile("s_nop 7" : "+v"(tc[0][0]), "+v"(tc[0][1]), "+v"(tc[1][0]), "+v"(tc[1][1]));
}
__device__ __forceinline__ void project_end(u32x4 (&bb)[8]) {
    asm volatile("s_waitcnt lgkmcnt(0)" : "+v"(bb[0]), "+v"(bb[1]), "+v"(bb[2]), "+v"(bb[3]), "+v"(bb[4]), "+v"(bb[5]), "+v"(bb[6]), "+v"(bb[7]) :: "memory");
}
__device__ __forceinline__ void project0(u32x4 (&bb)[8], const LAS char* c0, const LAS char* c1) {
    ldfrag<0>(bb[0], c0); ldfrag<2048>(bb[1], c0); ldfrag<0>(bb[2], c1); ldfrag<2048>(bb[3], c1);
    ldfrag<4096>(bb[4], c0); ldfrag<6144>(bb[5], c0); ldfrag<4096>(bb[6], c1); ldfrag<6144>(bb[7], c1);
    project_end(bb);
}
constexpr int WLB = 20992;
template <bool O, class F> __device__ __forceinline__ void load_w(u32x8 (&wt)[2][10], const char* W, int hh, int w, int c0, int cd, char* st, F gap) {
    const int lane = (int)fresh_lane(), fr = lane & 15, fq = lane >> 4, x = w & 3;
    const __amdgpu_buffer_rsrc_t r = mkrsrc(W, D_ * HD * 2);
    u32x4 raw[40];
    const unsigned vo = O ? (unsigned)(lane * 16) : (unsigned)(fq * 8192 + fr * 16);
    unsigned so = O ? (unsigned)(hh * 128 + 8 * x) * 1280u : (unsigned)(8 * x * 8192 + hh * 256);
    asm volatile("" : "+s"(so));
    auto fetch = [&](int i) {
        if (O) raw[i] = __builtin_amdgcn_raw_buffer_load_b128(r, vo + (i % 5 & 3) * 1024u, so + (unsigned)((32 * (i / 10) + 4 * (i / 5 & 1)) * 1280 + (i % 5 >> 2) * 4096), WAUX);
        else raw[i] = __builtin_amdgcn_raw_buffer_load_b128(r, vo, so + (unsigned)(32 * (i >> 1) + 4 * (i & 1)) * 8192u, WAUX);
    };
#pragma unroll
    for (int i = 0; i < 5 * WLA; ++i) { fetch(i); if (i % 5 == 4) { BAR0(); gap(i / 5); } }
    st += (w >> 2) * WLB;
    const int row = 4 * fq + (fr >> 2);
    int wo[5];
#pragma unroll
    for (int e = 0; e < 5; ++e) {
        const int id = 64 * e + lane, rr = id / 80, ch = id - 80 * rr;
        wo[e] = O ? (4 * x + rr) * 1312 + (ch >> 1) * 32 + (ch & 1) * 4 : (4 * x + fq) * 256 + ((fr ^ (2 * ((4 * x + fq) & 7))) * 16) + e * 4096;
    }
    const int r0 = O ? row * 1312 + x * 320 + (fr & 3) * 8 : row * 256 + (((c0 + ((fr >> 1) & 1)) ^ (2 * (row & 7))) * 16) + (fr & 1) * 8;
    const int r1 = O ? 0 : row * 256 + (((c0 + cd + ((fr >> 1) & 1)) ^ (2 * (row & 7))) * 16) + (fr & 1) * 8;
#pragma unroll
    for (int p = 0; p < 8; ++p) {
        char* b = st + (p & 1) * 2 * WLB;
#pragma unroll
        for (int e = 0; e < 5; ++e) {
            const u32x4 v = raw[5 * p + e];
            if (O) {
                unsigned* d = (unsigned*)(b + wo[e]);
                d[0] = __builtin_amdgcn_perm(v[2], v[0], 0x05040100); d[2] = __builtin_amdgcn_perm(v[3], v[1], 0x05040100);
                d[4] = __builtin_amdgcn_perm(v[2], v[0], 0x07060302); d[6] = __builtin_amdgcn_perm(v[3], v[1], 0x07060302);
            } else *(u32x4*)(b + wo[e]) = v;
        }
        BAR();
#pragma unroll
        for (int e = 0; e < 5; ++e) if (p + WLA < 8) fetch(5 * (p + WLA) + e);
        SB();
        gap(p + WLA);
#pragma unroll
        for (int e = 0; e < 10; ++e) {
            const int P = O ? p : 5 * p + (e >> 1), d = 2 * (P & 3);
            const u32x2 y = ldtr(b + (O ? r0 + e * 32 : (e & 1 ? r1 : r0) + (e >> 1) * 4096));
            u32x8& t = O ? wt[P >> 2][e] : wt[e & 1][P >> 2];
            t[d] = y[0]; t[d + 1] = y[1];
        }
    }
    BAR();
}

__global__ __launch_bounds__(512, 2) B3ATTR void b3_kernel(RecArgs a) {
    extern __shared__ __attribute__((aligned(16))) char smem[];
    char* memL = smem;
    char* tokL = smem + L_TOK;
    char* hL = smem + L_H;
    int w = __builtin_amdgcn_readfirstlane(threadIdx.x >> 6);
    const __amdgpu_buffer_rsrc_t rwin = mkrsrc(a.win, W_END + 2 * PAIR);
    char* const ws = (char*)a.ctl;
    const __amdgpu_buffer_rsrc_t rws = mkrsrc(ws, (unsigned)OFF_WIN);
    unsigned long long tref = 0;
    const unsigned long long tstart = __builtin_amdgcn_s_memtime();
    auto get_tag = [&]() __attribute__((always_inline)) {
        unsigned long long e;
        asm volatile("s_load_dwordx2 %0, %1, 8\n\ts_waitcnt lgkmcnt(0)" : "=s"(e) : "s"(a.ctl) : "memory");
        e += 0x9E3779B97F4A7C15ull;
        return (u32x2){(unsigned)e, (unsigned)(e >> 32)};
    };
    auto role = [&](unsigned rot) {
        const unsigned vx = blockIdx.x + rot;
        const int j = blockIdx.x >> 3, x = vx & 3, hh = 8 * (blockIdx.x & 3) + (j & 7);
        int r;
        if (!(vx & 4)) r = j < 8 ? hh : j < 16 ? 32 + hh : j < 26 ? 80 + 10 * x + j : j < 30 ? 255 - 4 * (j - 26) - x : NXT + hh;
        else r = j < 8 ? NQT + hh : j < 16 ? 64 + hh : j < 26 ? NREC - 16 + 10 * x + j : NXT + hh - 2;
        return r;
    };
    int bid = role(xcc_id() - blockIdx.x + BADG * (blockIdx.x >> 3));
    unsigned nh = 0, nz = 0, mc = 0, qz = 0;
    auto hint = [&](unsigned vo, unsigned so) __attribute__((always_inline)) {
        const u32x2 tag = get_tag();
        u32x2 f;
        for (unsigned spins = 0; spins < SPIN_CAP; ++spins) {
            asm volatile("" ::: "memory");
            f = __builtin_amdgcn_raw_buffer_load_b64(rwin, vo, so, 16);
            if (!__any(f[1] != tag[1] || (f[0] ^ tag[0]) > 255u)) break;
            __builtin_amdgcn_s_sleep(4);
        }
        return f[0] & 255u;
    };
    auto tell = [&](unsigned so, unsigned v) __attribute__((always_inline)) {
        const u32x2 tag = get_tag();
        __builtin_amdgcn_raw_buffer_store_b64((u32x2){(tag[0] & ~255u) | v, tag[1]}, rwin, 0u, so, 16);
    };
    auto share = [&](unsigned v) __attribute__((always_inline)) {
        if (w == 0) *(volatile LAS unsigned*)(smem + L_S + 64) = v;
        BAR();
        v = __builtin_amdgcn_readfirstlane(*(volatile LAS unsigned*)(smem + L_S + 64));
        BAR();
        return v;
    };
    auto config = [&]() __attribute__((always_inline)) {
        unsigned v = 0;
        if (w == 0) {
            const unsigned l = fresh_lane(), f = hint(l * 8u, W_CFG), rot = __builtin_amdgcn_readfirstlane(hint(0u, W_ARM));
            const unsigned long long ok = __ballot((f & 4u) && !((l + rot) & 4u)), h = __ballot(f & 1u), q = __ballot(((f >> 1) ^ (l + rot)) & 1u);
            v = rot;
#pragma unroll
            for (int p = 0; p < 2; ++p) {
                unsigned long long m = ok & (p ? 0xFF00FF00FF00FF00ull : 0x00FF00FF00FF00FFull);
                const bool hb = 2 * __builtin_popcountll(m & h) > __builtin_popcountll(m);
                m &= hb ? h : ~h;
                v |= (hb ? 8u : 0u) << p | (2 * __builtin_popcountll(m & q) > __builtin_popcountll(m) ? 32u : 0u) << p;
            }
        }
        v = share(v);
        nh = (v >> 3 & 1u) * HALF; nz = (v >> 4 & 1u) * HALF;
        mc = ((blockIdx.x + v) & 3u) ^ (v >> 5 & 1u); qz = v >> 6 & 1u;
        return v;
    };
    auto zpair = [&](unsigned x, unsigned k) { return (((x >> 1) * 10u + k) * 2u + ((x & 1u) ^ qz)) * PAIR + nz; };
    unsigned mo, mo1, tro;
    auto two = [&]() { const unsigned l = fresh_lane(); return l < 4 ? (l >> 1) * PAIR + (l & 1) * 4096u + nh : 0x40000000u; };
    const u32x4 SENT = {0xFFFFFFFFu, 0xFFFFFFFFu, 0xFFFFFFFFu, 0xFFFFFFFFu};
    const f32x4 z4 = {0.f, 0.f, 0.f, 0.f};
    int hrdy = 0, budget = (int)SPIN_CAP;
    unsigned fc0 = 0, fc1 = 0, fc2 = 0;
    Clk ck = {0u, 0u, 1600u, 10486u};
    int lay = 0;
    unsigned fb = 0, zb = W_ZM, hb = 0;
    unsigned tpp = 0;
    int lt = LT1, xs = XS0;
    struct Tk { unsigned vo, tw, sl; const LAS char* sp; };
    auto tk_init = [&]() {
        const unsigned l = fresh_lane();
        Tk k = {l * 16u, two(), (l & 31u) * 128u + (l >> 5) * 4u, (const LAS char*)(memL + l * 64 + 4 + ((l >> 1) & 7) * 8)};
        PIN2(k.vo, k.tw); PIN2(k.sl, k.sp);
        return k;
    };
    auto sched2 = [&](Tk& k, int s, unsigned f) {
        const unsigned g0 = tpp - 0x400000u;
        tpp = g0 + (unsigned)read_sched(a, rwin, s, (unsigned)(s + 64 * lay), k.sl, tro + (unsigned)(s & 3) * 2u * PAIR, f, g0, lt, xs, fc1);
        k.sl &= ~4u;
        RT(6);
    };
    auto sched = [&](Tk& k, int s) { sched2(k, s, __builtin_amdgcn_raw_buffer_load_b32(rwin, k.sl, tro + (unsigned)(s & 3) * 2u * PAIR, 16)); };
    unsigned it = 0;
    auto fetch = [&](const Tk& k, int s, bool first, unsigned lim, unsigned dc, unsigned fo, bool tell) {
        const unsigned sb = (unsigned)(s & 3) * 6u * PAIR, ta0 = ck.ta, mm0 = ck.ma;
        if (__builtin_expect(s > 0, 1)) {
            if (__builtin_expect(first, 1)) while ((unsigned)sx24(tpp - TKE - 4u * (unsigned)__builtin_amdgcn_s_memrealtime()) < 20000u) __builtin_amdgcn_s_sleep(1);
            if (__builtin_expect(first, 1)) gather_at(tpp, ck, rwin, k.vo, mo + sb, mo1 + sb); else { const unsigned c = (mc + 1u + (it & 1u)) & 3u; gather_dma(rwin, k.vo, W_MEM + c * PAIR + nh + sb, W_MEM + (4u + (c & 1u)) * PAIR + nh + (c >> 1) * 2048u + sb); }
        }
        __builtin_amdgcn_s_setprio(0);
        RT(1);
        clk_rate(ck, ta0, mm0);
        unsigned tb = ck.ma - dc, tg = (unsigned)(s + 64 * lay) << 24, so = W_T + (unsigned)(s & 3) * 2u * PAIR + fo;
        asm volatile("" : "+s"(tb), "+s"(tg), "+s"(so));
        asm volatile("s_waitcnt vmcnt(%1) lgkmcnt(0)\n\ts_barrier\n\ts_waitcnt vmcnt(%2)\n\ts_barrier\n\ts_waitcnt vmcnt(0)\n\ts_memtime %0\n\ts_barrier"
                     : "=s"(tref) : "n"(10 - NB1), "n"(10 - NB2) : "memory");
        MT(1);
        if (!tell) { RT(3); return; }
        unsigned x0 = *(const LAS unsigned*)k.sp, x1 = *(const LAS unsigned*)(k.sp + 4096), x2 = *(const LAS unsigned*)(k.sp + 6144);
        __builtin_amdgcn_s_waitcnt(0xC07F);
        asm volatile("" : "+s"(tref) :: "memory");
        const unsigned fw = ((ck.ta + ((((unsigned)tref - tb) * ck.qpc) >> 16)) & 0xFFFFFFu) | tg;
        __builtin_amdgcn_raw_buffer_store_b32(fw, rwin, k.tw, __any(max(max(x0, x1), x2) > lim) ? 0x40000000u : so, 16);
        RT(3);
    };
    auto places = [&]() {
        mo = W_MEM + mc * PAIR + nh; mo1 = W_MEM + (4u + (mc & 1u)) * PAIR + nh + (mc >> 1) * 2048u;
        tro = W_T + (mc & 1u) * PAIR + nh + (mc >> 1) * 4096u;
    };
    auto fresh = [&]() {
        asm volatile("" : "+s"(bid), "+s"(w), "+s"(nh), "+s"(nz), "+s"(mc), "+s"(qz), "+s"(lay));
        fb = lay * W_FL; hb = O_H + (1 - lay) * (4u * 40960u); zb = W_ZM + lay * (NSEG % 3 * NOWN * PAIR);
        tref = 0; hrdy = 0; budget = (int)SPIN_CAP; ck = Clk{0u, 0u, 1600u, 10486u}; tpp = 0; lt = LT1; xs = XS0; it = 0;
    };
    auto a1_done = [&]() __attribute__((always_inline)) {
        VMWAIT();
        BAR();
        if (w == 0) __builtin_amdgcn_raw_buffer_store_b64(get_tag(), rwin, 0u, W_ST + (blockIdx.x - 128u >> 1) * 128u + (blockIdx.x & 1u) * 8u, 16);
        SST(29);
    };
    auto due = [&](int i, int d) __attribute__((always_inline)) {
        __builtin_amdgcn_raw_buffer_store_b32(nosent1((unsigned)__builtin_amdgcn_s_memrealtime() + d), rwin, 0u, W_E + fb + i * 128u, 16);
    };
    auto await = [&](int i, int n, int d, unsigned f = ~0u) __attribute__((always_inline)) {
        if (__any(f == ~0u)) for (unsigned spins = 0;;) {
            asm volatile("" ::: "memory");
            f = __builtin_amdgcn_raw_buffer_load_b32(rwin, min(fresh_lane(), n - 1u) * 128u, W_E + i * 128u, 16);
            if (!__any(f == ~0u)) break;
            if (spin_fail(spins, a.ctl)) break;
            __builtin_amdgcn_s_sleep(HSLP);
        }
        while (__any(f + d - (unsigned)__builtin_amdgcn_s_memrealtime() - 1u < 1000u)) {}
    };
    constexpr int E_OUT = 0, E_A1 = 8, E_A2 = 16, E_A3 = 24;
    auto soon = [&](int j) __attribute__((always_inline)) {
        if (w == 0) { if (j >= 4) wait_h(a, rwin, fb, j, hrdy); else await(E_A3 + fb / 128u, 4, 0); }
        BAR();
    };
    auto gap = [&](int p) __attribute__((always_inline)) {
        if (blockIdx.x < 128) { if (p == WLA - 1 && w == 7) clk_init(ck); }
        else if (XSLP && p < 7 && (long long)(__builtin_amdgcn_s_memtime() - tstart) > 50000) __builtin_amdgcn_s_sleep(XSLP);
    };
    auto retime = [&]() __attribute__((always_inline)) {
        if (lay) clk_init(ck);
        else { const unsigned ta0 = ck.ta, ma0 = ck.ma; wait_fine(4u * (unsigned)__builtin_amdgcn_s_memrealtime() + 60u, ck); clk_rate(ck, ta0, ma0); }
    };
    SST(27);
    {
        LANES();
        const unsigned u = blockIdx.x - 128u;
        if (blockIdx.x < 64) {
            filter_unit((const u16*)karg<9>(), (const u16*)karg<10>(), blockIdx.x * 32, rws, (u16*)smem, tid);
            VMWAIT();
            BAR();
            if (w == 0) __builtin_amdgcn_raw_buffer_store_b64(get_tag(), rwin, (lane & 1) * 8u, W_ST + blockIdx.x * 128u + 64u, 16);
        } else if (blockIdx.x < 128) {
            if (w == 0) tell(W_CFG + (blockIdx.x - 64u) * 8u, bid < 64 ? 4u | probe(rwin, (blockIdx.x & 8u ? W_END : W_PROBE) + ((blockIdx.x >> 1 & 24u) | (blockIdx.x & 7u)) * 128u, lane) : 0u);
            BAR0();
        } else a1_unit<8, 512>(mkrsrc(karg<3>(), S_ * D_ * 2), u * 16, false, rws, smem, row_tw(lane), w, lane, [&](int k) {
            if (k < 0) {
                if (u == 0 && w == 0) {
                    tell(W_ROT, xcc_id());
                    __builtin_amdgcn_raw_buffer_store_b32(0u, rws, 0u, 0u, 16);
                }
                u32x4 sv = SENT;
                PIN1(sv);
#pragma unroll
                for (int i = 0; i < 4; ++i) {
                    static_assert(ARM_END - OFF_ARM <= 512 * 8192 && W_PROBE + W_END - W_ZM <= 512 * 8192, "arming");
                    const unsigned g = (i * 128u + u) * 8192u;
                    if (g < W_PROBE + W_END - W_ZM) st128(sv, rwin, tid * 16u, g < W_PROBE ? g : g + W_ZM - W_PROBE, 16);
                    if (g < ARM_END - OFF_ARM) st128(sv, rws, tid * 16u, (unsigned)OFF_ARM + g, 16);
                }
            }
        });
        SST(28);
        if (blockIdx.x >= 128) a1_done();
        if (blockIdx.x >= 208) {
            const unsigned rot = share(w == 0 ? hint(0u, W_ROT) : 0u);
            bid = role(rot);
            if (bid == 255 && w == 0) {
                const u32x2 tag = get_tag();
                for (unsigned spins = 0; spins < SPIN_CAP; ++spins) {
                    asm volatile("" ::: "memory");
                    const u32x4 g = __builtin_amdgcn_raw_buffer_load_b128(rwin, lane * 128u, W_ST, 16);
                    if (!__any(((g[0] ^ tag[0]) | (g[1] ^ tag[1]) | (g[2] ^ tag[0]) | (g[3] ^ tag[1])) != 0u)) break;
                    __builtin_amdgcn_s_sleep(4);
                }
                tell(W_ARM, rot);
            }
            SST(30);
        }
    }
    if (bid < 96 || (bid >= NQT && bid < NWRK)) {
    u32x8 wt[2][10];
#pragma nounroll
    for (; lay < 2; ++lay) {
    fresh();
    const bool xt = bid >= NXT;
    SST(0);
    if ((bid >= 32 && bid < 96) || xt) {
        const int hh = (xt ? bid - NXT : bid) & 31;
        u32x4 m0[2];
        get_mem0(m0, w * 64 + fresh_lane());
        if (lay == 0 || xt) {
            wait_until(tstart + (bid < 64 ? DVO : xt ? DXT : DW1));
            if (w < 4) load_w<false>(wt, karg<7>(), hh, w, 2 * w, 8, tokL, [&](int p) { if (XSLP) gap(p); }); else load_w<true>(wt, karg<8>(), hh, w, 0, 0, tokL, gap);
            SST(5);
            if (lay == 0) bid = role(config());
        }
        places();
        const bool vt = bid >= 64;
        const unsigned long long mv = xt ? MXV : ~MXV;
        auto nextv = [&](int j) { return min(seg_after(mv, j), NSEG - 1); };
        const int jv1 = nextv(-1), jv2 = nextv(jv1);
        {
            LANES();
            put_mem0(m0, memL, tid);
            VMWAIT();
            if (tid < 8) *(unsigned*)(smem + L_S + tid * 4) = 0u;
            if (!vt && w == 7) retime();
            SST(1);
            if (vt) {
                soon(jv1);
                SST(2);
                const unsigned so = hb + (unsigned)(jv1 * 40960 + w * 1024), lo = L_H + w * 1024;
                stage_h<5, 8192>(rws, so, lo, lane);
                BAR0();
                if (w < 4) check_h<5, 8192, 0>(rws, so, lo, lane);
                else { stage_h<10, 4096>(rws, hb + (unsigned)(jv2 * 40960 + (w - 4) * 1024), lo + 36864u, lane); check_h<5, 8192, 10>(rws, so, lo, lane); }
            }
        }
        auto steps = [&](auto cc) {
            constexpr int C = decltype(cc)::v & 1;
            constexpr bool TK = decltype(cc)::v == 3;
            LANES();
            Tk tk = {0u, 0u, 0x40000000u, nullptr};
            if (TK) { tk = tk_init(); __builtin_amdgcn_s_setprio(TKP); }
            const unsigned zi = (unsigned)(hh * 128 + fr * 8) * 2u;
            unsigned zo0 = zpair(w & 3, fq) + zi, zo1 = zo0 + 8 * PAIR, zo2 = zpair(w & 3, 8 + (fq >> 1)) + zi + (fq & 1) * 8;
            const LAS char* va = (const LAS char*)(smem + L_Q + (fr & 1) * 544 + ((fr >> 1) & 1) * 1152 + (16 * fq + 2 * (fr >> 2)) * 4);
            LAS char* vw = (LAS char*)(smem + L_Q + (fq & 1) * 544 + (fq >> 1) * 1152 + (16 * w + bs2(fr)) * 4);
            const LAS char* mp0 = (const LAS char*)(memL + ma0);
            PIN2(zo0, zo1); PIN1(zo2);
            if (C == 0) { PIN2(mp0, vw); } else PIN1(va);
            u32x4 za = SENT, zc = SENT;
            u32x2 ze = {0u, 0u};
            auto put_z2 = [&](int s) {
                const unsigned z2b = s < NSEG ? O_Z2 + (unsigned)s * (NOWN * 8192u) + (unsigned)(10 * (w & 3) * 8192 + hh * 256) : 0x40000000u, l2 = fresh_lane();
                st128(za, rws, (l2 >> 4) * 8192u + (l2 & 15u) * 16u, z2b, 16);
                st128(zc, rws, (l2 >> 4) * 8192u + (l2 & 15u) * 16u, z2b + 4u * 8192u, 16);
                __builtin_amdgcn_raw_buffer_store_b64(ze, rws, (l2 >> 5) * 8192u + (l2 & 15u) * 16u + ((l2 >> 4) & 1u) * 8u, z2b + 8u * 8192u, 16);
            };
            for (int s = 0; s < NSEG; ++s) {
                RT(0);
                bool redo = false;
                unsigned sf = 0;
                do {
                    ++it;
                    if (C == 0) {
                        const unsigned lim = s > 0 ? poll_lim(budget) : ~0u;
                        asm volatile(
                            "v_mov_b64 v[200:201], 0\n\t"
                            "v_mov_b64 v[202:203], 0\n\t"
                            "v_mov_b64 v[204:205], 0\n\t"
                            "v_mov_b64 v[206:207], 0\n\t"
                            "v_mov_b32 v208, 0\n\t"
                            "s_setprio 2\n\t"
                            "s_waitcnt lgkmcnt(0)\n\t"
                            "s_barrier\n\t"
                            MTA(0)
                            "ds_read_b128 v[212:215], %[mp] offset:0\n\t"
                            "ds_read_b128 v[216:219], %[mp] offset:1024\n\t"
                            "ds_read_b128 v[220:223], %[mp] offset:2048\n\t"
                            "s_barrier\n\t"
                            "ds_read_b128 v[224:227], %[mp] offset:3072\n\t"
                            "ds_read_b128 v[228:231], %[mp] offset:4096\n\t"
                            "ds_read_b128 v[232:235], %[mp] offset:5120\n\t"
                            "s_waitcnt lgkmcnt(5)\n\t"
                            "v_smfmac_f32_16x16x64_bf16 v[200:203], v[212:215], %[w00], %[ix]\n\t"
                            "s_waitcnt lgkmcnt(4)\n\t"
                            "v_smfmac_f32_16x16x64_bf16 v[200:203], v[216:219], %[w01], %[ix]\n\t"
                            "s_barrier\n\t"
                            MTA(1)
                            "ds_read_b128 v[236:239], %[mp] offset:6144\n\t"
                            "s_waitcnt lgkmcnt(4)\n\t"
                            "v_smfmac_f32_16x16x64_bf16 v[200:203], v[220:223], %[w02], %[ix]\n\t"
                            "ds_read_b128 v[240:243], %[mp] offset:7168\n\t"
                            "s_waitcnt lgkmcnt(4)\n\t"
                            "v_smfmac_f32_16x16x64_bf16 v[200:203], v[224:227], %[w03], %[ix]\n\t"
                            "ds_read_b128 v[244:247], %[mp] offset:8192\n\t"
                            "s_waitcnt lgkmcnt(4)\n\t"
                            "v_smfmac_f32_16x16x64_bf16 v[200:203], v[228:231], %[w04], %[ix]\n\t"
                            "ds_read_b128 v[248:251], %[mp] offset:9216\n\t"
                            "s_waitcnt lgkmcnt(4)\n\t"
                            "v_smfmac_f32_16x16x64_bf16 v[200:203], v[232:235], %[w05], %[ix]\n\t"
                            "s_waitcnt lgkmcnt(3)\n\t"
                            "v_smfmac_f32_16x16x64_bf16 v[200:203], v[236:239], %[w06], %[ix]\n\t"
                            "s_waitcnt lgkmcnt(2)\n\t"
                            "v_smfmac_f32_16x16x64_bf16 v[200:203], v[240:243], %[w07], %[ix]\n\t"
                            "s_waitcnt lgkmcnt(1)\n\t"
                            "v_smfmac_f32_16x16x64_bf16 v[200:203], v[244:247], %[w08], %[ix]\n\t"
                            "s_waitcnt lgkmcnt(0)\n\t"
                            "v_smfmac_f32_16x16x64_bf16 v[200:203], v[248:251], %[w09], %[ix]\n\t"
                            "s_nop 2\n\t"
                            "v_smfmac_f32_16x16x64_bf16 v[204:207], v[212:215], %[w10], %[ix]\n\t"
                            "s_nop 2\n\t"
                            "v_smfmac_f32_16x16x64_bf16 v[204:207], v[216:219], %[w11], %[ix]\n\t"
                            "v_add_f32 v200, v200, v202\n\t"
                            "v_add_f32 v201, v201, v203\n\t"
                            "v_cvt_pk_bf16_f32 v200, v200, v201\n\t"
                            "ds_write_b32 %[vw], v200\n\t"
                            MTA(2)
                            "v_smfmac_f32_16x16x64_bf16 v[204:207], v[220:223], %[w12], %[ix]\n\t"
                            "v_max3_u32 v208, v208, v212, v213\n\t"
                            "v_max3_u32 v208, v208, v214, v215\n\t"
                            "v_smfmac_f32_16x16x64_bf16 v[204:207], v[224:227], %[w13], %[ix]\n\t"
                            "v_max3_u32 v208, v208, v216, v217\n\t"
                            "v_max3_u32 v208, v208, v218, v219\n\t"
                            "v_smfmac_f32_16x16x64_bf16 v[204:207], v[228:231], %[w14], %[ix]\n\t"
                            "v_max3_u32 v208, v208, v220, v221\n\t"
                            "v_max3_u32 v208, v208, v222, v223\n\t"
                            "v_smfmac_f32_16x16x64_bf16 v[204:207], v[232:235], %[w15], %[ix]\n\t"
                            "v_max3_u32 v208, v208, v224, v225\n\t"
                            "v_max3_u32 v208, v208, v226, v227\n\t"
                            "v_smfmac_f32_16x16x64_bf16 v[204:207], v[236:239], %[w16], %[ix]\n\t"
                            "v_max3_u32 v208, v208, v228, v229\n\t"
                            "v_max3_u32 v208, v208, v230, v231\n\t"
                            "v_smfmac_f32_16x16x64_bf16 v[204:207], v[240:243], %[w17], %[ix]\n\t"
                            "s_waitcnt lgkmcnt(0)\n\t"
                            "s_barrier\n\t"
                            MTA(5)
                            "v_smfmac_f32_16x16x64_bf16 v[204:207], v[244:247], %[w18], %[ix]\n\t"
                            "v_max3_u32 v208, v208, v232, v233\n\t"
                            "v_max3_u32 v208, v208, v234, v235\n\t"
                            "v_smfmac_f32_16x16x64_bf16 v[204:207], v[248:251], %[w19], %[ix]\n\t"
                            "v_max3_u32 v208, v208, v236, v237\n\t"
                            "v_max3_u32 v208, v208, v238, v239\n\t"
                            "v_max3_u32 v208, v208, v240, v241\n\t"
                            "v_max3_u32 v208, v208, v242, v243\n\t"
                            "v_max3_u32 v208, v208, v244, v245\n\t"
                            "v_max3_u32 v208, v208, v246, v247\n\t"
                            "v_max3_u32 v208, v208, v248, v249\n\t"
                            "v_max3_u32 v208, v208, v250, v251\n\t"
                            "v_cmp_lt_u32 vcc, %[lim], v208\n\t"
                            "v_add_f32 v204, v204, v206\n\t"
                            "v_add_f32 v205, v205, v207\n\t"
                            "v_cvt_pk_bf16_f32 v204, v204, v205\n\t"
                            "ds_write_b32 %[vw], v204 offset:256\n\t"
                            MTA(3)
                            "s_cbranch_vccz 1f\n\t"
                            "v_mov_b32 v208, %[it]\n\t"
                            "v_mov_b32 v209, 0\n\t"
                            "ds_write_b32 v209, v208 offset:%[ls]\n\t"
                            "1:\n\t"
                            "s_waitcnt vmcnt(0) lgkmcnt(0)\n\t"
                            "s_barrier\n\t"
                            "s_setprio 0\n\t"
                            :: [mp] "v"(mp0), [vw] "v"(vw), [ix] "v"(sidx), [lim] "s"(lim), [it] "s"(it), [ls] "n"(L_S),
                              [w00] "v"(wt[0][0]), [w01] "v"(wt[0][1]), [w02] "v"(wt[0][2]), [w03] "v"(wt[0][3]), [w04] "v"(wt[0][4]), [w05] "v"(wt[0][5]), [w06] "v"(wt[0][6]), [w07] "v"(wt[0][7]), [w08] "v"(wt[0][8]), [w09] "v"(wt[0][9]), [w10] "v"(wt[1][0]), [w11] "v"(wt[1][1]), [w12] "v"(wt[1][2]), [w13] "v"(wt[1][3]), [w14] "v"(wt[1][4]), [w15] "v"(wt[1][5]), [w16] "v"(wt[1][6]), [w17] "v"(wt[1][7]), [w18] "v"(wt[1][8]), [w19] "v"(wt[1][9])
                            : "v200", "v201", "v202", "v203", "v204", "v205", "v206", "v207", "v208", "v209", "v212", "v213", "v214", "v215", "v216", "v217", "v218", "v219", "v220", "v221", "v222", "v223", "v224", "v225", "v226", "v227", "v228", "v229", "v230", "v231", "v232", "v233", "v234", "v235", "v236", "v237", "v238", "v239", "v240", "v241", "v242", "v243", "v244", "v245", "v246", "v247", "v248", "v249", "v250", "v251", "vcc", "memory");
                        MT(4);
                        redo = __builtin_amdgcn_readfirstlane(*(volatile LAS unsigned*)(smem + L_S)) == it;
                        if (redo) FC(fc0);
                    } else {
                        f32x16 ca, cb;
                        f32x8 cc;
                        asm volatile(".irp r,212,214,216,218,220,222,224,226,228,230,232,234,236,238,240,242,244,246,248,250\n\tv_mov_b64 v[\\r:\\r+1], 0\n\t.endr"
                                     : "={v[212:227]}"(ca), "={v[228:243]}"(cb), "={v[244:251]}"(cc));
                        unsigned sr = tro + (unsigned)(s & 3) * 2u * PAIR;
                        asm volatile("" : "+s"(sr));
                        if (TK) fetch(tk, s, !redo, s > 0 ? poll_lim(budget) : ~0u, DVC, (unsigned)hh * 128u + 4u, __builtin_expect(s == 0, 0));
                        else asm volatile("s_waitcnt lgkmcnt(0)\n\ts_barrier\n\ts_barrier\n\ts_barrier" ::: "memory");
                        unsigned vd, vv, zs = zb;
                        u32x4 af1;
                        asm volatile(
                            "s_setprio 1\n\t"
                            "s_waitcnt vmcnt(0) lgkmcnt(0)\n\t"
                            "s_barrier\n\t"
                            "ds_read_b64_tr_b16 v[192:193], %[va]\n\t"
                            "ds_read_b64_tr_b16 v[194:195], %[va] offset:32\n\t"
                            "s_waitcnt lgkmcnt(0)\n\t"
                            "v_smfmac_f32_16x16x64_bf16 v[212:215], v[192:195], %[w0], %[ix]\n\t"
                            "v_smfmac_f32_16x16x64_bf16 v[220:223], v[192:195], %[w2], %[ix]\n\t"
                            "v_smfmac_f32_16x16x64_bf16 v[216:219], v[192:195], %[w1], %[ix]\n\t"
                            "v_smfmac_f32_16x16x64_bf16 v[224:227], v[192:195], %[w3], %[ix]\n\t"
                            "s_barrier\n\t"
                            MTA(4)
                            "ds_read_b64_tr_b16 v[196:197], %[va] offset:256\n\t"
                            "v_smfmac_f32_16x16x64_bf16 v[228:231], v[192:195], %[w4], %[ix]\n\t"
                            "ds_read_b64_tr_b16 v[198:199], %[va] offset:288\n\t"
                            "v_smfmac_f32_16x16x64_bf16 v[236:239], v[192:195], %[w6], %[ix]\n\t"
                            "ds_read_b32 v210, %[vz] offset:%[ls]\n\t"
                            "v_smfmac_f32_16x16x64_bf16 v[232:235], v[192:195], %[w5], %[ix]\n\t"
                            "v_smfmac_f32_16x16x64_bf16 v[240:243], v[192:195], %[w7], %[ix]\n\t"
                            "v_smfmac_f32_16x16x64_bf16 v[244:247], v[192:195], %[w8], %[ix]\n\t"
                            "v_smfmac_f32_16x16x64_bf16 v[248:251], v[192:195], %[w9], %[ix]\n\t"
                            "buffer_load_dword %[sf], %[sl], %[rw], %[sr] offen sc1\n\t"
                            : [sf] "=&v"(sf), "+{v[212:227]}"(ca), "+{v[228:243]}"(cb), "+{v[244:251]}"(cc), "=&{v[196:199]}"(af1), "=&{v210}"(vv)
                            : [va] "v"(va), [vz] "v"(0u), [ls] "n"(L_S), [ix] "v"(sidx), [sl] "v"(tk.sl), [sr] "s"(sr), [rw] "s"(rwin),
                              [w0] "v"(wt[0][0]), [w1] "v"(wt[0][1]), [w2] "v"(wt[0][2]), [w3] "v"(wt[0][3]), [w4] "v"(wt[0][4]), [w5] "v"(wt[0][5]), [w6] "v"(wt[0][6]), [w7] "v"(wt[0][7]), [w8] "v"(wt[0][8]), [w9] "v"(wt[0][9])
                            : "v192", "v193", "v194", "v195", "memory");
                        asm volatile(
                            "s_waitcnt lgkmcnt(0)\n\t"
                            "v_smfmac_f32_16x16x64_bf16 v[212:215], v[196:199], %[w0], %[ix]\n\t"
                            "v_readfirstlane_b32 %[vd], v210\n\t"
                            "v_smfmac_f32_16x16x64_bf16 v[220:223], v[196:199], %[w2], %[ix]\n\t"
                            "s_cmp_eq_u32 %[vd], %[it]\n\t"
                            "s_cselect_b32 %[zs], 2.0, %[zs]\n\t"
                            "v_smfmac_f32_16x16x64_bf16 v[216:219], v[196:199], %[w1], %[ix]\n\t"
                            "s_nop 1\n\t"
                            "v_smfmac_f32_16x16x64_bf16 v[224:227], v[196:199], %[w3], %[ix]\n\t"
                            "v_add_f32 v212, v212, v214\n\t"
                            "v_smfmac_f32_16x16x64_bf16 v[228:231], v[196:199], %[w4], %[ix]\n\t"
                            "v_add_f32 v213, v213, v215\n\t"
                            "v_smfmac_f32_16x16x64_bf16 v[236:239], v[196:199], %[w6], %[ix]\n\t"
                            "v_add_f32 v220, v220, v222\n\t"
                            "v_smfmac_f32_16x16x64_bf16 v[232:235], v[196:199], %[w5], %[ix]\n\t"
                            "v_add_f32 v221, v221, v223\n\t"
                            "v_smfmac_f32_16x16x64_bf16 v[240:243], v[196:199], %[w7], %[ix]\n\t"
                            "v_cvt_pk_bf16_f32 v200, v212, v213\n\t"
                            "v_smfmac_f32_16x16x64_bf16 v[244:247], v[196:199], %[w8], %[ix]\n\t"
                            "v_cvt_pk_bf16_f32 v201, v220, v221\n\t"
                            "v_smfmac_f32_16x16x64_bf16 v[248:251], v[196:199], %[w9], %[ix]\n\t"
                            "v_add_f32 v216, v216, v218\n\t"
                            "v_add_f32 v217, v217, v219\n\t"
                            "v_add_f32 v224, v224, v226\n\t"
                            "v_add_f32 v225, v225, v227\n\t"
                            "v_cvt_pk_bf16_f32 v202, v216, v217\n\t"
                            "v_cvt_pk_bf16_f32 v203, v224, v225\n\t"
                            "v_permlane32_swap_b32 v200, v201\n\t"
                            "v_pk_add_f32 v[228:229], v[228:229], v[230:231]\n\t"
                            "v_permlane32_swap_b32 v202, v203\n\t"
                            "v_min_u32 v201, -2, v201\n\t"
                            "v_pk_add_f32 v[236:237], v[236:237], v[238:239]\n\t"
                            "v_min_u32 v203, -2, v203\n\t"
                            "v_permlane16_swap_b32 v200, v202\n\t"
                            "v_pk_add_f32 v[232:233], v[232:233], v[234:235]\n\t"
                            "v_permlane16_swap_b32 v201, v203\n\t"
                            "v_pk_add_f32 v[240:241], v[240:241], v[242:243]\n\t"
                            MTA(5)
                            "buffer_store_dwordx4 v[200:203], %[z0], %[rw], %[zs] offen sc1\n\t"
                            "v_cvt_pk_bf16_f32 v204, v228, v229\n\t"
                            "v_cvt_pk_bf16_f32 v205, v236, v237\n\t"
                            "v_cvt_pk_bf16_f32 v206, v232, v233\n\t"
                            "v_cvt_pk_bf16_f32 v207, v240, v241\n\t"
                            "v_permlane32_swap_b32 v204, v205\n\t"
                            "v_pk_add_f32 v[244:245], v[244:245], v[246:247]\n\t"
                            "v_permlane32_swap_b32 v206, v207\n\t"
                            "v_min_u32 v205, -2, v205\n\t"
                            "v_pk_add_f32 v[248:249], v[248:249], v[250:251]\n\t"
                            "v_min_u32 v207, -2, v207\n\t"
                            "v_permlane16_swap_b32 v204, v206\n\t"
                            "v_cvt_pk_bf16_f32 v208, v244, v245\n\t"
                            "v_permlane16_swap_b32 v205, v207\n\t"
                            "v_cvt_pk_bf16_f32 v209, v248, v249\n\t"
                            "buffer_store_dwordx4 v[204:207], %[z1], %[rw], %[zs] offen sc1\n\t"
                            "s_nop 0\n\t"
                            "v_permlane32_swap_b32 v208, v209\n\t"
                            "v_min_u32 v209, -2, v209\n\t"
                            "buffer_store_dwordx2 v[208:209], %[z2], %[rw], %[zs] offen sc1\n\t"
                            : [vd] "=&s"(vd), [zs] "+s"(zs), "=&{v[200:203]}"(za), "=&{v[204:207]}"(zc), "=&{v[208:209]}"(ze), "+{v[212:227]}"(ca), "+{v[228:243]}"(cb), "+{v[244:251]}"(cc), "+v"(sf)
                            : [ix] "v"(sidx), [it] "s"(it), [rw] "s"(rwin), [z0] "v"(zo0), [z1] "v"(zo1), [z2] "v"(zo2), "{v[196:199]}"(af1), "{v210}"(vv),
                              [w0] "v"(wt[1][0]), [w1] "v"(wt[1][1]), [w2] "v"(wt[1][2]), [w3] "v"(wt[1][3]), [w4] "v"(wt[1][4]), [w5] "v"(wt[1][5]), [w6] "v"(wt[1][6]), [w7] "v"(wt[1][7]), [w8] "v"(wt[1][8]), [w9] "v"(wt[1][9])
                            : "scc", "memory");
                        redo = vd == it;
                        RT(4);
                        MT(6);
                        tref = __builtin_amdgcn_s_memtime();
                        __builtin_amdgcn_s_setprio(TKP);
                        asm volatile("s_waitcnt vmcnt(3)" : "+v"(sf) :: "memory");
                    }
                } while (__builtin_expect(redo, 0));
                if (s < 8) SST(8 + s);
                if (C == 0) {
                    if (s > TAILS && w == 0) __builtin_amdgcn_raw_buffer_store_b32((unsigned)s - 1u, rwin, 0u, W_FZ2 + fb + (unsigned)hh * 128u, 16);
                    if (s > 0) {
                        __builtin_amdgcn_s_sleep(DZR);
                        const unsigned zq = zb == W_ZM ? W_ZM + 2u * NOWN * PAIR : zb - NOWN * PAIR;
                        unsigned s1;
                        asm volatile("v_mov_b32 %0, -1" : "=v"(s1));
                        st128(((u32x4){s1, s1, s1, s1}), rwin, zo0, zq, 16);
                        st128(((u32x4){s1, s1, s1, s1}), rwin, zo1, zq, 16);
                        __builtin_amdgcn_raw_buffer_store_b64((u32x2){s1, s1}, rwin, zo2, zq, 16);
                        if (__builtin_expect(s == 1, 0)) {
                            const unsigned zn = zb == W_ZM + 2u * NOWN * PAIR ? W_ZM : zb + NOWN * PAIR;
                            st128(((u32x4){s1, s1, s1, s1}), rwin, zo0, zn, 16);
                            st128(((u32x4){s1, s1, s1, s1}), rwin, zo1, zn, 16);
                            __builtin_amdgcn_raw_buffer_store_b64((u32x2){s1, s1}, rwin, zo2, zn, 16);
                        }
                    }
                } else {
                    if (TK && s + 1 < NSEG) sched2(tk, s, sf);
                    if (!TK) wait_until(tref + DZ2);
                    if (s + 1 < NSEG) put_z2(s);
                }
                if (budget <= 0) __hip_atomic_store(a.ctl, 1u, __ATOMIC_RELAXED, __HIP_MEMORY_SCOPE_AGENT);
                zb = zb == W_ZM + 2u * NOWN * PAIR ? W_ZM : zb + NOWN * PAIR;
                RT(5);
                RTS();
                MT(7);
                MTS();
            }
        };
        if (!vt) {
            if (w < 4) steps(IC<0>()); else if (w < 7) steps(IC<1>()); else steps(IC<3>());
        } else {
            BAR();
            SST(3);
            LANES();
            const int ni = __builtin_popcountll(mv) + 1;
            if (w < 4) {
                const LAS char* c0 = (const LAS char*)(hL + hoff + ((fq ^ (fr & 7)) << 4));
                const LAS char* c1 = (const LAS char*)(hL + hoff + (((4 + fq) ^ (fr & 7)) << 4));
                const LAS char* n0 = c0 + 40960; const LAS char* n1 = c1 + 40960;
                u32x4 bb[8];
                project0(bb, c0, c1);
                for (int i = 1; i < ni; ++i) {
                    if (vt) MT(0);
                    f32x4 tc[2][2];
                    project<0>(wt, tc, bb, c0, c1, n0, n1,
                        [&]() {
                            MT(1);
                            asm volatile("s_waitcnt vmcnt(0) lgkmcnt(0)\n\ts_barrier" ::: "memory");
                            MT(2);
                        }, [](auto) {});
                    { const LAS char* x = c0; c0 = n0; n0 = x; x = c1; c1 = n1; n1 = x; }
                    char* tb = tokL + (i & 1) * 32 * QS + (16 * w + 4 * fq) * 2;
#pragma unroll
                    for (int g = 0; g < 2; ++g)
#pragma unroll
                        for (int tt = 0; tt < 2; ++tt) *(u32x2*)(tb + (prow + 8 * tt) * QS + g * 128) = (u32x2){pack2(tc[g][tt][0], tc[g][tt][1]), pack2(tc[g][tt][2], tc[g][tt][3])};
                    asm volatile("" ::: "memory");
                    *(volatile LAS int*)(smem + L_S + 16 + w * 4) = i;
                    if (vt) { MT(3); const int s = i & 63; MTRT(); MTS(); }
                }
                project_end(bb);
            } else {
                __builtin_amdgcn_s_setprio(OPR);
                const unsigned hv = lane * 16u, fz_ = W_FZ + fb + (unsigned)(hh * 128 + xt * 64 + (w - 4) * 4);
                unsigned fp = ~0u;
                u32x4 r = SENT;
                int jo = jv1, jh = jv2;
                check_h<10, 4096, 0>(rws, hb + (unsigned)(jv2 * 40960 + (w - 4) * 1024), L_H + 40960 + (w - 4) * 1024, lane);
                SST(16);
                auto rows = [&](int b) {
                    for (int t = 0; t < 10; ++t) dma1k(rws, hv, hb + (unsigned)(jh * 40960 + (w - 4) * 1024 + t * 4096), (unsigned)(L_H + ((b + 1) & 1) * 40960 + (w - 4) * 1024 + t * 4096));
                    VMWAIT();
                };
                for (int i = 1; i < ni; ++i, jo = nextv(jo)) {
                    const int b = i + (i >= ZLAG);
                    if (__builtin_expect(i == ZLAG, 0)) {
                        VMWAIT();
                        jh = nextv(jh);
                        wait_h(a, rwin, fb, jh, hrdy);
                        BAR0();
                        rows(i);
                    }
                    jh = nextv(jh);
                    const bool ok = hrdy > jh;
                    if (b < ni) BAR();
                    if (i == 1) SST(17);
                    if (vt) MT(1);
                    for (;;) {
                        const u32x4 f = *(volatile LAS u32x4*)(smem + L_S + 16);
                        if (__builtin_amdgcn_readfirstlane(min(min(f[0], f[1]), min(f[2], f[3]))) >= (unsigned)i) break;
                        __builtin_amdgcn_s_sleep(1);
                    }
                    asm volatile("" ::: "memory");
                    if (i == 1) SST(18);
                    if (vt) MT(0);
                    u32x4 vf[2][4];
#pragma unroll
                    for (int t = 0; t < 2; ++t)
#pragma unroll
                        for (int ks = 0; ks < 4; ++ks) vf[t][ks] = *(const u32x4*)(tokL + (i & 1) * 32 * QS + (16 * t + fr) * QS + ks * 64 + fq * 16);
                    const unsigned ztb = O_Z + (unsigned)(jo * ZTSEG) * 2u + (unsigned)(10 * (w - 4) * H_ + hh) * 1024u;
                    f32x4 c[2][2];
                    unsigned hs = hb + (unsigned)(jh * 40960 + (w - 4) * 1024), hd = (unsigned)(L_H + ((b + 1) & 1) * 40960 + (w - 4) * 1024);
                    asm volatile("" : "+s"(hs), "+s"(hd));
#pragma unroll
                    for (int nt = 0; nt < 11; ++nt) {
                        auto& cc = c[nt & 1]; auto& cp = c[(nt + 1) & 1];
#pragma unroll
                        for (int ks = 0; ks < 4; ++ks) {
                            if (nt < 10) { cc[0] = mfma32(vf[0][ks], half8(wt[ks >> 1][nt], ks & 1), ks ? cc[0] : z4); PIN1(cc[0]); }
                            if (nt > 0 && ks >= 1 && ks < 3) { r[2 * ks - 2] = pack2(cp[ks - 1][0], cp[ks - 1][1]); PIN1(r); }
                            if (nt < 10) { cc[1] = mfma32(vf[1][ks], half8(wt[ks >> 1][nt], ks & 1), ks ? cc[1] : z4); PIN1(cc[1]); }
                            if (nt > 0 && ks >= 1 && ks < 3) { r[2 * ks - 1] = pack2(cp[ks - 1][2], cp[ks - 1][3]); PIN1(r); }
                            if (nt > 0 && ks == 2) st128(r, rws, lane * 16u, ztb + (unsigned)((nt - 1) * H_) * 1024u, 16);
                            if (nt < 5 && !(ks & 1) && __builtin_expect(ok, 1)) dma1k(rws, hv, hs + (2 * nt + (ks >> 1)) * 4096u, hd + (2 * nt + (ks >> 1)) * 4096u);
                            if (nt == 5 && ks == 0 && __builtin_expect(ok, 1)) {
                                asm volatile("s_waitcnt vmcnt(14)" ::: "memory");
                                __builtin_amdgcn_raw_buffer_store_b32(fp, rwin, 0u, fz_, 16);
                            }
                            SB();
                        }
                    }
                    if (i == 1) SST(19);
                    if (vt) MT(2);
                    fp = (unsigned)jo;
                    if (__builtin_expect(hrdy <= nextv(nextv(jh)), 0)) hrdy = see_h(ask_h(rwin, fb));
                    if (__builtin_expect(jo <= ZEARLY || i + 1 == ni || !ok, 0)) {
                        VMWAIT();
                        __builtin_amdgcn_raw_buffer_store_b32(fp, rwin, 0u, fz_, 16);
                    } else asm volatile("s_waitcnt vmcnt(7)" ::: "memory");
                    if (i == 1) SST(20);
                    if (__builtin_expect(!ok, 0)) { wait_h(a, rwin, fb, jh, hrdy); rows(b); }
                    if (jo < 8) SST(8 + jo);
                    if (vt && (!BSPARSE || jo % 6 == 2)) { const int s = jo; RT(0); RTS(); }
                    if (vt) { MT(3); const int s = i & 63; MTRT(); MTS(); }
                }
            }
            __builtin_amdgcn_s_setprio(0);
            VMWAIT();
            BAR();
        }
    }
    if (bid < 32 || bid >= NQT) {
        LANES();
        const int hh = (bid < 32 ? bid : bid - NQT) & 31;
        u32x4 m0[2];
        get_mem0(m0, tid);
        if (lay == 0 || xt) {
            wait_until(tstart + (bid >= NQT ? DQT : DW1));
            load_w<false>(wt, karg<5>(w >> 2), hh, w, 4 * (w & 3), 2, tokL, gap);
            SST(5);
            if (!xt) bid = role(config());
        }
        places();
        const bool qt = bid >= NQT;
        const unsigned long long mq = xt ? MXQ : ~MXQ;
        auto nextq = [&](int j) { return min(seg_after(mq, j), NSEG - 1); };
        const int jq0 = qt ? nextq(1) : 0, jq1 = nextq(jq0);
        put_mem0(m0, memL, tid);
        if (!qt && w == 7) retime();
        if (tid < 4) *(unsigned*)(smem + L_S + tid * 4) = 0u;
        SST(1);
        soon(jq1);
        SST(2);
        const unsigned so1 = hb + (unsigned)(jq1 * 40960 + w * 1024), lo1 = L_H + 40960 + w * 1024;
        stage_h<5, 8192>(rws, hb + (unsigned)(jq0 * 40960 + w * 1024), lo1 - 40960u, lane);
        BAR0();
        stage_h<5, 8192>(rws, so1, lo1, lane);
        check_h<5, 8192, 5>(rws, hb + (unsigned)(jq0 * 40960 + w * 1024), lo1 - 40960u, lane);
        BAR();
        SST(3);
        int qrdy = 0;
        {
            LANES();
            const unsigned to0 = (unsigned)((w ^ 4) >> 2) * 8192u + (unsigned)((w & 3) * 64 + (lane ^ ((fq & 1) * (w < 4 ? 8 : 5)))) * 16u, to1 = to0 + 4096u, hv = lane * 16u;
            const LAS char* c0 = (const LAS char*)(hL + hoff + ((fq ^ (fr & 7)) << 4));
            const LAS char* c1 = (const LAS char*)(hL + hoff + (((4 + fq) ^ (fr & 7)) << 4));
            u32x4 bb[8], g0, g1;
            asm volatile("" : "=v"(g0), "=v"(g1));
            f32x4 tc[2][2];
            auto pack = [&]() {
                g0 = (u32x4){pack2(tc[0][0][0], tc[0][0][1]), pack2(tc[0][0][2], tc[0][0][3]), pack2(tc[1][0][0], tc[1][0][1]), pack2(tc[1][0][2], tc[1][0][3])};
                g1 = (u32x4){pack2(tc[0][1][0], tc[0][1][1]), pack2(tc[0][1][2], tc[0][1][3]), pack2(tc[1][1][0], tc[1][1][1]), pack2(tc[1][1][2], tc[1][1][3])};
            };
            const LAS char* n0 = c0 + 40960; const LAS char* n1 = c1 + 40960;
            if (!qt) {
                project0(bb, c0, c1);
                for (int j = 0; j < 2; ++j) {
                    project<0>(wt, tc, bb, c0, c1, n0, n1, [&]() { if (j == 0) { check_h<5, 8192, 0>(rws, so1, lo1, lane); BAR(); } }, [](auto) {});
                    pack();
                    *(u32x4*)(tokL + j * 16384 + to0) = g0; *(u32x4*)(tokL + j * 16384 + to1) = g1;
                    c0 = n0; c1 = n1;
                    if (j == 0 && w == 7) __builtin_amdgcn_raw_buffer_store_b32(((4u * (unsigned)__builtin_amdgcn_s_memrealtime() + (lay ? DQ0 : DQ00)) & 0xFFFFFFu) | (unsigned)lay << 30, rwin, two(), W_T + hh * 128u, 16);
                }
                project_end(bb);
                VMWAIT();
            } else {
                project0(bb, c0, c1);
                unsigned hs = hb + (unsigned)(jq1 * 40960 + w * 1024), hd = (unsigned)(L_H + 40960 + w * 1024), fv = tid == 0 ? 0u : 0x40000000u;
                const unsigned fq_ = W_FQ + fb + (unsigned)(hh * 128 + xt * 64);
                const int jl = 63 - __builtin_clzll(mq);
                for (int jp = 1, j = jq0, jn = jq1;; jp = j, j = jn, jn = nextq(jn)) {
                    MT(0);
                    project<0>(wt, tc, bb, c0, c1, n0, n1,
                        [&]() {
                            if (__builtin_expect(hrdy <= nextq(jn), 0)) hrdy = see_h(g0);
                            hs = hrdy > nextq(jn) ? hb + (unsigned)(nextq(jn) * 40960 + w * 1024) : 0x40000000u; hd = (unsigned)(2 * L_H + 40960 + 2 * w * 1024) - hd;
                            unsigned fj = (unsigned)jp;
                            asm volatile("" : "+s"(hs), "+s"(hd));
                            PIN1(fj);
                            MT(1);
                            if (__builtin_expect(j == jq0, 0)) check_h<5, 8192, 0>(rws, so1, lo1, lane);
                            VMWAIT();
                            BAR();
                            MT(2);
                            if (w == 0) __builtin_amdgcn_raw_buffer_store_b32(fj, rwin, fv, fq_, 16);
                        },
                        [&](auto uu) {
                            constexpr int u = decltype(uu)::v;
                            if (u == QEU && __builtin_expect(j <= QE2, 0)) {
                                asm volatile("s_waitcnt vmcnt(3)\n\ts_barrier" ::: "memory");
                                if (w == 0) __builtin_amdgcn_raw_buffer_store_b32((unsigned)jp, rwin, fv, fq_, 16);
                            }
                            if (u == 20 && __builtin_expect(hrdy <= nextq(jn), 0)) g0 = ask_h(rwin, fb);
                            if (u == 34 || u == 38 || u == 2 || u == 6 || u == 10) { constexpr int t = ((u + 6) % 40) / 4; dma1k(rws, hv, hs + t * 8192u, hd + t * 8192u); }
                        });
                    pack();
                    const unsigned qs = O_QK + (unsigned)(j * H_ + hh) * 16384u;
                    st128(g0, rws, to0, qs, 16);
                    st128(g1, rws, to1, qs, 16);
                    { const LAS char* x = c0; c0 = n0; n0 = x; x = c1; c1 = n1; n1 = x; }
                    if (__builtin_expect(j <= QEARLY || j == jl, 0)) {
                        VMWAIT();
                        BAR0();
                        if (tid == 0) __builtin_amdgcn_raw_buffer_store_b32((unsigned)j, rwin, 0u, fq_, 16);
                        if (j == jl) break;
                    }
                    if (__builtin_expect(hrdy <= nextq(jn), 0)) {
                        wait_h(a, rwin, fb, nextq(jn), hrdy);
                        hs = hb + (unsigned)(nextq(jn) * 40960 + w * 1024);
                        dma1k(rws, hv, hs, hd); dma1k(rws, hv, hs + 8192u, hd + 8192u);
                    }
                    MT(3);
                    if (j < 8) SST(8 + j);
                    if (!BSPARSE || j % 6 == 2) { const int s = j; RT(0); RTS(); MTRT(); MTS(); }
                }
                project_end(bb);
                VMWAIT();
                BAR();
            }
        }
        SST(4);
        auto steps = [&](auto cc) {
        constexpr int C = decltype(cc)::v >> 1, R = decltype(cc)::v & 1;
        LANES();
        const LAS char* ka = (const LAS char*)(smem + L_K + (fr & 1) * TQS + (4 * fq + (fr >> 2)) * 8);
        const LAS char* qa = (const LAS char*)(smem + L_Q + (fr & 1) * 544 + ((fr >> 1) & 1) * 1152 + (16 * fq + 2 * (fr >> 2)) * 4);
        LAS char* qw = (LAS char*)(C < 2 ? smem + L_Q + (fq & 1) * 544 + (fq >> 1) * 1152 + (32 * w + 8 * (fr >> 2) + bs2(fr & 3)) * 4
                                         : smem + L_K + (fq >> 1) * TQS + (32 * (w & 3) + fr) * 8 + (fq & 1) * 4);
        const LAS char* mp0 = (const LAS char*)(memL + ma0);
        const int z = 16 * (fr >> 3) + 4 * (fr & 3) + 2 * ((fr >> 2) & 1);
        const int ko = ((z >> 4) * 256 + fq * 16 + ((z & 15) ^ ((fq & 1) * 5))) * 16;
        unsigned pa0 = (4u * C + 2u * (hh >> 4)) * PAIR + (unsigned)((hh & 15) * 512 + R * 256 + lane * 4);
        unsigned pa2 = ((8u + (fr >> 3)) * PAIR + C * 4096u + (unsigned)((hh >> 1) * 256 + R * 128 + (hh & 1) * 64 + fq * 16 + (fr & 6) * 2)) | (fr & 1) << 30;
        PIN2(ka, qw); PIN1(mp0);
        constexpr bool TK = C == 2 && R == 1;
        Tk tk = {0u, 0u, 0x40000000u, nullptr};
        if (TK) { tk = tk_init(); __builtin_amdgcn_s_setprio(TKP); }
        if (C < 2) { PIN2(qa, pa0); PIN1(pa2); }
        int qf_ = 0;
        int tbo = 0;
        for (int s = 0; s < NSEG; ++s) {
            RT(0);
            const char* tb = tokL + tbo;
            unsigned pb = __builtin_amdgcn_readfirstlane(s + 1 < NSEG ? W_P + (unsigned)(s & 3) * 10u * PAIR + nh : 0x40000000u);
            asm volatile("" : "+s"(pb));
            if (s == 0) BAR();
            u32x8 kx[2][2];
            if (C < 2) {
#pragma unroll
                for (int u = 0; u < 4; ++u) kx[u >> 1][u & 1] = cat8(*(const u32x4*)(tb + (ko ^ ((u >> 1) * 16)) + (u & 1) * 2048), *(const u32x4*)(tb + (ko ^ ((u >> 1) * 16)) + (u & 1) * 2048 + 1024));
                PIN2(kx[0][0], kx[0][1]); PIN2(kx[1][0], kx[1][1]);
            }
            bool redo = false;
            unsigned sf = 0;
            do {
                ++it;
                f32x4 a0 = z4, a1 = z4;
                PIN2(a0, a1);
                u32x4 m[10];
                if (C < 2) {
                    __builtin_amdgcn_s_setprio(2);
                    BAR();
                    MT(0);
#pragma unroll
                    for (int t = 0; t < NB1; ++t) { m[t] = *(const LAS u32x4*)(mp0 + t * 1024); SB(); }
                    BAR0();
#pragma unroll
                    for (int t = 0; t < 10; ++t) {
                        if (t == QB2) { BAR0(); MT(1); }
                        if (t + NB1 < 10) m[t + NB1] = *(const LAS u32x4*)(mp0 + (t + NB1) * 1024);
                        SB();
                        a0 = smfmac(m[t], wt[0][t], a0, sidx); a1 = smfmac(m[t], wt[1][t], a1, sidx);
                        PIN2(a0, a1);
                    }
                } else {
                    unsigned mx = 0;
                    asm volatile("" : "+{v[200:203]}"(a0), "+{v[204:207]}"(a1), "+{v208}"(mx));
                    unsigned lim = s > 0 ? poll_lim(budget) : ~0u, sr = tro + (unsigned)(s & 3) * 2u * PAIR;
                    asm volatile("" : "+s"(lim), "+s"(sr));
                    if (TK) fetch(tk, s, !redo, lim, DQC, (unsigned)hh * 128u, __builtin_expect(s > 0, 1));
                    else asm volatile("s_waitcnt lgkmcnt(0)\n\ts_barrier\n\ts_barrier\n\ts_barrier" ::: "memory");
                    asm volatile(
                        "ds_read_b128 v[212:215], %[mp] offset:0\n\t"
                        "ds_read_b128 v[216:219], %[mp] offset:1024\n\t"
                        "ds_read_b128 v[220:223], %[mp] offset:2048\n\t"
                        "ds_read_b128 v[224:227], %[mp] offset:3072\n\t"
                        "ds_read_b128 v[228:231], %[mp] offset:4096\n\t"
                        "ds_read_b128 v[232:235], %[mp] offset:5120\n\t"
                        "ds_read_b128 v[236:239], %[mp] offset:6144\n\t"
                        "ds_read_b128 v[240:243], %[mp] offset:7168\n\t"
                        "ds_read_b128 v[244:247], %[mp] offset:8192\n\t"
                        "ds_read_b128 v[248:251], %[mp] offset:9216\n\t"
                        MTA(4)
                        "s_waitcnt lgkmcnt(9)\n\t"
                        "v_smfmac_f32_16x16x64_bf16 v[200:203], v[212:215], %[w00], %[ix]\n\t"
                        "v_max3_u32 v208, v208, v212, v213\n\t"
                        "v_smfmac_f32_16x16x64_bf16 v[204:207], v[212:215], %[w10], %[ix]\n\t"
                        "s_waitcnt lgkmcnt(8)\n\t"
                        "v_max3_u32 v208, v208, v214, v215\n\t"
                        "v_smfmac_f32_16x16x64_bf16 v[200:203], v[216:219], %[w01], %[ix]\n\t"
                        "v_max3_u32 v208, v208, v216, v217\n\t"
                        "v_smfmac_f32_16x16x64_bf16 v[204:207], v[216:219], %[w11], %[ix]\n\t"
                        "s_waitcnt lgkmcnt(7)\n\t"
                        "v_max3_u32 v208, v208, v218, v219\n\t"
                        "v_smfmac_f32_16x16x64_bf16 v[200:203], v[220:223], %[w02], %[ix]\n\t"
                        "v_max3_u32 v208, v208, v220, v221\n\t"
                        "v_smfmac_f32_16x16x64_bf16 v[204:207], v[220:223], %[w12], %[ix]\n\t"
                        "s_waitcnt lgkmcnt(6)\n\t"
                        "v_max3_u32 v208, v208, v222, v223\n\t"
                        "v_smfmac_f32_16x16x64_bf16 v[200:203], v[224:227], %[w03], %[ix]\n\t"
                        "v_max3_u32 v208, v208, v224, v225\n\t"
                        "v_smfmac_f32_16x16x64_bf16 v[204:207], v[224:227], %[w13], %[ix]\n\t"
                        "s_waitcnt lgkmcnt(0)\n\t"
                        "v_max3_u32 v208, v208, v226, v227\n\t"
                        "v_smfmac_f32_16x16x64_bf16 v[200:203], v[228:231], %[w04], %[ix]\n\t"
                        "v_max3_u32 v208, v208, v228, v229\n\t"
                        "v_smfmac_f32_16x16x64_bf16 v[204:207], v[228:231], %[w14], %[ix]\n\t"
                        "v_max3_u32 v208, v208, v230, v231\n\t"
                        "v_smfmac_f32_16x16x64_bf16 v[200:203], v[232:235], %[w05], %[ix]\n\t"
                        "v_max3_u32 v208, v208, v232, v233\n\t"
                        "v_smfmac_f32_16x16x64_bf16 v[204:207], v[232:235], %[w15], %[ix]\n\t"
                        "v_max3_u32 v208, v208, v234, v235\n\t"
                        "v_smfmac_f32_16x16x64_bf16 v[200:203], v[236:239], %[w06], %[ix]\n\t"
                        "v_max3_u32 v208, v208, v236, v237\n\t"
                        "v_smfmac_f32_16x16x64_bf16 v[204:207], v[236:239], %[w16], %[ix]\n\t"
                        "v_max3_u32 v208, v208, v238, v239\n\t"
                        "v_smfmac_f32_16x16x64_bf16 v[200:203], v[240:243], %[w07], %[ix]\n\t"
                        "v_max3_u32 v208, v208, v240, v241\n\t"
                        "v_smfmac_f32_16x16x64_bf16 v[204:207], v[240:243], %[w17], %[ix]\n\t"
                        "v_max3_u32 v208, v208, v242, v243\n\t"
                        "v_smfmac_f32_16x16x64_bf16 v[200:203], v[244:247], %[w08], %[ix]\n\t"
                        "v_max3_u32 v208, v208, v244, v245\n\t"
                        "v_smfmac_f32_16x16x64_bf16 v[204:207], v[244:247], %[w18], %[ix]\n\t"
                        "v_max3_u32 v208, v208, v246, v247\n\t"
                        "v_smfmac_f32_16x16x64_bf16 v[200:203], v[248:251], %[w09], %[ix]\n\t"
                        "v_max3_u32 v208, v208, v248, v249\n\t"
                        "v_smfmac_f32_16x16x64_bf16 v[204:207], v[248:251], %[w19], %[ix]\n\t"
                        "v_max3_u32 v208, v208, v250, v251\n\t"
                        "buffer_load_dword %[sf], %[sl], %[rw], %[sr] offen sc1\n\t"
                        "v_cmp_lt_u32 vcc, %[lim], v208\n\t"
                        "s_nop 3\n\t"
                        "v_pk_add_f32 v[200:201], v[200:201], v[202:203]\n\t"
                        "v_pk_add_f32 v[204:205], v[204:205], v[206:207]\n\t"
                        "v_cvt_pk_bf16_f32 v200, v200, v201\n\t"
                        "v_cvt_pk_bf16_f32 v204, v204, v205\n\t"
                        "ds_write2_b32 %[qw], v200, v204 offset1:32\n\t"
                        MTA(2)
                        "s_cbranch_vccz 1f\n\t"
                        "v_mov_b32 v208, %[it]\n\t"
                        "v_mov_b32 v209, 0\n\t"
                        "ds_write_b32 v209, v208 offset:%[ls]\n\t"
                        "1:\n\t"
                        "s_waitcnt vmcnt(%[nv]) lgkmcnt(0)\n\t"
                        "s_barrier\n\t"
                        "s_waitcnt vmcnt(0)\n\t"
                        : [sf] "=&v"(sf), "+v"(qf_), "+{v[200:203]}"(a0), "+{v[204:207]}"(a1), "+{v208}"(mx)
                        : [mp] "v"(mp0), [qw] "v"(qw), [ix] "v"(sidx), [lim] "s"(lim), [it] "s"(it), [ls] "n"(L_S), [sl] "v"(tk.sl), [sr] "s"(sr), [rw] "s"(rwin), [nv] "n"(TK ? 2 : 1),
                          [w00] "v"(wt[0][0]), [w01] "v"(wt[0][1]), [w02] "v"(wt[0][2]), [w03] "v"(wt[0][3]), [w04] "v"(wt[0][4]), [w05] "v"(wt[0][5]), [w06] "v"(wt[0][6]), [w07] "v"(wt[0][7]), [w08] "v"(wt[0][8]), [w09] "v"(wt[0][9]), [w10] "v"(wt[1][0]), [w11] "v"(wt[1][1]), [w12] "v"(wt[1][2]), [w13] "v"(wt[1][3]), [w14] "v"(wt[1][4]), [w15] "v"(wt[1][5]), [w16] "v"(wt[1][6]), [w17] "v"(wt[1][7]), [w18] "v"(wt[1][8]), [w19] "v"(wt[1][9])
                        : "v209", "v212", "v213", "v214", "v215", "v216", "v217", "v218", "v219", "v220", "v221", "v222", "v223", "v224", "v225", "v226", "v227", "v228", "v229", "v230", "v231", "v232", "v233", "v234", "v235", "v236", "v237", "v238", "v239", "v240", "v241", "v242", "v243", "v244", "v245", "v246", "v247", "v248", "v249", "v250", "v251", "vcc", "memory");
                }
                if (C < 2) {
                    *(LAS unsigned*)qw = hpack(a0); *(LAS unsigned*)(qw + 16) = hpack(a1);
                    SB();
                    MT(2);
                    unsigned vd;
#define SC0 "v[212:215]"
#define SC1 "v[216:219]"
#define SC2 "v[220:223]"
#define DPP(op, d, ctl) op " " d ", " d ", " d " " ctl " row_mask:0xf bank_mask:0xf\n\t"
#define TAIL(A0, A2, B0, B2, C0, C2) \
                        "v_mov_b64 v[212:213], 0\n\tv_mov_b64 v[214:215], 0\n\tv_mov_b64 v[216:217], 0\n\tv_mov_b64 v[218:219], 0\n\tv_mov_b64 v[220:221], 0\n\tv_mov_b64 v[222:223], 0\n\t" \
                        "s_setprio 3\n\t" \
                        "s_waitcnt vmcnt(0) lgkmcnt(0)\n\t" \
                        "s_barrier\n\t" MTA(3) \
                        "ds_read_b64_tr_b16 v[224:225], %[qa]\n\t" \
                        "ds_read_b64_tr_b16 v[226:227], %[qa] offset:32\n\t" \
                        "ds_read_b64_tr_b16 v[228:229], %[qa] offset:256\n\t" \
                        "ds_read_b64_tr_b16 v[230:231], %[qa] offset:288\n\t" \
                        "ds_read_b64_tr_b16 v[232:233], %[ka]\n\t" \
                        "ds_read_b64_tr_b16 v[234:235], %[ka] offset:128\n\t" \
                        "ds_read_b64_tr_b16 v[236:237], %[ka] offset:256\n\t" \
                        "ds_read_b64_tr_b16 v[238:239], %[ka] offset:384\n\t" \
                        "ds_read_b64_tr_b16 v[240:241], %[ka] offset:512\n\t" \
                        "ds_read_b64_tr_b16 v[242:243], %[ka] offset:640\n\t" \
                        "s_waitcnt lgkmcnt(8)\n\t" \
                        "v_smfmac_f32_16x16x64_bf16 " SC0 ", v[224:227], %[k00], %[ix]\n\t" \
                        "ds_read_b64_tr_b16 v[244:245], %[ka] offset:768\n\t" \
                        "v_smfmac_f32_16x16x64_bf16 " SC1 ", v[224:227], %[k10], %[ix]\n\t" \
                        "ds_read_b64_tr_b16 v[246:247], %[ka] offset:896\n\t" \
                        "s_waitcnt lgkmcnt(8)\n\t" \
                        "v_smfmac_f32_16x16x64_bf16 " SC0 ", v[228:231], %[k01], %[ix]\n\t" \
                        "ds_read_b32 v248, %[vz] offset:%[ls]\n\t" \
                        "v_smfmac_f32_16x16x64_bf16 " SC1 ", v[228:231], %[k11], %[ix]\n\t" \
                        "s_waitcnt lgkmcnt(5)\n\t" \
                        "v_smfmac_f32_16x16x64_bf16 " SC2 ", v[224:227], v[232:239], %[ix]\n\t" \
                        "s_waitcnt lgkmcnt(1)\n\t" \
                        "v_smfmac_f32_16x16x64_bf16 " SC2 ", v[228:231], v[240:247], %[ix]\n\t" \
                        "s_waitcnt lgkmcnt(0)\n\t" \
                        MTA(4) "v_readfirstlane_b32 %[vd], v248\n\t" \
                        "v_add_f32 v232, " A0 ", " A2 "\n\t" \
                        "s_nop 0\n\t" \
                        "v_add_f32 v233, " B0 ", " B2 "\n\t" \
                        "v_max_f32 v235, v232, v233\n\t" \
                        "s_cmp_eq_u32 %[vd], %[it]\n\t" \
                        "s_cselect_b32 %[p0], 2.0, %[p0]\n\t" \
                        DPP("v_max_f32_dpp", "v235", "row_ror:8") \
                        "v_add_f32 v234, " C0 ", " C2 "\n\t" \
                        "v_max_f32 v235, v235, v234\n\t" \
                        "s_add_u32 %[p1], %[p0], 0x4000\n\t" \
                        "s_nop 0\n\t" DPP("v_max_f32_dpp", "v235", "row_ror:4") \
                        "s_nop 1\n\t" DPP("v_max_f32_dpp", "v235", "row_ror:2") \
                        "s_nop 1\n\t" DPP("v_max_f32_dpp", "v235", "row_ror:1") \
                        "v_mul_f32 v235, 0xbe0293ee, v235\n\t" \
                        "v_fmamk_f32 v232, v232, 0x3e0293ee, v235\n\t" \
                        "v_fmamk_f32 v233, v233, 0x3e0293ee, v235\n\t" \
                        "v_exp_f32 v232, v232\n\t" \
                        "v_exp_f32 v233, v233\n\t" \
                        "v_fmamk_f32 v234, v234, 0x3e0293ee, v235\n\t" \
                        "v_add_f32 v235, v232, v233\n\t" \
                        "v_exp_f32 v234, v234\n\t" \
                        "s_nop 0\n\t" DPP("v_add_f32_dpp", "v235", "row_ror:8") \
                        "v_add_f32 v235, v235, v234\n\t" \
                        "v_mov_b32_dpp v236, v234 quad_perm:[1,1,3,3] row_mask:0xf bank_mask:0xf\n\t" \
                        "s_nop 0\n\t" DPP("v_add_f32_dpp", "v235", "row_ror:4") \
                        "s_nop 1\n\t" DPP("v_add_f32_dpp", "v235", "row_ror:2") \
                        "s_nop 1\n\t" DPP("v_add_f32_dpp", "v235", "row_ror:1") \
                        "v_rcp_f32 v235, v235\n\t" \
                        "s_nop 0\n\t" \
                        "v_mul_f32_e64 v232, v232, v235 clamp\n\t" \
                        "v_mul_f32_e64 v233, v233, v235 clamp\n\t" \
                        "v_cvt_pk_bf16_f32 v232, v232, v233\n\t" \
                        MTA(5) "buffer_store_dword v232, %[pa], %[rw], %[p0] offen sc1\n\t" \
                        "v_mul_f32_e64 v234, v234, v235 clamp\n\t" \
                        "v_mul_f32_e64 v236, v236, v235 clamp\n\t" \
                        "buffer_store_dword v232, %[pa], %[rw], %[p1] offen sc1\n\t" \
                        "v_cvt_pk_bf16_f32 v234, v234, v236\n\t" \
                        "buffer_store_dword v234, %[pm], %[rw], %[p0] offen sc1\n\t" \
                        "s_setprio 0"
#define TAILIO : [vd] "=&s"(vd), [p0] "+s"(ps), [p1] "=&s"(ps1) \
                        : [qa] "v"(qa), [ka] "v"(ka), [vz] "v"(0u), [ls] "n"(L_S), [ix] "v"(sidx), [it] "s"(it), [pa] "v"(pa0), [pm] "v"(pa2), [rw] "s"(rwin), \
                          [k00] "v"(kx[0][0]), [k01] "v"(kx[0][1]), [k10] "v"(kx[1][0]), [k11] "v"(kx[1][1]) \
                        : "v212", "v213", "v214", "v215", "v216", "v217", "v218", "v219", "v220", "v221", "v222", "v223", "v224", "v225", "v226", "v227", "v228", "v229", "v230", "v231", "v232", \
                          "v233", "v234", "v235", "v236", "v237", "v238", "v239", "v240", "v241", "v242", "v243", "v244", "v245", "v246", "v247", "v248", "scc", "memory"
                    unsigned ps = pb, ps1;
                    if (R == 0) asm volatile(TAIL("v212", "v214", "v216", "v218", "v220", "v222") TAILIO);
                    else asm volatile(TAIL("v213", "v215", "v217", "v219", "v221", "v223") TAILIO);
                    redo = vd == it;
                    RT(4);
                    MT(6);
                } else {
                    MT(3);
                    if (TK) { __builtin_amdgcn_s_setprio(TKP); if (TKS) __builtin_amdgcn_s_sleep(TKS); }
                    redo = __builtin_amdgcn_readfirstlane(*(volatile LAS unsigned*)(smem + L_S)) == it;
                    if (redo) FC(fc0);
                }
            } while (__builtin_expect(redo, 0));
            if (s < 8) SST(8 + s);
            if (s >= 56) SST(s - 40);
            if (C + R == 1) {
                if (s > 0) {
                    __builtin_amdgcn_s_sleep(DDMA);
                    unsigned l2 = fresh_lane(), s1;
                    asm volatile("v_mov_b32 %0, -1" : "=v"(s1));
                    const unsigned base = W_P + (unsigned)((s - 1) & 3) * 10u * PAIR + nh;
                    asm volatile("s_waitcnt vmcnt(3)" ::: "memory");
                    st128(((u32x4){s1, s1, s1, s1}), rwin, (l2 >> 5) * PAIR + (l2 & 31u) * 16u, base + (unsigned)(R * 4 + 2 * (hh >> 4)) * PAIR + (unsigned)(hh & 15) * 512u, 16);
                    if (R) __builtin_amdgcn_raw_buffer_store_b64((u32x2){s1, s1}, rwin, (l2 >> 5) * PAIR + (l2 & 16u) * 256u + (l2 & 8u) * 16u + (l2 & 7u) * 8u, base + 8u * PAIR + (unsigned)((hh >> 1) * 256 + (hh & 1) * 64), 16);
                    else __builtin_amdgcn_raw_buffer_store_b32(s1, rwin, two(), W_T + (unsigned)((s + 2) & 3) * 2u * PAIR + (unsigned)hh * 128u, 16);
                }
            } else if (TK) {
                if (s + 1 < NSEG) sched2(tk, s, sf);
            } else if (C == 2 && w == 4) {
                if (s > TAILS) __builtin_amdgcn_raw_buffer_store_b32((unsigned)s - 1u, rwin, 0u, W_FPT + fb + (unsigned)hh * 128u, 16);
                if (s + 2 < NSEG) {
                    qrdy = max(qrdy, seg_done(MXQ, __builtin_amdgcn_readlane(qf_, 0), __builtin_amdgcn_readlane(qf_, 1)));
                    if (__builtin_expect(qrdy < s + 2, 0)) for (unsigned spins = 0;;) {
                        asm volatile("" ::: "memory");
                        const int f = (int)__builtin_amdgcn_raw_buffer_load_b32(rwin, (unsigned)(lane & 1) * 64u, W_FQ + fb + (unsigned)hh * 128u, 16);
                        qrdy = seg_done(MXQ, __builtin_amdgcn_readlane(f, 0), __builtin_amdgcn_readlane(f, 1));
                        if (qrdy >= s + 2) break;
                        FC(fc2);
                        if (spin_fail(spins, a.ctl)) break;
                        __builtin_amdgcn_s_sleep(2);
                    }
                    const unsigned so = O_QK + (unsigned)(((s + 2) * H_ + hh) * 16384), lo = (unsigned)(L_TOK + (tbo ? tbo - 16384 : 32768)), l2 = fresh_lane();
#pragma unroll
                    for (int i = 0; i < 4; ++i) dma4k(rws, l2 * 16u, so + i * 4096u, lo + i * 4096u);
                    if (qrdy < min(s + 5, NSEG - 1))
                        asm volatile("buffer_load_dword %0, %1, %2, %3 offen sc1" : "+v"(qf_) : "v"((l2 & 1u) * 64u), "s"(rwin), "s"(W_FQ + fb + (unsigned)hh * 128u) : "memory");
                }
            } else {
                if (C == 2 && s + 1 < NSEG) __builtin_amdgcn_s_sleep(DPT);
                const float scale = 0.08838834764831845f, l2e = 1.44269504088896340736f * (NORND ? scale : 1.f);
                const int tq = (w >> 1) & 1, qh = w & 1;
                f32x4 sc[3] = {z4, z4, z4};
                u32x4 kt[2][4], qf[4], kf[4];
                const unsigned l2 = fresh_lane(), kto = ((l2 & 8u) * 512u + (l2 & 51u) * 16u + (l2 & 4u) * 32u) ^ ((l2 & 16u) * 5u), qto = ((l2 & 55u) * 16u + qh * 128u) ^ ((l2 & 16u) * 8u);
#pragma unroll
                for (int ks = 0; ks < 4; ++ks) {
                    qf[ks] = *(const u32x4*)(tb + 8192 + (tq * 4 + ks) * 1024 + qto);
                    kt[0][ks] = *(const u32x4*)(tb + ks * 1024 + kto);
                    kt[1][ks] = *(const u32x4*)(tb + ks * 1024 + (kto ^ 64u));
                    SB();
                }
#pragma unroll
                for (int ks = 0; ks < 4; ++ks) {
                    sc[0] = mfma32(kt[0][ks], qf[ks], sc[0]); PIN1(sc[0]);
                    kf[ks] = ldtr2(ka + ks * 256, ka + ks * 256 + 128);
                    SB();
                    sc[1] = mfma32(kt[1][ks], qf[ks], sc[1]); PIN1(sc[1]);
                }
#pragma unroll
                for (int ks = 0; ks < 4; ++ks) sc[2] = mfma32(kf[ks], qf[ks], sc[2]);
                const bool hi = (l2 & 8u) != 0u;
                float v[6];
#pragma unroll
                for (int r = 0; r < 4; ++r) v[r] = hi ? sc[1][r] : sc[0][r];
                v[4] = hi ? sc[2][2] : sc[2][0]; v[5] = hi ? sc[2][3] : sc[2][1];
#pragma unroll
                for (int r = 0; r < 6; r += 2) {
                    if (NORND) break;
                    const unsigned b0 = pack2(v[r], v[r + 1]);
                    const unsigned b1 = pack2(lo2f(b0) * scale, hi2f(b0) * scale);
                    v[r] = lo2f(b1); v[r + 1] = hi2f(b1);
                }
                const int lim = 8 * (w & 3) + (int)(l2 & 7u) - 8 * (int)(l2 >> 4) - (int)((l2 & 8u) >> 1);
#pragma unroll
                for (int r = 0; r < 4; ++r) if (r > lim) v[r] = NORND ? -1000000000.0f / scale : bfr(-1000000000.0f);
                float mx = fmx(fmx(fmx(fmx(fmx(v[0], v[1]), v[2]), v[3]), v[4]), v[5]);
                mx = xmax3216(fmx(mx, ror8f(mx)));
                const float mb = -mx * l2e;
#pragma unroll
                for (int r = 0; r < 6; ++r) v[r] = __builtin_amdgcn_exp2f(fmaf(v[r], l2e, mb));
                float sum = fmaf(v[4] + v[5], 0.5f, (v[0] + v[1]) + (v[2] + v[3]));
                sum = xsum3216(sum + ror8f(sum));
                const float inv = __builtin_amdgcn_rcpf(sum);
                const unsigned p0 = pack2(v[0] * inv, v[1] * inv), p1 = nosent1(pack2(v[2] * inv, v[3] * inv)), p2 = nosent1(pack2(v[4] * inv, v[5] * inv));
                const unsigned base = s + 1 < NSEG ? O_PT + (unsigned)(s * PTSEG) * 2u : W_PL + fb;
                const __amdgpu_buffer_rsrc_t rp = s + 1 < NSEG ? rws : rwin;
                const unsigned oob = (l2 & 8u) << 27;
                st128(((u32x4){p0, p1, dppu<0x128>(p0), dppu<0x128>(p1)}), rp, (l2 * 16u) | oob, base + (unsigned)((hh * 2 + tq) * 1024 + qh * 128), 16);
                __builtin_amdgcn_raw_buffer_store_b64((u32x2){p2, dppu<0x128>(p2)}, rp, ((l2 & 7u) * 16u + (l2 >> 4) * 8u) | oob | (l2 & 32u) << 25, base + 65536u + (unsigned)((tq * 32 + hh) * 256 + qh * 128), 16);
            }
            tbo = tbo == 32768 ? 0 : tbo + 16384;
            if (C == 2 && __builtin_expect(budget <= 0, 0)) __hip_atomic_store(a.ctl, 1u, __ATOMIC_RELAXED, __HIP_MEMORY_SCOPE_AGENT);
            RT(5);
            RTS();
            MT(7);
            MTS();
        }
        };
        if (qt) {} else if (w == 0) steps(IC<0>()); else if (w == 1) steps(IC<1>()); else if (w == 2) steps(IC<2>()); else if (w == 3) steps(IC<3>()); else if (w < 7) steps(IC<4>()); else steps(IC<5>());
        __builtin_amdgcn_s_setprio(0);
    }
    SST(31);
    }
    } else
#pragma nounroll
    for (; lay < 2; ++lay) {
    fresh();
    SST(0);
    {
        LANES();
        const int G = bid < NWRK ? blockIdx.x - 128 : bid == NWRK ? 80 : -1;
        const int fi = bid >= NWRK ? 255 - bid : 16 + G;
        const bool first = fi < 32;
        auto a2_done = [&]() { if (w == 0) __builtin_amdgcn_raw_buffer_store_b32(0u, rwin, 0u, W_FB + fb + (unsigned)((G >> 2) * 128 + (G & 3) * 4), 16); };
        u32x2 tag = {0u, 0u};
        if (lay == 0) tag = get_tag();
        const unsigned fv = lay ? min(lane, 6) * 1024u : (lane & 7) * 1024u + (lane & 8) * 8u, fs = (lay ? W_FA : W_ST) + w * 128u;
        auto ask = [&]() { asm volatile("" ::: "memory"); return __builtin_amdgcn_raw_buffer_load_b128(rwin, fv, fs, 16); };
        auto miss = [&](u32x4 f) { return (unsigned)__ballot(((f[0] ^ tag[0]) | (f[1] ^ tag[1]) | (f[2] ^ tag[0]) | (f[3] ^ tag[1])) != 0u); };
        if (G < 0) for (unsigned spins = 0; spins < SPIN_CAP && (miss(ask()) & 255u); ++spins) __builtin_amdgcn_s_sleep(ASLP);
        BAR();
        RowTw tw;
        if (G < 0) tw = row_tw(lane);
        else {
            unsigned m = 0xFFFFu, e = ~0u;
            u32x4 f = SENT;
            a2_unit(rws, G, smem, w, lane, lay == 0, [&](int r) {
                if (r == 5) {
                    if (lay) { f = ask(); e = __builtin_amdgcn_raw_buffer_load_b32(rwin, 0u, W_E + (E_A1 + w) * 128u, 16); }
                    SB();
                    tw = row_tw(lane);
                    return;
                }
                if (r == 6) { if (lay) m = miss(f); else if (DFA) __builtin_amdgcn_s_sleep(DFA); }
                if (r == 7 && lay) await(E_A1 + w, 1, 0, e);
                else for (unsigned spins = 0; spins < SPIN_CAP && (m & (r == 6 ? 127u : r == 7 ? 128u : 0xFF00u)); ++spins) {
                    if (spins) __builtin_amdgcn_s_sleep(ASLP);
                    m = miss(ask());
                }
                if (r == 7) SST(5);
            }, [&](int k) {
                if (k == 4 && w == 7 && G < 8) due(E_A2 + G, lay ? DA2 : DA20);
                if (k == 7 && YSLP) __builtin_amdgcn_s_sleep(YSLP);
                if (bid < NREC) SST(8 + k);
            });
            SST(6);
            if (!first) VMWAIT();
            BAR0();
            if (!first) a2_done();
            SST(7);
        }
        auto columns = [&](bool all) {
            if (w == 7) await(E_A2 + fb / 128u, 8, all ? DFB : 0);
            if (w == 7 && all) for (unsigned spins = 0;;) {
                asm volatile("" ::: "memory");
                const u32x4 f = __builtin_amdgcn_raw_buffer_load_b128(rwin, (unsigned)min(lane, 20) * 128u, W_FB + fb, 16);
                if (!__any((f[0] | (lane < 20 ? f[1] | f[2] | f[3] : 0u)) != 0u)) break;
                if (spin_fail(spins, a.ctl)) break;
                __builtin_amdgcn_s_sleep(ASLP);
            }
            BAR0();
            if (!all || !first) SST(3);
        };
        const __amdgpu_buffer_rsrc_t rx = mkrsrc(lay ? ws + OFF_X2 : karg<3>(), S_ * D_ * 2);
        if (first) {
            const unsigned fo = fb + (unsigned)((fi >> 2) * 128 + (fi & 3) * 4);
            a3_unit<4>(rws, rx, fi * 8, lay, smem, tw, w, lane, [&](int k) {
                if (k < 0) { if (G < 0) columns(false); } else if (fi < 16) SST(16 + k);
                if (k == 0 && G >= 0) a2_done();
                if (k == 0 && w == 7 && fi < 4) due(E_A3 + fi, lay ? DA3 : DA30);
            });
            if (fi < 16) SST(21);
            VMWAIT();
            BAR0();
            if (tid == 0) __builtin_amdgcn_raw_buffer_store_b32(0u, rwin, 0u, W_H + fo, 16);
            SST(4);
        }
        if (lay == 0 && bid < NWRK) bid = role(config());
        if (bid >= NREC && bid < NWRK) {
            columns(true);
            for (int u = 16 + (bid - NREC) / 10 + 4 * (9 - (bid - NREC) % 10); u < 128; u += NOWN) {
                a3_unit<8>(rws, rx, u * 16, lay, smem, tw, w, lane, [&](int k) {});
                VMWAIT();
                BAR0();
                if (tid == 0) __builtin_amdgcn_raw_buffer_store_b64((u32x2){0u, 0u}, rwin, 0u, W_H + fb + (unsigned)((u >> 1) * 128 + (u & 1) * 8), 16);
                if (u < 56) SST(2);
            }
        }
        SST(1);
        places();
    }
    const __amdgpu_buffer_rsrc_t rout = mkrsrc(lay ? karg<4>() : ws + OFF_X2, S_ * D_ * 2);
    if (bid < NREC) {
        LANES();
        const int o = bid - 96, hg = w & 3;
        const unsigned ztoff = (unsigned)((o * H_ + 8 * hg) * 64 + lane) * 16u;
        const unsigned zoff = zpair(o / 10, o % 10) + (unsigned)(lane * 16);
        const unsigned poff = (4u * (mc >> 1) + 2u * (hg >> 1) + (mc & 1u)) * PAIR + nh + (unsigned)((hg & 1) * 4096 + (fq >> 1) * 512 + (fr >> 2) * 128 + (fr & 1) * 64 + (fq & 1) * 32 + (fr & 2) * 8);
        const unsigned pmoff = (8u + (mc & 1u)) * PAIR + nh + (mc >> 1) * 4096u + (unsigned)(fq * 256 + (fr >> 3) * 128 + (fr & 2) * 32 + (fr & 4) * 8 + (fr & 1) * 16);
        const unsigned fzo = (unsigned)(8 * hg + (lane & 7)) * 128u + (unsigned)(o / 10) * 4u + (lane & 8) * 8u;
        const unsigned moff = (o < 32 ? (unsigned)o * 256u : 4u * PAIR + (unsigned)(o - 32) * 256u) + nh + (unsigned)(lane * 4);
        const unsigned mc2 = o < 32 ? 2u * PAIR : 2048u;
        char* op = smem + 8192;
        SST(2);
        if (w == 7) clk_init(ck);
        if (w < 4) {
            u32x8 zf[4];
            int zr = -1;
            unsigned zso = O_Z;
            const unsigned ra = (o < 32 ? (unsigned)fq * PAIR + (unsigned)o * 256u : (4u + (fq & 1)) * PAIR + (unsigned)(fq >> 1) * 2048u + (unsigned)(o - 32) * 256u) + nh + (unsigned)(fr * 16);
            auto zt_miss = [&](int f, int s) { return __any(f < s && ((lane >> 3) & 1) == (int)((MXV >> s) & 1)); };
            await(E_A3 + fb / 128u, 4, lay ? DZ0 : DZ00);
            for (int s = 0;; ++s) {
                const unsigned pb = W_P + (unsigned)(s & 3) * 10u * PAIR;
                if (__builtin_expect(zr < s, 0)) for (unsigned spins = 0;;) {
                    asm volatile("" ::: "memory");
                    if (!zt_miss((int)__builtin_amdgcn_raw_buffer_load_b32(rwin, fzo, W_FZ + fb, 16), s)) break;
                    FC(fc2);
                    if (spin_fail(spins, a.ctl)) break;
                    __builtin_amdgcn_s_sleep(ZPS);
                }
                if (s == 0) SST(1);
                asm volatile(
                    "buffer_load_dwordx4 v[160:163], %[zt], %[rs], %[so] offen sc1\n\t"
                    "buffer_load_dwordx4 v[164:167], %[zt], %[rs], %[so] offen offset:1024 sc1\n\t"
                    "buffer_load_dwordx4 v[168:171], %[zt], %[rs], %[so] offen offset:2048 sc1\n\t"
                    "buffer_load_dwordx4 v[172:175], %[zt], %[rs], %[so] offen offset:3072 sc1\n\t"
                    "buffer_load_dwordx4 v[176:179], %[zt], %[rs], %[s4] offen sc1\n\t"
                    "buffer_load_dwordx4 v[180:183], %[zt], %[rs], %[s4] offen offset:1024 sc1\n\t"
                    "buffer_load_dwordx4 v[184:187], %[zt], %[rs], %[s4] offen offset:2048 sc1\n\t"
                    "buffer_load_dwordx4 v[188:191], %[zt], %[rs], %[s4] offen offset:3072 sc1"
                    : "=&{v[160:167]}"(zf[0]), "=&{v[168:175]}"(zf[1]), "=&{v[176:183]}"(zf[2]), "=&{v[184:191]}"(zf[3])
                    : [zt] "v"(ztoff), [rs] "s"(rws), [so] "s"(zso), [s4] "s"(zso + 4096u) : "memory");
                zso = O_Z + (unsigned)((s + 1) * ZTSEG) * 2u;
                if (__builtin_expect(budget <= 0, 0)) __hip_atomic_store(a.ctl, 1u, __ATOMIC_RELAXED, __HIP_MEMORY_SCOPE_AGENT);
                RT(6);
                if (s + 1 == NSEG) break;
                const unsigned spa = (unsigned)((s & 1) * 2048 + (w < 3 ? w * 512 : 0) + lane * 8);
#define OST(r)
                unsigned long long t0_, t1_, t2_, t3_, t4_ = 0, bad;
                int tries = budget;
                unsigned po = poff, pm = pmoff, x0 = 4u * PAIR, x1 = 4096u, nb = 0;
                const unsigned mb = W_MEM + (unsigned)((s + 1) & 3) * 6u * PAIR;
                if (w < 3) asm volatile(
                    "s_barrier\n\t"
                    OST("%[t0]")
                    "v_mov_b64 v[96:97], 0\n\t"
                    "v_mov_b64 v[98:99], 0\n\t"
                    "v_mov_b32 v100, 0\n\t"
                    "s_barrier\n\t"
                    "1:\n\t"
                    "buffer_load_dwordx4 v[208:211], %[po], %[rw], %[pb] offen sc1\n\t"
                    "buffer_load_dwordx4 v[212:215], %[po], %[rw], %[pb] offen offset:1024 sc1\n\t"
                    "buffer_load_dwordx4 v[216:219], %[po], %[rw], %[pb] offen offset:2048 sc1\n\t"
                    "buffer_load_dwordx4 v[220:223], %[po], %[rw], %[pb] offen offset:3072 sc1\n\t"
                    OST("%[t1]")
                    "s_waitcnt vmcnt(3)\n\t"
                    OST("%[t3]")
                    "v_smfmac_f32_16x16x64_bf16 v[96:99], v[208:211], v[160:167], %[ix]\n\t"
                    "v_max3_u32 v100, v100, v208, v209\n\t"
                    "v_max3_u32 v100, v100, v210, v211\n\t"
                    "s_waitcnt vmcnt(2)\n\t"
                    "v_smfmac_f32_16x16x64_bf16 v[96:99], v[212:215], v[168:175], %[ix]\n\t"
                    "v_max3_u32 v100, v100, v212, v213\n\t"
                    "v_max3_u32 v100, v100, v214, v215\n\t"
                    "s_waitcnt vmcnt(1)\n\t"
                    "v_smfmac_f32_16x16x64_bf16 v[96:99], v[216:219], v[176:183], %[ix]\n\t"
                    "v_max3_u32 v100, v100, v216, v217\n\t"
                    "v_max3_u32 v100, v100, v218, v219\n\t"
                    "s_waitcnt vmcnt(0)\n\t"
                    "v_smfmac_f32_16x16x64_bf16 v[96:99], v[220:223], v[184:191], %[ix]\n\t"
                    "v_max3_u32 v100, v100, v220, v221\n\t"
                    "v_max3_u32 v100, v100, v222, v223\n\t"
                    "v_cmp_eq_u32 vcc, -1, v100\n\t"
                    OST("%[t2]")
                    "s_nop 4\n\t"
                    "v_pk_add_f32 v[96:97], v[96:97], v[98:99]\n\t"
                    "ds_write_b64 %[sp], v[96:97]\n\t"
                    "s_cbranch_vccz 2f\n\t"
                    "s_sub_i32 %[tr], %[tr], 1\n\t"
                    "s_cmp_lt_i32 %[tr], 1\n\t"
                    "s_cbranch_scc1 2f\n\t"
                    "v_xor_b32 %[po], %[x0], %[po]\n\t"
                    "s_xor_b32 %[x0], %[x0], 0x14000\n\t"
                    "s_waitcnt lgkmcnt(0)\n\t"
                    "v_mov_b64 v[96:97], 0\n\t"
                    "v_mov_b64 v[98:99], 0\n\t"
                    "v_mov_b32 v100, 0\n\t"
                    "s_branch 1b\n\t"
                    "2:\n\t"
                    "s_waitcnt lgkmcnt(0)\n\t"
                    "s_barrier\n\t"
                    "s_sleep 3\n\t"
                    : [tr] "+s"(tries), [po] "+v"(po), [x0] "+s"(x0), [t0] "=&s"(t0_), [t1] "=&s"(t1_), [t2] "=&s"(t2_), [t3] "=&s"(t3_)
                    : [sp] "v"(spa), [ix] "v"(sidx), [rw] "s"(rwin), [pb] "s"(pb), "{v[160:167]}"(zf[0]), "{v[168:175]}"(zf[1]), "{v[176:183]}"(zf[2]), "{v[184:191]}"(zf[3])
                    : "v96", "v97", "v98", "v99", "v100", "v208", "v209", "v210", "v211", "v212", "v213", "v214", "v215", "v216", "v217", "v218", "v219", "v220", "v221", "v222", "v223", "vcc", "scc", "memory");
                else asm volatile(
                    "s_barrier\n\t"
                    "buffer_load_dwordx4 v[128:131], %[zo], %[rw], %[zb] offen sc1\n\t"
                    "buffer_load_dwordx4 v[132:135], %[zo], %[rw], %[zb] offen offset:1024 sc1\n\t"
                    "buffer_load_dwordx4 v[136:139], %[zo], %[rw], %[zb] offen offset:2048 sc1\n\t"
                    "buffer_load_dwordx4 v[140:143], %[zo], %[rw], %[zb] offen offset:3072 sc1\n\t"
                    "buffer_load_dwordx4 v[144:147], %[zo], %[rw], %[z4] offen sc1\n\t"
                    "buffer_load_dwordx4 v[148:151], %[zo], %[rw], %[z4] offen offset:1024 sc1\n\t"
                    "buffer_load_dwordx4 v[152:155], %[zo], %[rw], %[z4] offen offset:2048 sc1\n\t"
                    "buffer_load_dwordx4 v[156:159], %[zo], %[rw], %[z4] offen offset:3072 sc1\n\t"
                    OST("%[t0]")
                    "v_mov_b64 v[96:97], 0\n\t"
                    "v_mov_b64 v[98:99], 0\n\t"
                    "v_mov_b32 v100, 0\n\t"
                    "s_barrier\n\t"
                    "s_sleep %[fd]\n\t"
                    "buffer_load_dwordx4 v[208:211], %[po], %[rw], %[pb] offen sc1\n\t"
                    "buffer_load_dwordx4 v[212:215], %[po], %[rw], %[pb] offen offset:1024 sc1\n\t"
                    "buffer_load_dwordx4 v[216:219], %[po], %[rw], %[pb] offen offset:2048 sc1\n\t"
                    "buffer_load_dwordx4 v[220:223], %[po], %[rw], %[pb] offen offset:3072 sc1\n\t"
                    "buffer_load_dwordx4 v[224:227], %[pm], %[rw], %[pb] offen sc1\n\t"
                    "buffer_load_dwordx4 v[228:231], %[pm], %[rw], %[pb] offen offset:1024 sc1\n\t"
                    "buffer_load_dwordx4 v[232:235], %[pm], %[rw], %[pb] offen offset:2048 sc1\n\t"
                    "buffer_load_dwordx4 v[236:239], %[pm], %[rw], %[pb] offen offset:3072 sc1\n\t"
                    OST("%[t1]")
                    "s_waitcnt vmcnt(8)\n\t"
                    "v_max3_u32 v101, v129, v131, v133\n\t"
                    "v_max3_u32 v101, v101, v135, v137\n\t"
                    "v_max3_u32 v101, v101, v139, v141\n\t"
                    "v_max3_u32 v101, v101, v143, v145\n\t"
                    "v_max3_u32 v101, v101, v147, v149\n\t"
                    "v_max3_u32 v101, v101, v151, v153\n\t"
                    "v_max3_u32 v101, v101, v155, v157\n\t"
                    "v_max_u32 v101, v101, v159\n\t"
                    "v_cmp_eq_u32 vcc, -1, v101\n\t"
                    "s_cbranch_vccz 3f\n\t"
                    "buffer_load_dwordx4 v[128:131], %[zo], %[rw], %[zb] offen sc1\n\t"
                    "buffer_load_dwordx4 v[132:135], %[zo], %[rw], %[zb] offen offset:1024 sc1\n\t"
                    "buffer_load_dwordx4 v[136:139], %[zo], %[rw], %[zb] offen offset:2048 sc1\n\t"
                    "buffer_load_dwordx4 v[140:143], %[zo], %[rw], %[zb] offen offset:3072 sc1\n\t"
                    "buffer_load_dwordx4 v[144:147], %[zo], %[rw], %[z4] offen sc1\n\t"
                    "buffer_load_dwordx4 v[148:151], %[zo], %[rw], %[z4] offen offset:1024 sc1\n\t"
                    "buffer_load_dwordx4 v[152:155], %[zo], %[rw], %[z4] offen offset:2048 sc1\n\t"
                    "buffer_load_dwordx4 v[156:159], %[zo], %[rw], %[z4] offen offset:3072 sc1\n\t"
                    "s_waitcnt vmcnt(0)\n\t"
                    "v_max3_u32 v101, v129, v131, v133\n\t"
                    "v_max3_u32 v101, v101, v135, v137\n\t"
                    "v_max3_u32 v101, v101, v139, v141\n\t"
                    "v_max3_u32 v101, v101, v143, v145\n\t"
                    "v_max3_u32 v101, v101, v147, v149\n\t"
                    "v_max3_u32 v101, v101, v151, v153\n\t"
                    "v_max3_u32 v101, v101, v155, v157\n\t"
                    "v_max_u32 v101, v101, v159\n\t"
                    "v_mov_b32 v100, v101\n\t"
                    "3:\n\t"
                    "v_permlane16_swap_b32 v128, v129\n\t"
                    "v_permlane16_swap_b32 v130, v131\n\t"
                    "v_permlane16_swap_b32 v132, v133\n\t"
                    "v_permlane16_swap_b32 v134, v135\n\t"
                    "v_permlane16_swap_b32 v136, v137\n\t"
                    "v_permlane16_swap_b32 v138, v139\n\t"
                    "v_permlane16_swap_b32 v140, v141\n\t"
                    "v_permlane16_swap_b32 v142, v143\n\t"
                    "v_permlane16_swap_b32 v144, v145\n\t"
                    "v_permlane16_swap_b32 v146, v147\n\t"
                    "v_permlane16_swap_b32 v148, v149\n\t"
                    "v_permlane16_swap_b32 v150, v151\n\t"
                    "v_permlane16_swap_b32 v152, v153\n\t"
                    "v_permlane16_swap_b32 v154, v155\n\t"
                    "v_permlane16_swap_b32 v156, v157\n\t"
                    "v_permlane16_swap_b32 v158, v159\n\t"
                    "s_mov_b32 vcc_lo, 0x00ff00ff\n\t"
                    "s_mov_b32 vcc_hi, 0x00ff00ff\n\t"
                    "s_waitcnt vmcnt(7)\n\t"
                    OST("%[t3]")
                    "v_smfmac_f32_16x16x64_bf16 v[96:99], v[208:211], v[160:167], %[ix]\n\t"
                    "v_max3_u32 v100, v100, v208, v209\n\t"
                    "v_max3_u32 v100, v100, v210, v211\n\t"
                    "s_waitcnt vmcnt(6)\n\t"
                    "v_smfmac_f32_16x16x64_bf16 v[96:99], v[212:215], v[168:175], %[ix]\n\t"
                    "v_max3_u32 v100, v100, v212, v213\n\t"
                    "v_max3_u32 v100, v100, v214, v215\n\t"
                    "s_waitcnt vmcnt(5)\n\t"
                    "v_smfmac_f32_16x16x64_bf16 v[96:99], v[216:219], v[176:183], %[ix]\n\t"
                    "v_max3_u32 v100, v100, v216, v217\n\t"
                    "v_max3_u32 v100, v100, v218, v219\n\t"
                    "s_waitcnt vmcnt(4)\n\t"
                    "v_smfmac_f32_16x16x64_bf16 v[96:99], v[220:223], v[184:191], %[ix]\n\t"
                    "v_max3_u32 v100, v100, v220, v221\n\t"
                    "v_max3_u32 v100, v100, v222, v223\n\t"
                    "s_waitcnt vmcnt(3)\n\t"
                    "v_smfmac_f32_16x16x64_bf16 v[96:99], v[224:227], v[128:135], %[ix]\n\t"
                    "v_max3_u32 v100, v100, v224, v225\n\t"
                    "v_max3_u32 v100, v100, v226, v227\n\t"
                    E3("v_cmp_eq_u32 %[bad], -1, v100\n\t")
                    "s_waitcnt vmcnt(2)\n\t"
                    "v_smfmac_f32_16x16x64_bf16 v[96:99], v[228:231], v[136:143], %[ix]\n\t"
                    E3("s_cmp_lg_u64 %[bad], 0\n\t" "s_cbranch_scc1 6f\n\t")
                    "s_barrier\n\t"
                    "ds_read_b64 v[102:103], %[sp]\n\t"
                    "ds_read_b64 v[104:105], %[sp] offset:512\n\t"
                    "ds_read_b64 v[106:107], %[sp] offset:1024\n\t"
                    "v_max3_u32 v100, v100, v228, v229\n\t"
                    "v_max3_u32 v100, v100, v230, v231\n\t"
                    "s_waitcnt vmcnt(1)\n\t"
                    "v_smfmac_f32_16x16x64_bf16 v[96:99], v[232:235], v[144:151], %[ix]\n\t"
                    "v_max3_u32 v100, v100, v232, v233\n\t"
                    "v_max3_u32 v100, v100, v234, v235\n\t"
                    "s_waitcnt vmcnt(0)\n\t"
                    "v_smfmac_f32_16x16x64_bf16 v[96:99], v[236:239], v[152:159], %[ix]\n\t"
                    "v_max3_u32 v100, v100, v236, v237\n\t"
                    "v_max3_u32 v100, v100, v238, v239\n\t"
                    OST("%[t2]")
                    "v_cmp_eq_u32 %[bad], -1, v100\n\t"
                    "s_waitcnt lgkmcnt(0)\n\t"
                    "v_add_f32 v102, v102, v104\n\t"
                    "v_add_f32 v103, v103, v105\n\t"
                    "v_add_f32 v102, v102, v106\n\t"
                    "v_add_f32 v103, v103, v107\n\t"
                    "v_add_f32 v96, v96, v102\n\t"
                    "v_add_f32 v97, v97, v103\n\t"
                    "v_add_f32 v96, v96, v98\n\t"
                    "v_add_f32 v97, v97, v99\n\t"
                    "s_cmp_lg_u64 %[bad], 0\n\t"
                    "s_cbranch_scc1 4f\n\t"
                    "2:\n\t"
                    "v_cndmask_b32_dpp v98, v97, v96, vcc row_ror:8 row_mask:0xf bank_mask:0xf\n\t"
                    "s_not_b64 vcc, vcc\n\t"
                    "v_cndmask_b32_dpp v99, v96, v97, vcc row_ror:8 row_mask:0xf bank_mask:0xf\n\t"
                    "v_cvt_pk_bf16_f32 v98, v98, v99\n\t"
                    "v_min_u32 v98, -2, v98\n\t"
                    "buffer_store_dword v98, %[mo], %[rw], %[m0] offen sc1\n\t"
                    "buffer_store_dword v98, %[mo], %[rw], %[m1] offen sc1\n\t"
                    "buffer_store_dword v98, %[mo], %[rw], %[m2] offen sc1\n\t"
                    "buffer_store_dword v98, %[mo], %[rw], %[m3] offen sc1\n\t"
                    OST("%[t4]")
                    "s_branch 5f\n\t"
                    "6:\n\t"
                    "s_mov_b32 %[nb], 1\n\t"
                    "s_waitcnt vmcnt(0)\n\t"
                    "s_nop 7\n\t"
                    "4:\n\t"
                    "s_sub_i32 %[tr], %[tr], 1\n\t"
                    "s_cmp_lt_i32 %[tr], 1\n\t"
                    "s_cbranch_scc1 7f\n\t"
                    "v_xor_b32 %[po], %[x0], %[po]\n\t"
                    "s_xor_b32 %[x0], %[x0], 0x14000\n\t"
                    "v_xor_b32 %[pm], %[x1], %[pm]\n\t"
                    "s_xor_b32 %[x1], %[x1], 0x5000\n\t"
                    "buffer_load_dwordx4 v[128:131], %[zo], %[rw], %[zb] offen sc1\n\t"
                    "buffer_load_dwordx4 v[132:135], %[zo], %[rw], %[zb] offen offset:1024 sc1\n\t"
                    "buffer_load_dwordx4 v[136:139], %[zo], %[rw], %[zb] offen offset:2048 sc1\n\t"
                    "buffer_load_dwordx4 v[140:143], %[zo], %[rw], %[zb] offen offset:3072 sc1\n\t"
                    "buffer_load_dwordx4 v[144:147], %[zo], %[rw], %[z4] offen sc1\n\t"
                    "buffer_load_dwordx4 v[148:151], %[zo], %[rw], %[z4] offen offset:1024 sc1\n\t"
                    "buffer_load_dwordx4 v[152:155], %[zo], %[rw], %[z4] offen offset:2048 sc1\n\t"
                    "buffer_load_dwordx4 v[156:159], %[zo], %[rw], %[z4] offen offset:3072 sc1\n\t"
                    "buffer_load_dwordx4 v[208:211], %[po], %[rw], %[pb] offen sc1\n\t"
                    "buffer_load_dwordx4 v[212:215], %[po], %[rw], %[pb] offen offset:1024 sc1\n\t"
                    "buffer_load_dwordx4 v[216:219], %[po], %[rw], %[pb] offen offset:2048 sc1\n\t"
                    "buffer_load_dwordx4 v[220:223], %[po], %[rw], %[pb] offen offset:3072 sc1\n\t"
                    "buffer_load_dwordx4 v[224:227], %[pm], %[rw], %[pb] offen sc1\n\t"
                    "buffer_load_dwordx4 v[228:231], %[pm], %[rw], %[pb] offen offset:1024 sc1\n\t"
                    "buffer_load_dwordx4 v[232:235], %[pm], %[rw], %[pb] offen offset:2048 sc1\n\t"
                    "buffer_load_dwordx4 v[236:239], %[pm], %[rw], %[pb] offen offset:3072 sc1\n\t"
                    "v_mov_b64 v[96:97], 0\n\t"
                    "v_mov_b64 v[98:99], 0\n\t"
                    "v_mov_b32 v100, 0\n\t"
                    "s_waitcnt vmcnt(0)\n\t"
                    "v_max3_u32 v101, v129, v131, v133\n\t"
                    "v_max3_u32 v101, v101, v135, v137\n\t"
                    "v_max3_u32 v101, v101, v139, v141\n\t"
                    "v_max3_u32 v101, v101, v143, v145\n\t"
                    "v_max3_u32 v101, v101, v147, v149\n\t"
                    "v_max3_u32 v101, v101, v151, v153\n\t"
                    "v_max3_u32 v101, v101, v155, v157\n\t"
                    "v_max_u32 v101, v101, v159\n\t"
                    "v_mov_b32 v100, v101\n\t"
                    "v_permlane16_swap_b32 v128, v129\n\t"
                    "v_permlane16_swap_b32 v130, v131\n\t"
                    "v_permlane16_swap_b32 v132, v133\n\t"
                    "v_permlane16_swap_b32 v134, v135\n\t"
                    "v_permlane16_swap_b32 v136, v137\n\t"
                    "v_permlane16_swap_b32 v138, v139\n\t"
                    "v_permlane16_swap_b32 v140, v141\n\t"
                    "v_permlane16_swap_b32 v142, v143\n\t"
                    "v_permlane16_swap_b32 v144, v145\n\t"
                    "v_permlane16_swap_b32 v146, v147\n\t"
                    "v_permlane16_swap_b32 v148, v149\n\t"
                    "v_permlane16_swap_b32 v150, v151\n\t"
                    "v_permlane16_swap_b32 v152, v153\n\t"
                    "v_permlane16_swap_b32 v154, v155\n\t"
                    "v_permlane16_swap_b32 v156, v157\n\t"
                    "v_permlane16_swap_b32 v158, v159\n\t"
                    "s_nop 1\n\t"
                    "v_smfmac_f32_16x16x64_bf16 v[96:99], v[208:211], v[160:167], %[ix]\n\t"
                    "v_max3_u32 v100, v100, v208, v209\n\t"
                    "v_max3_u32 v100, v100, v210, v211\n\t"
                    "v_smfmac_f32_16x16x64_bf16 v[96:99], v[212:215], v[168:175], %[ix]\n\t"
                    "v_max3_u32 v100, v100, v212, v213\n\t"
                    "v_max3_u32 v100, v100, v214, v215\n\t"
                    "v_smfmac_f32_16x16x64_bf16 v[96:99], v[216:219], v[176:183], %[ix]\n\t"
                    "v_max3_u32 v100, v100, v216, v217\n\t"
                    "v_max3_u32 v100, v100, v218, v219\n\t"
                    "v_smfmac_f32_16x16x64_bf16 v[96:99], v[220:223], v[184:191], %[ix]\n\t"
                    "v_max3_u32 v100, v100, v220, v221\n\t"
                    "v_max3_u32 v100, v100, v222, v223\n\t"
                    "v_smfmac_f32_16x16x64_bf16 v[96:99], v[224:227], v[128:135], %[ix]\n\t"
                    "v_max3_u32 v100, v100, v224, v225\n\t"
                    "v_max3_u32 v100, v100, v226, v227\n\t"
                    "v_smfmac_f32_16x16x64_bf16 v[96:99], v[228:231], v[136:143], %[ix]\n\t"
                    "v_max3_u32 v100, v100, v228, v229\n\t"
                    "v_max3_u32 v100, v100, v230, v231\n\t"
                    "v_smfmac_f32_16x16x64_bf16 v[96:99], v[232:235], v[144:151], %[ix]\n\t"
                    "v_max3_u32 v100, v100, v232, v233\n\t"
                    "v_max3_u32 v100, v100, v234, v235\n\t"
                    "v_smfmac_f32_16x16x64_bf16 v[96:99], v[236:239], v[152:159], %[ix]\n\t"
                    "v_max3_u32 v100, v100, v236, v237\n\t"
                    "v_max3_u32 v100, v100, v238, v239\n\t"
                    "v_cmp_eq_u32 %[bad], -1, v100\n\t"
                    "s_nop 7\n\t"
                    "s_cmp_lg_u64 %[bad], 0\n\t"
                    "s_cbranch_scc1 4b\n\t"
                    "7:\n\t"
                    "s_cmp_eq_u32 %[nb], 0\n\t"
                    "s_cbranch_scc1 8f\n\t"
                    "s_barrier\n\t"
                    "ds_read_b64 v[102:103], %[sp]\n\t"
                    "ds_read_b64 v[104:105], %[sp] offset:512\n\t"
                    "ds_read_b64 v[106:107], %[sp] offset:1024\n\t"
                    "s_waitcnt lgkmcnt(0)\n\t"
                    "v_add_f32 v102, v102, v104\n\t"
                    "v_add_f32 v103, v103, v105\n\t"
                    "v_add_f32 v102, v102, v106\n\t"
                    "v_add_f32 v103, v103, v107\n\t"
                    "8:\n\t"
                    "v_add_f32 v96, v96, v102\n\t"
                    "v_add_f32 v97, v97, v103\n\t"
                    "v_add_f32 v96, v96, v98\n\t"
                    "v_add_f32 v97, v97, v99\n\t"
                    "s_nop 0\n\t"
                    "s_branch 2b\n\t"
                    "5:\n\t"
                    : [tr] "+s"(tries), [po] "+v"(po), [pm] "+v"(pm), [x0] "+s"(x0), [x1] "+s"(x1), [nb] "+s"(nb), [bad] "=&s"(bad), [t0] "=&s"(t0_), [t1] "=&s"(t1_), [t2] "=&s"(t2_), [t3] "=&s"(t3_), [t4] "=&s"(t4_)
                    : [zo] "v"(zoff), [sp] "v"(spa), [ix] "v"(sidx), [rw] "s"(rwin), [zb] "s"(zb), [z4] "s"(zb + 4096u), [pb] "s"(pb), [mo] "v"(moff), [fd] "n"(FDL),
                      [m0] "s"(mb), [m1] "s"(mb + PAIR), [m2] "s"(mb + mc2), [m3] "s"(mb + mc2 + PAIR), "{v[160:167]}"(zf[0]), "{v[168:175]}"(zf[1]), "{v[176:183]}"(zf[2]), "{v[184:191]}"(zf[3])
                    : "v96", "v97", "v98", "v99", "v100", "v101", "v102", "v103", "v104", "v105", "v106", "v107", "v128", "v129", "v130", "v131", "v132", "v133", "v134", "v135", "v136", "v137", "v138", "v139", "v140", "v141", "v142", "v143", "v144", "v145", "v146", "v147", "v148", "v149", "v150", "v151", "v152", "v153", "v154", "v155", "v156", "v157", "v158", "v159", "v208", "v209", "v210", "v211", "v212", "v213", "v214", "v215", "v216", "v217", "v218", "v219", "v220", "v221", "v222", "v223", "v224", "v225", "v226", "v227", "v228", "v229", "v230", "v231", "v232", "v233", "v234", "v235", "v236", "v237", "v238", "v239", "vcc", "scc", "memory");
                budget = tries;
                if (w == 3) *(volatile LAS int*)(smem + 4100) = s;
                __builtin_amdgcn_s_sleep(DPS - 4);
                if (w == 3) {
                    st128(SENT, rwin, ra, W_MEM + (unsigned)(s & 3) * 6u * PAIR, 16);
                    if (lane == 0) __builtin_amdgcn_raw_buffer_store_b64((u32x2){(unsigned)s - 1u, (unsigned)max(zr, s)}, rwin, 0u, W_GO + fb + (unsigned)o * 128u, 16);
                }
                for (;;) {
                    const u32x2 x = *(volatile LAS u32x2*)(smem + 4096);
                    zr = __builtin_amdgcn_readfirstlane(x[0]);
                    if (__builtin_amdgcn_readfirstlane(x[1]) == s) break;
                    __builtin_amdgcn_s_sleep(2);
                }
                zb = zb == W_ZM + 2u * NOWN * PAIR ? W_ZM : zb + NOWN * PAIR;
            }
            asm volatile("s_waitcnt vmcnt(0)" : "+{v[160:167]}"(zf[0]), "+{v[168:175]}"(zf[1]), "+{v[176:183]}"(zf[2]), "+{v[184:191]}"(zf[3]) :: "memory");
            BAR0();
            BAR0();
            f32x4 c[2];
            for (unsigned spins = 0;;) {
                asm volatile("" ::: "memory");
                u32x4 pt[16], ptm[2][2], zm[2];
#pragma unroll
                for (int q = 0; q < 2; ++q) zm[q] = __builtin_amdgcn_raw_buffer_load_b128(rwin, zoff + hg * 2048u + q * 1024u, zb, 16);
#pragma unroll
                for (int q = 0; q < 16; ++q) pt[q] = __builtin_amdgcn_raw_buffer_load_b128(rwin, (unsigned)(16 * hg) * 1024u + lane * 16u, W_PL + fb + q * 1024u, 16);
#pragma unroll
                for (int q = 0; q < 4; ++q) ptm[q >> 1][q & 1] = __builtin_amdgcn_raw_buffer_load_b128(rwin, (unsigned)(hg * 2048 + lane * 16), W_PL + fb + 65536u + (q >> 1) * 8192u + (q & 1) * 1024u, 16);
                unsigned m = max(smax(zm[0]), smax(zm[1]));
#pragma unroll
                for (int t = 0; t < 2; ++t) {
                    c[t] = z4;
#pragma unroll
                    for (int q = 0; q < 8; ++q) { c[t] = mfma32(half8(zf[q >> 1], q & 1), pt[2 * q + t], c[t]); m = max(m, smax(pt[2 * q + t])); }
#pragma unroll
                    for (int q = 0; q < 2; ++q) { c[t] = mfma32((u32x4){zm[q][0], zm[q][2], zm[q][1], zm[q][3]}, ptm[t][q], c[t]); m = max(m, smax(ptm[t][q])); }
                }
                if (!__any(m == 0xFFFFFFFFu)) break;
                if (spin_fail(spins, a.ctl)) break;
            }
#pragma unroll
            for (int t = 0; t < 2; ++t)
#pragma unroll
                for (int r = 0; r < 4; ++r) *(float*)(op + hg * 2048 + (16 * t + fr) * 64 + pi16(4 * fq + r) * 4) = c[t][r];
            BAR();
            if (w == 3) st128(SENT, rwin, ra, W_MEM + 18u * PAIR, 16);
        } else if (w == 4) {
            const unsigned fo = (unsigned)(lane & 31) * 128u + (unsigned)(o / 10) * 4u + (lane & 32) * 2u;
            int zk = -1;
            if (lane < 2) *(volatile LAS int*)(smem + 4096 + lane * 4) = -1;
            for (int s = 0; s < NSEG; ++s) {
                asm volatile("s_waitcnt lgkmcnt(0)\n\ts_barrier\n\ts_barrier" ::: "memory");
                int f = 0;
                const bool look = zk < min(s + ZLA, NSEG - 1);
                if (look) { __builtin_amdgcn_s_sleep(6); f = (int)__builtin_amdgcn_raw_buffer_load_b32(rwin, fo, W_FZ + fb, 16); }
                SB();
                BAR0();
                if (look) {
                    f = min(f, __builtin_amdgcn_update_dpp(f, f, 0xB1, 0xF, 0xF, false)); f = min(f, __builtin_amdgcn_update_dpp(f, f, 0x4E, 0xF, 0xF, false));
                    f = min(f, __builtin_amdgcn_update_dpp(f, f, 0x141, 0xF, 0xF, false)); f = min(f, __builtin_amdgcn_update_dpp(f, f, 0x140, 0xF, 0xF, false));
                    zk = seg_done(MXV, min(__builtin_amdgcn_readlane(f, 0), __builtin_amdgcn_readlane(f, 16)), min(__builtin_amdgcn_readlane(f, 32), __builtin_amdgcn_readlane(f, 48)));
                    *(volatile LAS int*)(smem + 4096) = zk;
                }
            }
        } else if (w == 5) {
            for (int s = 0; s < 3 * NSEG; ++s) BAR0();
        } else if (w == 6) {
            const unsigned ooff = (unsigned)((S_ - SEG + (lane >> 1)) * D_ + o * 16 + (lane & 1) * 8) * 2u;
            for (int s = 0; s < 3 * NSEG - 2; ++s) BAR0();
            const u32x4 xr = __builtin_amdgcn_raw_buffer_load_b128(rws, ooff, O_X1, 16);
            asm volatile("s_barrier\n\ts_barrier ; 6" ::: "memory");
            const char* src = op + lane * 32;
            f32x4 x = *(const f32x4*)src, y = *(const f32x4*)(src + 16);
#pragma unroll
            for (int ww = 1; ww < 4; ++ww) { x += *(const f32x4*)(src + ww * 2048); y += *(const f32x4*)(src + ww * 2048 + 16); }
            const u32x4 r = {pack2(lo2f(xr[0]) + bfr(x[0]), hi2f(xr[0]) + bfr(x[1])), pack2(lo2f(xr[1]) + bfr(x[2]), hi2f(xr[1]) + bfr(x[3])),
                             pack2(lo2f(xr[2]) + bfr(y[0]), hi2f(xr[2]) + bfr(y[1])), pack2(lo2f(xr[3]) + bfr(y[2]), hi2f(xr[3]) + bfr(y[3]))};
            st128(((u32x4){nosent(r[0]), nosent(r[1]), nosent(r[2]), nosent(r[3])}), rout, ooff, 0u, 16);
            if (lay && o == 0) __builtin_amdgcn_raw_buffer_store_b64(get_tag(), rws, 0u, 8u, 16);
        } else {
            Tk tk = tk_init();
            __builtin_amdgcn_s_setprio(3);
            for (int s = 0; s < NSEG; ++s) {
                const unsigned ta0 = ck.ta, mm0 = ck.ma;
                unsigned tp = tpp + lt + xs + M1;
                if (s < SAD) { sched(tk, s); tp = tpp - M2 - XS0 - (lay ? PEN0 : PEN00); }
                if (s + 1 == NSEG && o == 0) due(E_OUT, (sx24(tp + ML - 4u * (unsigned)__builtin_amdgcn_s_memrealtime()) >> 2) + DOUT);
                wait_fine_bar(tp - DZP, ck);
                wait_cyc_bar(cyc_at(ck, sx24(tp + (s + 1 < NSEG ? 0 : ML) - ck.ta)));
                clk_rate(ck, ta0, mm0);
                asm volatile("s_barrier ; 7" ::: "memory");
                if (s >= SAD && s + 1 < NSEG) sched(tk, s);
                RTS();
            }
            __builtin_amdgcn_s_setprio(0);
        }
        SST(23);
    } else if (bid < NQT) {
        LANES();
        const int o = bid - NREC, cg = o >> 1;
        const unsigned ztoff = (unsigned)((2 * cg * H_ + 4 * w) * 64 + lane) * 16u;
        const unsigned ptoff = (unsigned)(8 * w * 64 + lane) * 16u, ptmoff = 65536u + (unsigned)(4 * w * 16 + lane) * 16u;
        const unsigned ooff = (unsigned)(((w & 1) * 16 + (lane >> 2)) * D_ + cg * 32 + (lane & 3) * 8) * 2u;
        const unsigned tfoff = lane < 32 ? (lane < 16 ? W_FPT : W_FZ2) + (unsigned)(4 * w + (lane & 3)) * 128u : W_GO + (unsigned)(o * 128 + (lane & 1) * 4);
        int rdy = -1, go = -1;
        unsigned pend = 0;
        auto flush = [&]() { if (pend) { __builtin_amdgcn_raw_buffer_store_b32(0u, rwin, 0u, pend, 16); pend = 0; } };
        auto ask = [&](int s) {
            asm volatile("" ::: "memory");
            return (int)__builtin_amdgcn_raw_buffer_load_b32(rwin, s < TAILS ? W_GO + (unsigned)(o * 128 + (lane & 1) * 4) : tfoff, fb, 16);
        };
        auto look = [&](int f, int s) {
            go = __builtin_amdgcn_readlane(f, 33);
            rdy = max(rdy, __builtin_amdgcn_readlane(f, 32));
            if (!(unsigned)__ballot(f < s)) rdy = max(rdy, s);
        };
        auto wait_rdy = [&](int& x, int v, int s) {
            for (unsigned spins = 0; x < v;) {
                look(ask(s), s);
                VMWAIT();
                flush();
                if (x >= v) break;
                if (spin_fail(spins, a.ctl)) break;
                if (s >= TAILS) __builtin_amdgcn_s_sleep(OSLP2); else __builtin_amdgcn_s_sleep(OSLP);
            }
        };
        struct Seg { u32x4 xr, zf[2][4], zx[2], pt[8], ptm[2]; };
        auto load_z = [&](Seg& g, int s) {
            if (w >= 6) g.xr = __builtin_amdgcn_raw_buffer_load_b128(rws, ooff, O_X1 + (unsigned)(s * SEG * D_ * 2), 16);
#pragma unroll
            for (int q = 0; q < 8; ++q) g.zf[q >> 2][q & 3] = __builtin_amdgcn_raw_buffer_load_b128(rws, ztoff + (q & 3) * 1024u, O_Z + (unsigned)(s * ZTSEG) * 2u + (q >> 2) * (H_ * 1024u), 16);
        };
        auto load_p = [&](Seg& g, int s) {
            const unsigned ptb = O_PT + (unsigned)(s * PTSEG) * 2u;
#pragma unroll
            for (int q = 0; q < 8; ++q) g.pt[q] = __builtin_amdgcn_raw_buffer_load_b128(rws, ptoff + q * 1024u, ptb, 16);
#pragma unroll
            for (int q = 0; q < 2; ++q) {
                g.zx[q] = __builtin_amdgcn_raw_buffer_load_b128(rws, tid * 16u, O_Z2 + (unsigned)(s * NOWN + 2 * cg + q) * 8192u, 16);
                g.ptm[q] = __builtin_amdgcn_raw_buffer_load_b128(rws, ptmoff + q * 8192u, ptb, 16);
            }
        };
        auto one = [&](Seg& g, Seg& n, int s) {
            RT(0);
            wait_rdy(rdy, s, s);
            RT(1);
            if (s >= NSEG - 8 && cg == 0 && w == 0) due(E_OUT + NSEG - 1 - s, DOU2);
            load_p(g, s);
            const int f = ask(s + 2);
            SB();
            char* op = smem + ((s >> 1) & 1) * 32768;
#pragma unroll
            for (int u = 0; u < 4; ++u) {
                const int tq = u >> 1, t = u & 1;
                f32x4 c = z4;
#pragma unroll
                for (int q = 0; q < 4; ++q) c = mfma32(g.zf[t][q], g.pt[2 * q + tq], c);
                c = mfma32((u32x4){g.zx[t][0], g.zx[t][2], g.zx[t][1], g.zx[t][3]}, g.ptm[tq], c);
                *(f32x4*)(op + w * 4096 + (16 * tq + fr) * 128 + (((t * 4 + fq) ^ (fr & 7)) << 4)) = c;
            }
            look(f, s + 2);
            asm volatile("" ::: "memory");
            flush();
            BAR();
            const bool pre = go >= s + 2 && w < 6 && s + 2 < NSEG - 1;
            if (pre) load_z(n, s + 2);
            if (w >= 6) {
                const int tk = (w & 1) * 16 + (lane >> 2);
                f32x2 x[4];
#pragma unroll
                for (int q = 0; q < 4; ++q) {
                    const char* sp = op + tk * 128 + ((((lane & 2) * 2 + q) ^ (tk & 7)) << 4) + (lane & 1) * 8;
                    x[q] = *(const f32x2*)sp;
#pragma unroll
                    for (int ww = 1; ww < 8; ++ww) x[q] += *(const f32x2*)(sp + ww * 4096);
                }
                const u32x4 xr = g.xr;
                const u32x4 r = {pack2(lo2f(xr[0]) + bfr(x[0][0]), hi2f(xr[0]) + bfr(x[2][0])), pack2(lo2f(xr[1]) + bfr(x[1][0]), hi2f(xr[1]) + bfr(x[3][0])),
                                 pack2(lo2f(xr[2]) + bfr(x[0][1]), hi2f(xr[2]) + bfr(x[2][1])), pack2(lo2f(xr[3]) + bfr(x[1][1]), hi2f(xr[3]) + bfr(x[3][1]))};
                st128(((u32x4){nosent(r[0]), nosent(r[1]), nosent(r[2]), nosent(r[3])}), rout, ooff, (unsigned)(s * SEG * D_ * 2), 16);
                if (lay == 0 && s < NSEG - 8) pend = W_OUT + (unsigned)(s * 64 + 2 * cg + (w & 1)) * 4u;
            }
            RT(2);
            RTS();
            if (s < 8) SST(8 + s);
            if (s >= 56) SST(s - 40);
            if (!pre && s + 2 < NSEG - 1) { wait_rdy(go, s + 2, 0); load_z(n, s + 2); }
        };
        Seg g0 = {}, g1 = {};
        wait_rdy(go, o & 1, 0);
        if (w >= 6) wait_h(a, rwin, fb, 3, hrdy);
        load_z(g0, o & 1);
        for (int s = o & 1; s < NSEG - 1; s += 4) {
            one(g0, g1, s);
            if (s + 2 < NSEG - 1) one(g1, g0, s + 2);
        }
    } else if (lay == 0) {
        LANES();
        const RowTw tw = row_tw(lane);
        for (int u = (256 - bid) & 15; u < 256; u += 16) {
            if (w == 0) {
                if (u >= 224) await(E_OUT + (255 - u >> 2), 1, 0);
                else for (unsigned spins = 0;;) {
                    asm volatile("" ::: "memory");
                    const unsigned f = __builtin_amdgcn_raw_buffer_load_b32(rwin, (unsigned)min(lane, NOWN - 1) * 4u, W_OUT + (unsigned)(u >> 2) * 256u, 16);
                    if (!__any(f != 0u)) break;
                    if (spin_fail(spins, a.ctl)) break;
                    __builtin_amdgcn_s_sleep(WSLP);
                }
            }
            BAR0();
            SST(24);
            const unsigned fo = W_FA + (unsigned)((u >> 2) * 128 + (u & 3) * 4);
            a1_unit<4, 512>(rout, u * 8, u >= 224, rws, smem, tw, w, lane, [&](int k) {
                if (k == 0 && w == 0 && u >= 224 && !(u & 3)) due(E_A1 + (u - 224 >> 2), DA1);
                if (k >= 0) SST(8 + k);
            });
            SST(25);
            if (u >= 224) continue;
            VMWAIT();
            BAR0();
            if (w == 0) __builtin_amdgcn_raw_buffer_store_b32(0u, rwin, 0u, fo, 16);
        }
    }
    SST(31);
    }
}

constexpr int L_DBG = 0;

extern "C" void kernel_launch(void* const* d_in, const int* in_sizes, int n_in, void* d_out, int out_size, void* d_ws, size_t ws_size,
                              hipStream_t stream) {
    char* ws = (char*)d_ws;
    hipFuncSetAttribute((const void*)b3_kernel, hipFuncAttributeMaxDynamicSharedMemorySize, L_END + L_DBG);
    char* win = (char*)(((uintptr_t)(ws + OFF_WIN) + 0x1FFFFFu) & ~(uintptr_t)0x1FFFFFu);
    RecArgs ra{(unsigned*)ws, win, (const u16*)d_in[4], (const u16*)d_in[0], (u16*)d_out, {(const u16*)d_in[5], (const u16*)d_in[6], (const u16*)d_in[7], (const u16*)d_in[8], (const u16*)d_in[1], (const u16*)d_in[2]}};
    b3_kernel<<<256, 512, L_END + L_DBG, stream>>>(ra);
}
```

```cpp
#include <hip/hip_runtime.h>
#include <hip/hip_bf16.h>
#include <stdint.h>

typedef unsigned short u16;
typedef __attribute__((ext_vector_type(8))) __bf16 bf16x8;
typedef __attribute__((ext_vector_type(4))) short s16x4;
typedef __attribute__((ext_vector_type(4))) float f32x4;
typedef __attribute__((ext_vector_type(8))) float f32x8;
typedef __attribute__((ext_vector_type(16))) float f32x16;
typedef __attribute__((ext_vector_type(4))) unsigned u32x4;
typedef __attribute__((ext_vector_type(2))) unsigned u32x2;
typedef __attribute__((ext_vector_type(8))) unsigned u32x8;
typedef __attribute__((ext_vector_type(16))) __bf16 bf16x16;
typedef __attribute__((ext_vector_type(2))) unsigned short u16x2;
#define LAS __attribute__((address_space(3)))
template <int N> struct IC { static constexpr int v = N; };

constexpr int S_ = 2048, D_ = 640, DF = 321, H_ = 32, DH = 128, HD = 4096, NM = 8, SEG = 32, NSEG = 64;
constexpr int NQKV = 3 * HD;
constexpr int NOWN = 40;
constexpr int ZTR = NSEG;
constexpr size_t ZTSEG = (size_t)NOWN * H_ * 512;
constexpr unsigned PAIR = 16384, HALF = 8192;
constexpr unsigned W_MEM = 0, W_P = W_MEM + 24 * PAIR, W_T = W_P + 40 * PAIR, W_GO = W_T + 8 * PAIR, W_OUT = W_GO + PAIR, W_FZ = W_OUT + PAIR, W_FQ = W_FZ + 12288,
                   W_H = W_FZ + PAIR, W_FA = W_H + 8192, W_FB = W_GO + 8192, W_E = W_FB + 4096, W_PL = W_H + PAIR, W_FL = W_PL + 10 * PAIR - W_GO, W_PROBE = W_GO + 2 * W_FL,
                   W_ZM = 2u * 1024 * 1024, W_END = W_ZM + 120 * PAIR;
constexpr unsigned W_ST = W_PROBE + 2 * PAIR, W_ROT = W_ST + 8192, W_ARM = W_ROT + 128, W_CFG = W_ARM + 128;
static_assert(W_ROT + PAIR <= 2u * 1024 * 1024 && W_PROBE % (2 * PAIR) == 0, "window");
constexpr int PTSEG = 32768 + 8192;

#define WAUX 0
#define FPAD 512
constexpr int GF = 65536 + FPAD, YS = 24576 + FPAD;
constexpr int NG = 81;
constexpr size_t al(size_t x) { return (x + 255) & ~(size_t)255; }
constexpr size_t OFF_CTL  = 0;
constexpr size_t OFF_WFT  = 4096 + 4 * 1024 * 1024;
constexpr size_t OFF_F    = OFF_WFT + al((size_t)4 * NG * S_ * 4);
constexpr size_t OFF_X1   = OFF_F + (size_t)NG * GF;
constexpr size_t OFF_X2   = OFF_X1 + al((size_t)S_ * D_ * 2);
constexpr size_t OFF_Y0   = OFF_X2 + al((size_t)S_ * D_ * 2);
constexpr size_t OFF_H    = OFF_Y0 + (size_t)NG * YS;
constexpr size_t OFF_ARM  = OFF_Y0 - 256 * D_ * 2, ARM_END = OFF_H + 8 * 40960;
constexpr size_t OFF_Z    = OFF_H + (size_t)(NSEG + 4) * 40960;
constexpr size_t OFF_Z2   = OFF_Z + al(ZTSEG * ZTR * 2);
constexpr size_t OFF_PT   = OFF_Z2 + al((size_t)NSEG * NOWN * 8192);
constexpr size_t OFF_QK   = OFF_PT + al((size_t)NSEG * PTSEG * 2);
constexpr size_t OFF_WIN  = OFF_QK + (size_t)NSEG * H_ * 16384;
constexpr size_t OFF_END  = OFF_WIN + (size_t)6 * 1024 * 1024;
static_assert(OFF_END <= (size_t)256 * 1024 * 1024, "workspace");

#define st128(v, r, vo, so, aux) do { const u32x4 d_ = (v); __builtin_amdgcn_raw_buffer_store_b128(d_, r, vo, so, aux); asm volatile("s_nop 1" :: "v"(d_)); } while (0)
__device__ __forceinline__ float bf2f(u16 v) { return __uint_as_float((unsigned)v << 16); }
__device__ __forceinline__ u16 f2bf(float f) { __bf16 b = (__bf16)f; return __builtin_bit_cast(u16, b); }
__device__ __forceinline__ float bfr(float f) { return (float)(__bf16)f; }
typedef __attribute__((ext_vector_type(2))) float f32x2;
typedef __attribute__((ext_vector_type(2))) __bf16 bf16x2;
__device__ __forceinline__ unsigned pack2(float a, float b) { return __builtin_bit_cast(unsigned, __builtin_convertvector((f32x2){a, b}, bf16x2)); }
__device__ __forceinline__ float lo2f(unsigned v) { return __uint_as_float(v << 16); }
__device__ __forceinline__ float hi2f(unsigned v) { return __uint_as_float(v & 0xFFFF0000u); }
__device__ __forceinline__ float fmx(float a, float b) { return __builtin_elementwise_maximum(a, b); }
__device__ __forceinline__ f32x4 mfma32(u32x4 a, u32x4 b, f32x4 c) {
    return __builtin_amdgcn_mfma_f32_16x16x32_bf16(__builtin_bit_cast(bf16x8, a), __builtin_bit_cast(bf16x8, b), c, 0, 0, 0);
}
__device__ __forceinline__ f32x4 mfma16(u32x2 a, u32x2 b, f32x4 c) {
    return __builtin_amdgcn_mfma_f32_16x16x16bf16_1k(__builtin_bit_cast(s16x4, a), __builtin_bit_cast(s16x4, b), c, 0, 0, 0);
}
__device__ __forceinline__ f32x4 smfmac(u32x4 a, u32x8 b, f32x4 c, int idx) {
    return __builtin_amdgcn_smfmac_f32_16x16x64_bf16(__builtin_bit_cast(bf16x8, a), __builtin_bit_cast(bf16x16, b), c, idx, 0, 0);
}
__device__ __forceinline__ int pi16(int k) { return ((k & 3) << 2) | ((k >> 1) & 2) | (k >> 3); }
__device__ __forceinline__ float addswap32(float a, float b) {
    auto r = __builtin_amdgcn_permlane32_swap(__float_as_uint(a), __float_as_uint(b), false, false);
    return __uint_as_float(r[0]) + __uint_as_float(r[1]);
}
__device__ __forceinline__ float xmax3216(float v) {
    auto r = __builtin_amdgcn_permlane32_swap(__float_as_uint(v), __float_as_uint(v), false, false);
    v = fmx(__uint_as_float(r[0]), __uint_as_float(r[1]));
    auto q = __builtin_amdgcn_permlane16_swap(__float_as_uint(v), __float_as_uint(v), false, false);
    return fmx(__uint_as_float(q[0]), __uint_as_float(q[1]));
}
__device__ __forceinline__ float xsum3216(float v) {
    auto r = __builtin_amdgcn_permlane32_swap(__float_as_uint(v), __float_as_uint(v), false, false);
    v = __uint_as_float(r[0]) + __uint_as_float(r[1]);
    auto q = __builtin_amdgcn_permlane16_swap(__float_as_uint(v), __float_as_uint(v), false, false);
    return __uint_as_float(q[0]) + __uint_as_float(q[1]);
}
template <int CTRL> __device__ __forceinline__ float dppf(float v) { return __uint_as_float((unsigned)__builtin_amdgcn_update_dpp(0, (int)__float_as_uint(v), CTRL, 0xF, 0xF, false)); }
__device__ __forceinline__ float wsum(float v) {
    v += dppf<0xB1>(v); v += dppf<0x4E>(v); v += dppf<0x141>(v); v += dppf<0x140>(v);
    return xsum3216(v);
}
__device__ __forceinline__ unsigned ror8(unsigned v) { return (unsigned)__builtin_amdgcn_update_dpp(0, (int)v, 0x128, 0xF, 0xF, false); }
__device__ __forceinline__ float ror8f(float v) { return __uint_as_float(ror8(__float_as_uint(v))); }
__device__ __forceinline__ unsigned nosent(unsigned v) {
    u16x2 lim = {0xFFFE, 0xFFFE};
    return __builtin_bit_cast(unsigned, __builtin_elementwise_min(__builtin_bit_cast(u16x2, v), lim));
}
__device__ __forceinline__ unsigned pkmax(unsigned a, unsigned b) {
    return __builtin_bit_cast(unsigned, __builtin_elementwise_max(__builtin_bit_cast(u16x2, a), __builtin_bit_cast(u16x2, b)));
}
__device__ __forceinline__ bool has_sent(u32x4 v) {
    unsigned m = pkmax(pkmax(v.x, v.y), pkmax(v.z, v.w));
    return (m & 0xFFFFu) == 0xFFFFu || (m >> 16) == 0xFFFFu;
}

typedef f32x2 c32;
__device__ __forceinline__ c32 fma2(c32 a, c32 b, c32 c) { return __builtin_elementwise_fma(a, b, c); }
__device__ __forceinline__ c32 dup(float v) { return (c32){v, v}; }
template <int SG> __device__ __forceinline__ c32 addi(c32 a, c32 b) {
    c32 d;
    if (SG > 0) asm("v_pk_add_f32 %0, %1, %2 op_sel:[0,1] op_sel_hi:[1,0] neg_lo:[0,1]" : "=v"(d) : "v"(a), "v"(b));
    else asm("v_pk_add_f32 %0, %1, %2 op_sel:[0,1] op_sel_hi:[1,0] neg_hi:[0,1]" : "=v"(d) : "v"(a), "v"(b));
    return d;
}
template <int SG> __device__ __forceinline__ c32 ctw(c32 a, c32 w) {
    c32 d = a * w.xx;
    if (SG < 0) asm("v_pk_fma_f32 %0, %1, %2, %0 op_sel:[1,1,0] op_sel_hi:[0,1,1] neg_lo:[0,1,0]" : "+v"(d) : "v"(a), "v"(w));
    else asm("v_pk_fma_f32 %0, %1, %2, %0 op_sel:[1,1,0] op_sel_hi:[0,1,1] neg_hi:[0,1,0]" : "+v"(d) : "v"(a), "v"(w));
    return d;
}
__device__ __forceinline__ c32 cis(float rev) { return (c32){__builtin_amdgcn_cosf(rev), __builtin_amdgcn_sinf(rev)}; }
template <int N> __device__ __forceinline__ void cpow(c32 (&t)[N], float rev) {
    t[0] = cis(rev);
    asm volatile("s_nop 0" : "+v"(t[0]));
#pragma unroll
    for (int i = 1; i < N; ++i) { t[i] = ctw<-1>(t[(i - 1) / 2], t[i / 2]); asm volatile("" : "+v"(t[i])); }
}
#define SB() __builtin_amdgcn_sched_barrier(0)
template <int SG, int I0, int N, class F> __device__ __forceinline__ void ctwn(c32 (&u)[N], F w) {
    c32 d[N];
#pragma unroll
    for (int i = I0; i < N; ++i) asm("v_pk_mul_f32 %0, %1, %2 op_sel_hi:[1,0]" : "=v"(d[i]) : "v"(u[i]), "v"(w(i)));
    SB();
#pragma unroll
    for (int i = I0; i < N; ++i) {
        if (SG < 0) asm("v_pk_fma_f32 %0, %1, %2, %0 op_sel:[1,1,0] op_sel_hi:[0,1,1] neg_lo:[0,1,0]" : "+v"(d[i]) : "v"(u[i]), "v"(w(i)));
        else asm("v_pk_fma_f32 %0, %1, %2, %0 op_sel:[1,1,0] op_sel_hi:[0,1,1] neg_hi:[0,1,0]" : "+v"(d[i]) : "v"(u[i]), "v"(w(i)));
        u[i] = d[i];
    }
    SB();
}
template <int SG, int M, int A, int B, int CI, int N> __device__ __forceinline__ void dft4n(c32 (&u)[N]) {
    c32 t[M][4];
#pragma unroll
    for (int i = 0; i < M; ++i) {
        const c32 a = u[A * i], b = u[A * i + B], c = u[A * i + 2 * B], d = u[A * i + 3 * B];
        t[i][0] = i == CI ? addi<SG>(a, c) : a + c; t[i][1] = i == CI ? addi<-SG>(a, c) : a - c; t[i][2] = b + d; t[i][3] = b - d;
        if (B == 4) SB();
    }
    SB();
#pragma unroll
    for (int i = 0; i < M; ++i) { u[A * i] = t[i][0] + t[i][2]; u[A * i + 2 * B] = t[i][0] - t[i][2]; u[A * i + B] = addi<SG>(t[i][1], t[i][3]); u[A * i + 3 * B] = addi<-SG>(t[i][1], t[i][3]); }
    SB();
}
template <int SG> __device__ __forceinline__ void dft8x2(c32 (&u)[16]) {
    dft4n<SG, 4, 1, 4, -1>(u);
    const float r = 0.70710678118654752440f;
    c32 o[16];
#pragma unroll
    for (int e = 0; e < 2; ++e) { u[6 + e] = addi<SG>(u[6 + e], u[6 + e]); u[14 + e] = addi<-SG>(u[14 + e], u[14 + e]); }
#pragma unroll
    for (int e = 0; e < 2; ++e) { o[e] = u[e] + u[2 + e]; o[8 + e] = u[e] - u[2 + e]; o[4 + e] = addi<SG>(u[8 + e], u[10 + e]); o[12 + e] = addi<-SG>(u[8 + e], u[10 + e]); }
    SB();
#pragma unroll
    for (int e = 0; e < 2; ++e) { o[2 + e] = fma2(u[6 + e], dup(r), u[4 + e]); o[10 + e] = fma2(u[6 + e], dup(-r), u[4 + e]); o[6 + e] = fma2(u[14 + e], dup(-r), u[12 + e]); o[14 + e] = fma2(u[14 + e], dup(r), u[12 + e]); }
    SB();
#pragma unroll
    for (int i = 0; i < 16; ++i) u[i] = o[i];
}
template <int SG> __device__ __forceinline__ void dft16(c32 (&u)[16]) {
    const float c1 = 0.92387953251128675613f, s1 = 0.38268343236508977173f, r = 0.70710678118654752440f;
    dft4n<SG, 4, 1, 4, -1>(u);
    const int ix[8] = {5, 9, 13, 6, 14, 7, 11, 15};
    const float wx[8] = {c1, r, s1, r, -r, s1, -r, -c1}, wy[8] = {s1, r, c1, r, r, c1, r, -s1};
    c32 d[8];
#pragma unroll
    for (int i = 0; i < 8; ++i) d[i] = u[ix[i]] * dup(wx[i]);
    SB();
#pragma unroll
    for (int i = 0; i < 8; ++i) u[ix[i]] = fma2(u[ix[i]].yx, (c32){-SG * wy[i], SG * wy[i]}, d[i]);
    SB();
    dft4n<SG, 4, 4, 1, 2>(u);
    c32 o[16];
#pragma unroll
    for (int i = 0; i < 16; ++i) o[(i >> 2) + 4 * (i & 3)] = u[i];
#pragma unroll
    for (int i = 0; i < 16; ++i) u[i] = o[i];
}
template <int SG> __device__ __forceinline__ void dft10(c32 (&u)[10]) {
    const float c1 = 0.30901699437494742410f, c2 = -0.80901699437494742410f, s1 = 0.95105651629515357212f, s2 = 0.58778525229247312917f;
    c32 x[2][5], t[2][4], a[2][5];
#pragma unroll
    for (int i = 0; i < 5; ++i) { x[0][i] = u[2 * i] + u[(2 * i + 5) % 10]; x[1][i] = u[2 * i] - u[(2 * i + 5) % 10]; }
    SB();
#pragma unroll
    for (int e = 0; e < 2; ++e) { t[e][0] = x[e][1] + x[e][4]; t[e][1] = x[e][2] + x[e][3]; t[e][2] = x[e][1] - x[e][4]; t[e][3] = x[e][2] - x[e][3]; }
    SB();
#pragma unroll
    for (int e = 0; e < 2; ++e) { a[e][0] = t[e][0] + t[e][1]; a[e][1] = fma2(t[e][0], dup(c1), x[e][0]); a[e][2] = fma2(t[e][0], dup(c2), x[e][0]); a[e][3] = t[e][2] * dup(s1); a[e][4] = t[e][2] * dup(s2); }
    SB();
#pragma unroll
    for (int e = 0; e < 2; ++e) { a[e][0] += x[e][0]; a[e][1] = fma2(t[e][1], dup(c2), a[e][1]); a[e][2] = fma2(t[e][1], dup(c1), a[e][2]); a[e][3] = fma2(t[e][3], dup(s2), a[e][3]); a[e][4] = fma2(t[e][3], dup(-s1), a[e][4]); }
    SB();
#pragma unroll
    for (int e = 0; e < 2; ++e) {
        u[5 * e] = a[e][0];
        u[(6 + 5 * e) % 10] = addi<SG>(a[e][1], a[e][3]); u[(24 + 5 * e) % 10] = addi<-SG>(a[e][1], a[e][3]);
        u[(12 + 5 * e) % 10] = addi<SG>(a[e][2], a[e][4]); u[(18 + 5 * e) % 10] = addi<-SG>(a[e][2], a[e][4]);
    }
    SB();
}
template <int ST> __device__ __forceinline__ void lds16(c32 (&u)[16], const char* p) {
    const LAS char* q = (const LAS char*)p;
#pragma unroll
    for (int i = 0; i < 16; ++i) asm volatile("ds_read_b64 %0, %1 offset:%2" : "=v"(u[(i >> 2) + 4 * (i & 3)]) : "v"(q), "n"(((i >> 2) + 4 * (i & 3)) * ST) : "memory");
#pragma unroll
    for (int i = 0; i < 4; ++i) asm volatile("s_waitcnt lgkmcnt(%4)" : "+v"(u[i]), "+v"(u[i + 4]), "+v"(u[i + 8]), "+v"(u[i + 12]) : "n"(12 - 4 * i) : "memory");
}
__device__ __forceinline__ c32 unpack2(unsigned v) { return (c32){lo2f(v), hi2f(v)}; }
__device__ __forceinline__ float wsum2(c32 v) {
    float t = addswap32(v.x, v.y);
    auto q = __builtin_amdgcn_permlane16_swap(__float_as_uint(t), __float_as_uint(t), false, false);
    t = __uint_as_float(q[0]) + __uint_as_float(q[1]);
    t += dppf<0xB1>(t); t += dppf<0x4E>(t); t += dppf<0x141>(t); t += dppf<0x140>(t);
    return t;
}
__device__ __forceinline__ c32 halves(float t) { return (c32){__uint_as_float(__builtin_amdgcn_readlane(__float_as_uint(t), 0)), __uint_as_float(__builtin_amdgcn_readlane(__float_as_uint(t), 32))}; }
__device__ __forceinline__ void ln_rows(const c32 (&v)[10], unsigned (&o)[10]) {
    const float mean = wsum2(((v[0] + v[1]) + (v[2] + v[3])) + ((v[4] + v[5]) + (v[6] + v[7])) + (v[8] + v[9])) * (1.f / 640.f);
    const c32 m = halves(mean), mb = halves(bfr(mean));
    c32 d[10], e[10];
#pragma unroll
    for (int r = 0; r < 10; ++r) d[r] = v[r] - m;
    SB();
#pragma unroll
    for (int r = 0; r < 5; ++r) e[r] = d[r] * d[r];
#pragma unroll
    for (int r = 0; r < 5; ++r) d[r] = fma2(d[r + 5], d[r + 5], e[r]);
    SB();
#pragma unroll
    for (int r = 0; r < 10; ++r) { const c32 y = v[r] - mb; e[r] = unpack2(pack2(y.x, y.y)); }
    const float var = wsum2((d[0] + d[1]) + (d[2] + d[3]) + d[4]) * (1.f / 640.f);
    const c32 rs = halves(__builtin_amdgcn_rcpf(bfr(__builtin_amdgcn_sqrtf(bfr(bfr(var) + 1e-5f)))));
    SB();
#pragma unroll
    for (int r = 0; r < 10; ++r) e[r] *= rs;
    SB();
#pragma unroll
    for (int r = 0; r < 10; ++r) o[r] = pack2(e[r].x, e[r].y);
    SB();
}

__device__ __forceinline__ __amdgpu_buffer_rsrc_t mkrsrc(const void* p, unsigned bytes) {
    return __builtin_amdgcn_make_buffer_rsrc((void*)p, 0, bytes, 0x00020000);
}

constexpr unsigned O_F = (unsigned)OFF_F, O_Y0 = (unsigned)OFF_Y0;
__device__ __forceinline__ unsigned nosent1(unsigned v) { return min(v, 0xFFFFFFFEu); }
__device__ __forceinline__ u32x4 nosent4(u32x4 v) { v[1] = nosent1(v[1]); v[3] = nosent1(v[3]); return v; }
__device__ __forceinline__ unsigned smax(u32x4 v) { return max(v[1], v[3]); }
constexpr unsigned SPIN_CAP = 400000u;
constexpr int RW = 5920;
struct RowTw { c32 a[9], b, c; };
__device__ __forceinline__ RowTw row_tw(int lane) {
    RowTw t;
    t.b = cis((float)(lane & 31) * (-1.f / 64.f)); t.c = cis((float)(lane & 15) * (-1.f / 32.f));
    asm volatile("" : "+v"(t.b), "+v"(t.c));
    cpow(t.a, (float)lane * (-1.f / 640.f));
    return t;
}
template <int B> __device__ __forceinline__ void cswap(c32& x, c32& y) {
#pragma unroll
    for (int e = 0; e < 2; ++e) {
        auto r = B == 32 ? __builtin_amdgcn_permlane32_swap(__float_as_uint(x[e]), __float_as_uint(y[e]), false, false) : __builtin_amdgcn_permlane16_swap(__float_as_uint(x[e]), __float_as_uint(y[e]), false, false);
        x[e] = __uint_as_float(r[0]); y[e] = __uint_as_float(r[1]);
    }
}
template <int B> __device__ __forceinline__ void cswap5(c32 (&p)[5], c32 (&q)[5]) {
#pragma unroll
    for (int i = 0; i < 5; ++i) cswap<B>(p[i], q[i]);
    SB();
}
__device__ __forceinline__ void bfly5(c32 (&p)[5], c32 (&q)[5]) {
#pragma unroll
    for (int i = 0; i < 5; ++i) { const c32 t = p[i] - q[i]; p[i] += q[i]; q[i] = t; }
    SB();
}
__device__ __forceinline__ void row_x(u16 (&v)[20], __amdgpu_buffer_rsrc_t rx, int row, int lane) {
#pragma unroll
    for (int r = 0; r < 20; ++r) v[r] = __builtin_amdgcn_raw_buffer_load_b16(rx, (lane & 63) * 2u + 128u * (r >> 1) + 1280u * (r & 1), (unsigned)row * (D_ * 2), 16);
    __builtin_amdgcn_sched_barrier(0);
}
template <int NW, int NT, class FS> __device__ __forceinline__ void a1_unit(__amdgpu_buffer_rsrc_t rx, int row0, bool tail, __amdgpu_buffer_rsrc_t rws, char* sm, const RowTw& tw, int w, int lane, FS st_) {
    const unsigned fo = tail ? O_Y0 + 16384u + (unsigned)(row0 & 255) * 32u : O_F + (unsigned)row0 * 32u, gs = tail ? YS : GF;
    if (w < NW) {
        c32 u[10];
        unsigned h[10];
        u16 x[20];
        row_x(x, rx, row0 + 2 * w, lane);
        if (tail) for (unsigned spins = 0; spins < SPIN_CAP; ++spins) {
            unsigned m = 0;
#pragma unroll
            for (int r = 0; r < 20; ++r) m = max(m, (unsigned)x[r]);
            if (!__any(m == 0xFFFFu)) break;
            asm volatile("" ::: "memory");
            row_x(x, rx, row0 + 2 * w, lane);
        }
        st_(-1);
#pragma unroll
        for (int r = 0; r < 10; ++r) u[r] = (c32){bf2f(x[2 * r]), bf2f(x[2 * r + 1])};
        st_(0);
        ln_rows(u, h);
        st_(1);
#pragma unroll
        for (int r = 0; r < 10; ++r) u[r] = unpack2(h[r]);
        dft10<-1>(u);
        char* mine = sm + w * RW;
        char* ex = mine + ((lane >> 5) + 10 * ((lane >> 4) & 1)) * 144 + (lane & 15) * 8;
        ctwn<-1, 1>(u, [&](int i) { return tw.a[i - 1]; });
        c32 p[5], q[5];
#pragma unroll
        for (int i = 0; i < 5; ++i) { p[i] = u[2 * i]; q[i] = u[2 * i + 1]; }
        cswap5<32>(p, q); bfly5(p, q); ctwn<-1, 0>(q, [&](int) { return tw.b; });
        cswap5<16>(p, q); bfly5(p, q);
#pragma unroll
        for (int i = 0; i < 5; ++i) *(c32*)(ex + i * 288) = p[i];
        ctwn<-1, 0>(q, [&](int) { return tw.c; });
#pragma unroll
        for (int i = 0; i < 5; ++i) *(c32*)(ex + i * 288 + 2880) = q[i];
        asm volatile("" ::: "memory");
        st_(2);
        if (lane < 40) {
            c32 v[16];
#pragma unroll
            for (int m = 0; m < 8; ++m) { const f32x4 t = *(const f32x4*)(mine + lane * 144 + m * 16); v[2 * m] = t.xy; v[2 * m + 1] = t.zw; }
            dft16<-1>(v);
            asm volatile("" ::: "memory");
#pragma unroll
            for (int m = 0; m < 16; ++m) *(c32*)(mine + lane * 8 + m * 320) = v[m];
        }
        st_(3);
    }
    asm volatile("s_waitcnt lgkmcnt(0)\n\ts_barrier" ::: "memory");
    st_(4);
    const int tid = w * 64 + lane, k0 = tid / (4 * NW) * 4 + (tid & 3), id = tid % (4 * NW);
    const char* X = sm + (id >> 2) * RW;
#pragma unroll
    for (int c = 0; c * NT < 320 * NW; ++c) {
        const int k = k0 + c * (NT / NW);
        if ((c + 1) * NT <= 320 * NW || k < 320) {
            const c32 zk = *(const c32*)(X + k * 8), zn = *(const c32*)(X + (k == 0 ? 0 : 640 - k) * 8);
            const f32x4 o = {0.5f * (zk.x + zn.x), 0.5f * (zk.y - zn.y), 0.5f * (zk.y + zn.y), 0.5f * (zn.x - zk.x)};
            st128(nosent4(__builtin_bit_cast(u32x4, o)), rws, (unsigned)(k >> 2) * gs + id * 16u, fo, 16);
        }
    }
    if (k0 < 4) {
        const c32 z = *(const c32*)(X + 2560);
        st128(((u32x4){__float_as_uint(z.x), 0u, __float_as_uint(z.y), 0u}), rws, 80u * gs + id * 16u, fo, 16);
    }
}
template <int NW, class FS, class FD> __device__ __forceinline__ void a3_units(__amdgpu_buffer_rsrc_t rws, __amdgpu_buffer_rsrc_t rx, int row0, int step, int lay, char* sm, const RowTw& tw, int w, int lane, FS st_, FD done) {
    constexpr int NT = 512, NC = (320 * NW + NT - 1) / NT, GS = NW == 4 ? YS : GF;
    const int tid = w * 64 + lane, k0 = tid / (4 * NW) * 4 + (tid & 3), id = tid % (4 * NW);
    u32x4 fv[NC], fz = {0u, 0u, 0u, 0u};
    u16 x[20];
    auto get = [&](int row) {
        const unsigned fo = (NW == 4 ? O_Y0 + lay * 8192u : O_F) + (unsigned)row * 32u;
#pragma unroll
        for (int c = 0; c < NC; ++c) fv[c] = __builtin_amdgcn_raw_buffer_load_b128(rws, (unsigned)((min(k0 + c * (NT / NW), 319) >> 2) * GS + id * 16), fo, 16);
        if (w == 0) fz = __builtin_amdgcn_raw_buffer_load_b128(rws, (unsigned)(80 * GS + id * 16), fo, 16);
        __builtin_amdgcn_sched_barrier(0);
    };
    if (NW == 4 && w < NW) row_x(x, rx, row0 + 2 * w, lane);
    st_(-1);
    for (unsigned spins = 0; spins < SPIN_CAP; ++spins) {
        asm volatile("" ::: "memory");
        get(row0);
        if (NW == 8 || !__any(max(max(smax(fv[0]), smax(fv[1])), max(smax(fv[2]), smax(fz))) == 0xFFFFFFFFu)) break;
    }
    for (int n = 0;; ++n, row0 += step, sm += n & 1 ? 8 * RW : -8 * RW) {
    char* X = sm + (id >> 2) * RW;
#pragma unroll
    for (int c = 0; c < NC; ++c) {
        const int k = k0 + c * (NT / NW);
        const f32x4 v = __builtin_bit_cast(f32x4, fv[c]);
        c32 zk = addi<1>(v.xy, v.zw), zn = (c32){v[0] + v[3], v[2] - v[1]};
        if (c == 0 && k == 0) { zk = (c32){v[0], v[2]}; zn = (c32){__uint_as_float(fz[0]), __uint_as_float(fz[2])}; }
        if ((c + 1) * NT <= 320 * NW || k < 320) { *(c32*)(X + k * 8) = zk; *(c32*)(X + (k == 0 ? 320 : 640 - k) * 8) = zn; }
    }
    asm volatile("s_waitcnt vmcnt(0) lgkmcnt(0)\n\ts_barrier" ::: "memory");
    st_(0);
    if (NW == 8) {
        if (n) done(row0 - step);
        row_x(x, rx, row0 + 2 * w, lane);
        if (row0 + step < S_) get(row0 + step);
    }
    if (w < NW) {
        char* mine = sm + w * RW;
        if (lane < 40) {
            c32 v[16];
            lds16<320>(v, mine + lane * 8);
            dft16<1>(v);
            asm volatile("" ::: "memory");
#pragma unroll
            for (int m = 0; m < 8; ++m) *(f32x4*)(mine + lane * 144 + m * 16) = (f32x4){v[2 * m].x, v[2 * m].y, v[2 * m + 1].x, v[2 * m + 1].y};
        }
        asm volatile("" ::: "memory");
        st_(1);
        const char* ex = mine + ((lane >> 5) + 10 * ((lane >> 4) & 1)) * 144 + (lane & 15) * 8;
        c32 u[10];
        c32 p[5], q[5];
#pragma unroll
        for (int i = 0; i < 5; ++i) q[i] = *(const c32*)(ex + i * 288 + 2880);
#pragma unroll
        for (int i = 0; i < 5; ++i) p[i] = *(const c32*)(ex + i * 288);
        SB();
        ctwn<1, 0>(q, [&](int) { return tw.c; }); bfly5(p, q); cswap5<16>(p, q);
        ctwn<1, 0>(q, [&](int) { return tw.b; }); bfly5(p, q); cswap5<32>(p, q);
#pragma unroll
        for (int i = 0; i < 5; ++i) { u[2 * i] = p[i]; u[2 * i + 1] = q[i]; }
        ctwn<1, 1>(u, [&](int i) { return tw.a[i - 1]; });
        dft10<1>(u);
        st_(2);
        asm volatile("" ::: "memory");
        char* st = mine + lane * 2;
        unsigned h[10];
#pragma unroll
        for (int r = 0; r < 10; ++r) {
            const c32 y = (c32){bf2f(x[2 * r]), bf2f(x[2 * r + 1])} + unpack2(pack2(u[r].x, u[r].y));
            const unsigned p = pack2(y.x, y.y);
            u[r] = unpack2(p);
            *(u16*)(st + r * 128) = (u16)p; *(u16*)(st + 1280 + r * 128) = (u16)(p >> 16);
        }
        st_(3);
        ln_rows(u, h);
        st_(4);
#pragma unroll
        for (int r = 0; r < 10; ++r) { *(u16*)(st + 2560 + r * 128) = (u16)h[r]; *(u16*)(st + 3840 + r * 128) = (u16)(h[r] >> 16); }
        asm volatile("" ::: "memory");
        u32x4 o[6];
#pragma unroll
        for (int c = 0; c < 6; ++c) o[c] = *(const u32x4*)(mine + (c < 3 ? 2560 : 0) + min(c % 3 * 64 + lane, 159) * 16);
#pragma unroll
        for (int c = 0; c < 3; ++c) {
            const int i = min(c * 64 + lane, 159), rr = i >= 80, ch = i - 80 * rr, row = (row0 + 2 * w + rr) & 31;
            st128(nosent4(o[c]), rws, (unsigned)((ch >> 3) * 4096 + row * 128 + (((ch & 7) ^ (row & 7)) << 4)) | (c == 2 ? (lane & 32u) << 25 : 0u), (unsigned)OFF_H + (unsigned)((row0 >> 5) + 4 - 4 * lay) * 40960u, 16);
        }
#pragma unroll
        for (int c = 0; c < 3; ++c) st128(o[3 + c], rws, (unsigned)(c * 64 + lane) * 16u | (c == 2 ? (lane & 32u) << 25 : 0u), (unsigned)OFF_X1 + (unsigned)(row0 + 2 * w) * (D_ * 2), 16);
    }
    if (NW == 4 || row0 + step >= S_) break;
    }
    st_(5);
    asm volatile("s_waitcnt vmcnt(0)\n\ts_barrier" ::: "memory");
    done(row0);
}

__device__ __forceinline__ unsigned xcc_id() { unsigned x; asm volatile("s_getreg_b32 %0, hwreg(HW_REG_XCC_ID)" : "=s"(x)); return x & 7u; }
__device__ __forceinline__ unsigned probe(__amdgpu_buffer_rsrc_t rw, unsigned base) {
    unsigned mn[4] = {~0u, ~0u, ~0u, ~0u};
#pragma unroll 1
    for (int r = 0; r < 2; ++r) {
#pragma unroll
        for (int i = 0; i < 4; ++i) {
            const unsigned long long t1 = __builtin_amdgcn_s_memtime();
            asm volatile("s_waitcnt lgkmcnt(0)" ::: "memory");
            unsigned v = __builtin_amdgcn_raw_buffer_load_b32(rw, i * HALF, base + r * 4096u, 16);
            asm volatile("s_waitcnt vmcnt(0)" : "+v"(v) :: "memory");
            const unsigned long long t2 = __builtin_amdgcn_s_memtime();
            mn[i] = min(mn[i], (unsigned)(t2 - t1) + v);
        }
    }
    return mn[1] + mn[3] < mn[0] + mn[2] ? (mn[3] < mn[1] ? 3u : 1u) : (mn[2] < mn[0] ? 2u : 0u);
}
__device__ __forceinline__ void filter_unit(const u16* wr, const u16* wi, int s0, __amdgpu_buffer_rsrc_t rws, u16* tf, int tid) {
    u32x4 v[6];
#pragma unroll
    for (int i = 0; i < 6; ++i) v[i] = ((const u32x4*)((i < 3 ? wr : wi) + (size_t)s0 * DF))[min(tid + 512 * (i % 3), 1283)];
    __builtin_amdgcn_sched_barrier(0);
#pragma unroll
    for (int i = 0; i < 6; ++i) ((u32x4*)tf)[min(tid + 512 * (i % 3), 1283) + i / 3 * 1284] = v[i];
    __syncthreads();
    const int k = tid >> 3, sq = (tid & 7) * 4;
    u16 t[6][8];
#pragma unroll
    for (int i = 0; i < 6; ++i)
#pragma unroll
        for (int j = 0; j < 8; ++j) t[i][j] = tf[((j & 1) * 32 + sq + (j >> 1)) * DF + k + 64 * i];
    __builtin_amdgcn_sched_barrier(0);
#pragma unroll
    for (int i = 0; i < 6; ++i) {
        u32x4 o;
#pragma unroll
        for (int j = 0; j < 4; ++j) o[j] = (unsigned)t[i][2 * j] | (unsigned)t[i][2 * j + 1] << 16;
        if (i < 5 || tid < 8) st128(o, rws, (unsigned)(k * S_ + sq) * 4u, (unsigned)OFF_WFT + (unsigned)(s0 * 4 + i * 64 * S_ * 4), 16);
    }
}

constexpr int A2W = 8720;
template <class FW, class FS> __device__ __forceinline__ void a2_unit(__amdgpu_buffer_rsrc_t rws, int G, char* sm, int w, int lane, bool late, FW rdy, FS st_) {
    const int c = lane & 3, j = lane >> 2, p = 16 * w + j;
    const unsigned fo = O_F + (unsigned)(G * GF + w * 1024);
    u32x4 x[8];
    unsigned wv[16];
    c32 t1[2][7], t2[15], wf[16];
    auto ldw = [&]() {
#pragma unroll
        for (int r = 0; r < 16; ++r) wv[r] = __builtin_amdgcn_raw_buffer_load_b32(rws, (unsigned)(j * 32 + c * 4 + (r & 7) * 512), (unsigned)OFF_WFT + (unsigned)(min(4 * G + (w >> 1), 320) * 8192 + (w & 1) * 16 + (r >> 3) * 4096), 16);
        __builtin_amdgcn_sched_barrier(0);
    };
    auto cvw = [&]() {
#pragma unroll
        for (int m = 0; m < 16; ++m) { wf[m] = unpack2(wv[m]) * dup(1.f / ((float)S_ * (float)D_)); asm volatile("" : "+v"(wf[m])); }
    };
#pragma unroll
    for (int m = 0; m < 16; ++m) wv[m] = 0u;
    rdy(5);
    cpow(t1[0], (float)p * (-1.f / 1024.f)); cpow(t1[1], (float)(2 * p + 1) * (-1.f / 2048.f)); cpow(t2, (float)j * (-1.f / 256.f));
    rdy(6);
#pragma unroll
    for (int r = 0; r < 7; ++r) x[r] = __builtin_amdgcn_raw_buffer_load_b128(rws, lane * 16u, fo + r * 8192u, 16);
    if (!late) ldw();
    rdy(7);
    const unsigned yo = O_Y0 + (unsigned)(G * YS + w * 1024);
    x[7] = __builtin_amdgcn_raw_buffer_load_b128(rws, lane * 16u, late ? fo + 7 * 8192u : yo + 16384u, 16);
    __builtin_amdgcn_sched_barrier(0);
    cvw();
    for (unsigned spins = 0; spins < SPIN_CAP && __any(smax(x[7]) == 0xFFFFFFFFu); ++spins) {
        asm volatile("" ::: "memory");
        x[7] = __builtin_amdgcn_raw_buffer_load_b128(rws, lane * 16u, yo + 16384u, 16);
    }
    __builtin_amdgcn_sched_barrier(0);
    c32 u[16];
#pragma unroll
    for (int r = 0; r < 8; ++r) { const f32x4 v = __builtin_bit_cast(f32x4, x[r]); u[2 * r] = v.xy; u[2 * r + 1] = v.zw; }
    st_(0);
    dft8x2<-1>(u);
    ctwn<-1, 2>(u, [&](int i) { return t1[i & 1][(i >> 1) - 1]; });
    char* e1 = sm + 2 * c * A2W + p * 16;
    char* mine = sm + w * A2W;
#pragma unroll
    for (int q = 0; q < 8; ++q) *(f32x4*)(e1 + (q >> 2) * A2W + (q & 3) * 2112) = (f32x4){u[2 * q].x, u[2 * q].y, u[2 * q + 1].x, u[2 * q + 1].y};
    st_(1);
    rdy(8);
    asm volatile("s_waitcnt lgkmcnt(0)\n\ts_barrier" ::: "memory");
    st_(2);
    if (late) ldw();
    lds16<128>(u, mine + c * 2112 + j * 8);
    dft16<-1>(u);
    auto turn = [&](auto sg) {
        ctwn<decltype(sg)::v, 1>(u, [&](int i) { return t2[i - 1]; });
#pragma unroll
        for (int m = 0; m < 16; ++m) *(c32*)(mine + m * 544 + lane * 8) = u[m];
        lds16<32>(u, mine + j * 544 + c * 8);
    };
    turn(IC<-1>());
    st_(3);
    dft16<-1>(u);
    if (late) cvw();
    ctwn<-1, 0>(u, [&](int i) { return wf[i]; });
    dft16<1>(u);
    turn(IC<1>());
    st_(4);
    dft16<1>(u);
#pragma unroll
    for (int m = 0; m < 16; ++m) *(c32*)(mine + (w >> 1) * 32 + c * 2080 + j * 8 + m * 128) = u[m];
    st_(5);
    asm volatile("s_waitcnt lgkmcnt(0)\n\ts_barrier" ::: "memory");
    st_(6);
#pragma unroll
    for (int q = 0; q < 8; ++q) {
        const f32x4 v = *(const f32x4*)(e1 + c * 32 + (q >> 2) * A2W + (q & 3) * 2080);
        u[2 * q] = v.xy; u[2 * q + 1] = v.zw;
    }
    SB();
    ctwn<1, 2>(u, [&](int i) { return t1[i & 1][(i >> 1) - 1]; });
    dft8x2<1>(u);
#pragma unroll
    for (int r = 0; r < 8; ++r) {
        const u32x4 o = __builtin_bit_cast(u32x4, (f32x4){u[2 * r].x, u[2 * r].y, u[2 * r + 1].x, u[2 * r + 1].y});
        st128(r ? o : nosent4(o), rws, lane * 16u, r ? fo + r * 8192u : late ? yo : yo + 8192u, 16);
        if (r == 0) st_(7);
    }
}

struct RecArgs {
    unsigned* ctl; char* win; const u16* mem0; const u16* xin; u16* out;
    const u16* W[6];
};
template <int I> __device__ __forceinline__ char* karg(int i = 0) {
    unsigned long long p;
    asm volatile("s_load_dwordx2 %0, %1, %2\n\ts_waitcnt lgkmcnt(0)" : "=s"(p) : "s"(__builtin_amdgcn_kernarg_segment_ptr()), "s"(I * 8 + i * 8) : "memory");
    return (char*)p;
}
constexpr unsigned O_H = (unsigned)OFF_H, O_X1 = (unsigned)OFF_X1, O_Z = (unsigned)OFF_Z, O_Z2 = (unsigned)OFF_Z2, O_PT = (unsigned)OFF_PT, O_QK = (unsigned)OFF_QK;
#define MXV 0xAAAAAull
#define MXQ 0x88888000000ull
static_assert((MXV & 15) == 10 && !(MXQ & 15) && !((MXV | MXQ) >> 63), "segment sets");
constexpr int NREC = 96 + NOWN, NQT = NREC + NOWN, NXT = NQT + 32, NWRK = NXT + 32;
__device__ __forceinline__ bool spin_fail(unsigned& spins, __amdgpu_buffer_rsrc_t rws) {
    if (++spins > SPIN_CAP) { __builtin_amdgcn_raw_buffer_store_b32(1u, rws, 0u, 0u, 16); return true; }
    return (spins & 1023u) == 0u && __builtin_amdgcn_readfirstlane(__builtin_amdgcn_raw_buffer_load_b32(rws, 0u, 0u, 16));
}
#define STR2(x) #x
#define STR(x) STR2(x)
#define TPRE 0
#define QP 2
#define BQN 7
#define BQ(n) ".if " #n "==" STR(BQN) "\n\ts_barrier\n\t.endif\n\t"
#define SST(k) do {} while (0)
#define LSEL 1
#define RT(k) do {} while (0)
#define RTS() do {} while (0)
#define B3ATTR
#define MTA(k)
#define MTRT() do {} while (0)
#define MT(k) do {} while (0)
#define MTS() do {} while (0)
#define FC(x) do {} while (0)
#define DQC 1130
#define DVC 1200
#define M1 44
#define MZ 40
#define MZP 20
#define M2 224
#define LTP (LT0 + 15)
#define LTZ 340
#define PEN 280
#define KSL 1
#define SVO 64
#define DZP 46
#define LT0 330
#define SAD 1
#define PEN2 80
#define PEN1 50
#define PEN0 60
#define DDM2 0
#define DRA 700
#define TSLP 1
#define PFS 0
#define PFOFF 1
#define OPR 2
#define BSPARSE 0
#define ZLAG 7
#define TKP 0
#define DPS 12
#define RZ 63
#define RQ 63
#define R2 63
#define DXT 3000
#define SSW 99
#define LTM 12
#define XS0 10
#define XSU 8
#define XSM 32
#define LTU 4
#define LTD 1
#define QUIET 0
#define QUAD 1
#define LTS 3
#define XSS 3
#define TKS 1
#define LT1 358
#define FDL 2
#define ZLA 3
#define DPT2 3
#define NORND 0
#define LTX 24
#define TKE 200
#define DVO 0
#define ZPS 4
#define ASLP 4
#define BADG 0
#define DW1 0
#define WLA 4
#define XSLP 30
#define DOUT 170
#define DA1 145
#define DA2 100
#define DA20 120
#define DFB 150
#define DA3 170
#define DA30 230
#define YSLP 0
#define QEU 14
#define QE2 10
#define PEN00 180
#define DQ0 1020
#define DQ00 1350
#define DZ0 420
#define DZ00 430
#define DOU2 200
#define DFA 0
#define SRG 0x5A000000u
#define OBN 5
#define OB(n) ".if " #n "==" STR(OBN) "\n\ts_barrier\n\tds_read_b64 v[102:103], %[sp]\n\tds_read_b64 v[104:105], %[sp] offset:512\n\tds_read_b64 v[106:107], %[sp] offset:1024\n\t.endif\n\t"
#define VSLP 0
#define CPF 1
#define DST 11
#define XST 0
#define ODL 8
#define VTS 1
#define XF 7
#define XFS 32
#define DOU1 50
#define DR0 0
#define HLA 8
#define PSLP 16
#define PFD 4
#define NPF 3
#define KSL2 1
#define KBQ 4
#define VB1 6
#define VB2 5
#define KR1 3
#define QB2 2
#define NB2 6
#define DZR 24
#define ML 150
#define MN 12
#define CF 190
#define DB 1500
#define DP 200
#define ZSL 1
#define NB1 3
#define DPT 3
#define DDMA 8
#define DZ2 800
#define QEARLY 0
#define OSLP 12
#define OSLP2 4
#define WSLP 32
#define DO0 12000
#define DW0 1200
#define DQT 9000
#define DQ4 700
#define ZEARLY 1
#define HSLP 4
#define RMAP 0
#define DIE 0
#define VMWAIT() do { asm volatile("" ::: "memory"); __builtin_amdgcn_s_waitcnt(0x0F70); asm volatile("" ::: "memory"); } while (0)
#define BAR() asm volatile("s_waitcnt lgkmcnt(0)\n\ts_barrier" ::: "memory")
#define PIN1(a) asm volatile("" : "+v"(a) :: "memory")
#define PIN2(a, b) asm volatile("" : "+v"(a), "+v"(b) :: "memory")
#define BAR0() asm volatile("s_barrier" ::: "memory")
__device__ __forceinline__ unsigned fresh_lane() { unsigned l; asm volatile("v_mbcnt_lo_u32_b32 %0, -1, 0\n\tv_mbcnt_hi_u32_b32 %0, -1, %0" : "=v"(l)); __builtin_assume(l < 64u); return l; }
__device__ __forceinline__ void wait_until(unsigned long long t) { while ((long long)(__builtin_amdgcn_s_memtime() - t) < 0) __builtin_amdgcn_s_sleep(1); }
struct Clk { unsigned ta, ma, cpq, qpc; };
__device__ __forceinline__ int sx24(unsigned v) { return (int)(v << 8) >> 8; }
#define WAIT_FINE \
        "s_memrealtime vcc\n\t" \
        "s_memtime s[100:101]\n\t" \
        "s_brev_b32 %[fi], -2\n\t" \
        "s_waitcnt lgkmcnt(0)\n\t" \
        "s_mov_b32 %[rp], vcc_lo\n\t" \
        "s_lshl_b32 vcc_hi, vcc_lo, 2\n\t" \
        "s_sub_u32 %[x], %[t], vcc_hi\n\t" \
        "s_bfe_i32 %[x], %[x], 0x180000\n\t" \
        "s_min_i32 %[x], %[x], 4000\n\t" \
        "s_max_i32 %[x], %[x], -4000\n\t" \
        "s_add_u32 %[t], vcc_hi, %[x]\n\t" \
        "s_mul_i32 %[x], %[x], %[cq]\n\t" \
        "s_ashr_i32 %[x], %[x], 8\n\t" \
        "s_sub_u32 %[tm], %[t], %[ta]\n\t" \
        "s_bfe_i32 %[tm], %[tm], 0x180000\n\t" \
        "s_min_i32 %[tm], %[tm], 1000000\n\t" \
        "s_max_i32 %[tm], %[tm], -1000000\n\t" \
        "s_mul_i32 %[tm], %[tm], %[cq]\n\t" \
        "s_ashr_i32 %[tm], %[tm], 8\n\t" \
        "s_add_u32 %[tm], %[tm], %[ma]\n\t" \
        "s_sub_u32 %[tm], %[tm], s100\n\t" \
        "s_lshr_b32 vcc_hi, %[cq], 4\n\t" \
        "s_sub_u32 vcc_hi, %[x], vcc_hi\n\t" \
        "s_max_i32 %[tm], %[tm], vcc_hi\n\t" \
        "s_min_i32 %[tm], %[tm], %[x]\n\t" \
        "s_add_u32 %[tm], %[tm], s100\n" \
        "1:\n\t" \
        "s_memrealtime vcc\n\t" \
        "s_memtime s[100:101]\n\t" \
        "s_waitcnt lgkmcnt(0)\n\t" \
        "s_cmp_eq_u32 vcc_lo, %[rp]\n\t" \
        "s_cbranch_scc1 2f\n\t" \
        "s_mov_b32 %[rp], vcc_lo\n\t" \
        "s_lshl_b32 %[x], vcc_lo, 2\n\t" \
        "s_sub_u32 %[x], %[t], %[x]\n\t" \
        "s_bfe_i32 %[x], %[x], 0x180000\n\t" \
        "s_mul_i32 %[x], %[x], %[cq]\n\t" \
        "s_ashr_i32 %[x], %[x], 8\n\t" \
        "s_add_u32 %[x], %[x], s100\n\t" \
        "s_sub_u32 s101, %[x], %[tm]\n\t" \
        "s_cmp_lt_i32 s101, %[fi]\n\t" \
        "s_cselect_b32 %[tm], %[x], %[tm]\n\t" \
        "s_mov_b32 %[fi], 0\n" \
        "2:\n\t" \
        "s_sub_u32 %[x], %[tm], s100\n\t" \
        "s_cmp_gt_i32 %[x], 260\n\t" \
        "s_cbranch_scc1 1b\n\t" \
        "s_cmp_eq_u32 %[fi], 0\n\t" \
        "s_cselect_b32 %[ta], %[t], %[ta]\n\t" \
        "s_cselect_b32 %[ma], %[tm], %[ma]\n" \
        "3:\n\t" \
        "s_memtime s[100:101]\n\t" \
        "s_waitcnt lgkmcnt(0)\n\t" \
        "s_sub_u32 %[x], %[tm], s100\n\t" \
        "s_cmp_gt_i32 %[x], 92\n\t" \
        "s_cbranch_scc1 3b\n\t" \
        "s_max_i32 %[x], %[x], 0\n\t" \
        "s_andn2_b32 %[x], %[x], 3\n\t" \
        "s_sub_u32 %[x], 104, %[x]\n\t" \
        "s_getpc_b64 vcc\n\t" \
        "s_add_u32 vcc_lo, vcc_lo, %[x]\n\t" \
        "s_addc_u32 vcc_hi, vcc_hi, 0\n\t" \
        "s_setpc_b64 vcc\n\t" \
        ".rept 23\n\t" \
        "s_nop 0\n\t" \
        ".endr\n\t"
#define WAIT_FINE_OUT [tm] "=&s"(tm), [rp] "=&s"(rp), [x] "=&s"(x), [fi] "=&s"(fi), [t] "+s"(t), [ta] "+s"(c.ta), [ma] "+s"(c.ma)
__device__ __forceinline__ void wait_fine(unsigned t, Clk& c) {
    unsigned tm, rp, x, fi;
    asm volatile(WAIT_FINE : WAIT_FINE_OUT : [cq] "s"(c.cpq) : "vcc", "scc", "s100", "s101", "memory");
}
__device__ __forceinline__ void wait_fine_bar(unsigned t, Clk& c) {
    unsigned tm, rp, x, fi;
    asm volatile(WAIT_FINE "s_barrier" : WAIT_FINE_OUT : [cq] "s"(c.cpq) : "vcc", "scc", "s100", "s101", "memory");
}
__device__ __forceinline__ void wait_cyc_bar(unsigned tm) {
    unsigned x;
    asm volatile(
        "3:\n\t"
        "s_memtime s[100:101]\n\t"
        "s_waitcnt lgkmcnt(0)\n\t"
        "s_sub_u32 %0, %1, s100\n\t"
        "s_cmp_gt_i32 %0, 92\n\t"
        "s_cbranch_scc1 3b\n\t"
        "s_max_i32 %0, %0, 0\n\t"
        "s_andn2_b32 %0, %0, 3\n\t"
        "s_sub_u32 %0, 104, %0\n\t"
        "s_getpc_b64 vcc\n\t"
        "s_add_u32 vcc_lo, vcc_lo, %0\n\t"
        "s_addc_u32 vcc_hi, vcc_hi, 0\n\t"
        "s_setpc_b64 vcc\n\t"
        ".rept 23\n\t"
        "s_nop 0\n\t"
        ".endr\n\t"
        "s_barrier"
        : "=&s"(x) : "s"(tm) : "vcc", "scc", "s100", "s101", "memory");
}
__device__ __forceinline__ unsigned cyc_at(const Clk& c, int d) {
    d = min(d, 20000);
    asm volatile("" : "+s"(d));
    return c.ma + (unsigned)((max(d, -4000) * (int)c.cpq) >> 8);
}
__device__ __forceinline__ void clk_rate(Clk& c, unsigned ta0, unsigned ma0) {
    const int dq = sx24(c.ta - ta0);
    if (dq > 200) {
        const float r = fminf(fmaxf((float)(c.ma - ma0) * __builtin_amdgcn_rcpf((float)dq), 1.f), 8.f);
        asm volatile("s_nop 1\n\tv_readfirstlane_b32 %0, %2\n\tv_readfirstlane_b32 %1, %3\n\ts_nop 3" : "=s"(c.cpq), "=s"(c.qpc)
                     : "v"((unsigned)(256.f * r + 0.5f)), "v"((unsigned)(65536.f * __builtin_amdgcn_rcpf(r))));
    }
}
__device__ __forceinline__ void clk_init(Clk& c) {
    c.ta = 4u * (unsigned)__builtin_amdgcn_s_memrealtime(); c.ma = (unsigned)__builtin_amdgcn_s_memtime();
    for (int i = 0; i < 2; ++i) {
        const unsigned ta0 = c.ta, ma0 = c.ma;
        wait_fine(c.ta + (i ? 360u : 60u), c);
        clk_rate(c, ta0, ma0);
    }
}
__device__ __forceinline__ int read_sched(__amdgpu_buffer_rsrc_t rws, __amdgpu_buffer_rsrc_t rwin, int s, unsigned tg, unsigned vo, unsigned to, unsigned f, unsigned g0, int& lt, int& xs, unsigned& fc) {
    int d = (int)((f - g0) & 0xFFFFFFu);
    if (__builtin_expect(__any((f >> 24) != tg) || s == 0, 0)) {
        for (unsigned spins = 0; __any((f >> 24) != tg);) {
            FC(fc);
            if (spin_fail(spins, rws)) break;
            __builtin_amdgcn_s_sleep(TSLP);
            asm volatile("" ::: "memory");
            if ((f >> 24) != tg) f = __builtin_amdgcn_raw_buffer_load_b32(rwin, vo, to, 16);
        }
        const unsigned f0 = __builtin_amdgcn_readfirstlane(f) - 0x100000u;
        d = (int)((f - f0) & 0xFFFFFFu) + (int)((f0 - g0) & 0xFFFFFFu);
    }
    d = max(d, __builtin_amdgcn_update_dpp(0, d, 0xB1, 0xF, 0xF, true));
    d = max(d, __builtin_amdgcn_update_dpp(0, d, 0x4E, 0xF, 0xF, true));
    d = max(d, __builtin_amdgcn_update_dpp(0, d, 0x141, 0xF, 0xF, true));
    d = max(d, __builtin_amdgcn_update_dpp(0, d, 0x140, 0xF, 0xF, true));
    d = max(d, __builtin_amdgcn_update_dpp(0, d, 0x142, 0xA, 0xF, true));
    d = max(__builtin_amdgcn_readlane(d, 16), __builtin_amdgcn_readlane(d, 48));
    const int l0 = lt, x0 = xs;
    d -= 0x400000;
    const bool miss = d > l0 + min(x0 + LTM, LTX);
    if (s >= SAD) { lt = miss ? l0 : min(max(d, l0 - 1 - ((l0 - d) >> LTS)), l0 + LTU); xs = miss ? min(x0 + XSU, XSM) : max(x0 - 1 - (x0 >> XSS), s < XFS ? XF : 0); }
    if (s == 1) lt = LT0;
    return 0x400000 + (s < SAD ? d + (tg ? PEN0 : PEN00) : miss ? d + PEN : l0 + x0) + M1 + M2 + x0;
}
constexpr int QS = 272;
constexpr int TQS = 1152;
constexpr int L_Q = 10240, L_K = L_Q + 2 * TQS, L_S = L_K + 2 * TQS, L_TOK = L_S + 256, L_H = L_TOK + 49152, L_END = L_H + 2 * 40960;
typedef __attribute__((ext_vector_type(4))) short v4i16_t;
__device__ __forceinline__ u32x2 ldtr(const char* p) { return __builtin_bit_cast(u32x2, __builtin_amdgcn_ds_read_tr16_b64_v4i16((LAS v4i16_t*)p)); }
__device__ __forceinline__ u32x2 ldtr1(const LAS char* p) { return __builtin_bit_cast(u32x2, __builtin_amdgcn_ds_read_tr16_b64_v4i16((LAS v4i16_t*)p)); }
__device__ __forceinline__ u32x4 ldtr2(const LAS char* p0, const LAS char* p1) {
    const u32x2 x = __builtin_bit_cast(u32x2, __builtin_amdgcn_ds_read_tr16_b64_v4i16((LAS v4i16_t*)p0)), y = __builtin_bit_cast(u32x2, __builtin_amdgcn_ds_read_tr16_b64_v4i16((LAS v4i16_t*)p1));
    return (u32x4){x[0], x[1], y[0], y[1]};
}
__device__ __forceinline__ u32x4 ldtr2(const char* p0, const char* p1) { const u32x2 x = ldtr(p0), y = ldtr(p1); return (u32x4){x[0], x[1], y[0], y[1]}; }

__device__ __forceinline__ unsigned poll_lim(int& budget) {
    --budget;
    unsigned lim = budget > 0 ? 0xFFFFFFFEu : 0xFFFFFFFFu;
    asm volatile("" : "+s"(lim));
    return lim;
}
__device__ __forceinline__ bool mem_missing(unsigned lds, unsigned lim) {
    const LAS char* p = (const LAS char*)(size_t)(lds + fresh_lane() * 16u);
    unsigned m = 0;
#pragma unroll 1
    for (int h = 0; h < 2; ++h) {
        u32x4 v[5];
#pragma unroll
        for (int i = 0; i < 5; ++i) v[i] = *(const LAS u32x4*)(p + h * 5120 + i * 1024);
        SB();
#pragma unroll
        for (int i = 0; i < 5; ++i) m = max(max(m, max(v[i][0], v[i][1])), max(v[i][2], v[i][3]));
    }
    return __any(m > lim);
}
#define GATHER \
        "buffer_load_dwordx4 %[vo], %[rw], %[s0] offen sc1 lds\n\t" \
        "buffer_load_dwordx4 %[vo], %[rw], %[s0] offen offset:1024 sc1 lds\n\t" \
        "buffer_load_dwordx4 %[vo], %[rw], %[s0] offen offset:2048 sc1 lds\n\t" \
        "buffer_load_dwordx4 %[vo], %[rw], %[s0] offen offset:3072 sc1 lds\n\t" \
        "s_movk_i32 m0, 0x1000\n\t" \
        "s_nop 0\n\t" \
        "buffer_load_dwordx4 %[vo], %[rw], %[s4] offen sc1 lds\n\t" \
        "buffer_load_dwordx4 %[vo], %[rw], %[s4] offen offset:1024 sc1 lds\n\t" \
        "buffer_load_dwordx4 %[vo], %[rw], %[s4] offen offset:2048 sc1 lds\n\t" \
        "buffer_load_dwordx4 %[vo], %[rw], %[s4] offen offset:3072 sc1 lds\n\t" \
        "s_movk_i32 m0, 0x2000\n\t" \
        "s_nop 0\n\t" \
        "buffer_load_dwordx4 %[vo], %[rw], %[s1] offen sc1 lds\n\t" \
        "buffer_load_dwordx4 %[vo], %[rw], %[s1] offen offset:1024 sc1 lds"
#define GATHER_IN [vo] "v"(vo), [rw] "s"(rwin), [s0] "s"(so0), [s4] "s"(so0 + 4096u), [s1] "s"(so1)
__device__ __forceinline__ void gather_at(unsigned t, Clk& c, __amdgpu_buffer_rsrc_t rwin, unsigned vo, unsigned so0, unsigned so1) {
    unsigned tm, rp, x, fi;
    asm volatile("s_mov_b32 m0, 0\n\t" WAIT_FINE GATHER : WAIT_FINE_OUT : [cq] "s"(c.cpq), GATHER_IN : "vcc", "scc", "s100", "s101", "m0", "memory");
}
__device__ __forceinline__ void dma4k(__amdgpu_buffer_rsrc_t r, unsigned vo, unsigned so, unsigned lds) {
    asm volatile(
        "s_mov_b32 m0, %3\n\t"
        "s_nop 0\n\t"
        "buffer_load_dwordx4 %0, %1, %2 offen sc1 lds\n\t"
        "buffer_load_dwordx4 %0, %1, %2 offen offset:1024 sc1 lds\n\t"
        "buffer_load_dwordx4 %0, %1, %2 offen offset:2048 sc1 lds\n\t"
        "buffer_load_dwordx4 %0, %1, %2 offen offset:3072 sc1 lds"
        :: "v"(vo), "s"(r), "s"(so), "s"(lds) : "m0", "memory");
}
__device__ __forceinline__ int bs2(int c) { return (c & ~3) | ((c & 1) << 1) | ((c >> 1) & 1); }
__device__ __forceinline__ void get_mem0(u32x4 (&m)[2], int t) {
    const u16* mem0 = (const u16*)karg<2>();
#pragma unroll
    for (int i = 0; i < 2; ++i) {
        const int c = min(t + 512 * i, 639);
        const unsigned* src = (const unsigned*)(mem0 + ((c & 6) + ((c >> 3) & 1)) * D_ + (c >> 4) * 16 + (c & 1) * 2);
        m[i] = (u32x4){src[0], src[2], src[4], src[6]};
    }
    SB();
}
__device__ __forceinline__ void put_mem0(const u32x4 (&m)[2], char* memL, int t) { *(u32x4*)(memL + t * 16) = m[0]; *(u32x4*)(memL + min(t + 512, 639) * 16) = m[1]; }
__device__ __forceinline__ f32x2 hadd(f32x4 c) { return (f32x2){c[0], c[1]} + (f32x2){c[2], c[3]}; }
__device__ __forceinline__ unsigned hpack(f32x4 c) { const f32x2 y = hadd(c); return pack2(y[0], y[1]); }
__device__ __forceinline__ u32x8 cat8(u32x4 lo, u32x4 hi) { return (u32x8){lo[0], lo[1], lo[2], lo[3], hi[0], hi[1], hi[2], hi[3]}; }
__device__ __forceinline__ u32x4 half8(const u32x8& v, int u) { return u ? (u32x4){v[4], v[5], v[6], v[7]} : (u32x4){v[0], v[1], v[2], v[3]}; }
__device__ __forceinline__ void dma1k(__amdgpu_buffer_rsrc_t r, unsigned vo, unsigned so, unsigned lds) {
    asm volatile("s_mov_b32 m0, %3\n\ts_nop 0\n\tbuffer_load_dwordx4 %0, %1, %2 offen sc1 lds" :: "v"(vo), "s"(r), "s"(so), "s"(lds) : "m0", "memory");
}
template <int N, int ST> __device__ __forceinline__ void stage_h(__amdgpu_buffer_rsrc_t rws, unsigned so, unsigned lds, int lane) {
#pragma unroll
    for (int i = 0; i < N; ++i) dma1k(rws, lane * 16u, so + i * ST, lds + i * ST);
}
template <int N, int ST, int M> __device__ __forceinline__ void check_h(__amdgpu_buffer_rsrc_t rws, unsigned so, unsigned lds, int lane) {
    asm volatile("s_waitcnt vmcnt(%0)" :: "n"(M) : "memory");
    for (unsigned spins = 0; spins < SPIN_CAP; ++spins) {
        unsigned m = 0;
#pragma unroll
        for (int i = 0; i < N; ++i) m = max(m, smax(*(const LAS u32x4*)(size_t)(lds + lane * 16u + i * ST)));
        if (!__any(m == 0xFFFFFFFFu)) break;
        stage_h<N, ST>(rws, so, lds, lane);
        asm volatile("s_waitcnt vmcnt(0)" ::: "memory");
    }
}
__device__ __forceinline__ void wait_h(__amdgpu_buffer_rsrc_t rws, __amdgpu_buffer_rsrc_t rfl, unsigned fb, int seg, int& hrdy) {
    unsigned lim = hrdy ? 63u : (unsigned)min(seg + HLA, 63);
    if (__builtin_expect(hrdy <= seg, 0)) for (unsigned spins = 0;;) {
        asm volatile("" ::: "memory");
        const u32x4 f = __builtin_amdgcn_raw_buffer_load_b128(rfl, min(fresh_lane(), lim) * 128u, W_H + fb, 16);
        const unsigned long long miss = __ballot((f[0] | f[1] | f[2] | f[3]) != 0u);
        hrdy = miss ? __builtin_ctzll(miss) : (int)lim + 1;
        if (hrdy > seg) break;
        if (spin_fail(spins, rws)) break;
        lim = (unsigned)min(seg + HLA, 63);
        __builtin_amdgcn_s_sleep(HSLP);
    }
}
__device__ __forceinline__ u32x4 ask_h(__amdgpu_buffer_rsrc_t rfl, unsigned fb, unsigned lim) { return __builtin_amdgcn_raw_buffer_load_b128(rfl, min(fresh_lane(), lim) * 128u, W_H + fb, 16); }
__device__ __forceinline__ int see_h(u32x4 f) { const unsigned long long miss = __ballot((f[0] | f[1] | f[2] | f[3]) != 0u); return miss ? __builtin_ctzll(miss) : 64; }
template <int CTRL> __device__ __forceinline__ unsigned dppu(unsigned v) { return (unsigned)__builtin_amdgcn_mov_dpp((int)v, CTRL, 0xF, 0xF, true); }
template <int CTRL> __device__ __forceinline__ u32x4 ror4(u32x4 v) { return (u32x4){dppu<CTRL>(v[0]), dppu<CTRL>(v[1]), dppu<CTRL>(v[2]), dppu<CTRL>(v[3])}; }
__device__ __forceinline__ float xmax16(float v) {
    auto q = __builtin_amdgcn_permlane16_swap(__float_as_uint(v), __float_as_uint(v), false, false);
    return fmx(__uint_as_float(q[0]), __uint_as_float(q[1]));
}
__device__ __forceinline__ float xsum16(float v) {
    auto q = __builtin_amdgcn_permlane16_swap(__float_as_uint(v), __float_as_uint(v), false, false);
    return __uint_as_float(q[0]) + __uint_as_float(q[1]);
}
__device__ __forceinline__ float addswap16(float a, float b) {
    auto r = __builtin_amdgcn_permlane16_swap(__float_as_uint(a), __float_as_uint(b), false, false);
    return __uint_as_float(r[0]) + __uint_as_float(r[1]);
}
#define LANES() \
    const int lane = (int)fresh_lane(), tid = w * 64 + lane, fr = lane & 15, fq = lane >> 4; \
    const int sidx = (lane & 2) ? 0xEEEE : 0x4444; \
    const int ma0 = fq * 256 + (fr & 1) * 128 + (fr >> 1) * 16; \
    const int prow = 16 * ((fr >> 2) & 1) + 4 * (fr >> 3) + (fr & 3), hoff = fr * 128; \
    (void)tid; (void)sidx; (void)ma0; (void)prow; (void)hoff

__device__ __forceinline__ int seg_after(unsigned long long m, int j) { m = j >= 63 ? 0ull : m >> (j + 1); return m ? j + 1 + __builtin_ctzll(m) : NSEG; }
__device__ __forceinline__ int seg_done(unsigned long long m, int f0, int f1) { return min(seg_after(~m, f0), seg_after(m, f1)) - 1; }
template <int OFF> __device__ __forceinline__ void ldfrag(u32x4& d, const LAS char* p) { asm volatile("ds_read_b128 %0, %1 offset:%2" : "=v"(d) : "v"(p), "n"(OFF) : "memory"); }
template <int U, class FM, class FV> __device__ __forceinline__ void project(const u32x8 (&wt)[2][10], f32x4 (&tc)[2][2], u32x4 (&bb)[8], const LAS char* c0, const LAS char* c1, const LAS char* n0, const LAS char* n1, FM mid, FV vm) {
    constexpr int t = U >> 2, k = U & 3, v = (U + 8) % 40;
    if (U == 32) mid();
    vm(IC<U>());
    const LAS char* p = U < 32 ? (k >> 1 ? c1 : c0) : (k >> 1 ? n1 : n0);
    if (U < 2) asm volatile("s_nop 0\n\ts_waitcnt lgkmcnt(7)\n\tv_mfma_f32_16x16x32_bf16 %0, %3, %2, 0\n\tv_mfma_f32_16x16x32_bf16 %1, %4, %2, 0\n\tds_read_b128 %2, %5 offset:%6"
                            : "=&v"(tc[0][k & 1]), "=&v"(tc[1][k & 1]), "+v"(bb[U & 7]) : "v"(half8(wt[0][t], k >> 1)), "v"(half8(wt[1][t], k >> 1)), "v"(p), "n"((v >> 2) * 4096 + (k & 1) * 2048) : "memory");
    else asm volatile(".rept %7\n\ts_nop 0\n\t.endr\n\ts_waitcnt lgkmcnt(7)\n\tv_mfma_f32_16x16x32_bf16 %0, %3, %2, %0\n\tv_mfma_f32_16x16x32_bf16 %1, %4, %2, %1\n\tds_read_b128 %2, %5 offset:%6"
                      : "+v"(tc[0][k & 1]), "+v"(tc[1][k & 1]), "+v"(bb[U & 7]) : "v"(half8(wt[0][t], k >> 1)), "v"(half8(wt[1][t], k >> 1)), "v"(p), "n"((v >> 2) * 4096 + (k & 1) * 2048), "n"(U < 8 ? 1 : 0) : "memory");
    if constexpr (U < 39) project<U + 1>(wt, tc, bb, c0, c1, n0, n1, mid, vm);
    else asm volatile("s_nop 7" : "+v"(tc[0][0]), "+v"(tc[0][1]), "+v"(tc[1][0]), "+v"(tc[1][1]));
}
__device__ __forceinline__ void project_end(u32x4 (&bb)[8]) {
    asm volatile("s_waitcnt lgkmcnt(0)" : "+v"(bb[0]), "+v"(bb[1]), "+v"(bb[2]), "+v"(bb[3]), "+v"(bb[4]), "+v"(bb[5]), "+v"(bb[6]), "+v"(bb[7]) :: "memory");
}
__device__ __forceinline__ void project0(u32x4 (&bb)[8], const LAS char* c0, const LAS char* c1) {
    ldfrag<0>(bb[0], c0); ldfrag<2048>(bb[1], c0); ldfrag<0>(bb[2], c1); ldfrag<2048>(bb[3], c1);
    ldfrag<4096>(bb[4], c0); ldfrag<6144>(bb[5], c0); ldfrag<4096>(bb[6], c1); ldfrag<6144>(bb[7], c1);
    project_end(bb);
}
constexpr int WLB = 20992;
template <bool O, class F> __device__ __forceinline__ void load_w(u32x8 (&wt)[2][10], const char* W, int hh, int w, int c0, int cd, char* st, F gap) {
    const int lane = (int)fresh_lane(), fr = lane & 15, fq = lane >> 4, x = w & 3;
    const __amdgpu_buffer_rsrc_t r = mkrsrc(W, D_ * HD * 2);
    u32x4 raw[40];
    const unsigned vo = O ? (unsigned)(lane * 16) : (unsigned)(fq * 8192 + fr * 16);
    unsigned so = O ? (unsigned)(hh * 128 + 8 * x) * 1280u : (unsigned)(8 * x * 8192 + hh * 256);
    asm volatile("" : "+s"(so));
    auto fetch = [&](int i) {
        gap(-1);
        if (O) raw[i] = __builtin_amdgcn_raw_buffer_load_b128(r, vo + (i % 5 & 3) * 1024u, so + (unsigned)((32 * (i / 10) + 4 * (i / 5 & 1)) * 1280 + (i % 5 >> 2) * 4096), WAUX);
        else raw[i] = __builtin_amdgcn_raw_buffer_load_b128(r, vo, so + (unsigned)(32 * (i >> 1) + 4 * (i & 1)) * 8192u, WAUX);
    };
#pragma unroll
    for (int i = 0; i < 5 * WLA; ++i) { fetch(i); if (i % 5 == 4) { BAR0(); gap(i / 5); } }
    st += (w >> 2) * WLB;
    const int row = 4 * fq + (fr >> 2);
    int wo[5];
#pragma unroll
    for (int e = 0; e < 5; ++e) {
        const int id = 64 * e + lane, rr = id / 80, ch = id - 80 * rr;
        wo[e] = O ? (4 * x + rr) * 1312 + (ch >> 1) * 32 + (ch & 1) * 4 : (4 * x + fq) * 256 + ((fr ^ (2 * ((4 * x + fq) & 7))) * 16) + e * 4096;
    }
    const int r0 = O ? row * 1312 + x * 320 + (fr & 3) * 8 : row * 256 + (((c0 + ((fr >> 1) & 1)) ^ (2 * (row & 7))) * 16) + (fr & 1) * 8;
    const int r1 = O ? 0 : row * 256 + (((c0 + cd + ((fr >> 1) & 1)) ^ (2 * (row & 7))) * 16) + (fr & 1) * 8;
#pragma unroll
    for (int p = 0; p < 8; ++p) {
        char* b = st + (p & 1) * 2 * WLB;
#pragma unroll
        for (int e = 0; e < 5; ++e) {
            const u32x4 v = raw[5 * p + e];
            if (O) {
                unsigned* d = (unsigned*)(b + wo[e]);
                d[0] = __builtin_amdgcn_perm(v[2], v[0], 0x05040100); d[2] = __builtin_amdgcn_perm(v[3], v[1], 0x05040100);
                d[4] = __builtin_amdgcn_perm(v[2], v[0], 0x07060302); d[6] = __builtin_amdgcn_perm(v[3], v[1], 0x07060302);
            } else *(u32x4*)(b + wo[e]) = v;
        }
        BAR();
#pragma unroll
        for (int e = 0; e < 5; ++e) if (p + WLA < 8) fetch(5 * (p + WLA) + e);
        SB();
        gap(p + WLA);
#pragma unroll
        for (int e = 0; e < 10; ++e) {
            const int P = O ? p : 5 * p + (e >> 1), d = 2 * (P & 3);
            const u32x2 y = ldtr(b + (O ? r0 + e * 32 : (e & 1 ? r1 : r0) + (e >> 1) * 4096));
            u32x8& t = O ? wt[P >> 2][e] : wt[e & 1][P >> 2];
            t[d] = y[0]; t[d + 1] = y[1];
        }
    }
    BAR();
}

__global__ __launch_bounds__(512, 2) B3ATTR void b3_kernel(RecArgs a) {
    extern __shared__ __attribute__((aligned(16))) char smem[];
    char* memL = smem;
    char* tokL = smem + L_TOK;
    char* hL = smem + L_H;
    int w = __builtin_amdgcn_readfirstlane(threadIdx.x >> 6);
    __amdgpu_buffer_rsrc_t rwin = mkrsrc(a.win, W_END + 2 * PAIR);
    __amdgpu_buffer_rsrc_t rws = mkrsrc(a.ctl, (unsigned)OFF_WIN);
    asm volatile("" : "+s"(rwin), "+s"(rws));
    unsigned long long tref = 0;
    const unsigned long long tstart = __builtin_amdgcn_s_memtime();
    auto get_tag = [&]() __attribute__((always_inline)) {
        unsigned long long e;
        asm volatile("s_buffer_load_dwordx2 %0, %1, 8\n\ts_waitcnt lgkmcnt(0)" : "=s"(e) : "s"(rws) : "memory");
        e += 0x9E3779B97F4A7C15ull;
        return (u32x2){(unsigned)e, (unsigned)(e >> 32)};
    };
    auto role = [&](unsigned rot) {
        const unsigned vx = blockIdx.x + rot;
        const int j = blockIdx.x >> 3, x = vx & 3, hh = 8 * (blockIdx.x & 3) + (j & 7);
        int r;
        if (!(vx & 4)) r = j < 8 ? hh : j < 16 ? 32 + hh : j < 26 ? 80 + 10 * x + j : j < 30 ? 255 - 4 * (j - 26) - x : NREC + 2 + 2 * x + j;
        else r = j < 8 ? NQT + hh : j < 16 ? 64 + hh : j < 24 ? NREC - 16 + 8 * x + j : NXT + hh;
        return r;
    };
    int bid = role(xcc_id() - blockIdx.x + BADG * (blockIdx.x >> 3));
    unsigned nh = 0, nz = 0, mc = 0, qz = 0;
    auto hint = [&](unsigned vo, unsigned so) __attribute__((always_inline)) {
        const u32x2 tag = get_tag();
        u32x2 f;
        for (unsigned spins = 0; spins < SPIN_CAP; ++spins) {
            asm volatile("" ::: "memory");
            f = __builtin_amdgcn_raw_buffer_load_b64(rwin, vo, so, 16);
            if (!__any(f[1] != tag[1] || (f[0] ^ tag[0]) > 255u)) break;
            __builtin_amdgcn_s_sleep(4);
        }
        return f[0] & 255u;
    };
    auto tell = [&](unsigned so, unsigned v) __attribute__((always_inline)) {
        const u32x2 tag = get_tag();
        __builtin_amdgcn_raw_buffer_store_b64((u32x2){(tag[0] & ~255u) | v, tag[1]}, rwin, 0u, so, 16);
    };
    auto share = [&](unsigned v) __attribute__((always_inline)) {
        if (w == 0) *(volatile LAS unsigned*)(smem + L_S + 64) = v;
        BAR();
        v = __builtin_amdgcn_readfirstlane(*(volatile LAS unsigned*)(smem + L_S + 64));
        BAR();
        return v;
    };
    auto config = [&]() __attribute__((always_inline)) {
        unsigned v = 0;
        if (w == 0) {
            const unsigned l = fresh_lane(), f = hint(l * 8u, W_CFG), rot = __builtin_amdgcn_readfirstlane(hint(0u, W_ARM));
            const unsigned long long ok = __ballot((f & 4u) && !((l + rot) & 4u)), h = __ballot(f & 1u), q = __ballot(((f >> 1) ^ (l + rot)) & 1u);
            v = rot;
#pragma unroll
            for (int p = 0; p < 2; ++p) {
                unsigned long long m = ok & (p ? 0xFF00FF00FF00FF00ull : 0x00FF00FF00FF00FFull);
                const bool hb = 2 * __builtin_popcountll(m & h) > __builtin_popcountll(m);
                m &= hb ? h : ~h;
                v |= (hb ? 8u : 0u) << p | (2 * __builtin_popcountll(m & q) > __builtin_popcountll(m) ? 32u : 0u) << p;
            }
        }
        v = share(v);
        nh = (v >> 3 & 1u) * HALF; nz = (v >> 4 & 1u) * HALF;
        mc = ((blockIdx.x + v) & 3u) ^ (v >> 5 & 1u); qz = v >> 6 & 1u;
        return v;
    };
    auto zpair = [&](unsigned x, unsigned k) { return (((x >> 1) * 10u + k) * 2u + ((x & 1u) ^ qz)) * PAIR + nz; };
    unsigned mo, mo1, tro;
    auto two = [&]() { const unsigned l = fresh_lane(); return l < 4 ? (l >> 1) * PAIR + (l & 1) * 4096u + nh : 0x40000000u; };
    const u32x4 SENT = {0xFFFFFFFFu, 0xFFFFFFFFu, 0xFFFFFFFFu, 0xFFFFFFFFu};
    const f32x4 z4 = {0.f, 0.f, 0.f, 0.f};
    int hrdy = 0, budget = (int)SPIN_CAP;
    unsigned fc0 = 0, fc1 = 0, fc2 = 0;
    Clk ck = {0u, 0u, 1600u, 10486u};
    int lay = 0;
    unsigned fb = 0, zb = W_ZM, hb = 0;
    unsigned tpp = 0;
    int lt = LT1, xs = XS0;
    struct Tk { unsigned vo, tw, sl; const LAS char* sp; };
    auto tk_init = [&]() {
        const unsigned l = fresh_lane();
        Tk k = {l * 16u, two(), (l & 31u) * 128u + (l >> 5) * 4u, (const LAS char*)(memL + l * 64 + 4 + ((l >> 1) & 7) * 8)};
        PIN2(k.vo, k.tw); PIN2(k.sl, k.sp);
        return k;
    };
    auto sched2 = [&](Tk& k, int s, unsigned f) {
        const unsigned g0 = tpp - 0x400000u;
        tpp = g0 + (unsigned)read_sched(rws, rwin, s, (unsigned)(s + 64 * lay), k.sl, tro + (unsigned)(s & 3) * 2u * PAIR, f, g0, lt, xs, fc1);
        k.sl &= ~4u;
        RT(6);
    };
    auto sched = [&](Tk& k, int s) { sched2(k, s, __builtin_amdgcn_raw_buffer_load_b32(rwin, k.sl, tro + (unsigned)(s & 3) * 2u * PAIR, 16)); };
    auto fetch = [&](const Tk& k, int s, bool first, unsigned lim, unsigned dc, unsigned fo, bool tell) {
        const unsigned sb = (unsigned)(s & 3) * 6u * PAIR, ta0 = ck.ta, mm0 = ck.ma;
        if (__builtin_expect(s > 0, 1)) {
            if (__builtin_expect(first, 1)) while ((unsigned)sx24(tpp - TKE - 4u * (unsigned)__builtin_amdgcn_s_memrealtime()) < 20000u) __builtin_amdgcn_s_sleep(1);
            if (__builtin_expect(first, 1)) gather_at(tpp, ck, rwin, k.vo, mo + sb, mo1 + sb);
        }
        __builtin_amdgcn_s_setprio(0);
        RT(1);
        clk_rate(ck, ta0, mm0);
        unsigned tb = ck.ma - dc, tg = (unsigned)(s + 64 * lay) << 24, so = W_T + (unsigned)(s & 3) * 2u * PAIR + fo;
        asm volatile("" : "+s"(tb), "+s"(tg), "+s"(so));
        asm volatile("s_waitcnt vmcnt(%1) lgkmcnt(0)\n\ts_barrier\n\ts_waitcnt vmcnt(%2)\n\ts_barrier\n\ts_waitcnt vmcnt(0)\n\ts_memtime %0\n\ts_barrier"
                     : "=s"(tref) : "n"(10 - NB1), "n"(10 - NB2) : "memory");
        MT(1);
        if (!tell) { RT(3); return; }
        unsigned x0 = *(const LAS unsigned*)k.sp, x1 = *(const LAS unsigned*)(k.sp + 4096), x2 = *(const LAS unsigned*)(k.sp + 6144);
        __builtin_amdgcn_s_waitcnt(0xC07F);
        asm volatile("" : "+s"(tref) :: "memory");
        const unsigned fw = ((ck.ta + ((((unsigned)tref - tb) * ck.qpc) >> 16)) & 0xFFFFFFu) | tg;
        __builtin_amdgcn_raw_buffer_store_b32(fw, rwin, k.tw, __any(max(max(x0, x1), x2) > lim) ? 0x40000000u : so, 16);
        RT(3);
    };
    unsigned mb = 0;
    auto again = [&](bool tk, int s, unsigned lim, const LAS char*& p, const LAS char*& q) __attribute__((always_inline)) {
        const unsigned nb = mb == 0x10000u ? 0x14000u : 0x10000u;
        if (tk) {
            const unsigned c = (mc + 1u + (nb >> 14 & 1u)) & 3u, sb = W_MEM + (unsigned)(s & 3) * 6u * PAIR + nh, vo = fresh_lane() * 16u, t = sb + (4u + (c & 1u)) * PAIR + (c >> 1) * 2048u;
            dma4k(rwin, vo, sb + c * PAIR, nb); dma4k(rwin, vo, sb + c * PAIR + 4096u, nb + 4096u);
            dma1k(rwin, vo, t, nb + 8192u); dma1k(rwin, vo, t + 1024u, nb + 9216u);
        }
        const bool r = mem_missing(mb, lim);
        if (r) { p += nb - mb; q += nb - mb; mb = nb; FC(fc0); }
        return r;
    };
    auto places = [&]() {
        mo = W_MEM + mc * PAIR + nh; mo1 = W_MEM + (4u + (mc & 1u)) * PAIR + nh + (mc >> 1) * 2048u;
        tro = W_T + (mc & 1u) * PAIR + nh + (mc >> 1) * 4096u;
    };
    auto fresh = [&]() {
        asm volatile("" : "+s"(bid), "+s"(w), "+s"(nh), "+s"(nz), "+s"(mc), "+s"(qz), "+s"(lay));
        fb = lay * W_FL; hb = O_H + (1 - lay) * (4u * 40960u); zb = W_ZM + lay * (NSEG % 3 * NOWN * PAIR);
        tref = 0; hrdy = 0; budget = (int)SPIN_CAP; ck = Clk{0u, 0u, 1600u, 10486u}; tpp = 0; lt = LT1; xs = XS0;
    };
    auto filter = [&]() __attribute__((always_inline)) {
        if ((bid >= 32 && bid < 64) || (bid >= NQT && bid < NXT)) {
            const unsigned u = bid < 64 ? bid : bid - NQT, l = fresh_lane();
            filter_unit((const u16*)karg<9>(), (const u16*)karg<10>(), u * 32, rws, (u16*)tokL, w * 64 + l);
            VMWAIT();
            BAR();
            if (w == 0) __builtin_amdgcn_raw_buffer_store_b64(get_tag(), rwin, (l & 1u) * 8u, W_ST + u * 128u + 64u, 16);
        }
    };
    auto settle = [&]() __attribute__((always_inline)) { const int b = role(config()); if (__builtin_expect(b != bid, 0)) { bid = b; filter(); } };
    auto warm = [&]() __attribute__((always_inline)) {
        unsigned long long pc;
        unsigned len;
        asm volatile("s_getpc_b64 %0\n1:\n\ts_mov_b32 %1, 2f-1b\n\t.subsection 1\n2:\n\t.subsection 0" : "=s"(pc), "=s"(len));
        if (CPF) dma1k(mkrsrc((const void*)pc, len), fresh_lane() * 16u, (blockIdx.x >> 3) * 1024u + w * 16384u, L_END - 8192 + w * 1024);
    };
    auto a1_done = [&]() __attribute__((always_inline)) {
        VMWAIT();
        BAR();
        if (w == 0) __builtin_amdgcn_raw_buffer_store_b64(get_tag(), rwin, 0u, W_ST + (blockIdx.x - 128u >> 1) * 128u + (blockIdx.x & 1u) * 8u, 16);
        SST(29);
    };
    auto due = [&](int i, int d) __attribute__((always_inline)) {
        __builtin_amdgcn_raw_buffer_store_b32(nosent1((unsigned)__builtin_amdgcn_s_memrealtime() + d), rwin, 0u, W_E + fb + i * 128u, 16);
    };
    auto await = [&](int i, int n, int d, unsigned f = ~0u) __attribute__((always_inline)) {
        if (__any(f == ~0u)) for (unsigned spins = 0;;) {
            asm volatile("" ::: "memory");
            f = __builtin_amdgcn_raw_buffer_load_b32(rwin, min(fresh_lane(), n - 1u) * 128u, W_E + i * 128u, 16);
            if (!__any(f == ~0u)) break;
            if (spin_fail(spins, rws)) break;
            __builtin_amdgcn_s_sleep(HSLP);
        }
        while (__any(f + d - (unsigned)__builtin_amdgcn_s_memrealtime() - 1u < 1000u)) {}
    };
    constexpr int E_OUT = 0, E_A1 = 8, E_A2 = 16, E_A3 = 24;
    auto soon = [&](int j) __attribute__((always_inline)) {
        if (w == 0) { if (j >= 4) wait_h(rws, rwin, fb, j, hrdy); else await(E_A3 + fb / 128u, 4, 0); }
        BAR();
    };
    bool slow = false;
    auto gap = [&](int p) __attribute__((always_inline)) {
        if (blockIdx.x < 128) { if (p == WLA - 1 && w == 7) clk_init(ck); }
        else if (slow) { if (p < 0) { if (XST) __builtin_amdgcn_s_sleep(XST); } else if (XSLP && p < 7) __builtin_amdgcn_s_sleep(XSLP); }
    };
    auto retime = [&]() __attribute__((always_inline)) {
        if (lay) clk_init(ck);
        else { const unsigned ta0 = ck.ta, ma0 = ck.ma; wait_fine(4u * (unsigned)__builtin_amdgcn_s_memrealtime() + 60u, ck); clk_rate(ck, ta0, ma0); }
    };
    SST(27);
    {
        LANES();
        const unsigned u = blockIdx.x - 128u;
        if (blockIdx.x < 128) {
            const unsigned po = (blockIdx.x & 8u ? W_END : W_PROBE) + ((blockIdx.x >> 1 & 24u) | (blockIdx.x & 7u)) * 128u;
            const bool pr = bid >= 32 && bid < 64 && w == 7;
            if (pr) __builtin_amdgcn_raw_buffer_store_b32(0u, rwin, (lane & 3) * HALF + (lane >> 2 & 1) * 4096u, po, 16);
            warm();
            filter();
            if (blockIdx.x >= 64 && w == 7) tell(W_CFG + (blockIdx.x - 64u) * 8u, pr ? 4u | probe(rwin, po) : 0u);
            BAR0();
        } else a1_unit<8, 512>(mkrsrc(karg<3>(), S_ * D_ * 2), u * 16, false, rws, smem, row_tw(lane), w, lane, [&](int k) {
            if (k == -1) {
                if (u == 0 && w == 0) {
                    tell(W_ROT, xcc_id());
                    __builtin_amdgcn_raw_buffer_store_b32(0u, rws, 0u, 0u, 16);
                }
                u32x4 sv = SENT;
                PIN1(sv);
#pragma unroll
                for (int i = 0; i < 4; ++i) {
                    static_assert(ARM_END - OFF_ARM <= 512 * 8192 && W_PROBE + W_END - W_ZM <= 512 * 8192, "arming");
                    const unsigned g = (i * 128u + u) * 8192u;
                    if (g < W_PROBE + W_END - W_ZM) st128(sv, rwin, tid * 16u, g < W_PROBE ? g : g + W_ZM - W_PROBE, 16);
                    if (g < ARM_END - OFF_ARM) st128(sv, rws, tid * 16u, (unsigned)OFF_ARM + g, 16);
                }
            }
        });
        SST(28);
        if (blockIdx.x >= 128) {
            if (w == 0) *(volatile LAS unsigned*)(smem + L_END - 16) = hint(0u, W_ROT);
            a1_done();
            const unsigned rot = __builtin_amdgcn_readfirstlane(*(volatile LAS unsigned*)(smem + L_END - 16));
            bid = role(rot);
            if (bid == 255 && w == 0) {
                const u32x2 tag = get_tag();
                for (unsigned spins = 0; spins < SPIN_CAP; ++spins) {
                    asm volatile("" ::: "memory");
                    const u32x4 g = __builtin_amdgcn_raw_buffer_load_b128(rwin, lane * 128u, W_ST, 16);
                    if (!__any(((g[0] ^ tag[0]) | (g[1] ^ tag[1]) | (g[2] ^ tag[0]) | (g[3] ^ tag[1])) != 0u)) break;
                    __builtin_amdgcn_s_sleep(4);
                }
                tell(W_ARM, rot);
            }
            SST(30);
        }
    }
    if (bid < 96 || (bid >= NQT && bid < NWRK)) {
    u32x8 wt[2][10];
#pragma nounroll
    for (; lay < 2; ++lay) {
    fresh();
    const bool xt = bid >= NXT;
    SST(0);
    if ((bid >= 32 && bid < 96) || xt) {
        const int hh = (xt ? bid - NXT : bid) & 31;
        u32x4 m0[2];
        get_mem0(m0, w * 64 + fresh_lane());
        if (lay == 0 || xt) {
            wait_until(tstart + (bid < 64 ? DVO : xt ? DXT : DW1));
            if (w < 4) load_w<false>(wt, karg<7>(), hh, w, 2 * w, 8, tokL, gap); else load_w<true>(wt, karg<8>(), hh, w, 0, 0, tokL, gap);
            SST(5);
            slow = true;
            if (lay == 0) settle();
        }
        places();
        const bool vt = bid >= 64;
        const unsigned long long mv = xt ? MXV : ~MXV;
        auto nextv = [&](int j) { return min(seg_after(mv, j), NSEG - 1); };
        const int jv1 = nextv(-1), jv2 = nextv(jv1);
        {
            LANES();
            put_mem0(m0, memL, tid);
            VMWAIT();
            if (tid < 8) *(unsigned*)(smem + L_S + tid * 4) = 0u;
            if (!vt && w == 7) retime();
            SST(1);
            if (vt) {
                soon(jv1);
                SST(2);
                const unsigned so = hb + (unsigned)(jv1 * 40960 + w * 1024), lo = L_H + w * 1024;
                stage_h<5, 8192>(rws, so, lo, lane);
                BAR0();
                if (w < 4) check_h<5, 8192, 0>(rws, so, lo, lane);
                else { stage_h<10, 4096>(rws, hb + (unsigned)(jv2 * 40960 + (w - 4) * 1024), lo + 36864u, lane); check_h<5, 8192, 10>(rws, so, lo, lane); }
            }
        }
        auto steps = [&](auto cc) {
            constexpr int C = decltype(cc)::v & 1;
            constexpr bool TK = decltype(cc)::v == 3;
            LANES();
            Tk tk = {0u, 0u, 0x40000000u, nullptr};
            if (TK) { tk = tk_init(); __builtin_amdgcn_s_setprio(TKP); }
            const unsigned zi = (unsigned)(hh * 128 + fr * 8) * 2u;
            unsigned zo0 = zpair(w & 3, fq) + zi, zo1 = zo0 + 8 * PAIR, zo2 = zpair(w & 3, 8 + (fq >> 1)) + zi + (fq & 1) * 8;
            const LAS char* va = (const LAS char*)(smem + L_Q + (fr & 1) * 544 + ((fr >> 1) & 1) * 1152 + (16 * fq + 2 * (fr >> 2)) * 4);
            LAS char* vw = (LAS char*)(smem + L_Q + (fq & 1) * 544 + (fq >> 1) * 1152 + (16 * w + bs2(fr)) * 4);
            const LAS char* mp0 = (const LAS char*)(memL + ma0);
            PIN2(zo0, zo1); PIN1(zo2);
            if (C == 0) { PIN2(mp0, vw); } else PIN1(va);
            u32x4 za = SENT, zc = SENT;
            u32x2 ze = {0u, 0u};
            auto put_z2 = [&](int s) {
                const unsigned z2b = s < NSEG ? O_Z2 + (unsigned)s * (NOWN * 8192u) + (unsigned)(10 * (w & 3) * 8192 + hh * 256) : 0x40000000u, l2 = fresh_lane();
                st128(za, rws, (l2 >> 4) * 8192u + (l2 & 15u) * 16u, z2b, 16);
                st128(zc, rws, (l2 >> 4) * 8192u + (l2 & 15u) * 16u, z2b + 4u * 8192u, 16);
                __builtin_amdgcn_raw_buffer_store_b64(ze, rws, (l2 >> 5) * 8192u + (l2 & 15u) * 16u + ((l2 >> 4) & 1u) * 8u, z2b + 8u * 8192u, 16);
            };
            for (int s = 0; s < NSEG; ++s) {
                RT(0);
                bool redo = false;
                unsigned sf = 0;
                do {
                    unsigned lim = s > 0 ? poll_lim(budget) : ~0u;
                    unsigned long long sus;
                    asm volatile("" : "+s"(lim));
                    if (C == 0) {
                        asm volatile(
                            "v_mov_b64 v[200:201], 0\n\t"
                            "v_mov_b64 v[202:203], 0\n\t"
                            "v_mov_b64 v[204:205], 0\n\t"
                            "v_mov_b64 v[206:207], 0\n\t"
                            "s_setprio 2\n\t"
                            "s_waitcnt lgkmcnt(0)\n\t"
                            "s_barrier\n\t"
                            MTA(0)
                            "ds_read_b128 v[212:215], %[mp] offset:0\n\t"
                            "ds_read_b128 v[216:219], %[mp] offset:1024\n\t"
                            "ds_read_b128 v[220:223], %[mp] offset:2048\n\t"
                            "s_barrier\n\t"
                            "ds_read_b128 v[224:227], %[mp] offset:3072\n\t"
                            "ds_read_b128 v[228:231], %[mp] offset:4096\n\t"
                            "ds_read_b128 v[232:235], %[mp] offset:5120\n\t"
                            "s_waitcnt lgkmcnt(5)\n\t"
                            "v_smfmac_f32_16x16x64_bf16 v[200:203], v[212:215], %[w00], %[ix]\n\t"
                            "v_smfmac_f32_16x16x64_bf16 v[204:207], v[212:215], %[w10], %[ix]\n\t"
                            "s_waitcnt lgkmcnt(4)\n\t"
                            "v_smfmac_f32_16x16x64_bf16 v[200:203], v[216:219], %[w01], %[ix]\n\t"
                            "v_smfmac_f32_16x16x64_bf16 v[204:207], v[216:219], %[w11], %[ix]\n\t"
                            "s_barrier\n\t"
                            MTA(1)
                            "ds_read_b128 v[236:239], %[mp] offset:6144\n\t"
                            "ds_read_b128 v[240:243], %[mp] offset:7168\n\t"
                            "s_waitcnt lgkmcnt(5)\n\t"
                            "v_smfmac_f32_16x16x64_bf16 v[200:203], v[220:223], %[w02], %[ix]\n\t"
                            "ds_read_b128 v[244:247], %[mp] offset:8192\n\t"
                            "ds_read_b128 v[248:251], %[mp] offset:9216\n\t"
                            "s_waitcnt lgkmcnt(4)\n\t"
                            "v_smfmac_f32_16x16x64_bf16 v[200:203], v[224:227], %[w03], %[ix]\n\t"
                            "v_smfmac_f32_16x16x64_bf16 v[200:203], v[228:231], %[w04], %[ix]\n\t"
                            "v_smfmac_f32_16x16x64_bf16 v[200:203], v[232:235], %[w05], %[ix]\n\t"
                            "s_waitcnt lgkmcnt(3)\n\t"
                            "v_smfmac_f32_16x16x64_bf16 v[200:203], v[236:239], %[w06], %[ix]\n\t"
                            "s_waitcnt lgkmcnt(2)\n\t"
                            "v_smfmac_f32_16x16x64_bf16 v[200:203], v[240:243], %[w07], %[ix]\n\t"
                            "s_waitcnt lgkmcnt(1)\n\t"
                            "v_smfmac_f32_16x16x64_bf16 v[200:203], v[244:247], %[w08], %[ix]\n\t"
                            "s_waitcnt lgkmcnt(0)\n\t"
                            "v_smfmac_f32_16x16x64_bf16 v[200:203], v[248:251], %[w09], %[ix]\n\t"
                            "v_smfmac_f32_16x16x64_bf16 v[204:207], v[220:223], %[w12], %[ix]\n\t"
                            "s_nop 2\n\t"
                            "v_smfmac_f32_16x16x64_bf16 v[204:207], v[224:227], %[w13], %[ix]\n\t"
                            "s_nop 2\n\t"
                            "v_add_f32 v200, v200, v202\n\t"
                            "v_add_f32 v201, v201, v203\n\t"
                            "v_smfmac_f32_16x16x64_bf16 v[204:207], v[228:231], %[w14], %[ix]\n\t"
                            "v_cvt_pk_bf16_f32 v200, v200, v201\n\t"
                            "ds_write_b32 %[vw], v200\n\t"
                            MTA(2)
                            "v_smfmac_f32_16x16x64_bf16 v[204:207], v[232:235], %[w15], %[ix]\n\t"
                            "v_smfmac_f32_16x16x64_bf16 v[204:207], v[236:239], %[w16], %[ix]\n\t"
                            "v_smfmac_f32_16x16x64_bf16 v[204:207], v[240:243], %[w17], %[ix]\n\t"
                            "v_smfmac_f32_16x16x64_bf16 v[204:207], v[244:247], %[w18], %[ix]\n\t"
                            "s_waitcnt lgkmcnt(0)\n\t"
                            "s_barrier\n\t"
                            MTA(5)
                            "v_smfmac_f32_16x16x64_bf16 v[204:207], v[248:251], %[w19], %[ix]\n\t"
                            "s_nop 7\n\t"
                            "v_pk_add_f32 v[204:205], v[204:205], v[206:207]\n\t"
                            "v_cvt_pk_bf16_f32 v200, v204, v205\n\t"
                            "ds_write_b32 %[vw], v200 offset:256\n\t"
                            MTA(3)
                            "s_waitcnt vmcnt(0) lgkmcnt(0)\n\t"
                            "s_barrier\n\t"
                            "s_setprio 0\n\t"
                            "v_cmp_u_f32 %[sus], v204, v205\n\t"
                            : [sus] "=s"(sus) : [mp] "v"(mp0), [vw] "v"(vw), [ix] "v"(sidx),
                              [w00] "v"(wt[0][0]), [w01] "v"(wt[0][1]), [w02] "v"(wt[0][2]), [w03] "v"(wt[0][3]), [w04] "v"(wt[0][4]), [w05] "v"(wt[0][5]), [w06] "v"(wt[0][6]), [w07] "v"(wt[0][7]), [w08] "v"(wt[0][8]), [w09] "v"(wt[0][9]), [w10] "v"(wt[1][0]), [w11] "v"(wt[1][1]), [w12] "v"(wt[1][2]), [w13] "v"(wt[1][3]), [w14] "v"(wt[1][4]), [w15] "v"(wt[1][5]), [w16] "v"(wt[1][6]), [w17] "v"(wt[1][7]), [w18] "v"(wt[1][8]), [w19] "v"(wt[1][9])
                            : "v200", "v201", "v202", "v203", "v204", "v205", "v206", "v207", "v208", "v209", "v212", "v213", "v214", "v215", "v216", "v217", "v218", "v219", "v220", "v221", "v222", "v223", "v224", "v225", "v226", "v227", "v228", "v229", "v230", "v231", "v232", "v233", "v234", "v235", "v236", "v237", "v238", "v239", "v240", "v241", "v242", "v243", "v244", "v245", "v246", "v247", "v248", "v249", "v250", "v251", "vcc", "memory");
                        MT(4);
                    } else {
                        f32x16 ca, cb;
                        f32x8 cc;
                        asm volatile(".irp r,212,214,216,218,220,222,224,226,228,230,232,234,236,238,240,242,244,246,248,250\n\tv_mov_b64 v[\\r:\\r+1], 0\n\t.endr"
                                     : "={v[212:227]}"(ca), "={v[228:243]}"(cb), "={v[244:251]}"(cc));
                        unsigned sr = tro + (unsigned)(s & 3) * 2u * PAIR;
                        asm volatile("" : "+s"(sr));
                        if (TK) fetch(tk, s, !redo, lim, DVC, (unsigned)hh * 128u + 4u, __builtin_expect(s == 0, 0));
                        else asm volatile("s_waitcnt lgkmcnt(0)\n\ts_barrier\n\ts_barrier\n\ts_barrier" ::: "memory");
                        unsigned zs = zb;
                        u32x4 af1;
                        asm volatile(
                            "s_setprio 1\n\t"
                            "s_waitcnt vmcnt(0) lgkmcnt(0)\n\t"
                            "s_barrier\n\t"
                            "ds_read_b64_tr_b16 v[192:193], %[va]\n\t"
                            "ds_read_b64_tr_b16 v[194:195], %[va] offset:32\n\t"
                            "s_waitcnt lgkmcnt(0)\n\t"
                            "v_smfmac_f32_16x16x64_bf16 v[212:215], v[192:195], %[w0], %[ix]\n\t"
                            "v_smfmac_f32_16x16x64_bf16 v[220:223], v[192:195], %[w2], %[ix]\n\t"
                            "v_smfmac_f32_16x16x64_bf16 v[216:219], v[192:195], %[w1], %[ix]\n\t"
                            "v_smfmac_f32_16x16x64_bf16 v[224:227], v[192:195], %[w3], %[ix]\n\t"
                            "s_barrier\n\t"
                            MTA(4)
                            "ds_read_b64_tr_b16 v[196:197], %[va] offset:256\n\t"
                            "v_smfmac_f32_16x16x64_bf16 v[228:231], v[192:195], %[w4], %[ix]\n\t"
                            "ds_read_b64_tr_b16 v[198:199], %[va] offset:288\n\t"
                            "v_smfmac_f32_16x16x64_bf16 v[236:239], v[192:195], %[w6], %[ix]\n\t"
                            "v_smfmac_f32_16x16x64_bf16 v[232:235], v[192:195], %[w5], %[ix]\n\t"
                            "v_smfmac_f32_16x16x64_bf16 v[240:243], v[192:195], %[w7], %[ix]\n\t"
                            "v_cmp_u_f32 %[sus], v212, v213\n\t"
                            "v_smfmac_f32_16x16x64_bf16 v[244:247], v[192:195], %[w8], %[ix]\n\t"
                            "v_smfmac_f32_16x16x64_bf16 v[248:251], v[192:195], %[w9], %[ix]\n\t"
                            : "+{v[212:227]}"(ca), "+{v[228:243]}"(cb), "+{v[244:251]}"(cc), "=&{v[196:199]}"(af1), [sus] "=&s"(sus)
                            : [va] "v"(va), [ix] "v"(sidx),
                              [w0] "v"(wt[0][0]), [w1] "v"(wt[0][1]), [w2] "v"(wt[0][2]), [w3] "v"(wt[0][3]), [w4] "v"(wt[0][4]), [w5] "v"(wt[0][5]), [w6] "v"(wt[0][6]), [w7] "v"(wt[0][7]), [w8] "v"(wt[0][8]), [w9] "v"(wt[0][9])
                            : "v192", "v193", "v194", "v195", "memory");
                        asm volatile(
                            "s_waitcnt lgkmcnt(0)\n\t"
                            "v_smfmac_f32_16x16x64_bf16 v[212:215], v[196:199], %[w0], %[ix]\n\t"
                            "s_nop 0\n\t"
                            "v_smfmac_f32_16x16x64_bf16 v[220:223], v[196:199], %[w2], %[ix]\n\t"
                            "s_cmp_lg_u64 %[sus], 0\n\t"
                            "s_cselect_b32 %[zs], 2.0, %[zs]\n\t"
                            "v_smfmac_f32_16x16x64_bf16 v[216:219], v[196:199], %[w1], %[ix]\n\t"
                            "s_nop 1\n\t"
                            "v_smfmac_f32_16x16x64_bf16 v[224:227], v[196:199], %[w3], %[ix]\n\t"
                            "v_add_f32 v212, v212, v214\n\t"
                            "v_smfmac_f32_16x16x64_bf16 v[228:231], v[196:199], %[w4], %[ix]\n\t"
                            "v_add_f32 v213, v213, v215\n\t"
                            "v_smfmac_f32_16x16x64_bf16 v[236:239], v[196:199], %[w6], %[ix]\n\t"
                            "v_add_f32 v220, v220, v222\n\t"
                            "v_smfmac_f32_16x16x64_bf16 v[232:235], v[196:199], %[w5], %[ix]\n\t"
                            "v_add_f32 v221, v221, v223\n\t"
                            "v_smfmac_f32_16x16x64_bf16 v[240:243], v[196:199], %[w7], %[ix]\n\t"
                            "v_cvt_pk_bf16_f32 v200, v212, v213\n\t"
                            "v_smfmac_f32_16x16x64_bf16 v[244:247], v[196:199], %[w8], %[ix]\n\t"
                            "v_cvt_pk_bf16_f32 v201, v220, v221\n\t"
                            "v_smfmac_f32_16x16x64_bf16 v[248:251], v[196:199], %[w9], %[ix]\n\t"
                            "buffer_load_dword %[sf], %[sl], %[rw], %[sr] offen sc1\n\t"
                            "v_add_f32 v216, v216, v218\n\t"
                            "v_add_f32 v217, v217, v219\n\t"
                            "v_add_f32 v224, v224, v226\n\t"
                            "v_add_f32 v225, v225, v227\n\t"
                            "v_cvt_pk_bf16_f32 v202, v216, v217\n\t"
                            "v_cvt_pk_bf16_f32 v203, v224, v225\n\t"
                            "v_permlane32_swap_b32 v200, v201\n\t"
                            "v_pk_add_f32 v[228:229], v[228:229], v[230:231]\n\t"
                            "v_permlane32_swap_b32 v202, v203\n\t"
                            "v_min_u32 v201, -2, v201\n\t"
                            "v_pk_add_f32 v[236:237], v[236:237], v[238:239]\n\t"
                            "v_min_u32 v203, -2, v203\n\t"
                            "v_permlane16_swap_b32 v200, v202\n\t"
                            "v_pk_add_f32 v[232:233], v[232:233], v[234:235]\n\t"
                            "v_permlane16_swap_b32 v201, v203\n\t"
                            "v_pk_add_f32 v[240:241], v[240:241], v[242:243]\n\t"
                            MTA(5)
                            "buffer_store_dwordx4 v[200:203], %[z0], %[rw], %[zs] offen sc1\n\t"
                            "v_cvt_pk_bf16_f32 v204, v228, v229\n\t"
                            "v_cvt_pk_bf16_f32 v205, v236, v237\n\t"
                            "v_cvt_pk_bf16_f32 v206, v232, v233\n\t"
                            "v_cvt_pk_bf16_f32 v207, v240, v241\n\t"
                            "v_permlane32_swap_b32 v204, v205\n\t"
                            "v_pk_add_f32 v[244:245], v[244:245], v[246:247]\n\t"
                            "v_permlane32_swap_b32 v206, v207\n\t"
                            "v_min_u32 v205, -2, v205\n\t"
                            "v_pk_add_f32 v[248:249], v[248:249], v[250:251]\n\t"
                            "v_min_u32 v207, -2, v207\n\t"
                            "v_permlane16_swap_b32 v204, v206\n\t"
                            "v_cvt_pk_bf16_f32 v208, v244, v245\n\t"
                            "v_permlane16_swap_b32 v205, v207\n\t"
                            "v_cvt_pk_bf16_f32 v209, v248, v249\n\t"
                            "buffer_store_dwordx4 v[204:207], %[z1], %[rw], %[zs] offen sc1\n\t"
                            "s_nop 0\n\t"
                            "v_permlane32_swap_b32 v208, v209\n\t"
                            "v_min_u32 v209, -2, v209\n\t"
                            "buffer_store_dwordx2 v[208:209], %[z2], %[rw], %[zs] offen sc1\n\t"
                            : [sf] "+&v"(sf), [zs] "+s"(zs), "=&{v[200:203]}"(za), "=&{v[204:207]}"(zc), "=&{v[208:209]}"(ze), "+{v[212:227]}"(ca), "+{v[228:243]}"(cb), "+{v[244:251]}"(cc)
                            : [ix] "v"(sidx), [sus] "s"(sus), [sl] "v"(tk.sl), [sr] "s"(sr), [rw] "s"(rwin), [z0] "v"(zo0), [z1] "v"(zo1), [z2] "v"(zo2), "{v[196:199]}"(af1),
                              [w0] "v"(wt[1][0]), [w1] "v"(wt[1][1]), [w2] "v"(wt[1][2]), [w3] "v"(wt[1][3]), [w4] "v"(wt[1][4]), [w5] "v"(wt[1][5]), [w6] "v"(wt[1][6]), [w7] "v"(wt[1][7]), [w8] "v"(wt[1][8]), [w9] "v"(wt[1][9])
                            : "scc", "memory");
                        RT(4);
                        MT(6);
                        tref = __builtin_amdgcn_s_memtime();
                        __builtin_amdgcn_s_setprio(TKP);
                    }
                    redo = false;
                    if (__builtin_expect(sus != 0, 0)) {
                        redo = again(TK, s, lim, mp0, tk.sp);
                        if (!redo && C == 1) { st128(za, rwin, zo0, zb, 16); st128(zc, rwin, zo1, zb, 16); __builtin_amdgcn_raw_buffer_store_b64(ze, rwin, zo2, zb, 16); }
                    }
                } while (__builtin_expect(redo, 0));
                if (C == 1) asm volatile("s_waitcnt vmcnt(3)" : "+v"(sf) :: "memory");
                if (__builtin_expect(mb != 0, 0)) { mp0 -= mb; mb = 0; }
                if (s < 8) SST(8 + s);
                if (C == 0) {
                    if (s > 0 && s + 1 < NSEG) {
                        __builtin_amdgcn_s_sleep(DZR);
                        const unsigned zq = zb == W_ZM ? W_ZM + 2u * NOWN * PAIR : zb - NOWN * PAIR;
                        unsigned s1;
                        asm volatile("v_mov_b32 %0, -1" : "=v"(s1));
                        st128(((u32x4){s1, s1, s1, s1}), rwin, zo0, zq, 16);
                        st128(((u32x4){s1, s1, s1, s1}), rwin, zo1, zq, 16);
                        __builtin_amdgcn_raw_buffer_store_b64((u32x2){s1, s1}, rwin, zo2, zq, 16);
                    }
                } else {
                    if (TK && s + 1 < NSEG) sched2(tk, s, sf);
                    if (!TK) wait_until(tref + DZ2);
                    if (s + 2 < NSEG) put_z2(s);
                }
                if (budget <= 0) __builtin_amdgcn_raw_buffer_store_b32(1u, rws, 0u, 0u, 16);
                zb = zb == W_ZM + 2u * NOWN * PAIR ? W_ZM : zb + NOWN * PAIR;
                RT(5);
                RTS();
                MT(7);
                MTS();
            }
        };
        if (!vt) {
            if (w < 4) steps(IC<0>()); else if (w < 7) steps(IC<1>()); else steps(IC<3>());
        } else {
            BAR();
            SST(3);
            LANES();
            const int ni = __builtin_popcountll(mv) + 1;
            if (w < 4) {
                const LAS char* c0 = (const LAS char*)(hL + hoff + ((fq ^ (fr & 7)) << 4));
                const LAS char* c1 = (const LAS char*)(hL + hoff + (((4 + fq) ^ (fr & 7)) << 4));
                const LAS char* n0 = c0 + 40960; const LAS char* n1 = c1 + 40960;
                u32x4 bb[8];
                project0(bb, c0, c1);
                for (int i = 1; i < ni; ++i) {
                    if (vt) MT(0);
                    f32x4 tc[2][2];
                    project<0>(wt, tc, bb, c0, c1, n0, n1,
                        [&]() {
                            MT(1);
                            if (__builtin_expect(i > 1, 1)) BAR();
                            MT(2);
                        }, [](auto) {});
                    { const LAS char* x = c0; c0 = n0; n0 = x; x = c1; c1 = n1; n1 = x; }
                    char* tb = tokL + (i & 1) * 32 * QS + (16 * w + 4 * fq) * 2;
#pragma unroll
                    for (int g = 0; g < 2; ++g)
#pragma unroll
                        for (int tt = 0; tt < 2; ++tt) *(u32x2*)(tb + (prow + 8 * tt) * QS + g * 128) = (u32x2){pack2(tc[g][tt][0], tc[g][tt][1]), pack2(tc[g][tt][2], tc[g][tt][3])};
                    asm volatile("" ::: "memory");
                    *(volatile LAS int*)(smem + L_S + 16 + w * 4) = i;
                    if (__builtin_expect(i == 1, 0)) { BAR(); if (VSLP) __builtin_amdgcn_s_sleep(VSLP); project0(bb, c0, c1); }
                    if (vt) { MT(3); const int s = i & 63; MTRT(); MTS(); }
                }
                project_end(bb);
            } else {
                __builtin_amdgcn_s_setprio(OPR);
                const unsigned hv = lane * 16u, fz_ = W_FZ + fb + (unsigned)(hh * 128 + xt * 64 + (w - 4) * 4);
                unsigned fp = ~0u;
                u32x4 r = SENT;
                int jo = jv1, jh = jv2;
                check_h<10, 4096, 0>(rws, hb + (unsigned)(jv2 * 40960 + (w - 4) * 1024), L_H + 40960 + (w - 4) * 1024, lane);
                SST(16);
                auto rows = [&](int b) {
                    for (int t = 0; t < 10; ++t) dma1k(rws, hv, hb + (unsigned)(jh * 40960 + (w - 4) * 1024 + t * 4096), (unsigned)(L_H + ((b + 1) & 1) * 40960 + (w - 4) * 1024 + t * 4096));
                    VMWAIT();
                };
                for (int i = 1; i < ni; ++i, jo = nextv(jo)) {
                    const int b = i + (i >= ZLAG);
                    if (__builtin_expect(i == ZLAG, 0)) {
                        VMWAIT();
                        jh = nextv(jh);
                        wait_h(rws, rwin, fb, jh, hrdy);
                        BAR0();
                        rows(i);
                    }
                    jh = nextv(jh);
                    u32x4 hf;
                    asm volatile("" : "=v"(hf));
                    if (__builtin_expect(hrdy <= jh, 0)) asm volatile("buffer_load_dwordx4 %0, %1, %2, %3 offen sc1" : "+v"(hf) : "v"(min(lane, jh + 16) * 128), "s"(rwin), "s"(W_H + fb) : "memory");
                    if (b < ni) BAR();
                    if (i == 1) SST(17);
                    if (vt) MT(1);
                    for (;;) {
                        const u32x4 f = *(volatile LAS u32x4*)(smem + L_S + 16);
                        if (__builtin_amdgcn_readfirstlane(min(min(f[0], f[1]), min(f[2], f[3]))) >= (unsigned)i) break;
                        __builtin_amdgcn_s_sleep(1);
                    }
                    asm volatile("" ::: "memory");
                    if (__builtin_expect(hrdy <= jh, 0)) { asm volatile("s_waitcnt vmcnt(0)" : "+v"(hf) :: "memory"); hrdy = min(see_h(hf), jh + 17); }
                    const bool ok = hrdy > jh;
                    if (__builtin_expect(i < ZLAG || !ok, 0)) { VMWAIT(); __builtin_amdgcn_raw_buffer_store_b32(fp, rwin, 0u, fz_, 16); }
                    if (i == 1) SST(18);
                    if (vt) MT(0);
                    u32x4 vf[2][4];
#pragma unroll
                    for (int t = 0; t < 2; ++t)
#pragma unroll
                        for (int ks = 0; ks < 4; ++ks) vf[t][ks] = *(const u32x4*)(tokL + (i & 1) * 32 * QS + (16 * t + fr) * QS + ks * 64 + fq * 16);
                    const unsigned ztb = O_Z + (unsigned)(jo * ZTSEG) * 2u + (unsigned)(10 * (w - 4) * H_ + hh) * 1024u;
                    f32x4 c[2][2];
                    unsigned hs = hb + (unsigned)(jh * 40960 + (w - 4) * 1024), hd = (unsigned)(L_H + ((b + 1) & 1) * 40960 + (w - 4) * 1024);
                    asm volatile("" : "+s"(hs), "+s"(hd));
#pragma unroll
                    for (int nt = 0; nt < 11; ++nt) {
                        auto& cc = c[nt & 1]; auto& cp = c[(nt + 1) & 1];
#pragma unroll
                        for (int ks = 0; ks < 4; ++ks) {
                            if (nt < 10) { cc[0] = mfma32(vf[0][ks], half8(wt[ks >> 1][nt], ks & 1), ks ? cc[0] : z4); PIN1(cc[0]); }
                            if (nt > 0 && ks >= 1 && ks < 3) { r[2 * ks - 2] = pack2(cp[ks - 1][0], cp[ks - 1][1]); PIN1(r); }
                            if (nt < 10) { cc[1] = mfma32(vf[1][ks], half8(wt[ks >> 1][nt], ks & 1), ks ? cc[1] : z4); PIN1(cc[1]); }
                            if (nt > 0 && ks >= 1 && ks < 3) { r[2 * ks - 1] = pack2(cp[ks - 1][2], cp[ks - 1][3]); PIN1(r); }
                            if (nt > 0 && ks == 2) st128(r, rws, lane * 16u, ztb + (unsigned)((nt - 1) * H_) * 1024u, 16);
                            if (nt < 5 && !(ks & 1) && __builtin_expect(ok, 1)) dma1k(rws, hv, hs + (2 * nt + (ks >> 1)) * 4096u, hd + (2 * nt + (ks >> 1)) * 4096u);
                            if (nt == 5 && ks == 0 && __builtin_expect(ok, 1)) {
                                asm volatile("s_waitcnt vmcnt(14)" ::: "memory");
                                __builtin_amdgcn_raw_buffer_store_b32(fp, rwin, 0u, fz_, 16);
                            }
                            SB();
                        }
                    }
                    if (i == 1) SST(19);
                    if (vt) MT(2);
                    fp = (unsigned)jo;
                    if (__builtin_expect(jo <= ZEARLY || i + 1 == ni || i + 1 == ZLAG || !ok, 0)) {
                        VMWAIT();
                        __builtin_amdgcn_raw_buffer_store_b32(fp, rwin, 0u, fz_, 16);
                    } else asm volatile("s_waitcnt vmcnt(7)" ::: "memory");
                    if (i == 1) SST(20);
                    if (__builtin_expect(!ok, 0)) { wait_h(rws, rwin, fb, jh, hrdy); rows(b); }
                    if (jo < 8) SST(8 + jo);
                    if (vt && (!BSPARSE || jo % 6 == 2)) { const int s = jo; RT(0); RTS(); }
                    if (vt) { MT(3); const int s = i & 63; MTRT(); MTS(); }
                }
            }
            __builtin_amdgcn_s_setprio(0);
            VMWAIT();
            BAR();
        }
    }
    if (bid < 32 || bid >= NQT) {
        LANES();
        const int hh = (bid < 32 ? bid : bid - NQT) & 31;
        u32x4 m0[2];
        get_mem0(m0, tid);
        if (lay == 0 || xt) {
            wait_until(tstart + (bid >= NQT ? DQT : DW1));
            load_w<false>(wt, karg<5>(w >> 2), hh, w, 4 * (w & 3), 2, tokL, gap);
            SST(5);
            if (!xt) settle();
        }
        places();
        const bool qt = bid >= NQT;
        const unsigned long long mq = xt ? MXQ : ~MXQ;
        auto nextq = [&](int j) { return min(seg_after(mq, j), NSEG - 1); };
        const int jq0 = qt ? nextq(1) : 0, jq1 = nextq(jq0);
        put_mem0(m0, memL, tid);
        if (!qt && w == 7) retime();
        if (tid < 4) *(unsigned*)(smem + L_S + tid * 4) = ~0u;
        SST(1);
        soon(jq1);
        SST(2);
        const unsigned so1 = hb + (unsigned)(jq1 * 40960 + w * 1024), lo1 = L_H + 40960 + w * 1024;
        stage_h<5, 8192>(rws, hb + (unsigned)(jq0 * 40960 + w * 1024), lo1 - 40960u, lane);
        BAR0();
        stage_h<5, 8192>(rws, so1, lo1, lane);
        check_h<5, 8192, 5>(rws, hb + (unsigned)(jq0 * 40960 + w * 1024), lo1 - 40960u, lane);
        BAR();
        SST(3);
        int qrdy = 0;
        {
            LANES();
            const unsigned to0 = (unsigned)((w ^ 4) >> 2) * 8192u + (unsigned)((w & 3) * 64 + (lane ^ ((fq & 1) * (w < 4 ? 8 : 5)))) * 16u, to1 = to0 + 4096u, hv = lane * 16u;
            const LAS char* c0 = (const LAS char*)(hL + hoff + ((fq ^ (fr & 7)) << 4));
            const LAS char* c1 = (const LAS char*)(hL + hoff + (((4 + fq) ^ (fr & 7)) << 4));
            u32x4 bb[8], g0, g1;
            asm volatile("" : "=v"(g0), "=v"(g1));
            f32x4 tc[2][2];
            auto pack = [&]() {
                g0 = (u32x4){pack2(tc[0][0][0], tc[0][0][1]), pack2(tc[0][0][2], tc[0][0][3]), pack2(tc[1][0][0], tc[1][0][1]), pack2(tc[1][0][2], tc[1][0][3])};
                g1 = (u32x4){pack2(tc[0][1][0], tc[0][1][1]), pack2(tc[0][1][2], tc[0][1][3]), pack2(tc[1][1][0], tc[1][1][1]), pack2(tc[1][1][2], tc[1][1][3])};
            };
            const LAS char* n0 = c0 + 40960; const LAS char* n1 = c1 + 40960;
            if (!qt) {
                project0(bb, c0, c1);
                for (int j = 0; j < 2; ++j) {
                    project<0>(wt, tc, bb, c0, c1, n0, n1, [&]() { if (j == 0) { check_h<5, 8192, 0>(rws, so1, lo1, lane); BAR(); } }, [](auto) {});
                    pack();
                    *(u32x4*)(tokL + j * 16384 + to0) = g0; *(u32x4*)(tokL + j * 16384 + to1) = g1;
                    c0 = n0; c1 = n1;
                    if (j == 0 && w == 7) __builtin_amdgcn_raw_buffer_store_b32(((4u * (unsigned)__builtin_amdgcn_s_memrealtime() + (lay ? DQ0 : DQ00)) & 0xFFFFFFu) | (unsigned)lay << 30, rwin, two(), W_T + hh * 128u, 16);
                }
                project_end(bb);
                VMWAIT();
            } else {
                project0(bb, c0, c1);
                unsigned hs = hb + (unsigned)(jq1 * 40960 + w * 1024), hd = (unsigned)(L_H + 40960 + w * 1024), fv = tid == 0 ? 0u : 0x40000000u;
                const unsigned fq_ = W_FQ + fb + (unsigned)(hh * 128 + xt * 64);
                const int jl = 63 - __builtin_clzll(mq);
                for (int jp = 1, j = jq0, jn = jq1;; jp = j, j = jn, jn = nextq(jn)) {
                    MT(0);
                    project<0>(wt, tc, bb, c0, c1, n0, n1,
                        [&]() {
                            if (__builtin_expect(hrdy <= nextq(jn), 0)) hrdy = min(see_h(g0), nextq(jn) + 17);
                            hs = hrdy > nextq(jn) ? hb + (unsigned)(nextq(jn) * 40960 + w * 1024) : 0x40000000u; hd = (unsigned)(2 * L_H + 40960 + 2 * w * 1024) - hd;
                            unsigned fj = (unsigned)jp;
                            asm volatile("" : "+s"(hs), "+s"(hd));
                            PIN1(fj);
                            MT(1);
                            if (__builtin_expect(j == jq0, 0)) check_h<5, 8192, 0>(rws, so1, lo1, lane);
                            VMWAIT();
                            BAR();
                            MT(2);
                            if (w == 0) __builtin_amdgcn_raw_buffer_store_b32(fj, rwin, fv, fq_, 16);
                        },
                        [&](auto uu) {
                            constexpr int u = decltype(uu)::v;
                            if (u == QEU && __builtin_expect(j <= QE2, 0)) {
                                asm volatile("s_waitcnt vmcnt(3)\n\ts_barrier" ::: "memory");
                                if (w == 0) __builtin_amdgcn_raw_buffer_store_b32((unsigned)jp, rwin, fv, fq_, 16);
                            }
                            if (u == 20 && __builtin_expect(hrdy <= nextq(jn), 0)) g0 = ask_h(rwin, fb, nextq(jn) + 16);
                            if (u == 34 || u == 38 || u == 2 || u == 6 || u == 10) { constexpr int t = ((u + 6) % 40) / 4; dma1k(rws, hv, hs + t * 8192u, hd + t * 8192u); }
                        });
                    pack();
                    const unsigned qs = O_QK + (unsigned)(j * H_ + hh) * 16384u;
                    st128(g0, rws, to0, qs, 16);
                    st128(g1, rws, to1, qs, 16);
                    { const LAS char* x = c0; c0 = n0; n0 = x; x = c1; c1 = n1; n1 = x; }
                    if (__builtin_expect(j <= QEARLY || j == jl, 0)) {
                        VMWAIT();
                        BAR0();
                        if (tid == 0) __builtin_amdgcn_raw_buffer_store_b32((unsigned)j, rwin, 0u, fq_, 16);
                        if (j == jl) break;
                    }
                    if (__builtin_expect(hrdy <= nextq(jn), 0)) {
                        wait_h(rws, rwin, fb, nextq(jn), hrdy);
                        hs = hb + (unsigned)(nextq(jn) * 40960 + w * 1024);
                        dma1k(rws, hv, hs, hd); dma1k(rws, hv, hs + 8192u, hd + 8192u);
                    }
                    MT(3);
                    if (j < 8) SST(8 + j);
                    if (!BSPARSE || j % 6 == 2) { const int s = j; RT(0); RTS(); MTRT(); MTS(); }
                }
                project_end(bb);
                VMWAIT();
                BAR();
            }
        }
        SST(4);
        auto steps = [&](auto cc) {
        constexpr int C = decltype(cc)::v >> 1, R = decltype(cc)::v & 1;
        LANES();
        const LAS char* ka = (const LAS char*)(smem + L_K + (fr & 1) * TQS + (4 * fq + (fr >> 2)) * 8);
        const LAS char* qa = (const LAS char*)(smem + L_Q + (fr & 1) * 544 + ((fr >> 1) & 1) * 1152 + (16 * fq + 2 * (fr >> 2)) * 4);
        LAS char* qw = (LAS char*)(C < 2 ? smem + L_Q + (fq & 1) * 544 + (fq >> 1) * 1152 + (32 * w + 8 * (fr >> 2) + bs2(fr & 3)) * 4
                                         : smem + L_K + (fq >> 1) * TQS + (32 * (w & 3) + fr) * 8 + (fq & 1) * 4);
        const LAS char* mp0 = (const LAS char*)(memL + ma0);
        const int z = 16 * (fr >> 3) + 4 * (fr & 3) + 2 * ((fr >> 2) & 1);
        const int ko = ((z >> 4) * 256 + fq * 16 + ((z & 15) ^ ((fq & 1) * 5))) * 16;
        unsigned pa0 = (4u * C + 2u * (hh >> 4)) * PAIR + (unsigned)((hh & 15) * 512 + R * 256 + lane * 4);
        unsigned pa2 = ((8u + (fr >> 3)) * PAIR + C * 4096u + (unsigned)((hh >> 1) * 256 + R * 128 + (hh & 1) * 64 + fq * 16 + (fr & 6) * 2)) | (fr & 1) << 30;
        PIN2(ka, qw); PIN1(mp0);
        constexpr bool TK = C == 2 && R == 1;
        Tk tk = {0u, 0u, 0x40000000u, nullptr};
        if (TK) { tk = tk_init(); __builtin_amdgcn_s_setprio(TKP); }
        if (C < 2) { PIN2(qa, pa0); PIN1(pa2); }
        unsigned fqv = w == 4 ? (lane & 1) * 64u : 0x40000000u;
        if (C == 2 && !TK) PIN1(fqv);
        int qf = 1, tbo = 0;
        for (int s = 0; s < NSEG; ++s) {
            RT(0);
            const char* tb = tokL + tbo;
            unsigned pb = __builtin_amdgcn_readfirstlane(s + 1 < NSEG ? W_P + (unsigned)(s & 3) * 10u * PAIR + nh : 0x40000000u);
            unsigned pb1 = pb + PAIR;
            asm volatile("" : "+s"(pb), "+s"(pb1));
            if (s == 0) BAR();
            u32x8 kx[2][2];
            auto keys = [&]() {
#pragma unroll
                for (int u = 0; u < 4; ++u) kx[u >> 1][u & 1] = cat8(*(const u32x4*)(tb + (ko ^ ((u >> 1) * 16)) + (u & 1) * 2048), *(const u32x4*)(tb + (ko ^ ((u >> 1) * 16)) + (u & 1) * 2048 + 1024));
                PIN2(kx[0][0], kx[0][1]); PIN2(kx[1][0], kx[1][1]);
            };
            if (C < 2) keys();
            bool redo = false;
            unsigned sf = 0;
            do {
                unsigned lim = s > 0 ? poll_lim(budget) : ~0u;
                unsigned long long sus;
                f32x4 sc;
                if (C < 2) {
                    f32x4 s0, s1, s2;
                    unsigned late;
                    asm volatile(
                        ".irp r,232,234,236,238,240,242,244,246,248,250\n\tv_mov_b64 v[\\r:\\r+1], 0\n\t.endr\n\t"
                        "s_setprio 2\n\t"
                        "s_waitcnt lgkmcnt(0)\n\t"
                        "s_barrier\n\t"
                        MTA(0)
                        "ds_read_b128 v[208:211], %[mp] offset:0\n\t"
                        "ds_read_b128 v[212:215], %[mp] offset:1024\n\t"
                        "ds_read_b128 v[216:219], %[mp] offset:2048\n\t"
                        "s_barrier\n\t"
                        "ds_read_b128 v[220:223], %[mp] offset:3072\n\t"
                        "s_waitcnt lgkmcnt(3)\n\t"
                        "v_smfmac_f32_16x16x64_bf16 v[232:235], v[208:211], %[w00], %[ix]\n\t"
                        "v_smfmac_f32_16x16x64_bf16 v[236:239], v[208:211], %[w10], %[ix]\n\t"
                        "ds_read_b128 v[224:227], %[mp] offset:4096\n\t"
                        "s_waitcnt lgkmcnt(3)\n\t"
                        "v_smfmac_f32_16x16x64_bf16 v[232:235], v[212:215], %[w01], %[ix]\n\t"
                        "v_smfmac_f32_16x16x64_bf16 v[236:239], v[212:215], %[w11], %[ix]\n\t"
                        "s_barrier\n\t"
                        MTA(1)
                        "ds_read_b128 v[228:231], %[mp] offset:5120\n\t"
                        "s_waitcnt lgkmcnt(3)\n\t"
                        "v_smfmac_f32_16x16x64_bf16 v[232:235], v[216:219], %[w02], %[ix]\n\t"
                        "v_smfmac_f32_16x16x64_bf16 v[236:239], v[216:219], %[w12], %[ix]\n\t"
                        "ds_read_b128 v[208:211], %[mp] offset:6144\n\t"
                        "s_waitcnt lgkmcnt(3)\n\t"
                        "v_smfmac_f32_16x16x64_bf16 v[232:235], v[220:223], %[w03], %[ix]\n\t"
                        "v_smfmac_f32_16x16x64_bf16 v[236:239], v[220:223], %[w13], %[ix]\n\t"
                        "ds_read_b128 v[212:215], %[mp] offset:7168\n\t"
                        "s_waitcnt lgkmcnt(3)\n\t"
                        "v_smfmac_f32_16x16x64_bf16 v[232:235], v[224:227], %[w04], %[ix]\n\t"
                        "v_smfmac_f32_16x16x64_bf16 v[236:239], v[224:227], %[w14], %[ix]\n\t"
                        "ds_read_b128 v[216:219], %[mp] offset:8192\n\t"
                        "s_waitcnt lgkmcnt(3)\n\t"
                        "v_smfmac_f32_16x16x64_bf16 v[232:235], v[228:231], %[w05], %[ix]\n\t"
                        "v_smfmac_f32_16x16x64_bf16 v[236:239], v[228:231], %[w15], %[ix]\n\t"
                        "ds_read_b128 v[220:223], %[mp] offset:9216\n\t"
                        "s_waitcnt lgkmcnt(3)\n\t"
                        "v_smfmac_f32_16x16x64_bf16 v[232:235], v[208:211], %[w06], %[ix]\n\t"
                        "v_smfmac_f32_16x16x64_bf16 v[236:239], v[208:211], %[w16], %[ix]\n\t"
                        "ds_read_b32 v207, v250 offset:%[ls]\n\t"
                        "s_waitcnt lgkmcnt(3)\n\t"
                        "v_smfmac_f32_16x16x64_bf16 v[232:235], v[212:215], %[w07], %[ix]\n\t"
                        "v_smfmac_f32_16x16x64_bf16 v[236:239], v[212:215], %[w17], %[ix]\n\t"
                        "s_waitcnt lgkmcnt(0)\n\t"
                        "v_smfmac_f32_16x16x64_bf16 v[232:235], v[216:219], %[w08], %[ix]\n\t"
                        "v_readfirstlane_b32 %[lt], v207\n\t"
                        "v_smfmac_f32_16x16x64_bf16 v[232:235], v[220:223], %[w09], %[ix]\n\t"
                        "v_smfmac_f32_16x16x64_bf16 v[236:239], v[216:219], %[w18], %[ix]\n\t"
                        "v_smfmac_f32_16x16x64_bf16 v[236:239], v[220:223], %[w19], %[ix]\n\t"
                        "s_setprio " STR(QP) "\n\t"
                        "s_nop 5\n\t"
                        "v_add_f32 v232, v232, v234\n\t"
                        "v_add_f32 v233, v233, v235\n\t"
                        "v_add_f32 v236, v236, v238\n\t"
                        "v_add_f32 v237, v237, v239\n\t"
                        "v_cvt_pk_bf16_f32 v232, v232, v233\n\t"
                        "v_cvt_pk_bf16_f32 v236, v236, v237\n\t"
                        "ds_write2_b32 %[qw], v232, v236 offset1:4\n\t"
                        MTA(2)
                        : [lt] "=&s"(late), "=&{v[240:243]}"(s0), "=&{v[244:247]}"(s1), "=&{v[248:251]}"(s2)
                        : [mp] "v"(mp0), [qw] "v"(qw), [ix] "v"(sidx), [ls] "n"(L_S), [w00] "v"(wt[0][0]), [w10] "v"(wt[1][0]), [w01] "v"(wt[0][1]), [w11] "v"(wt[1][1]), [w02] "v"(wt[0][2]), [w12] "v"(wt[1][2]), [w03] "v"(wt[0][3]), [w13] "v"(wt[1][3]), [w04] "v"(wt[0][4]), [w14] "v"(wt[1][4]), [w05] "v"(wt[0][5]), [w15] "v"(wt[1][5]), [w06] "v"(wt[0][6]), [w16] "v"(wt[1][6]), [w07] "v"(wt[0][7]), [w17] "v"(wt[1][7]), [w08] "v"(wt[0][8]), [w18] "v"(wt[1][8]), [w09] "v"(wt[0][9]), [w19] "v"(wt[1][9])
                        : "v207", "v208", "v209", "v210", "v211", "v212", "v213", "v214", "v215", "v216", "v217", "v218", "v219", "v220", "v221", "v222", "v223", "v224", "v225", "v226", "v227", "v228", "v229", "v230", "v231", "v232", "v233", "v234", "v235", "v236", "v237", "v238", "v239", "memory");
                    if (__builtin_expect(late == (unsigned)s, 0)) keys();
#define TAIL(A0, A2, B0, B2, C0, C2) \
                        ".if " STR(BQN) "==0\n\t" \
                        "s_setprio 3\n\t" \
                        "s_waitcnt vmcnt(0) lgkmcnt(0)\n\t" \
                        "s_barrier\n\t" \
                        MTA(3) \
                        "ds_read_b64_tr_b16 v[208:209], %[qa]\n\t" \
                        "ds_read_b64_tr_b16 v[210:211], %[qa] offset:32\n\t" \
                        "ds_read_b64_tr_b16 v[212:213], %[qa] offset:256\n\t" \
                        "ds_read_b64_tr_b16 v[214:215], %[qa] offset:288\n\t" \
                        "ds_read_b64_tr_b16 v[216:217], %[ka] offset:0\n\t" \
                        "ds_read_b64_tr_b16 v[218:219], %[ka] offset:128\n\t" \
                        "ds_read_b64_tr_b16 v[220:221], %[ka] offset:256\n\t" \
                        "ds_read_b64_tr_b16 v[222:223], %[ka] offset:384\n\t" \
                        "ds_read_b64_tr_b16 v[224:225], %[ka] offset:512\n\t" \
                        "ds_read_b64_tr_b16 v[226:227], %[ka] offset:640\n\t" \
                        "s_waitcnt lgkmcnt(8)\n\t" \
                        "v_smfmac_f32_16x16x64_bf16 v[240:243], v[208:211], %[k00], %[ix]\n\t" \
                        "ds_read_b64_tr_b16 v[228:229], %[ka] offset:768\n\t" \
                        "s_waitcnt lgkmcnt(7)\n\t" \
                        "v_smfmac_f32_16x16x64_bf16 v[240:243], v[212:215], %[k01], %[ix]\n\t" \
                        "ds_read_b64_tr_b16 v[230:231], %[ka] offset:896\n\t" \
                        "v_smfmac_f32_16x16x64_bf16 v[244:247], v[208:211], %[k10], %[ix]\n\t" \
                        "v_smfmac_f32_16x16x64_bf16 v[244:247], v[212:215], %[k11], %[ix]\n\t" \
                        "s_nop 4\n\t" \
                        ".elseif " STR(TPRE) "==0\n\t" \
                        "s_waitcnt lgkmcnt(0)\n\t" \
                        "s_barrier\n\t" \
                        "ds_read_b64_tr_b16 v[208:209], %[qa]\n\t" \
                        "ds_read_b64_tr_b16 v[210:211], %[qa] offset:32\n\t" \
                        "ds_read_b64_tr_b16 v[212:213], %[qa] offset:256\n\t" \
                        "ds_read_b64_tr_b16 v[214:215], %[qa] offset:288\n\t" \
                        "s_setprio 3\n\t" \
                        "s_waitcnt vmcnt(0) lgkmcnt(0)\n\t" \
                        "s_barrier\n\t" \
                        MTA(3) \
                        "ds_read_b64_tr_b16 v[216:217], %[ka] offset:0\n\t" \
                        "ds_read_b64_tr_b16 v[218:219], %[ka] offset:128\n\t" \
                        "ds_read_b64_tr_b16 v[220:221], %[ka] offset:256\n\t" \
                        "ds_read_b64_tr_b16 v[222:223], %[ka] offset:384\n\t" \
                        "v_smfmac_f32_16x16x64_bf16 v[240:243], v[208:211], %[k00], %[ix]\n\t" \
                        "ds_read_b64_tr_b16 v[224:225], %[ka] offset:512\n\t" \
                        "ds_read_b64_tr_b16 v[226:227], %[ka] offset:640\n\t" \
                        "v_smfmac_f32_16x16x64_bf16 v[240:243], v[212:215], %[k01], %[ix]\n\t" \
                        "ds_read_b64_tr_b16 v[228:229], %[ka] offset:768\n\t" \
                        "ds_read_b64_tr_b16 v[230:231], %[ka] offset:896\n\t" \
                        "v_smfmac_f32_16x16x64_bf16 v[244:247], v[208:211], %[k10], %[ix]\n\t" \
                        "v_smfmac_f32_16x16x64_bf16 v[244:247], v[212:215], %[k11], %[ix]\n\t" \
                        "s_nop 3\n\t" \
                        ".else\n\t" \
                        "s_waitcnt lgkmcnt(0)\n\t" \
                        "s_barrier\n\t" \
                        "ds_read_b64_tr_b16 v[208:209], %[qa]\n\t" \
                        "ds_read_b64_tr_b16 v[210:211], %[qa] offset:32\n\t" \
                        "ds_read_b64_tr_b16 v[212:213], %[qa] offset:256\n\t" \
                        "ds_read_b64_tr_b16 v[214:215], %[qa] offset:288\n\t" \
                        "s_setprio 0\n\t" \
                        "s_waitcnt vmcnt(0) lgkmcnt(0)\n\t" \
                        "v_smfmac_f32_16x16x64_bf16 v[240:243], v[208:211], %[k00], %[ix]\n\t" \
                        "v_smfmac_f32_16x16x64_bf16 v[240:243], v[212:215], %[k01], %[ix]\n\t" \
                        "s_setprio 3\n\t" \
                        "s_barrier\n\t" \
                        MTA(3) \
                        "ds_read_b64_tr_b16 v[216:217], %[ka] offset:0\n\t" \
                        "ds_read_b64_tr_b16 v[218:219], %[ka] offset:128\n\t" \
                        "ds_read_b64_tr_b16 v[220:221], %[ka] offset:256\n\t" \
                        "ds_read_b64_tr_b16 v[222:223], %[ka] offset:384\n\t" \
                        "v_smfmac_f32_16x16x64_bf16 v[244:247], v[208:211], %[k10], %[ix]\n\t" \
                        "ds_read_b64_tr_b16 v[224:225], %[ka] offset:512\n\t" \
                        "ds_read_b64_tr_b16 v[226:227], %[ka] offset:640\n\t" \
                        "v_smfmac_f32_16x16x64_bf16 v[244:247], v[212:215], %[k11], %[ix]\n\t" \
                        "ds_read_b64_tr_b16 v[228:229], %[ka] offset:768\n\t" \
                        "ds_read_b64_tr_b16 v[230:231], %[ka] offset:896\n\t" \
                        ".endif\n\t" \
                        "v_add_f32 v232, " A0 ", " A2 "\n\t" \
                        "v_mul_f32 v236, 0x3e0293ee, v232\n\t" \
                        "v_exp_f32 v236, v236\n\t" \
                        "s_waitcnt lgkmcnt(4)\n\t" \
                        "v_smfmac_f32_16x16x64_bf16 v[248:251], v[208:211], v[216:223], %[ix]\n\t" \
                        "s_waitcnt lgkmcnt(0)\n\t" \
                        "v_smfmac_f32_16x16x64_bf16 v[248:251], v[212:215], v[224:231], %[ix]\n\t" \
                        "v_add_f32 v233, " B0 ", " B2 "\n\t" \
                        "v_mul_f32 v237, 0x3e0293ee, v233\n\t" \
                        "v_exp_f32 v237, v237\n\t" \
                        "s_nop 0\n\t" \
                        "v_add_f32 v239, v236, v237\n\t" \
                        "s_nop 1\n\t" \
                        "v_add_f32_dpp v239, v239, v239 row_ror:8 row_mask:0xf bank_mask:0xf\n\t" \
                        MTA(4) \
                        "v_add_f32 v234, " C0 ", " C2 "\n\t" \
                        "v_mul_f32 v238, 0x3e0293ee, v234\n\t" \
                        "v_exp_f32 v238, v238\n\t" \
                        "s_nop 0\n\t" \
                        "v_add_f32 v239, v239, v238\n\t" \
                        "v_mov_b32_dpp v235, v238 quad_perm:[1,1,3,3] row_mask:0xf bank_mask:0xf\n\t" \
                        "s_nop 0\n\t" \
                        "v_add_f32_dpp v239, v239, v239 row_ror:4 row_mask:0xf bank_mask:0xf\n\t" \
                        "s_nop 1\n\t" \
                        "v_add_f32_dpp v239, v239, v239 row_ror:2 row_mask:0xf bank_mask:0xf\n\t" \
                        "s_nop 1\n\t" \
                        "v_add_f32_dpp v239, v239, v239 row_ror:1 row_mask:0xf bank_mask:0xf\n\t" \
                        "v_rcp_f32 v207, v239\n\t" \
                        "v_add_u32 v239, 0xe8800000, v239\n\t" \
                        "v_mul_f32 v236, v236, v207\n\t" \
                        "v_cmp_lt_u32 vcc, %[rg], v239\n\t" \
                        "v_mul_f32 v237, v237, v207\n\t" \
                        "v_cvt_pk_bf16_f32 v236, v236, v237\n\t" \
                        "s_cbranch_vccnz 3f\n\t" \
                        MTA(5) \
                        "buffer_store_dword v236, %[pa], %[rw], %[p0] offen sc1\n\t" \
                        "v_mul_f32 v238, v238, v207\n\t" \
                        "v_mul_f32 v235, v235, v207\n\t" \
                        "buffer_store_dword v236, %[pa], %[rw], %[p1] offen sc1\n\t" \
                        "v_cvt_pk_bf16_f32 v238, v238, v235\n\t" \
                        "buffer_store_dword v238, %[pm], %[rw], %[p0] offen sc1\n\t" \
                        "3:\n\t" \
                        "s_mov_b64 %[sus], vcc\n\t" \
                        "s_setprio 0\n\t"
#define TAILIO : [sus] "=&s"(sus), "=&{v[232:235]}"(sc), "+{v[240:243]}"(s0), "+{v[244:247]}"(s1), "+{v[248:251]}"(s2) \
                        : [qa] "v"(qa), [ka] "v"(ka), [ix] "v"(sidx), [pa] "v"(pa0), [pm] "v"(pa2), [rw] "s"(rwin), [p0] "s"(pb), [p1] "s"(pb1), [rg] "s"(SRG), \
                          [k00] "v"(kx[0][0]), [k01] "v"(kx[0][1]), [k10] "v"(kx[1][0]), [k11] "v"(kx[1][1]) \
                        : "v207", "v208", "v209", "v210", "v211", "v212", "v213", "v214", "v215", "v216", "v217", "v218", "v219", "v220", "v221", "v222", "v223", "v224", "v225", "v226", "v227", "v228", "v229", "v230", "v231", "v236", "v237", "v238", "v239", "vcc", "memory"
                    if (R == 0) asm volatile(TAIL("v240", "v242", "v244", "v246", "v248", "v250") TAILIO);
                    else asm volatile(TAIL("v241", "v243", "v245", "v247", "v249", "v251") TAILIO);
                    RT(4);
                    MT(6);
                } else {
                    f32x16 m0;
                    f32x8 m1, a01;
                    asm volatile("v_mov_b64 v[200:201], 0\n\tv_mov_b64 v[202:203], 0\n\tv_mov_b64 v[204:205], 0\n\tv_mov_b64 v[206:207], 0" : "={v[200:207]}"(a01));
                    unsigned sr = TK ? tro + (unsigned)(s & 3) * 2u * PAIR : qrdy < min(s + 3, NSEG - 1) ? W_FQ + fb + (unsigned)hh * 128u : 0x40000000u;
                    unsigned tg = (unsigned)(s + 64 * lay) << 24, so = s > 0 ? W_T + (unsigned)(s & 3) * 2u * PAIR + (unsigned)hh * 128u : 0x40000000u;
                    asm volatile("" : "+s"(lim), "+s"(sr));
                    if (TK) { asm volatile("" : "+s"(tg), "+s"(so)); fetch(tk, s, !redo, lim, DQC, 0u, false); }
                    else asm volatile("s_waitcnt lgkmcnt(0)\n\ts_barrier\n\ts_barrier\n\ts_waitcnt vmcnt(0)\n\ts_barrier" ::: "memory");
                    unsigned tb = ck.ma - DQC;
                    if (TK) {
                        asm volatile(
                        "s_setprio 1\n\t"
                        "s_waitcnt lgkmcnt(0)\n\t"
                        "ds_read2st64_b32 v[208:209], %[sp] offset1:16\n\t"
                        "ds_read_b32 v210, %[sp] offset:6144\n\t"
                        "ds_read_b128 v[212:215], %[mp] offset:0\n\t"
                        "ds_read_b128 v[216:219], %[mp] offset:1024\n\t"
                        "ds_read_b128 v[220:223], %[mp] offset:2048\n\t"
                        "ds_read_b128 v[224:227], %[mp] offset:3072\n\t"
                        "s_sub_u32 %[tb], %[tr], %[tb]\n\t"
                        "s_mul_i32 %[tb], %[tb], %[qc]\n\t"
                        "s_lshr_b32 %[tb], %[tb], 16\n\t"
                        "s_add_u32 %[tb], %[tb], %[ta]\n\t"
                        "s_and_b32 %[tb], %[tb], 0xffffff\n\t"
                        "s_or_b32 %[tb], %[tb], %[tg]\n\t"
                        "v_mov_b32 v211, %[tb]\n\t"
                        "s_waitcnt lgkmcnt(4)\n\t"
                        "v_max3_u32 v208, v208, v209, v210\n\t"
                        "v_cmp_lt_u32 vcc, %[lim], v208\n\t"
                        "ds_read_b128 v[228:231], %[mp] offset:4096\n\t"
                        "s_cmp_eq_u64 vcc, 0\n\t"
                        "s_cselect_b32 %[so], %[so], 2.0\n\t"
                        "buffer_store_dword v211, %[tw], %[rw], %[so] offen sc1\n\t"
                        "ds_read_b128 v[232:235], %[mp] offset:5120\n\t"
                        : [tb] "+s"(tb), [so] "+s"(so), "=&{v[212:227]}"(m0), "=&{v[228:235]}"(m1)
                        : [mp] "v"(mp0), [sp] "v"(tk.sp), [tr] "s"((unsigned)tref), [lim] "s"(lim), [qc] "s"(ck.qpc), [ta] "s"(ck.ta), [tg] "s"(tg), [tw] "v"(tk.tw), [rw] "s"(rwin)
                        : "v208", "v209", "v210", "v211", "vcc", "scc", "memory");
                    } else asm volatile(
                        "s_setprio 1\n\t"
                        "ds_read_b128 v[212:215], %[mp] offset:0\n\t"
                        "ds_read_b128 v[216:219], %[mp] offset:1024\n\t"
                        "ds_read_b128 v[220:223], %[mp] offset:2048\n\t"
                        "ds_read_b128 v[224:227], %[mp] offset:3072\n\t"
                        "ds_read_b128 v[228:231], %[mp] offset:4096\n\t"
                        "ds_read_b128 v[232:235], %[mp] offset:5120\n\t"
                        : "=&{v[212:227]}"(m0), "=&{v[228:235]}"(m1) : [mp] "v"(mp0) : "memory");
                    asm volatile(
                        ".if !%[tk]\n\tbuffer_load_dword %[sf], %[sl], %[rw], %[sr] offen sc1\n\t.endif\n\t"
                        MTA(4)
                        "s_waitcnt lgkmcnt(5)\n\t"
                        "v_smfmac_f32_16x16x64_bf16 v[200:203], v[212:215], %[w00], %[ix]\n\t"
                        "ds_read_b128 v[236:239], %[mp] offset:6144\n\t"
                        "v_smfmac_f32_16x16x64_bf16 v[204:207], v[212:215], %[w10], %[ix]\n\t"
                        "ds_read_b128 v[240:243], %[mp] offset:7168\n\t"
                        "s_waitcnt lgkmcnt(6)\n\t"
                        "v_smfmac_f32_16x16x64_bf16 v[200:203], v[216:219], %[w01], %[ix]\n\t"
                        "ds_read_b128 v[244:247], %[mp] offset:8192\n\t"
                        "v_smfmac_f32_16x16x64_bf16 v[204:207], v[216:219], %[w11], %[ix]\n\t"
                        "ds_read_b128 v[248:251], %[mp] offset:9216\n\t"
                        "s_waitcnt lgkmcnt(4)\n\t"
                        "v_smfmac_f32_16x16x64_bf16 v[200:203], v[220:223], %[w02], %[ix]\n\t"
                        "v_smfmac_f32_16x16x64_bf16 v[204:207], v[220:223], %[w12], %[ix]\n\t"
                        "v_smfmac_f32_16x16x64_bf16 v[200:203], v[224:227], %[w03], %[ix]\n\t"
                        "v_smfmac_f32_16x16x64_bf16 v[204:207], v[224:227], %[w13], %[ix]\n\t"
                        "v_smfmac_f32_16x16x64_bf16 v[200:203], v[228:231], %[w04], %[ix]\n\t"
                        "v_smfmac_f32_16x16x64_bf16 v[204:207], v[228:231], %[w14], %[ix]\n\t"
                        "v_smfmac_f32_16x16x64_bf16 v[200:203], v[232:235], %[w05], %[ix]\n\t"
                        "v_smfmac_f32_16x16x64_bf16 v[204:207], v[232:235], %[w15], %[ix]\n\t"
                        "s_waitcnt lgkmcnt(0)\n\t"
                        "v_smfmac_f32_16x16x64_bf16 v[200:203], v[236:239], %[w06], %[ix]\n\t"
                        "v_smfmac_f32_16x16x64_bf16 v[204:207], v[236:239], %[w16], %[ix]\n\t"
                        BQ(3)
                        "v_smfmac_f32_16x16x64_bf16 v[200:203], v[240:243], %[w07], %[ix]\n\t"
                        "v_smfmac_f32_16x16x64_bf16 v[204:207], v[240:243], %[w17], %[ix]\n\t"
                        BQ(5)
                        "v_smfmac_f32_16x16x64_bf16 v[200:203], v[244:247], %[w08], %[ix]\n\t"
                        "v_smfmac_f32_16x16x64_bf16 v[200:203], v[248:251], %[w09], %[ix]\n\t"
                        BQ(7)
                        "v_smfmac_f32_16x16x64_bf16 v[204:207], v[244:247], %[w18], %[ix]\n\t"
                        "v_smfmac_f32_16x16x64_bf16 v[204:207], v[248:251], %[w19], %[ix]\n\t"
                        BQ(9)
                        "s_nop 5\n\t"
                        "v_pk_add_f32 v[200:201], v[200:201], v[202:203]\n\t"
                        "v_cvt_pk_bf16_f32 v200, v200, v201\n\t"
                        "v_pk_add_f32 v[204:205], v[204:205], v[206:207]\n\t"
                        "v_cvt_pk_bf16_f32 v201, v204, v205\n\t"
                        "ds_write2_b32 %[qw], v200, v201 offset1:32\n\t"
                        ".if %[tk]\n\tbuffer_load_dword %[sf], %[sl], %[rw], %[sr] offen sc1\n\t.endif\n\t"
                        "v_cmp_u_f32 %[sus], v204, v205\n\t"
                        MTA(2)
                        "s_waitcnt lgkmcnt(0)\n\t"
                        "s_barrier\n\t"
                        "s_setprio 0\n\t"
                        : [sus] "=&s"(sus), [sf] "+&v"(sf), "+{v[200:207]}"(a01)
                        : [mp] "v"(mp0), [qw] "v"(qw), [ix] "v"(sidx), [sl] "v"(TK ? tk.sl : fqv), [tk] "n"(TK ? 1 : 0), [sr] "s"(sr), [rw] "s"(rwin), "{v[212:227]}"(m0), "{v[228:235]}"(m1), [w00] "v"(wt[0][0]), [w10] "v"(wt[1][0]), [w01] "v"(wt[0][1]), [w11] "v"(wt[1][1]), [w02] "v"(wt[0][2]), [w12] "v"(wt[1][2]), [w03] "v"(wt[0][3]), [w13] "v"(wt[1][3]), [w04] "v"(wt[0][4]), [w14] "v"(wt[1][4]), [w05] "v"(wt[0][5]), [w15] "v"(wt[1][5]), [w06] "v"(wt[0][6]), [w16] "v"(wt[1][6]), [w07] "v"(wt[0][7]), [w17] "v"(wt[1][7]), [w08] "v"(wt[0][8]), [w18] "v"(wt[1][8]), [w09] "v"(wt[0][9]), [w19] "v"(wt[1][9])
                        : "v236", "v237", "v238", "v239", "v240", "v241", "v242", "v243", "v244", "v245", "v246", "v247", "v248", "v249", "v250", "v251", "memory");
                    MT(3);
                }
                redo = false;
                if (__builtin_expect(sus != 0, 0)) {
                    redo = again(TK, s, lim, mp0, tk.sp);
                    if (!redo && C < 2) {
                        const float c = 0.08838834764831845f * 1.44269504088896340736f;
                        float mx = fmx(fmx(sc[0], sc[1]), sc[2]);
                        mx = fmx(mx, dppf<0x128>(mx)); mx = fmx(mx, dppf<0x124>(mx)); mx = fmx(mx, dppf<0x122>(mx)); mx = fmx(mx, dppf<0x121>(mx));
                        const float e0 = __builtin_amdgcn_exp2f((sc[0] - mx) * c), e1 = __builtin_amdgcn_exp2f((sc[1] - mx) * c), e2 = __builtin_amdgcn_exp2f((sc[2] - mx) * c);
                        float d = e0 + e1;
                        d += dppf<0x128>(d); d += e2; d += dppf<0x124>(d); d += dppf<0x122>(d); d += dppf<0x121>(d);
                        d = __builtin_amdgcn_rcpf(d);
                        const unsigned p0 = nosent1(pack2(e0 * d, e1 * d)), p1 = nosent1(pack2(e2 * d, dppf<0xF5>(e2) * d));
                        __builtin_amdgcn_raw_buffer_store_b32(p0, rwin, pa0, pb, 16); __builtin_amdgcn_raw_buffer_store_b32(p0, rwin, pa0, pb1, 16);
                        __builtin_amdgcn_raw_buffer_store_b32(p1, rwin, pa2, pb, 16);
                    }
                }
            } while (__builtin_expect(redo, 0));
            if (C == 2) asm volatile("s_waitcnt vmcnt(0)" : "+v"(sf) :: "memory");
            if (__builtin_expect(mb != 0, 0)) { mp0 -= mb; tk.sp -= mb; mb = 0; }
            if (s < 8) SST(8 + s);
            if (s >= 56) SST(s - 40);
            if (C + R == 1) {
                if (s > 0) {
                    __builtin_amdgcn_s_sleep(DDMA);
                    unsigned l2 = fresh_lane(), s1;
                    asm volatile("v_mov_b32 %0, -1" : "=v"(s1));
                    const unsigned base = W_P + (unsigned)((s - 1) & 3) * 10u * PAIR + nh;
                    asm volatile("s_waitcnt vmcnt(3)" ::: "memory");
                    st128(((u32x4){s1, s1, s1, s1}), rwin, (l2 >> 5) * PAIR + (l2 & 31u) * 16u, base + (unsigned)(R * 4 + 2 * (hh >> 4)) * PAIR + (unsigned)(hh & 15) * 512u, 16);
                    if (R) __builtin_amdgcn_raw_buffer_store_b64((u32x2){s1, s1}, rwin, (l2 >> 5) * PAIR + (l2 & 16u) * 256u + (l2 & 8u) * 16u + (l2 & 7u) * 8u, base + 8u * PAIR + (unsigned)((hh >> 1) * 256 + (hh & 1) * 64), 16);
                    else __builtin_amdgcn_raw_buffer_store_b32(s1, rwin, two(), W_T + (unsigned)((s + 2) & 3) * 2u * PAIR + (unsigned)hh * 128u, 16);
                }
            } else if (TK) {
                if (s + 1 < NSEG) sched2(tk, s, sf);
            } else if (C == 2 && w == 4) {
                qrdy = max(qrdy, seg_done(MXQ, __builtin_amdgcn_readlane(sf, 0), __builtin_amdgcn_readlane(sf, 1)));
                if (__builtin_expect(qf <= s, 0) && s + 1 < NSEG) {
                    for (unsigned spins = 0; qrdy <= s;) {
                        asm volatile("" ::: "memory");
                        const int f = (int)__builtin_amdgcn_raw_buffer_load_b32(rwin, fqv, W_FQ + fb + (unsigned)hh * 128u, 16);
                        qrdy = seg_done(MXQ, __builtin_amdgcn_readlane(f, 0), __builtin_amdgcn_readlane(f, 1));
                        if (qrdy > s) break;
                        FC(fc2);
                        if (spin_fail(spins, rws)) { qrdy = NSEG; break; }
                        __builtin_amdgcn_s_sleep(2);
                    }
                    *(volatile LAS int*)(smem + L_S) = s + 1;
                }
                while (qf < min(min(s + 2, qrdy), NSEG - 1)) {
                    ++qf;
                    const unsigned so = O_QK + (unsigned)((qf * H_ + hh) * 16384), lo = (unsigned)(L_TOK + qf % 3 * 16384), l2 = fresh_lane();
#pragma unroll
                    for (int i = 0; i < 4; ++i) dma4k(rws, l2 * 16u, so + i * 4096u, lo + i * 4096u);
                }
            } else {
                if (C == 2 && s + 1 < NSEG) __builtin_amdgcn_s_sleep(DPT);
                const float scale = 0.08838834764831845f, l2e = 1.44269504088896340736f * (NORND ? scale : 1.f);
                const int tq = (w >> 1) & 1, qh = w & 1;
                f32x4 sc[3] = {z4, z4, z4};
                u32x4 kt[2][4], qf[4], kf[4];
                const unsigned l2 = fresh_lane(), kto = ((l2 & 8u) * 512u + (l2 & 51u) * 16u + (l2 & 4u) * 32u) ^ ((l2 & 16u) * 5u), qto = ((l2 & 55u) * 16u + qh * 128u) ^ ((l2 & 16u) * 8u);
#pragma unroll
                for (int ks = 0; ks < 4; ++ks) {
                    qf[ks] = *(const u32x4*)(tb + 8192 + (tq * 4 + ks) * 1024 + qto);
                    kt[0][ks] = *(const u32x4*)(tb + ks * 1024 + kto);
                    kt[1][ks] = *(const u32x4*)(tb + ks * 1024 + (kto ^ 64u));
                    SB();
                }
#pragma unroll
                for (int ks = 0; ks < 4; ++ks) {
                    sc[0] = mfma32(kt[0][ks], qf[ks], sc[0]); PIN1(sc[0]);
                    kf[ks] = ldtr2(ka + ks * 256, ka + ks * 256 + 128);
                    SB();
                    sc[1] = mfma32(kt[1][ks], qf[ks], sc[1]); PIN1(sc[1]);
                }
#pragma unroll
                for (int ks = 0; ks < 4; ++ks) sc[2] = mfma32(kf[ks], qf[ks], sc[2]);
                const bool hi = (l2 & 8u) != 0u;
                float v[6];
#pragma unroll
                for (int r = 0; r < 4; ++r) v[r] = hi ? sc[1][r] : sc[0][r];
                v[4] = hi ? sc[2][2] : sc[2][0]; v[5] = hi ? sc[2][3] : sc[2][1];
#pragma unroll
                for (int r = 0; r < 6; r += 2) {
                    if (NORND) break;
                    const unsigned b0 = pack2(v[r], v[r + 1]);
                    const unsigned b1 = pack2(lo2f(b0) * scale, hi2f(b0) * scale);
                    v[r] = lo2f(b1); v[r + 1] = hi2f(b1);
                }
                const int lim = 8 * (w & 3) + (int)(l2 & 7u) - 8 * (int)(l2 >> 4) - (int)((l2 & 8u) >> 1);
#pragma unroll
                for (int r = 0; r < 4; ++r) if (r > lim) v[r] = NORND ? -1000000000.0f / scale : bfr(-1000000000.0f);
                float mx = fmx(fmx(fmx(fmx(fmx(v[0], v[1]), v[2]), v[3]), v[4]), v[5]);
                mx = xmax3216(fmx(mx, ror8f(mx)));
                const float mb = -mx * l2e;
#pragma unroll
                for (int r = 0; r < 6; ++r) v[r] = __builtin_amdgcn_exp2f(fmaf(v[r], l2e, mb));
                float sum = fmaf(v[4] + v[5], 0.5f, (v[0] + v[1]) + (v[2] + v[3]));
                sum = xsum3216(sum + ror8f(sum));
                const float inv = __builtin_amdgcn_rcpf(sum);
                const unsigned p0 = pack2(v[0] * inv, v[1] * inv), p1 = nosent1(pack2(v[2] * inv, v[3] * inv)), p2 = nosent1(pack2(v[4] * inv, v[5] * inv));
                const unsigned base = s + 2 < NSEG ? O_PT + (unsigned)(s * PTSEG) * 2u : W_PL + fb + (s & 1) * 5u * PAIR;
                const __amdgpu_buffer_rsrc_t rp = s + 2 < NSEG ? rws : rwin;
                const unsigned oob = (l2 & 8u) << 27;
                st128(((u32x4){p0, p1, dppu<0x128>(p0), dppu<0x128>(p1)}), rp, (l2 * 16u) | oob, base + (unsigned)((hh * 2 + tq) * 1024 + qh * 128), 16);
                __builtin_amdgcn_raw_buffer_store_b64((u32x2){p2, dppu<0x128>(p2)}, rp, ((l2 & 7u) * 16u + (l2 >> 4) * 8u) | oob | (l2 & 32u) << 25, base + 65536u + (unsigned)((tq * 32 + hh) * 256 + qh * 128), 16);
            }
            tbo = tbo == 32768 ? 0 : tbo + 16384;
            if (__builtin_expect(budget <= 0, 0)) __builtin_amdgcn_raw_buffer_store_b32(1u, rws, 0u, 0u, 16);
            RT(5);
            RTS();
            MT(7);
            MTS();
        }
        };
        if (qt) {} else if (w == 0) steps(IC<0>()); else if (w == 1) steps(IC<1>()); else if (w == 2) steps(IC<2>()); else if (w == 3) steps(IC<3>()); else if (w < 7) steps(IC<4>()); else steps(IC<5>());
        __builtin_amdgcn_s_setprio(0);
    }
    SST(31);
    }
    } else
#pragma nounroll
    for (; lay < 2; ++lay) {
    fresh();
    SST(0);
    {
        LANES();
        const int G = bid < NWRK ? bid - 96 : bid == NWRK ? 80 : -1;
        const int fi = bid >= NWRK ? 255 - bid : bid < NREC ? 16 + G / 10 + 4 * (G % 10) : 99;
        const bool first = fi < 32;
        auto a2_done = [&]() { if (w == 0) __builtin_amdgcn_raw_buffer_store_b32(0u, rwin, 0u, W_FB + fb + (unsigned)((G >> 2) * 128 + (G & 3) * 4), 16); };
        u32x2 tag = {0u, 0u};
        if (lay == 0) tag = get_tag();
        const unsigned fv = lay ? min(lane, 6) * 1024u : (lane & 7) * 1024u + (lane & 8) * 8u, fs = (lay ? W_FA : W_ST) + w * 128u;
        auto ask = [&]() { asm volatile("" ::: "memory"); return __builtin_amdgcn_raw_buffer_load_b128(rwin, fv, fs, 16); };
        auto miss = [&](u32x4 f) { return (unsigned)__ballot(((f[0] ^ tag[0]) | (f[1] ^ tag[1]) | (f[2] ^ tag[0]) | (f[3] ^ tag[1])) != 0u); };
        if (G < 0) for (unsigned spins = 0; spins < SPIN_CAP && (miss(ask()) & 255u); ++spins) __builtin_amdgcn_s_sleep(ASLP);
        BAR();
        RowTw tw;
        if (G < 0) tw = row_tw(lane);
        else {
            unsigned m = 0xFFFFu, e = ~0u;
            u32x4 f = SENT;
            a2_unit(rws, G, smem, w, lane, lay == 0, [&](int r) {
                if (r == 5) {
                    if (lay) { f = ask(); e = __builtin_amdgcn_raw_buffer_load_b32(rwin, 0u, W_E + (E_A1 + w) * 128u, 16); }
                    SB();
                    tw = row_tw(lane);
                    return;
                }
                if (r == 6) { if (lay) m = miss(f); else if (DFA) __builtin_amdgcn_s_sleep(DFA); }
                if (r == 7 && lay) await(E_A1 + w, 1, 0, e);
                else for (unsigned spins = 0; spins < SPIN_CAP && (m & (r == 6 ? 127u : r == 7 ? 128u : 0xFF00u)); ++spins) {
                    if (spins) __builtin_amdgcn_s_sleep(ASLP);
                    m = miss(ask());
                }
                if (r == 7) SST(5);
            }, [&](int k) {
                if (k == 4 && w == 7 && blockIdx.x < 136) due(E_A2 + (blockIdx.x & 7), lay ? DA2 : DA20);
                if (k == 7 && YSLP) __builtin_amdgcn_s_sleep(YSLP);
                if (bid < NREC) SST(8 + k);
            });
            SST(6);
            if (!first) VMWAIT();
            BAR0();
            if (!first) a2_done();
            SST(7);
        }
        auto columns = [&](int all) {
            if (w == 7) await(E_A2 + fb / 128u, 8, all);
            if (w == 7 && all) for (unsigned spins = 0;;) {
                asm volatile("" ::: "memory");
                const u32x4 f = __builtin_amdgcn_raw_buffer_load_b128(rwin, (unsigned)min(lane, 20) * 128u, W_FB + fb, 16);
                if (!__any((f[0] | (lane < 20 ? f[1] | f[2] | f[3] : 0u)) != 0u)) break;
                if (spin_fail(spins, rws)) break;
                __builtin_amdgcn_s_sleep(ASLP);
            }
            BAR0();
            if (!all || !first) SST(3);
        };
        const __amdgpu_buffer_rsrc_t rx = mkrsrc(lay ? karg<0>() + OFF_X2 : karg<3>(), S_ * D_ * 2);
        if (first) {
            const unsigned fo = fb + (unsigned)((fi >> 2) * 128 + (fi & 3) * 4);
            a3_units<4>(rws, rx, fi * 8, 0, lay, smem, tw, w, lane, [&](int k) {
                if (k < 0) { if (G < 0) columns(0); } else if (fi < 16) SST(16 + k);
                if (k == 0 && G >= 0) a2_done();
                if (k == 0 && w == 7 && fi < 4) due(E_A3 + fi, lay ? DA3 : DA30);
            }, [&](int) { if (tid == 0) __builtin_amdgcn_raw_buffer_store_b32(0u, rwin, 0u, W_H + fo, 16); });
            SST(4);
        }
        if (lay == 0 && bid < NREC) config();
        if (bid >= NREC && bid < NREC + 32) {
            const int u0 = (bid - NREC) / 8 + 4 * ((bid - NREC) % 8);
            columns(DFB + u0 * DST);
            a3_units<8>(rws, rx, (16 + u0) * 16, 512, lay, smem, tw, w, lane, [](int) {}, [&](int r) {
                if (tid == 0) __builtin_amdgcn_raw_buffer_store_b64((u32x2){0u, 0u}, rwin, 0u, W_H + fb + (unsigned)((r >> 5) * 128 + (r & 16) / 2), 16);
                if (r < 768) SST(2);
            });
            VMWAIT();
        }
        SST(1);
        places();
    }
    const __amdgpu_buffer_rsrc_t rout = mkrsrc(lay ? karg<4>() : karg<0>() + OFF_X2, S_ * D_ * 2);
    if (bid < NREC) {
        LANES();
        const int o = bid - 96, hg = w & 3;
        const unsigned ztoff = (unsigned)((o * H_ + 8 * hg) * 64 + lane) * 16u;
        const unsigned zoff = zpair(o / 10, o % 10) + (unsigned)(lane * 16);
        const unsigned poff = (4u * (mc >> 1) + 2u * (hg >> 1) + (mc & 1u)) * PAIR + nh + (unsigned)((hg & 1) * 4096 + (fq >> 1) * 512 + (fr >> 2) * 128 + (fr & 1) * 64 + (fq & 1) * 32 + (fr & 2) * 8);
        const unsigned pmoff = (8u + (mc & 1u)) * PAIR + nh + (mc >> 1) * 4096u + (unsigned)(fq * 256 + (fr >> 3) * 128 + (fr & 2) * 32 + (fr & 4) * 8 + (fr & 1) * 16);
        const unsigned fzo = (unsigned)(8 * hg + (lane & 7)) * 128u + (unsigned)(o / 10) * 4u + (lane & 8) * 8u;
        const unsigned moff = (o < 32 ? (unsigned)o * 256u : 4u * PAIR + (unsigned)(o - 32) * 256u) + nh + (unsigned)(lane * 4);
        const unsigned mc2 = o < 32 ? 2u * PAIR : 2048u;
        char* op = smem + 8192;
        SST(2);
        if (w == 7) clk_init(ck);
        if (w < 4) {
            u32x8 zf[4];
            int zr = -1;
            unsigned zso = O_Z;
            const unsigned ra = (o < 32 ? (unsigned)fq * PAIR + (unsigned)o * 256u : (4u + (fq & 1)) * PAIR + (unsigned)(fq >> 1) * 2048u + (unsigned)(o - 32) * 256u) + nh + (unsigned)(fr * 16);
            auto zt_miss = [&](int f, int s) { return __any(f < s && ((lane >> 3) & 1) == (int)((MXV >> s) & 1)); };
            auto rows = [&](int e) __attribute__((always_inline)) {
            unsigned pl = W_PL + fb + e * 5u * PAIR;
            asm volatile("" : "+s"(pl));
            f32x4 c[2];
            for (unsigned spins = 0;;) {
                asm volatile("" ::: "memory");
                u32x4 pt[16], ptm[2][2], zm[2];
#pragma unroll
                for (int q = 0; q < 2; ++q) zm[q] = __builtin_amdgcn_raw_buffer_load_b128(rwin, zoff + hg * 2048u + q * 1024u, zb, 16);
#pragma unroll
                for (int q = 0; q < 16; ++q) pt[q] = __builtin_amdgcn_raw_buffer_load_b128(rwin, (unsigned)(16 * hg) * 1024u + lane * 16u, pl + q * 1024u, 16);
#pragma unroll
                for (int q = 0; q < 4; ++q) ptm[q >> 1][q & 1] = __builtin_amdgcn_raw_buffer_load_b128(rwin, (unsigned)(hg * 2048 + lane * 16), pl + 65536u + (q >> 1) * 8192u + (q & 1) * 1024u, 16);
                unsigned m = max(smax(zm[0]), smax(zm[1]));
#pragma unroll
                for (int t = 0; t < 2; ++t) {
                    c[t] = z4;
#pragma unroll
                    for (int q = 0; q < 8; ++q) { c[t] = mfma32(half8(zf[q >> 1], q & 1), pt[2 * q + t], c[t]); m = max(m, smax(pt[2 * q + t])); }
#pragma unroll
                    for (int q = 0; q < 2; ++q) { c[t] = mfma32((u32x4){zm[q][0], zm[q][2], zm[q][1], zm[q][3]}, ptm[t][q], c[t]); m = max(m, smax(ptm[t][q])); }
                }
                if (!__any(m == 0xFFFFFFFFu)) break;
                if (spin_fail(spins, rws)) break;
            }
#pragma unroll
            for (int t = 0; t < 2; ++t)
#pragma unroll
                for (int r = 0; r < 4; ++r) *(float*)(op + e * 8192 + hg * 2048 + (16 * t + fr) * 64 + pi16(4 * fq + r) * 4) = c[t][r];
            };
            await(E_A3 + fb / 128u, 4, lay ? DZ0 : DZ00);
            for (int s = 0;; ++s) {
                const unsigned pb = W_P + (unsigned)(s & 3) * 10u * PAIR;
                if (__builtin_expect(zr < s, 0)) for (unsigned spins = 0;;) {
                    asm volatile("" ::: "memory");
                    if (!zt_miss((int)__builtin_amdgcn_raw_buffer_load_b32(rwin, fzo, W_FZ + fb, 16), s)) break;
                    FC(fc2);
                    if (spin_fail(spins, rws)) break;
                    __builtin_amdgcn_s_sleep(ZPS);
                }
                if (s == 0) SST(1);
                asm volatile(
                    "buffer_load_dwordx4 v[160:163], %[zt], %[rs], %[so] offen sc1\n\t"
                    "buffer_load_dwordx4 v[164:167], %[zt], %[rs], %[so] offen offset:1024 sc1\n\t"
                    "buffer_load_dwordx4 v[168:171], %[zt], %[rs], %[so] offen offset:2048 sc1\n\t"
                    "buffer_load_dwordx4 v[172:175], %[zt], %[rs], %[so] offen offset:3072 sc1\n\t"
                    "buffer_load_dwordx4 v[176:179], %[zt], %[rs], %[s4] offen sc1\n\t"
                    "buffer_load_dwordx4 v[180:183], %[zt], %[rs], %[s4] offen offset:1024 sc1\n\t"
                    "buffer_load_dwordx4 v[184:187], %[zt], %[rs], %[s4] offen offset:2048 sc1\n\t"
                    "buffer_load_dwordx4 v[188:191], %[zt], %[rs], %[s4] offen offset:3072 sc1"
                    : "=&{v[160:167]}"(zf[0]), "=&{v[168:175]}"(zf[1]), "=&{v[176:183]}"(zf[2]), "=&{v[184:191]}"(zf[3])
                    : [zt] "v"(ztoff), [rs] "s"(rws), [so] "s"(zso), [s4] "s"(zso + 4096u) : "memory");
                zso = O_Z + (unsigned)((s + 1) * ZTSEG) * 2u;
                if (__builtin_expect(budget <= 0, 0)) __builtin_amdgcn_raw_buffer_store_b32(1u, rws, 0u, 0u, 16);
                RT(6);
                if (s + 1 == NSEG) break;
                const unsigned spa = (unsigned)((s & 1) * 2048 + (w < 3 ? w * 512 : 0) + lane * 8);
#define OST(r)
                unsigned long long t0_, t1_, t2_, t3_, t4_ = 0, bad;
                int tries = budget;
                unsigned po = poff, pm = pmoff, x0 = 4u * PAIR, x1 = 4096u;
                const unsigned mb = W_MEM + (unsigned)((s + 1) & 3) * 6u * PAIR;
                if (w < 3) asm volatile(
                    "s_barrier\n\t"
                    OST("%[t0]")
                    "v_mov_b64 v[96:97], 0\n\t"
                    "v_mov_b64 v[98:99], 0\n\t"
                    "s_barrier\n\t"
                    "1:\n\t"
                    "buffer_load_dwordx4 v[208:211], %[po], %[rw], %[pb] offen sc1\n\t"
                    "buffer_load_dwordx4 v[212:215], %[po], %[rw], %[pb] offen offset:1024 sc1\n\t"
                    "buffer_load_dwordx4 v[216:219], %[po], %[rw], %[pb] offen offset:2048 sc1\n\t"
                    "buffer_load_dwordx4 v[220:223], %[po], %[rw], %[pb] offen offset:3072 sc1\n\t"
                    OST("%[t1]")
                    "s_waitcnt vmcnt(3)\n\t"
                    OST("%[t3]")
                    "v_smfmac_f32_16x16x64_bf16 v[96:99], v[208:211], v[160:167], %[ix]\n\t"
                    "s_waitcnt vmcnt(2)\n\t"
                    "v_smfmac_f32_16x16x64_bf16 v[96:99], v[212:215], v[168:175], %[ix]\n\t"
                    "s_waitcnt vmcnt(1)\n\t"
                    "v_smfmac_f32_16x16x64_bf16 v[96:99], v[216:219], v[176:183], %[ix]\n\t"
                    "s_waitcnt vmcnt(0)\n\t"
                    "v_smfmac_f32_16x16x64_bf16 v[96:99], v[220:223], v[184:191], %[ix]\n\t"
                    OST("%[t2]")
                    "s_nop 7\n\t"
                    "v_pk_add_f32 v[96:97], v[96:97], v[98:99]\n\t"
                    "ds_write_b64 %[sp], v[96:97]\n\t"
                    "v_cmp_u_f32 vcc, v96, v97\n\t"
                    "s_cbranch_vccz 2f\n\t"
                    "v_max3_u32 v100, v208, v208, v209\n\t"
                    "v_max3_u32 v100, v100, v210, v211\n\t"
                    "v_max3_u32 v100, v100, v212, v213\n\t"
                    "v_max3_u32 v100, v100, v214, v215\n\t"
                    "v_max3_u32 v100, v100, v216, v217\n\t"
                    "v_max3_u32 v100, v100, v218, v219\n\t"
                    "v_max3_u32 v100, v100, v220, v221\n\t"
                    "v_max3_u32 v100, v100, v222, v223\n\t"
                    "v_cmp_eq_u32 vcc, -1, v100\n\t"
                    "s_cbranch_vccz 2f\n\t"
                    "s_sub_i32 %[tr], %[tr], 1\n\t"
                    "s_cmp_lt_i32 %[tr], 1\n\t"
                    "s_cbranch_scc1 2f\n\t"
                    "v_xor_b32 %[po], %[x0], %[po]\n\t"
                    "s_xor_b32 %[x0], %[x0], 0x14000\n\t"
                    "s_waitcnt lgkmcnt(0)\n\t"
                    "v_mov_b64 v[96:97], 0\n\t"
                    "v_mov_b64 v[98:99], 0\n\t"
                    "s_branch 1b\n\t"
                    "2:\n\t"
                    "s_waitcnt lgkmcnt(0)\n\t"
                    "s_barrier\n\t"
                    "s_sleep 3\n\t"
                    : [tr] "+s"(tries), [po] "+v"(po), [x0] "+s"(x0), [t0] "=&s"(t0_), [t1] "=&s"(t1_), [t2] "=&s"(t2_), [t3] "=&s"(t3_)
                    : [sp] "v"(spa), [ix] "v"(sidx), [rw] "s"(rwin), [pb] "s"(pb), "{v[160:167]}"(zf[0]), "{v[168:175]}"(zf[1]), "{v[176:183]}"(zf[2]), "{v[184:191]}"(zf[3])
                    : "v96", "v97", "v98", "v99", "v100", "v208", "v209", "v210", "v211", "v212", "v213", "v214", "v215", "v216", "v217", "v218", "v219", "v220", "v221", "v222", "v223", "vcc", "scc", "memory");
                else asm volatile(
                    "s_barrier\n\t"
                    "buffer_load_dwordx4 v[128:131], %[zo], %[rw], %[zb] offen sc1\n\t"
                    "buffer_load_dwordx4 v[132:135], %[zo], %[rw], %[zb] offen offset:1024 sc1\n\t"
                    "buffer_load_dwordx4 v[136:139], %[zo], %[rw], %[zb] offen offset:2048 sc1\n\t"
                    "buffer_load_dwordx4 v[140:143], %[zo], %[rw], %[zb] offen offset:3072 sc1\n\t"
                    "buffer_load_dwordx4 v[144:147], %[zo], %[rw], %[z4] offen sc1\n\t"
                    "buffer_load_dwordx4 v[148:151], %[zo], %[rw], %[z4] offen offset:1024 sc1\n\t"
                    "buffer_load_dwordx4 v[152:155], %[zo], %[rw], %[z4] offen offset:2048 sc1\n\t"
                    "buffer_load_dwordx4 v[156:159], %[zo], %[rw], %[z4] offen offset:3072 sc1\n\t"
                    OST("%[t0]")
                    "v_mov_b64 v[96:97], 0\n\t"
                    "v_mov_b64 v[98:99], 0\n\t"
                    "s_barrier\n\t"
                    "s_sleep %[fd]\n\t"
                    "buffer_load_dwordx4 v[208:211], %[po], %[rw], %[pb] offen sc1\n\t"
                    "buffer_load_dwordx4 v[212:215], %[po], %[rw], %[pb] offen offset:1024 sc1\n\t"
                    "buffer_load_dwordx4 v[216:219], %[po], %[rw], %[pb] offen offset:2048 sc1\n\t"
                    "buffer_load_dwordx4 v[220:223], %[po], %[rw], %[pb] offen offset:3072 sc1\n\t"
                    "buffer_load_dwordx4 v[224:227], %[pm], %[rw], %[pb] offen sc1\n\t"
                    "buffer_load_dwordx4 v[228:231], %[pm], %[rw], %[pb] offen offset:1024 sc1\n\t"
                    "buffer_load_dwordx4 v[232:235], %[pm], %[rw], %[pb] offen offset:2048 sc1\n\t"
                    "buffer_load_dwordx4 v[236:239], %[pm], %[rw], %[pb] offen offset:3072 sc1\n\t"
                    OST("%[t1]")
                    "s_waitcnt vmcnt(8)\n\t"
                    "v_max3_u32 v101, v129, v131, v133\n\t"
                    "v_max3_u32 v101, v101, v135, v137\n\t"
                    "v_max3_u32 v101, v101, v139, v141\n\t"
                    "v_max3_u32 v101, v101, v143, v145\n\t"
                    "v_max3_u32 v101, v101, v147, v149\n\t"
                    "v_max3_u32 v101, v101, v151, v153\n\t"
                    "v_max3_u32 v101, v101, v155, v157\n\t"
                    "v_max_u32 v101, v101, v159\n\t"
                    "v_cmp_eq_u32 vcc, -1, v101\n\t"
                    "s_cbranch_vccz 3f\n\t"
                    "buffer_load_dwordx4 v[128:131], %[zo], %[rw], %[zb] offen sc1\n\t"
                    "buffer_load_dwordx4 v[132:135], %[zo], %[rw], %[zb] offen offset:1024 sc1\n\t"
                    "buffer_load_dwordx4 v[136:139], %[zo], %[rw], %[zb] offen offset:2048 sc1\n\t"
                    "buffer_load_dwordx4 v[140:143], %[zo], %[rw], %[zb] offen offset:3072 sc1\n\t"
                    "buffer_load_dwordx4 v[144:147], %[zo], %[rw], %[z4] offen sc1\n\t"
                    "buffer_load_dwordx4 v[148:151], %[zo], %[rw], %[z4] offen offset:1024 sc1\n\t"
                    "buffer_load_dwordx4 v[152:155], %[zo], %[rw], %[z4] offen offset:2048 sc1\n\t"
                    "buffer_load_dwordx4 v[156:159], %[zo], %[rw], %[z4] offen offset:3072 sc1\n\t"
                    "s_waitcnt vmcnt(0)\n\t"
                    "v_max3_u32 v101, v129, v131, v133\n\t"
                    "v_max3_u32 v101, v101, v135, v137\n\t"
                    "v_max3_u32 v101, v101, v139, v141\n\t"
                    "v_max3_u32 v101, v101, v143, v145\n\t"
                    "v_max3_u32 v101, v101, v147, v149\n\t"
                    "v_max3_u32 v101, v101, v151, v153\n\t"
                    "v_max3_u32 v101, v101, v155, v157\n\t"
                    "v_max_u32 v101, v101, v159\n\t"
                    "3:\n\t"
                    "v_permlane16_swap_b32 v128, v129\n\t"
                    "v_permlane16_swap_b32 v130, v131\n\t"
                    "v_permlane16_swap_b32 v132, v133\n\t"
                    "v_permlane16_swap_b32 v134, v135\n\t"
                    "v_permlane16_swap_b32 v136, v137\n\t"
                    "v_permlane16_swap_b32 v138, v139\n\t"
                    "v_permlane16_swap_b32 v140, v141\n\t"
                    "v_permlane16_swap_b32 v142, v143\n\t"
                    "v_permlane16_swap_b32 v144, v145\n\t"
                    "v_permlane16_swap_b32 v146, v147\n\t"
                    "v_permlane16_swap_b32 v148, v149\n\t"
                    "v_permlane16_swap_b32 v150, v151\n\t"
                    "v_permlane16_swap_b32 v152, v153\n\t"
                    "v_permlane16_swap_b32 v154, v155\n\t"
                    "v_permlane16_swap_b32 v156, v157\n\t"
                    "v_permlane16_swap_b32 v158, v159\n\t"
                    "s_mov_b32 vcc_lo, 0x00ff00ff\n\t"
                    "s_mov_b32 vcc_hi, 0x00ff00ff\n\t"
                    "s_waitcnt vmcnt(7)\n\t"
                    OST("%[t3]")
                    "v_smfmac_f32_16x16x64_bf16 v[96:99], v[208:211], v[160:167], %[ix]\n\t"
                    "s_waitcnt vmcnt(6)\n\t"
                    "v_smfmac_f32_16x16x64_bf16 v[96:99], v[212:215], v[168:175], %[ix]\n\t"
                    "s_waitcnt vmcnt(5)\n\t"
                    "v_smfmac_f32_16x16x64_bf16 v[96:99], v[216:219], v[176:183], %[ix]\n\t"
                    "s_waitcnt vmcnt(4)\n\t"
                    "v_smfmac_f32_16x16x64_bf16 v[96:99], v[220:223], v[184:191], %[ix]\n\t"
                    OB(4)
                    "s_waitcnt vmcnt(3)\n\t"
                    "v_smfmac_f32_16x16x64_bf16 v[96:99], v[224:227], v[128:135], %[ix]\n\t"
                    OB(5)
                    "s_waitcnt vmcnt(2)\n\t"
                    "v_smfmac_f32_16x16x64_bf16 v[96:99], v[228:231], v[136:143], %[ix]\n\t"
                    OB(6)
                    "s_waitcnt vmcnt(1)\n\t"
                    "v_smfmac_f32_16x16x64_bf16 v[96:99], v[232:235], v[144:151], %[ix]\n\t"
                    "s_waitcnt vmcnt(0)\n\t"
                    "v_smfmac_f32_16x16x64_bf16 v[96:99], v[236:239], v[152:159], %[ix]\n\t"
                    OST("%[t2]")
                    "s_waitcnt lgkmcnt(0)\n\t"
                    "v_pk_add_f32 v[102:103], v[102:103], v[104:105]\n\t"
                    "v_pk_add_f32 v[102:103], v[102:103], v[106:107]\n\t"
                    "s_nop 4\n\t"
                    "6:\n\t"
                    "v_pk_add_f32 v[96:97], v[96:97], v[102:103]\n\t"
                    "v_pk_add_f32 v[96:97], v[96:97], v[98:99]\n\t"
                    "v_cmp_u_f32 %[bad], v96, v97\n\t"
                    "s_nop 0\n\t"
                    "v_cndmask_b32_dpp v98, v97, v96, vcc row_ror:8 row_mask:0xf bank_mask:0xf\n\t"
                    "s_not_b64 vcc, vcc\n\t"
                    "v_cndmask_b32_dpp v99, v96, v97, vcc row_ror:8 row_mask:0xf bank_mask:0xf\n\t"
                    "s_cmp_lg_u64 %[bad], 0\n\t"
                    "v_cvt_pk_bf16_f32 v98, v98, v99\n\t"
                    "s_cbranch_scc1 4f\n\t"
                    "2:\n\t"
                    "buffer_store_dword v98, %[mo], %[rw], %[m0] offen sc1\n\t"
                    "buffer_store_dword v98, %[mo], %[rw], %[m1] offen sc1\n\t"
                    "buffer_store_dword v98, %[mo], %[rw], %[m2] offen sc1\n\t"
                    "buffer_store_dword v98, %[mo], %[rw], %[m3] offen sc1\n\t"
                    OST("%[t4]")
                    "s_branch 5f\n\t"
                    "4:\n\t"
                    "v_min_u32 v98, -2, v98\n\t"
                    "v_max3_u32 v100, v101, v208, v209\n\t"
                    "v_max3_u32 v100, v100, v210, v211\n\t"
                    "v_max3_u32 v100, v100, v212, v213\n\t"
                    "v_max3_u32 v100, v100, v214, v215\n\t"
                    "v_max3_u32 v100, v100, v216, v217\n\t"
                    "v_max3_u32 v100, v100, v218, v219\n\t"
                    "v_max3_u32 v100, v100, v220, v221\n\t"
                    "v_max3_u32 v100, v100, v222, v223\n\t"
                    "v_max3_u32 v100, v100, v224, v225\n\t"
                    "v_max3_u32 v100, v100, v226, v227\n\t"
                    "v_max3_u32 v100, v100, v228, v229\n\t"
                    "v_max3_u32 v100, v100, v230, v231\n\t"
                    "v_max3_u32 v100, v100, v232, v233\n\t"
                    "v_max3_u32 v100, v100, v234, v235\n\t"
                    "v_max3_u32 v100, v100, v236, v237\n\t"
                    "v_max3_u32 v100, v100, v238, v239\n\t"
                    "v_cmp_eq_u32 %[bad], -1, v100\n\t"
                    "s_not_b64 vcc, vcc\n\t"
                    "s_cmp_eq_u64 %[bad], 0\n\t"
                    "s_cbranch_scc1 2b\n\t"
                    "s_sub_i32 %[tr], %[tr], 1\n\t"
                    "s_cmp_lt_i32 %[tr], 1\n\t"
                    "s_cbranch_scc1 2b\n\t"
                    "v_xor_b32 %[po], %[x0], %[po]\n\t"
                    "s_xor_b32 %[x0], %[x0], 0x14000\n\t"
                    "v_xor_b32 %[pm], %[x1], %[pm]\n\t"
                    "s_xor_b32 %[x1], %[x1], 0x5000\n\t"
                    "buffer_load_dwordx4 v[128:131], %[zo], %[rw], %[zb] offen sc1\n\t"
                    "buffer_load_dwordx4 v[132:135], %[zo], %[rw], %[zb] offen offset:1024 sc1\n\t"
                    "buffer_load_dwordx4 v[136:139], %[zo], %[rw], %[zb] offen offset:2048 sc1\n\t"
                    "buffer_load_dwordx4 v[140:143], %[zo], %[rw], %[zb] offen offset:3072 sc1\n\t"
                    "buffer_load_dwordx4 v[144:147], %[zo], %[rw], %[z4] offen sc1\n\t"
                    "buffer_load_dwordx4 v[148:151], %[zo], %[rw], %[z4] offen offset:1024 sc1\n\t"
                    "buffer_load_dwordx4 v[152:155], %[zo], %[rw], %[z4] offen offset:2048 sc1\n\t"
                    "buffer_load_dwordx4 v[156:159], %[zo], %[rw], %[z4] offen offset:3072 sc1\n\t"
                    "buffer_load_dwordx4 v[208:211], %[po], %[rw], %[pb] offen sc1\n\t"
                    "buffer_load_dwordx4 v[212:215], %[po], %[rw], %[pb] offen offset:1024 sc1\n\t"
                    "buffer_load_dwordx4 v[216:219], %[po], %[rw], %[pb] offen offset:2048 sc1\n\t"
                    "buffer_load_dwordx4 v[220:223], %[po], %[rw], %[pb] offen offset:3072 sc1\n\t"
                    "buffer_load_dwordx4 v[224:227], %[pm], %[rw], %[pb] offen sc1\n\t"
                    "buffer_load_dwordx4 v[228:231], %[pm], %[rw], %[pb] offen offset:1024 sc1\n\t"
                    "buffer_load_dwordx4 v[232:235], %[pm], %[rw], %[pb] offen offset:2048 sc1\n\t"
                    "buffer_load_dwordx4 v[236:239], %[pm], %[rw], %[pb] offen offset:3072 sc1\n\t"
                    "v_mov_b64 v[96:97], 0\n\t"
                    "v_mov_b64 v[98:99], 0\n\t"
                    "s_waitcnt vmcnt(0)\n\t"
                    "v_max3_u32 v101, v129, v131, v133\n\t"
                    "v_max3_u32 v101, v101, v135, v137\n\t"
                    "v_max3_u32 v101, v101, v139, v141\n\t"
                    "v_max3_u32 v101, v101, v143, v145\n\t"
                    "v_max3_u32 v101, v101, v147, v149\n\t"
                    "v_max3_u32 v101, v101, v151, v153\n\t"
                    "v_max3_u32 v101, v101, v155, v157\n\t"
                    "v_max_u32 v101, v101, v159\n\t"
                    "v_permlane16_swap_b32 v128, v129\n\t"
                    "v_permlane16_swap_b32 v130, v131\n\t"
                    "v_permlane16_swap_b32 v132, v133\n\t"
                    "v_permlane16_swap_b32 v134, v135\n\t"
                    "v_permlane16_swap_b32 v136, v137\n\t"
                    "v_permlane16_swap_b32 v138, v139\n\t"
                    "v_permlane16_swap_b32 v140, v141\n\t"
                    "v_permlane16_swap_b32 v142, v143\n\t"
                    "v_permlane16_swap_b32 v144, v145\n\t"
                    "v_permlane16_swap_b32 v146, v147\n\t"
                    "v_permlane16_swap_b32 v148, v149\n\t"
                    "v_permlane16_swap_b32 v150, v151\n\t"
                    "v_permlane16_swap_b32 v152, v153\n\t"
                    "v_permlane16_swap_b32 v154, v155\n\t"
                    "v_permlane16_swap_b32 v156, v157\n\t"
                    "v_permlane16_swap_b32 v158, v159\n\t"
                    "s_nop 1\n\t"
                    "v_smfmac_f32_16x16x64_bf16 v[96:99], v[208:211], v[160:167], %[ix]\n\t"
                    "v_smfmac_f32_16x16x64_bf16 v[96:99], v[212:215], v[168:175], %[ix]\n\t"
                    "v_smfmac_f32_16x16x64_bf16 v[96:99], v[216:219], v[176:183], %[ix]\n\t"
                    "v_smfmac_f32_16x16x64_bf16 v[96:99], v[220:223], v[184:191], %[ix]\n\t"
                    "v_smfmac_f32_16x16x64_bf16 v[96:99], v[224:227], v[128:135], %[ix]\n\t"
                    "v_smfmac_f32_16x16x64_bf16 v[96:99], v[228:231], v[136:143], %[ix]\n\t"
                    "v_smfmac_f32_16x16x64_bf16 v[96:99], v[232:235], v[144:151], %[ix]\n\t"
                    "v_smfmac_f32_16x16x64_bf16 v[96:99], v[236:239], v[152:159], %[ix]\n\t"
                    "s_nop 7\n\t"
                    "s_branch 6b\n\t"
                    "5:\n\t"
                    : [tr] "+s"(tries), [po] "+v"(po), [pm] "+v"(pm), [x0] "+s"(x0), [x1] "+s"(x1), [bad] "=&s"(bad), [t0] "=&s"(t0_), [t1] "=&s"(t1_), [t2] "=&s"(t2_), [t3] "=&s"(t3_), [t4] "=&s"(t4_)
                    : [zo] "v"(zoff), [sp] "v"(spa), [ix] "v"(sidx), [rw] "s"(rwin), [zb] "s"(zb), [z4] "s"(zb + 4096u), [pb] "s"(pb), [mo] "v"(moff), [fd] "n"(FDL),
                      [m0] "s"(mb), [m1] "s"(mb + PAIR), [m2] "s"(mb + mc2), [m3] "s"(mb + mc2 + PAIR), "{v[160:167]}"(zf[0]), "{v[168:175]}"(zf[1]), "{v[176:183]}"(zf[2]), "{v[184:191]}"(zf[3])
                    : "v96", "v97", "v98", "v99", "v100", "v101", "v102", "v103", "v104", "v105", "v106", "v107", "v128", "v129", "v130", "v131", "v132", "v133", "v134", "v135", "v136", "v137", "v138", "v139", "v140", "v141", "v142", "v143", "v144", "v145", "v146", "v147", "v148", "v149", "v150", "v151", "v152", "v153", "v154", "v155", "v156", "v157", "v158", "v159", "v208", "v209", "v210", "v211", "v212", "v213", "v214", "v215", "v216", "v217", "v218", "v219", "v220", "v221", "v222", "v223", "v224", "v225", "v226", "v227", "v228", "v229", "v230", "v231", "v232", "v233", "v234", "v235", "v236", "v237", "v238", "v239", "vcc", "scc", "memory");
                budget = tries;
                if (w == 3) *(volatile LAS int*)(smem + 4100) = s;
                __builtin_amdgcn_s_sleep(DPS - 4);
                if (w == 3) {
                    st128(SENT, rwin, ra, W_MEM + (unsigned)(s & 3) * 6u * PAIR, 16);
                    if (lane == 0) __builtin_amdgcn_raw_buffer_store_b64((u32x2){(unsigned)s - 1u, (unsigned)max(zr, s)}, rwin, 0u, W_GO + fb + (unsigned)o * 128u, 16);
                }
                for (;;) {
                    const u32x2 x = *(volatile LAS u32x2*)(smem + 4096);
                    zr = __builtin_amdgcn_readfirstlane(x[0]);
                    if (__builtin_amdgcn_readfirstlane(x[1]) == s) break;
                    __builtin_amdgcn_s_sleep(2);
                }
                if (__builtin_expect(s + 2 == NSEG, 0)) { if (DR0) __builtin_amdgcn_s_sleep(DR0); rows(0); VMWAIT(); }
                zb = zb == W_ZM + 2u * NOWN * PAIR ? W_ZM : zb + NOWN * PAIR;
            }
            asm volatile("s_waitcnt vmcnt(0)" : "+{v[160:167]}"(zf[0]), "+{v[168:175]}"(zf[1]), "+{v[176:183]}"(zf[2]), "+{v[184:191]}"(zf[3]) :: "memory");
            BAR();
            BAR0();
            rows(1);
            BAR();
            __builtin_amdgcn_s_sleep(8);
            u32x4 sv = SENT;
            PIN1(sv);
#pragma unroll
            for (int q = 0; q < 4; ++q) st128(sv, rwin, zoff + hg * 2048u + (q & 1) * 1024u, q < 2 ? zb : zb == W_ZM ? W_ZM + 2u * NOWN * PAIR : zb - NOWN * PAIR, 16);
            if (w == 3) st128(sv, rwin, ra, W_MEM + 18u * PAIR, 16);
        } else if (w == 4) {
            const unsigned fo = (unsigned)(lane & 31) * 128u + (unsigned)(o / 10) * 4u + (lane & 32) * 2u;
            int zk = -1;
            if (lane < 2) *(volatile LAS int*)(smem + 4096 + lane * 4) = -1;
            for (int s = 0; s < NSEG; ++s) {
                asm volatile("s_waitcnt lgkmcnt(0)\n\ts_barrier\n\ts_barrier" ::: "memory");
                int f = 0;
                const bool look = zk < min(s + ZLA, NSEG - 1);
                if (look) { __builtin_amdgcn_s_sleep(6); f = (int)__builtin_amdgcn_raw_buffer_load_b32(rwin, fo, W_FZ + fb, 16); }
                SB();
                BAR0();
                if (look) {
                    f = min(f, __builtin_amdgcn_update_dpp(f, f, 0xB1, 0xF, 0xF, false)); f = min(f, __builtin_amdgcn_update_dpp(f, f, 0x4E, 0xF, 0xF, false));
                    f = min(f, __builtin_amdgcn_update_dpp(f, f, 0x141, 0xF, 0xF, false)); f = min(f, __builtin_amdgcn_update_dpp(f, f, 0x140, 0xF, 0xF, false));
                    zk = seg_done(MXV, min(__builtin_amdgcn_readlane(f, 0), __builtin_amdgcn_readlane(f, 16)), min(__builtin_amdgcn_readlane(f, 32), __builtin_amdgcn_readlane(f, 48)));
                    *(volatile LAS int*)(smem + 4096) = zk;
                }
            }
        } else if (w < 7) {
            const unsigned ooff = (unsigned)((S_ - SEG * (7 - w) + (lane >> 1)) * D_ + o * 16 + (lane & 1) * 8) * 2u;
            for (int s = 0; s < 3 * NSEG - 3; ++s) BAR0();
            __builtin_amdgcn_s_sleep(DPS);
            const u32x4 xr = __builtin_amdgcn_raw_buffer_load_b128(rws, ooff, O_X1, 16);
            BAR0();
            if (w == 6) { BAR0(); BAR0(); }
            const char* src = op + (w - 5) * 8192 + lane * 32;
            f32x4 x = *(const f32x4*)src, y = *(const f32x4*)(src + 16);
#pragma unroll
            for (int ww = 1; ww < 4; ++ww) { x += *(const f32x4*)(src + ww * 2048); y += *(const f32x4*)(src + ww * 2048 + 16); }
            const u32x4 r = {pack2(lo2f(xr[0]) + bfr(x[0]), hi2f(xr[0]) + bfr(x[1])), pack2(lo2f(xr[1]) + bfr(x[2]), hi2f(xr[1]) + bfr(x[3])),
                             pack2(lo2f(xr[2]) + bfr(y[0]), hi2f(xr[2]) + bfr(y[1])), pack2(lo2f(xr[3]) + bfr(y[2]), hi2f(xr[3]) + bfr(y[3]))};
            st128(((u32x4){nosent(r[0]), nosent(r[1]), nosent(r[2]), nosent(r[3])}), rout, ooff, 0u, 16);
            if (w == 5) { BAR0(); BAR0(); }
            else if (lay && o == 0) __builtin_amdgcn_raw_buffer_store_b64(get_tag(), rws, 0u, 8u, 16);
        } else {
            Tk tk = tk_init();
            __builtin_amdgcn_s_setprio(3);
            for (int s = 0; s < NSEG; ++s) {
                const unsigned ta0 = ck.ta, mm0 = ck.ma;
                unsigned tp = tpp + lt + xs + M1;
                if (s < SAD) { sched(tk, s); tp = tpp - M2 - XS0 - (lay ? PEN0 : PEN00); }
                if (s + 1 == NSEG && o < 2) due(E_OUT + o, (sx24(tp + ML - 4u * (unsigned)__builtin_amdgcn_s_memrealtime()) >> 2) + (o ? DOU1 : DOUT));
                wait_fine_bar(tp - DZP, ck);
                wait_cyc_bar(cyc_at(ck, sx24(tp + (s + 1 < NSEG ? 0 : ML) - ck.ta)));
                clk_rate(ck, ta0, mm0);
                asm volatile("s_barrier ; 7" ::: "memory");
                if (s >= SAD && s + 1 < NSEG) sched(tk, s);
                RTS();
            }
            __builtin_amdgcn_s_setprio(0);
        }
        SST(23);
    } else if (bid < NREC + 30) {
        LANES();
        const int cg = (bid - NREC) % 10;
        const unsigned ztoff = (unsigned)((4 * cg * H_ + 4 * w) * 64 + lane) * 16u;
        const unsigned ptoff = (unsigned)(8 * w * 64 + lane) * 16u, ptmoff = 65536u + (unsigned)(4 * w * 16 + lane) * 16u;
        const int tk = (w & 3) * 8 + (lane >> 3);
        const unsigned ooff = (unsigned)(tk * D_ + cg * 64 + (lane & 7) * 8) * 2u;
        const unsigned gooff = W_GO + (unsigned)((4 * cg + (lane & 2 ? 3 : 0)) * 128 + (lane & 1) * 4);
        int rdy = -1, go = -1;
        auto ask = [&]() {
            asm volatile("" ::: "memory");
            return (int)__builtin_amdgcn_raw_buffer_load_b32(rwin, gooff, fb, 16);
        };
        auto look = [&](int f) {
            go = min(__builtin_amdgcn_readlane(f, 33), __builtin_amdgcn_readlane(f, 35));
            rdy = max(rdy, max(__builtin_amdgcn_readlane(f, 32), __builtin_amdgcn_readlane(f, 34)));
        };
        auto wait_rdy = [&](int& x, int v, int s) {
            for (unsigned spins = 0; x < v;) {
                look(ask());
                if (x >= v) break;
                if (spin_fail(spins, rws)) break;
                if (s >= NSEG - 5) __builtin_amdgcn_s_sleep(OSLP2); else __builtin_amdgcn_s_sleep(OSLP);
            }
        };
        if (w >= 4) wait_h(rws, rwin, fb, 3, hrdy);
        u32x4 zf[4][4];
        auto load_z = [&](int s) {
#pragma unroll
            for (int q = 0; q < 16; ++q) zf[q >> 2][q & 3] = __builtin_amdgcn_raw_buffer_load_b128(rws, ztoff + (q & 3) * 1024u, O_Z + (unsigned)(s * ZTSEG) * 2u + (q >> 2) * (H_ * 1024u), 16);
            SB();
        };
        wait_rdy(go, (bid - NREC) / 10, 0);
        load_z((bid - NREC) / 10);
        for (int s = (bid - NREC) / 10;; s += 3) {
            u32x4 xr, zx[4], pt[8], ptm[2];
            asm volatile("" : "=v"(xr));
            RT(0);
            wait_rdy(rdy, s, s);
            RT(1);
            if (s >= NSEG - 8 && cg == 0 && w == 0) due(E_OUT + NSEG - 1 - s, DOU2);
            const unsigned ptb = O_PT + (unsigned)(s * PTSEG) * 2u;
#pragma unroll
            for (int q = 0; q < 8; ++q) pt[q] = __builtin_amdgcn_raw_buffer_load_b128(rws, ptoff + q * 1024u, ptb, 16);
#pragma unroll
            for (int q = 0; q < 2; ++q) ptm[q] = __builtin_amdgcn_raw_buffer_load_b128(rws, ptmoff + q * 8192u, ptb, 16);
#pragma unroll
            for (int q = 0; q < 4; ++q) zx[q] = __builtin_amdgcn_raw_buffer_load_b128(rws, tid * 16u, O_Z2 + (unsigned)(s * NOWN + 4 * cg + q) * 8192u, 16);
            if (w >= 4) xr = __builtin_amdgcn_raw_buffer_load_b128(rws, ooff, O_X1 + (unsigned)(s * SEG * D_ * 2), 16);
            const int f = ask();
            SB();
            char* op = smem + (s & 1) * 65536;
            f32x4 c[8];
#pragma unroll
            for (int q = 0; q < 4; ++q)
#pragma unroll
                for (int u = 0; u < 8; ++u) c[u] = mfma32(zf[u >> 1][q], pt[2 * q + (u & 1)], q ? c[u] : z4);
#pragma unroll
            for (int u = 0; u < 8; ++u) {
                const int tq = u & 1, t = u >> 1;
                *(f32x4*)(op + w * 8192 + (16 * tq + fr) * 256 + (((t * 4 + fq) ^ (fr & 7)) << 4)) = mfma32((u32x4){zx[t][0], zx[t][2], zx[t][1], zx[t][3]}, ptm[tq], c[u]);
            }
            look(f);
            const bool pre = go >= s + 3 && s < NSEG - 14;
            if (pre) load_z(s + 3);
            BAR();
            if (lay == 0 && s >= 3 && s < NSEG - 11 && tid == 0) __builtin_amdgcn_raw_buffer_store_b32(0u, rwin, 0u, W_OUT + (unsigned)((s - 3) * 256 + cg * 4), 16);
            if (w >= 4) {
                f32x2 x[4];
#pragma unroll
                for (int q = 0; q < 4; ++q) {
                    const char* sp = op + tk * 256 + ((((lane & 6) * 2 + q) ^ (tk & 7)) << 4) + (lane & 1) * 8;
                    x[q] = *(const f32x2*)sp;
#pragma unroll
                    for (int ww = 1; ww < 8; ++ww) x[q] += *(const f32x2*)(sp + ww * 8192);
                }
                const u32x4 r = {pack2(lo2f(xr[0]) + bfr(x[0][0]), hi2f(xr[0]) + bfr(x[2][0])), pack2(lo2f(xr[1]) + bfr(x[1][0]), hi2f(xr[1]) + bfr(x[3][0])),
                                 pack2(lo2f(xr[2]) + bfr(x[0][1]), hi2f(xr[2]) + bfr(x[2][1])), pack2(lo2f(xr[3]) + bfr(x[1][1]), hi2f(xr[3]) + bfr(x[3][1]))};
                st128(((u32x4){nosent(r[0]), nosent(r[1]), nosent(r[2]), nosent(r[3])}), rout, ooff, (unsigned)(s * SEG * D_ * 2), 16);
            } else if (!pre) __builtin_amdgcn_s_sleep(ODL);
            if (lay == 0 && s >= NSEG - 14 && s < NSEG - 8) {
                VMWAIT();
                BAR0();
                if (tid == 0) __builtin_amdgcn_raw_buffer_store_b32(0u, rwin, 0u, W_OUT + (unsigned)(s * 256 + cg * 4), 16);
            }
            RT(2);
            RTS();
            if (s < 8) SST(8 + s);
            if (s >= 56) SST(s - 40);
            if (s + 3 >= NSEG - 2) break;
            if (!pre) { wait_rdy(go, s + 3, 0); load_z(s + 3); }
        }
    } else if (lay == 0 && bid < NWRK) {
        if (w == 0) for (unsigned spins = 0; (int)__builtin_amdgcn_raw_buffer_load_b32(rwin, 0u, W_E + (E_OUT + 3) * 128u, 16) == -1 && !spin_fail(spins, rws);) {
            __builtin_amdgcn_s_sleep(64);
            asm volatile("" ::: "memory");
        }
    } else if (lay == 0) {
        LANES();
        const RowTw tw = row_tw(lane);
        for (int u = (256 - bid) & 15; u < 256; u += 16) {
            if (w == 0) {
                if (u >= 224) await(E_OUT + (255 - u >> 2), 1, 0);
                else for (unsigned spins = 0;;) {
                    asm volatile("" ::: "memory");
                    const unsigned f = __builtin_amdgcn_raw_buffer_load_b32(rwin, (unsigned)min(lane, 9) * 4u, W_OUT + (unsigned)(u >> 2) * 256u, 16);
                    if (!__any(f != 0u)) break;
                    if (spin_fail(spins, rws)) break;
                    __builtin_amdgcn_s_sleep(WSLP);
                }
            }
            BAR0();
            SST(24);
            const unsigned fo = W_FA + (unsigned)((u >> 2) * 128 + (u & 3) * 4);
            a1_unit<4, 512>(rout, u * 8, u >= 224, rws, smem, tw, w, lane, [&](int k) {
                if (k == 0 && w == 0 && u >= 224 && !(u & 3)) due(E_A1 + (u - 224 >> 2), DA1);
                if (k >= 0) SST(8 + k);
            });
            SST(25);
            if (u >= 224) continue;
            VMWAIT();
            BAR0();
            if (tid == 0) __builtin_amdgcn_raw_buffer_store_b32(0u, rwin, 0u, fo, 16);
        }
    }
    SST(31);
    }
}

constexpr int L_DBG = 0;

extern "C" void kernel_launch(void* const* d_in, const int* in_sizes, int n_in, void* d_out, int out_size, void* d_ws, size_t ws_size,
                              hipStream_t stream) {
    char* ws = (char*)d_ws;
    hipFuncSetAttribute((const void*)b3_kernel, hipFuncAttributeMaxDynamicSharedMemorySize, L_END + L_DBG);
    char* win = (char*)(((uintptr_t)(ws + OFF_WIN) + 0x1FFFFFu) & ~(uintptr_t)0x1FFFFFu);
    RecArgs ra{(unsigned*)ws, win, (const u16*)d_in[4], (const u16*)d_in[0], (u16*)d_out, {(const u16*)d_in[5], (const u16*)d_in[6], (const u16*)d_in[7], (const u16*)d_in[8], (const u16*)d_in[1], (const u16*)d_in[2]}};
    b3_kernel<<<256, 512, L_END + L_DBG, stream>>>(ra);
}
```

```cpp
#include <hip/hip_runtime.h>
#include <hip/hip_bf16.h>
#include <stdint.h>

typedef unsigned short u16;
typedef __attribute__((ext_vector_type(8))) __bf16 bf16x8;
typedef __attribute__((ext_vector_type(4))) short s16x4;
typedef __attribute__((ext_vector_type(4))) float f32x4;
typedef __attribute__((ext_vector_type(8))) float f32x8;
typedef __attribute__((ext_vector_type(16))) float f32x16;
typedef __attribute__((ext_vector_type(4))) unsigned u32x4;
typedef __attribute__((ext_vector_type(2))) unsigned u32x2;
typedef __attribute__((ext_vector_type(8))) unsigned u32x8;
typedef __attribute__((ext_vector_type(16))) __bf16 bf16x16;
typedef __attribute__((ext_vector_type(2))) unsigned short u16x2;
#define LAS __attribute__((address_space(3)))
template <int N> struct IC { static constexpr int v = N; };

constexpr int S_ = 2048, D_ = 640, DF = 321, H_ = 32, DH = 128, HD = 4096, NM = 8, SEG = 32, NSEG = 64;
constexpr int NQKV = 3 * HD;
constexpr int NOWN = 40;
constexpr int ZTR = NSEG;
constexpr size_t ZTSEG = (size_t)NOWN * H_ * 512;
constexpr unsigned PAIR = 16384, HALF = 8192;
constexpr unsigned W_MEM = 0, W_P = W_MEM + 24 * PAIR, W_T = W_P + 40 * PAIR, W_GO = W_T + 8 * PAIR, W_OUT = W_GO + PAIR, W_FQ = W_OUT + 8192, W_FZ = W_OUT + PAIR,
                   W_H = W_FZ + 2 * PAIR, W_FA = W_H + 8192, W_FB = W_GO + 8192, W_E = W_FB + 4096, W_PL = W_H + PAIR, W_FL = W_PL + 10 * PAIR - W_GO, W_PROBE = W_GO + 2 * W_FL,
                   W_ZM = 2u * 1024 * 1024, W_END = W_ZM + 120 * PAIR;
constexpr unsigned W_ST = W_PROBE + 2 * PAIR, W_ROT = W_ST + 8192, W_ARM = W_ROT + 128, W_CFG = W_ARM + 128;
static_assert(W_ROT + PAIR <= 2u * 1024 * 1024 && W_PROBE % (2 * PAIR) == 0, "window");
constexpr int PTSEG = 32768 + 8192;

#define WAUX 0
#define FPAD 512
constexpr int GF = 65536 + FPAD, YS = 24576 + FPAD;
constexpr int NG = 81;
constexpr size_t al(size_t x) { return (x + 255) & ~(size_t)255; }
constexpr size_t OFF_CTL  = 0;
constexpr size_t OFF_WFT  = 4096 + 4 * 1024 * 1024;
constexpr size_t OFF_F    = OFF_WFT + al((size_t)4 * NG * S_ * 4);
constexpr size_t OFF_X1   = OFF_F + (size_t)NG * GF;
constexpr size_t OFF_X2   = OFF_X1 + al((size_t)S_ * D_ * 2);
constexpr size_t OFF_Y0   = OFF_X2 + al((size_t)S_ * D_ * 2);
constexpr size_t OFF_H    = OFF_Y0 + (size_t)NG * YS;
constexpr size_t OFF_ARM  = OFF_Y0 - 256 * D_ * 2, ARM_END = OFF_H + 8 * 40960;
constexpr size_t OFF_Z    = OFF_H + (size_t)(NSEG + 4) * 40960;
constexpr size_t OFF_Z2   = OFF_Z + al(ZTSEG * ZTR * 2);
constexpr size_t OFF_PT   = OFF_Z2 + al((size_t)NSEG * NOWN * 8192);
constexpr size_t OFF_QK   = OFF_PT + al((size_t)NSEG * PTSEG * 2);
constexpr size_t OFF_WIN  = OFF_QK + (size_t)NSEG * H_ * 16384;
constexpr size_t OFF_END  = OFF_WIN + (size_t)6 * 1024 * 1024;
static_assert(OFF_END <= (size_t)256 * 1024 * 1024, "workspace");

#define st128(v, r, vo, so, aux) do { const u32x4 d_ = (v); __builtin_amdgcn_raw_buffer_store_b128(d_, r, vo, so, aux); asm volatile("s_nop 1" :: "v"(d_)); } while (0)
__device__ __forceinline__ float bf2f(u16 v) { return __uint_as_float((unsigned)v << 16); }
__device__ __forceinline__ u16 f2bf(float f) { __bf16 b = (__bf16)f; return __builtin_bit_cast(u16, b); }
__device__ __forceinline__ float bfr(float f) { return (float)(__bf16)f; }
typedef __attribute__((ext_vector_type(2))) float f32x2;
typedef __attribute__((ext_vector_type(2))) __bf16 bf16x2;
__device__ __forceinline__ unsigned pack2(float a, float b) { return __builtin_bit_cast(unsigned, __builtin_convertvector((f32x2){a, b}, bf16x2)); }
__device__ __forceinline__ float lo2f(unsigned v) { return __uint_as_float(v << 16); }
__device__ __forceinline__ float hi2f(unsigned v) { return __uint_as_float(v & 0xFFFF0000u); }
__device__ __forceinline__ float fmx(float a, float b) { return __builtin_elementwise_maximum(a, b); }
__device__ __forceinline__ f32x4 mfma32(u32x4 a, u32x4 b, f32x4 c) {
    return __builtin_amdgcn_mfma_f32_16x16x32_bf16(__builtin_bit_cast(bf16x8, a), __builtin_bit_cast(bf16x8, b), c, 0, 0, 0);
}
__device__ __forceinline__ f32x4 mfma16(u32x2 a, u32x2 b, f32x4 c) {
    return __builtin_amdgcn_mfma_f32_16x16x16bf16_1k(__builtin_bit_cast(s16x4, a), __builtin_bit_cast(s16x4, b), c, 0, 0, 0);
}
__device__ __forceinline__ f32x4 smfmac(u32x4 a, u32x8 b, f32x4 c, int idx) {
    return __builtin_amdgcn_smfmac_f32_16x16x64_bf16(__builtin_bit_cast(bf16x8, a), __builtin_bit_cast(bf16x16, b), c, idx, 0, 0);
}
__device__ __forceinline__ int pi16(int k) { return ((k & 3) << 2) | ((k >> 1) & 2) | (k >> 3); }
__device__ __forceinline__ float addswap32(float a, float b) {
    auto r = __builtin_amdgcn_permlane32_swap(__float_as_uint(a), __float_as_uint(b), false, false);
    return __uint_as_float(r[0]) + __uint_as_float(r[1]);
}
__device__ __forceinline__ float xmax3216(float v) {
    auto r = __builtin_amdgcn_permlane32_swap(__float_as_uint(v), __float_as_uint(v), false, false);
    v = fmx(__uint_as_float(r[0]), __uint_as_float(r[1]));
    auto q = __builtin_amdgcn_permlane16_swap(__float_as_uint(v), __float_as_uint(v), false, false);
    return fmx(__uint_as_float(q[0]), __uint_as_float(q[1]));
}
__device__ __forceinline__ float xsum3216(float v) {
    auto r = __builtin_amdgcn_permlane32_swap(__float_as_uint(v), __float_as_uint(v), false, false);
    v = __uint_as_float(r[0]) + __uint_as_float(r[1]);
    auto q = __builtin_amdgcn_permlane16_swap(__float_as_uint(v), __float_as_uint(v), false, false);
    return __uint_as_float(q[0]) + __uint_as_float(q[1]);
}
template <int CTRL> __device__ __forceinline__ float dppf(float v) { return __uint_as_float((unsigned)__builtin_amdgcn_update_dpp(0, (int)__float_as_uint(v), CTRL, 0xF, 0xF, false)); }
__device__ __forceinline__ float wsum(float v) {
    v += dppf<0xB1>(v); v += dppf<0x4E>(v); v += dppf<0x141>(v); v += dppf<0x140>(v);
    return xsum3216(v);
}
__device__ __forceinline__ unsigned ror8(unsigned v) { return (unsigned)__builtin_amdgcn_update_dpp(0, (int)v, 0x128, 0xF, 0xF, false); }
__device__ __forceinline__ float ror8f(float v) { return __uint_as_float(ror8(__float_as_uint(v))); }
__device__ __forceinline__ unsigned nosent(unsigned v) {
    u16x2 lim = {0xFFFE, 0xFFFE};
    return __builtin_bit_cast(unsigned, __builtin_elementwise_min(__builtin_bit_cast(u16x2, v), lim));
}
__device__ __forceinline__ unsigned pkmax(unsigned a, unsigned b) {
    return __builtin_bit_cast(unsigned, __builtin_elementwise_max(__builtin_bit_cast(u16x2, a), __builtin_bit_cast(u16x2, b)));
}
__device__ __forceinline__ bool has_sent(u32x4 v) {
    unsigned m = pkmax(pkmax(v.x, v.y), pkmax(v.z, v.w));
    return (m & 0xFFFFu) == 0xFFFFu || (m >> 16) == 0xFFFFu;
}

typedef f32x2 c32;
__device__ __forceinline__ c32 fma2(c32 a, c32 b, c32 c) { return __builtin_elementwise_fma(a, b, c); }
__device__ __forceinline__ c32 dup(float v) { return (c32){v, v}; }
template <int SG> __device__ __forceinline__ c32 addi(c32 a, c32 b) {
    c32 d;
    if (SG > 0) asm("v_pk_add_f32 %0, %1, %2 op_sel:[0,1] op_sel_hi:[1,0] neg_lo:[0,1]" : "=v"(d) : "v"(a), "v"(b));
    else asm("v_pk_add_f32 %0, %1, %2 op_sel:[0,1] op_sel_hi:[1,0] neg_hi:[0,1]" : "=v"(d) : "v"(a), "v"(b));
    return d;
}
template <int SG> __device__ __forceinline__ c32 ctw(c32 a, c32 w) {
    c32 d = a * w.xx;
    if (SG < 0) asm("v_pk_fma_f32 %0, %1, %2, %0 op_sel:[1,1,0] op_sel_hi:[0,1,1] neg_lo:[0,1,0]" : "+v"(d) : "v"(a), "v"(w));
    else asm("v_pk_fma_f32 %0, %1, %2, %0 op_sel:[1,1,0] op_sel_hi:[0,1,1] neg_hi:[0,1,0]" : "+v"(d) : "v"(a), "v"(w));
    return d;
}
__device__ __forceinline__ c32 cis(float rev) { return (c32){__builtin_amdgcn_cosf(rev), __builtin_amdgcn_sinf(rev)}; }
template <int N> __device__ __forceinline__ void cpow(c32 (&t)[N], float rev) {
    t[0] = cis(rev);
    asm volatile("s_nop 0" : "+v"(t[0]));
#pragma unroll
    for (int i = 1; i < N; ++i) { t[i] = ctw<-1>(t[(i - 1) / 2], t[i / 2]); asm volatile("" : "+v"(t[i])); }
}
#define SB() __builtin_amdgcn_sched_barrier(0)
template <int SG, int I0, int N, class F> __device__ __forceinline__ void ctwn(c32 (&u)[N], F w) {
    c32 d[N];
#pragma unroll
    for (int i = I0; i < N; ++i) asm("v_pk_mul_f32 %0, %1, %2 op_sel_hi:[1,0]" : "=v"(d[i]) : "v"(u[i]), "v"(w(i)));
    SB();
#pragma unroll
    for (int i = I0; i < N; ++i) {
        if (SG < 0) asm("v_pk_fma_f32 %0, %1, %2, %0 op_sel:[1,1,0] op_sel_hi:[0,1,1] neg_lo:[0,1,0]" : "+v"(d[i]) : "v"(u[i]), "v"(w(i)));
        else asm("v_pk_fma_f32 %0, %1, %2, %0 op_sel:[1,1,0] op_sel_hi:[0,1,1] neg_hi:[0,1,0]" : "+v"(d[i]) : "v"(u[i]), "v"(w(i)));
        u[i] = d[i];
    }
    SB();
}
template <int SG, int M, int A, int B, int CI, int N> __device__ __forceinline__ void dft4n(c32 (&u)[N]) {
    c32 t[M][4];
#pragma unroll
    for (int i = 0; i < M; ++i) {
        const c32 a = u[A * i], b = u[A * i + B], c = u[A * i + 2 * B], d = u[A * i + 3 * B];
        t[i][0] = i == CI ? addi<SG>(a, c) : a + c; t[i][1] = i == CI ? addi<-SG>(a, c) : a - c; t[i][2] = b + d; t[i][3] = b - d;
        if (B == 4) SB();
    }
    SB();
#pragma unroll
    for (int i = 0; i < M; ++i) { u[A * i] = t[i][0] + t[i][2]; u[A * i + 2 * B] = t[i][0] - t[i][2]; u[A * i + B] = addi<SG>(t[i][1], t[i][3]); u[A * i + 3 * B] = addi<-SG>(t[i][1], t[i][3]); }
    SB();
}
template <int SG> __device__ __forceinline__ void dft8x2(c32 (&u)[16]) {
    dft4n<SG, 4, 1, 4, -1>(u);
    const float r = 0.70710678118654752440f;
    c32 o[16];
#pragma unroll
    for (int e = 0; e < 2; ++e) { u[6 + e] = addi<SG>(u[6 + e], u[6 + e]); u[14 + e] = addi<-SG>(u[14 + e], u[14 + e]); }
#pragma unroll
    for (int e = 0; e < 2; ++e) { o[e] = u[e] + u[2 + e]; o[8 + e] = u[e] - u[2 + e]; o[4 + e] = addi<SG>(u[8 + e], u[10 + e]); o[12 + e] = addi<-SG>(u[8 + e], u[10 + e]); }
    SB();
#pragma unroll
    for (int e = 0; e < 2; ++e) { o[2 + e] = fma2(u[6 + e], dup(r), u[4 + e]); o[10 + e] = fma2(u[6 + e], dup(-r), u[4 + e]); o[6 + e] = fma2(u[14 + e], dup(-r), u[12 + e]); o[14 + e] = fma2(u[14 + e], dup(r), u[12 + e]); }
    SB();
#pragma unroll
    for (int i = 0; i < 16; ++i) u[i] = o[i];
}
template <int SG> __device__ __forceinline__ void dft16(c32 (&u)[16]) {
    const float c1 = 0.92387953251128675613f, s1 = 0.38268343236508977173f, r = 0.70710678118654752440f;
    dft4n<SG, 4, 1, 4, -1>(u);
    const int ix[8] = {5, 9, 13, 6, 14, 7, 11, 15};
    const float wx[8] = {c1, r, s1, r, -r, s1, -r, -c1}, wy[8] = {s1, r, c1, r, r, c1, r, -s1};
    c32 d[8];
#pragma unroll
    for (int i = 0; i < 8; ++i) d[i] = u[ix[i]] * dup(wx[i]);
    SB();
#pragma unroll
    for (int i = 0; i < 8; ++i) u[ix[i]] = fma2(u[ix[i]].yx, (c32){-SG * wy[i], SG * wy[i]}, d[i]);
    SB();
    dft4n<SG, 4, 4, 1, 2>(u);
    c32 o[16];
#pragma unroll
    for (int i = 0; i < 16; ++i) o[(i >> 2) + 4 * (i & 3)] = u[i];
#pragma unroll
    for (int i = 0; i < 16; ++i) u[i] = o[i];
}
template <int SG> __device__ __forceinline__ void dft10(c32 (&u)[10]) {
    const float c1 = 0.30901699437494742410f, c2 = -0.80901699437494742410f, s1 = 0.95105651629515357212f, s2 = 0.58778525229247312917f;
    c32 x[2][5], t[2][4], a[2][5];
#pragma unroll
    for (int i = 0; i < 5; ++i) { x[0][i] = u[2 * i] + u[(2 * i + 5) % 10]; x[1][i] = u[2 * i] - u[(2 * i + 5) % 10]; }
    SB();
#pragma unroll
    for (int e = 0; e < 2; ++e) { t[e][0] = x[e][1] + x[e][4]; t[e][1] = x[e][2] + x[e][3]; t[e][2] = x[e][1] - x[e][4]; t[e][3] = x[e][2] - x[e][3]; }
    SB();
#pragma unroll
    for (int e = 0; e < 2; ++e) { a[e][0] = t[e][0] + t[e][1]; a[e][1] = fma2(t[e][0], dup(c1), x[e][0]); a[e][2] = fma2(t[e][0], dup(c2), x[e][0]); a[e][3] = t[e][2] * dup(s1); a[e][4] = t[e][2] * dup(s2); }
    SB();
#pragma unroll
    for (int e = 0; e < 2; ++e) { a[e][0] += x[e][0]; a[e][1] = fma2(t[e][1], dup(c2), a[e][1]); a[e][2] = fma2(t[e][1], dup(c1), a[e][2]); a[e][3] = fma2(t[e][3], dup(s2), a[e][3]); a[e][4] = fma2(t[e][3], dup(-s1), a[e][4]); }
    SB();
#pragma unroll
    for (int e = 0; e < 2; ++e) {
        u[5 * e] = a[e][0];
        u[(6 + 5 * e) % 10] = addi<SG>(a[e][1], a[e][3]); u[(24 + 5 * e) % 10] = addi<-SG>(a[e][1], a[e][3]);
        u[(12 + 5 * e) % 10] = addi<SG>(a[e][2], a[e][4]); u[(18 + 5 * e) % 10] = addi<-SG>(a[e][2], a[e][4]);
    }
    SB();
}
template <int ST> __device__ __forceinline__ void lds16(c32 (&u)[16], const char* p) {
    const LAS char* q = (const LAS char*)p;
#pragma unroll
    for (int i = 0; i < 16; ++i) asm volatile("ds_read_b64 %0, %1 offset:%2" : "=v"(u[(i >> 2) + 4 * (i & 3)]) : "v"(q), "n"(((i >> 2) + 4 * (i & 3)) * ST) : "memory");
#pragma unroll
    for (int i = 0; i < 4; ++i) asm volatile("s_waitcnt lgkmcnt(%4)" : "+v"(u[i]), "+v"(u[i + 4]), "+v"(u[i + 8]), "+v"(u[i + 12]) : "n"(12 - 4 * i) : "memory");
}
__device__ __forceinline__ c32 unpack2(unsigned v) { return (c32){lo2f(v), hi2f(v)}; }
__device__ __forceinline__ float wsum2(c32 v) {
    float t = addswap32(v.x, v.y);
    auto q = __builtin_amdgcn_permlane16_swap(__float_as_uint(t), __float_as_uint(t), false, false);
    t = __uint_as_float(q[0]) + __uint_as_float(q[1]);
    t += dppf<0xB1>(t); t += dppf<0x4E>(t); t += dppf<0x141>(t); t += dppf<0x140>(t);
    return t;
}
__device__ __forceinline__ c32 halves(float t) { return (c32){__uint_as_float(__builtin_amdgcn_readlane(__float_as_uint(t), 0)), __uint_as_float(__builtin_amdgcn_readlane(__float_as_uint(t), 32))}; }
__device__ __forceinline__ void ln_rows(const c32 (&v)[10], unsigned (&o)[10]) {
    const float mean = wsum2(((v[0] + v[1]) + (v[2] + v[3])) + ((v[4] + v[5]) + (v[6] + v[7])) + (v[8] + v[9])) * (1.f / 640.f);
    const c32 m = halves(mean), mb = halves(bfr(mean));
    c32 d[10], e[10];
#pragma unroll
    for (int r = 0; r < 10; ++r) d[r] = v[r] - m;
    SB();
#pragma unroll
    for (int r = 0; r < 5; ++r) e[r] = d[r] * d[r];
#pragma unroll
    for (int r = 0; r < 5; ++r) d[r] = fma2(d[r + 5], d[r + 5], e[r]);
    SB();
#pragma unroll
    for (int r = 0; r < 10; ++r) { const c32 y = v[r] - mb; e[r] = unpack2(pack2(y.x, y.y)); }
    const float var = wsum2((d[0] + d[1]) + (d[2] + d[3]) + d[4]) * (1.f / 640.f);
    const c32 rs = halves(__builtin_amdgcn_rcpf(bfr(__builtin_amdgcn_sqrtf(bfr(bfr(var) + 1e-5f)))));
    SB();
#pragma unroll
    for (int r = 0; r < 10; ++r) e[r] *= rs;
    SB();
#pragma unroll
    for (int r = 0; r < 10; ++r) o[r] = pack2(e[r].x, e[r].y);
    SB();
}

__device__ __forceinline__ __amdgpu_buffer_rsrc_t mkrsrc(const void* p, unsigned bytes) {
    return __builtin_amdgcn_make_buffer_rsrc((void*)p, 0, bytes, 0x00020000);
}

constexpr unsigned O_F = (unsigned)OFF_F, O_Y0 = (unsigned)OFF_Y0;
__device__ __forceinline__ unsigned nosent1(unsigned v) { return min(v, 0xFFFFFFFEu); }
__device__ __forceinline__ u32x4 nosent4(u32x4 v) { v[1] = nosent1(v[1]); v[3] = nosent1(v[3]); return v; }
__device__ __forceinline__ unsigned smax(u32x4 v) { return max(v[1], v[3]); }
constexpr unsigned SPIN_CAP = 400000u;
constexpr int RW = 5920;
struct RowTw { c32 a[9], b, c; };
__device__ __forceinline__ RowTw row_tw(int lane) {
    RowTw t;
    t.b = cis((float)(lane & 31) * (-1.f / 64.f)); t.c = cis((float)(lane & 15) * (-1.f / 32.f));
    asm volatile("" : "+v"(t.b), "+v"(t.c));
    cpow(t.a, (float)lane * (-1.f / 640.f));
    return t;
}
template <int B> __device__ __forceinline__ void cswap(c32& x, c32& y) {
#pragma unroll
    for (int e = 0; e < 2; ++e) {
        auto r = B == 32 ? __builtin_amdgcn_permlane32_swap(__float_as_uint(x[e]), __float_as_uint(y[e]), false, false) : __builtin_amdgcn_permlane16_swap(__float_as_uint(x[e]), __float_as_uint(y[e]), false, false);
        x[e] = __uint_as_float(r[0]); y[e] = __uint_as_float(r[1]);
    }
}
template <int B> __device__ __forceinline__ void cswap5(c32 (&p)[5], c32 (&q)[5]) {
#pragma unroll
    for (int i = 0; i < 5; ++i) cswap<B>(p[i], q[i]);
    SB();
}
__device__ __forceinline__ void bfly5(c32 (&p)[5], c32 (&q)[5]) {
#pragma unroll
    for (int i = 0; i < 5; ++i) { const c32 t = p[i] - q[i]; p[i] += q[i]; q[i] = t; }
    SB();
}
constexpr unsigned L_X = 16 * RW;
__device__ __forceinline__ void row_x(__amdgpu_buffer_rsrc_t rx, int row, int w, int lane) {
    asm volatile("s_mov_b32 m0, %3\n\ts_nop 0\n\tbuffer_load_dwordx4 %0, %1, %2 offen sc1 lds\n\tbuffer_load_dwordx4 %0, %1, %2 offen offset:1024 sc1 lds\n\tbuffer_load_dwordx4 %0, %1, %2 offen offset:1536 sc1 lds"
                 :: "v"(lane * 16), "s"(rx), "s"(row * (D_ * 2)), "s"(L_X + w * 2560u) : "m0", "memory");
}
template <int M> __device__ __forceinline__ void row_get(u16 (&v)[20], int w, int lane) {
    asm volatile("s_waitcnt vmcnt(%0)" :: "n"(M) : "memory");
#pragma unroll
    for (int r = 0; r < 20; ++r) v[r] = *(const LAS u16*)(size_t)(L_X + w * 2560u + lane * 2u + 128u * (r >> 1) + 1280u * (r & 1));
    SB();
}
template <int NW, int NT, class FS> __device__ __forceinline__ void a1_unit(__amdgpu_buffer_rsrc_t rx, int row0, bool tail, __amdgpu_buffer_rsrc_t rws, char* sm, const RowTw& tw, int w, int lane, FS st_) {
    const unsigned fo = tail ? O_Y0 + 16384u + (unsigned)(row0 & 255) * 32u : O_F + (unsigned)row0 * 32u, gs = tail ? YS : GF;
    if (w < NW) {
        c32 u[10];
        unsigned h[10];
        u16 x[20];
        row_x(rx, row0 + 2 * w, w, lane);
        st_(-1);
        row_get<NW == 8 ? 8 : 0>(x, w, lane);
        if (tail) for (unsigned spins = 0; spins < SPIN_CAP; ++spins) {
            unsigned m = 0;
#pragma unroll
            for (int r = 0; r < 20; ++r) m = max(m, (unsigned)x[r]);
            if (!__any(m == 0xFFFFu)) break;
            row_x(rx, row0 + 2 * w, w, lane);
            row_get<0>(x, w, lane);
        }
#pragma unroll
        for (int r = 0; r < 10; ++r) u[r] = (c32){bf2f(x[2 * r]), bf2f(x[2 * r + 1])};
        st_(0);
        ln_rows(u, h);
        st_(1);
#pragma unroll
        for (int r = 0; r < 10; ++r) u[r] = unpack2(h[r]);
        dft10<-1>(u);
        char* mine = sm + w * RW;
        char* ex = mine + ((lane >> 5) + 10 * ((lane >> 4) & 1)) * 144 + (lane & 15) * 8;
        ctwn<-1, 1>(u, [&](int i) { return tw.a[i - 1]; });
        c32 p[5], q[5];
#pragma unroll
        for (int i = 0; i < 5; ++i) { p[i] = u[2 * i]; q[i] = u[2 * i + 1]; }
        cswap5<32>(p, q); bfly5(p, q); ctwn<-1, 0>(q, [&](int) { return tw.b; });
        cswap5<16>(p, q); bfly5(p, q);
#pragma unroll
        for (int i = 0; i < 5; ++i) *(c32*)(ex + i * 288) = p[i];
        ctwn<-1, 0>(q, [&](int) { return tw.c; });
#pragma unroll
        for (int i = 0; i < 5; ++i) *(c32*)(ex + i * 288 + 2880) = q[i];
        asm volatile("" ::: "memory");
        st_(2);
        if (lane < 40) {
            c32 v[16];
#pragma unroll
            for (int m = 0; m < 8; ++m) { const f32x4 t = *(const f32x4*)(mine + lane * 144 + m * 16); v[2 * m] = t.xy; v[2 * m + 1] = t.zw; }
            dft16<-1>(v);
            asm volatile("" ::: "memory");
#pragma unroll
            for (int m = 0; m < 16; ++m) *(c32*)(mine + lane * 8 + m * 320) = v[m];
        }
        st_(3);
    }
    asm volatile("s_waitcnt lgkmcnt(0)\n\ts_barrier" ::: "memory");
    st_(4);
    const int tid = w * 64 + lane, k0 = tid / (4 * NW) * 4 + (tid & 3), id = tid % (4 * NW);
    const char* X = sm + (id >> 2) * RW;
#pragma unroll
    for (int c = 0; c * NT < 320 * NW; ++c) {
        const int k = k0 + c * (NT / NW);
        if ((c + 1) * NT <= 320 * NW || k < 320) {
            const c32 zk = *(const c32*)(X + k * 8), zn = *(const c32*)(X + (k == 0 ? 0 : 640 - k) * 8);
            const f32x4 o = {0.5f * (zk.x + zn.x), 0.5f * (zk.y - zn.y), 0.5f * (zk.y + zn.y), 0.5f * (zn.x - zk.x)};
            st128(nosent4(__builtin_bit_cast(u32x4, o)), rws, (unsigned)(k >> 2) * gs + id * 16u, fo, 16);
        }
    }
    if (k0 < 4) {
        const c32 z = *(const c32*)(X + 2560);
        st128(((u32x4){__float_as_uint(z.x), 0u, __float_as_uint(z.y), 0u}), rws, 80u * gs + id * 16u, fo, 16);
    }
}
template <int NW, class FS, class FD> __device__ __forceinline__ void a3_units(__amdgpu_buffer_rsrc_t rws, __amdgpu_buffer_rsrc_t rx, int row0, int step, int lay, char* sm, const RowTw& tw, int w, int lane, FS st_, FD done) {
    constexpr int NT = 512, NC = (320 * NW + NT - 1) / NT, GS = NW == 4 ? YS : GF;
    const int tid = w * 64 + lane, k0 = tid / (4 * NW) * 4 + (tid & 3), id = tid % (4 * NW);
    u32x4 fv[NC], fz = {0u, 0u, 0u, 0u};
    u16 x[20];
    auto get = [&](int row) {
        const unsigned fo = (NW == 4 ? O_Y0 + lay * 8192u : O_F) + (unsigned)row * 32u;
#pragma unroll
        for (int c = 0; c < NC; ++c) fv[c] = __builtin_amdgcn_raw_buffer_load_b128(rws, (unsigned)((min(k0 + c * (NT / NW), 319) >> 2) * GS + id * 16), fo, 16);
        if (w == 0) fz = __builtin_amdgcn_raw_buffer_load_b128(rws, (unsigned)(80 * GS + id * 16), fo, 16);
        __builtin_amdgcn_sched_barrier(0);
    };
    if (w < NW) row_x(rx, row0 + 2 * w, w, lane);
    st_(-1);
    for (unsigned spins = 0; spins < SPIN_CAP; ++spins) {
        asm volatile("" ::: "memory");
        get(row0);
        if (NW == 8 || !__any(max(max(smax(fv[0]), smax(fv[1])), max(smax(fv[2]), smax(fz))) == 0xFFFFFFFFu)) break;
    }
    for (int n = 0;; ++n, row0 += step, sm += n & 1 ? 8 * RW : -8 * RW) {
    char* X = sm + (id >> 2) * RW;
#pragma unroll
    for (int c = 0; c < NC; ++c) {
        const int k = k0 + c * (NT / NW);
        const f32x4 v = __builtin_bit_cast(f32x4, fv[c]);
        c32 zk = addi<1>(v.xy, v.zw), zn = (c32){v[0] + v[3], v[2] - v[1]};
        if (c == 0 && k == 0) { zk = (c32){v[0], v[2]}; zn = (c32){__uint_as_float(fz[0]), __uint_as_float(fz[2])}; }
        if ((c + 1) * NT <= 320 * NW || k < 320) { *(c32*)(X + k * 8) = zk; *(c32*)(X + (k == 0 ? 320 : 640 - k) * 8) = zn; }
    }
    asm volatile("s_waitcnt vmcnt(0) lgkmcnt(0)\n\ts_barrier" ::: "memory");
    st_(0);
    if (NW == 8) {
        if (n) done(row0 - step);
        row_get<63>(x, w, lane);
        asm volatile("s_waitcnt lgkmcnt(0)" ::: "memory");
        if (row0 + step < S_) { row_x(rx, row0 + step + 2 * w, w, lane); get(row0 + step); }
    }
    if (w < NW) {
        char* mine = sm + w * RW;
        if (lane < 40) {
            c32 v[16];
            lds16<320>(v, mine + lane * 8);
            dft16<1>(v);
            asm volatile("" ::: "memory");
#pragma unroll
            for (int m = 0; m < 8; ++m) *(f32x4*)(mine + lane * 144 + m * 16) = (f32x4){v[2 * m].x, v[2 * m].y, v[2 * m + 1].x, v[2 * m + 1].y};
        }
        asm volatile("" ::: "memory");
        st_(1);
        const char* ex = mine + ((lane >> 5) + 10 * ((lane >> 4) & 1)) * 144 + (lane & 15) * 8;
        c32 u[10];
        c32 p[5], q[5];
#pragma unroll
        for (int i = 0; i < 5; ++i) q[i] = *(const c32*)(ex + i * 288 + 2880);
#pragma unroll
        for (int i = 0; i < 5; ++i) p[i] = *(const c32*)(ex + i * 288);
        SB();
        ctwn<1, 0>(q, [&](int) { return tw.c; }); bfly5(p, q); cswap5<16>(p, q);
        ctwn<1, 0>(q, [&](int) { return tw.b; }); bfly5(p, q); cswap5<32>(p, q);
#pragma unroll
        for (int i = 0; i < 5; ++i) { u[2 * i] = p[i]; u[2 * i + 1] = q[i]; }
        ctwn<1, 1>(u, [&](int i) { return tw.a[i - 1]; });
        if (NW == 4) row_get<63>(x, w, lane);
        dft10<1>(u);
        st_(2);
        asm volatile("" ::: "memory");
        char* st = mine + lane * 2;
        unsigned h[10];
#pragma unroll
        for (int r = 0; r < 10; ++r) {
            const c32 y = (c32){bf2f(x[2 * r]), bf2f(x[2 * r + 1])} + unpack2(pack2(u[r].x, u[r].y));
            const unsigned p = pack2(y.x, y.y);
            u[r] = unpack2(p);
            *(u16*)(st + r * 128) = (u16)p; *(u16*)(st + 1280 + r * 128) = (u16)(p >> 16);
        }
        st_(3);
        ln_rows(u, h);
        st_(4);
#pragma unroll
        for (int r = 0; r < 10; ++r) { *(u16*)(st + 2560 + r * 128) = (u16)h[r]; *(u16*)(st + 3840 + r * 128) = (u16)(h[r] >> 16); }
        asm volatile("" ::: "memory");
        u32x4 o[6];
#pragma unroll
        for (int c = 0; c < 6; ++c) o[c] = *(const u32x4*)(mine + (c < 3 ? 2560 : 0) + min(c % 3 * 64 + lane, 159) * 16);
#pragma unroll
        for (int c = 0; c < 3; ++c) {
            const int i = min(c * 64 + lane, 159), rr = i >= 80, ch = i - 80 * rr, row = (row0 + 2 * w + rr) & 31;
            st128(nosent4(o[c]), rws, (unsigned)((ch >> 3) * 4096 + row * 128 + (((ch & 7) ^ (row & 7)) << 4)) | (c == 2 ? (lane & 32u) << 25 : 0u), (unsigned)OFF_H + (unsigned)((row0 >> 5) + 4 - 4 * lay) * 40960u, 16);
        }
#pragma unroll
        for (int c = 0; c < 3; ++c) st128(o[3 + c], rws, (unsigned)(c * 64 + lane) * 16u | (c == 2 ? (lane & 32u) << 25 : 0u), (unsigned)OFF_X1 + (unsigned)(row0 + 2 * w) * (D_ * 2), 16);
    }
    if (NW == 4 || row0 + step >= S_) break;
    }
    st_(5);
    asm volatile("s_waitcnt vmcnt(0)\n\ts_barrier" ::: "memory");
    done(row0);
}

__device__ __forceinline__ unsigned xcc_id() { unsigned x; asm volatile("s_getreg_b32 %0, hwreg(HW_REG_XCC_ID)" : "=s"(x)); return x & 7u; }
__device__ __forceinline__ unsigned probe(__amdgpu_buffer_rsrc_t rw, unsigned base) {
    unsigned mn[4] = {~0u, ~0u, ~0u, ~0u};
#pragma unroll 1
    for (int r = 0; r < 2; ++r) {
#pragma unroll
        for (int i = 0; i < 4; ++i) {
            const unsigned long long t1 = __builtin_amdgcn_s_memtime();
            asm volatile("s_waitcnt lgkmcnt(0)" ::: "memory");
            unsigned v = __builtin_amdgcn_raw_buffer_load_b32(rw, i * HALF, base + r * 4096u, 16);
            asm volatile("s_waitcnt vmcnt(0)" : "+v"(v) :: "memory");
            const unsigned long long t2 = __builtin_amdgcn_s_memtime();
            mn[i] = min(mn[i], (unsigned)(t2 - t1) + v);
        }
    }
    return mn[1] + mn[3] < mn[0] + mn[2] ? (mn[3] < mn[1] ? 3u : 1u) : (mn[2] < mn[0] ? 2u : 0u);
}
__device__ __forceinline__ void filter_unit(const u16* wr, const u16* wi, int s0, __amdgpu_buffer_rsrc_t rws, u16* tf, int tid) {
    u32x4 v[6];
#pragma unroll
    for (int i = 0; i < 6; ++i) v[i] = ((const u32x4*)((i < 3 ? wr : wi) + (size_t)s0 * DF))[min(tid + 512 * (i % 3), 1283)];
    __builtin_amdgcn_sched_barrier(0);
#pragma unroll
    for (int i = 0; i < 6; ++i) ((u32x4*)tf)[min(tid + 512 * (i % 3), 1283) + i / 3 * 1284] = v[i];
    __syncthreads();
    const int k = tid >> 3, sq = (tid & 7) * 4;
    u16 t[6][8];
#pragma unroll
    for (int i = 0; i < 6; ++i)
#pragma unroll
        for (int j = 0; j < 8; ++j) t[i][j] = tf[((j & 1) * 32 + sq + (j >> 1)) * DF + k + 64 * i];
    __builtin_amdgcn_sched_barrier(0);
#pragma unroll
    for (int i = 0; i < 6; ++i) {
        u32x4 o;
#pragma unroll
        for (int j = 0; j < 4; ++j) o[j] = (unsigned)t[i][2 * j] | (unsigned)t[i][2 * j + 1] << 16;
        if (i < 5 || tid < 8) st128(o, rws, (unsigned)(k * S_ + sq) * 4u, (unsigned)OFF_WFT + (unsigned)(s0 * 4 + i * 64 * S_ * 4), 16);
    }
}

constexpr int A2W = 8720;
template <class FW, class FS> __device__ __forceinline__ void a2_unit(__amdgpu_buffer_rsrc_t rws, int G, char* sm, int w, int lane, bool late, FW rdy, FS st_) {
    const int c = lane & 3, j = lane >> 2, p = 16 * w + j;
    const unsigned fo = O_F + (unsigned)(G * GF + w * 1024);
    u32x4 x[8];
    unsigned wv[16];
    c32 t1[2][7], t2[15], wf[16];
    auto ldw = [&]() {
#pragma unroll
        for (int r = 0; r < 16; ++r) wv[r] = __builtin_amdgcn_raw_buffer_load_b32(rws, (unsigned)(j * 32 + c * 4 + (r & 7) * 512), (unsigned)OFF_WFT + (unsigned)(min(4 * G + (w >> 1), 320) * 8192 + (w & 1) * 16 + (r >> 3) * 4096), 16);
        __builtin_amdgcn_sched_barrier(0);
    };
    auto cvw = [&]() {
#pragma unroll
        for (int m = 0; m < 16; ++m) { wf[m] = unpack2(wv[m]) * dup(1.f / ((float)S_ * (float)D_)); asm volatile("" : "+v"(wf[m])); }
    };
#pragma unroll
    for (int m = 0; m < 16; ++m) wv[m] = 0u;
    rdy(5);
    cpow(t1[0], (float)p * (-1.f / 1024.f)); cpow(t1[1], (float)(2 * p + 1) * (-1.f / 2048.f)); cpow(t2, (float)j * (-1.f / 256.f));
    rdy(6);
#pragma unroll
    for (int r = 0; r < 7; ++r) x[r] = __builtin_amdgcn_raw_buffer_load_b128(rws, lane * 16u, fo + r * 8192u, 16);
    if (!late) ldw();
    rdy(7);
    const unsigned yo = O_Y0 + (unsigned)(G * YS + w * 1024);
    x[7] = __builtin_amdgcn_raw_buffer_load_b128(rws, lane * 16u, late ? fo + 7 * 8192u : yo + 16384u, 16);
    __builtin_amdgcn_sched_barrier(0);
    cvw();
    for (unsigned spins = 0; spins < SPIN_CAP && __any(smax(x[7]) == 0xFFFFFFFFu); ++spins) {
        asm volatile("" ::: "memory");
        x[7] = __builtin_amdgcn_raw_buffer_load_b128(rws, lane * 16u, yo + 16384u, 16);
    }
    __builtin_amdgcn_sched_barrier(0);
    c32 u[16];
#pragma unroll
    for (int r = 0; r < 8; ++r) { const f32x4 v = __builtin_bit_cast(f32x4, x[r]); u[2 * r] = v.xy; u[2 * r + 1] = v.zw; }
    st_(0);
    dft8x2<-1>(u);
    ctwn<-1, 2>(u, [&](int i) { return t1[i & 1][(i >> 1) - 1]; });
    char* e1 = sm + 2 * c * A2W + p * 16;
    char* mine = sm + w * A2W;
#pragma unroll
    for (int q = 0; q < 8; ++q) *(f32x4*)(e1 + (q >> 2) * A2W + (q & 3) * 2112) = (f32x4){u[2 * q].x, u[2 * q].y, u[2 * q + 1].x, u[2 * q + 1].y};
    st_(1);
    rdy(8);
    asm volatile("s_waitcnt lgkmcnt(0)\n\ts_barrier" ::: "memory");
    st_(2);
    if (late) ldw();
    lds16<128>(u, mine + c * 2112 + j * 8);
    dft16<-1>(u);
    auto turn = [&](auto sg) {
        ctwn<decltype(sg)::v, 1>(u, [&](int i) { return t2[i - 1]; });
#pragma unroll
        for (int m = 0; m < 16; ++m) *(c32*)(mine + m * 544 + lane * 8) = u[m];
        lds16<32>(u, mine + j * 544 + c * 8);
    };
    turn(IC<-1>());
    st_(3);
    dft16<-1>(u);
    if (late) cvw();
    ctwn<-1, 0>(u, [&](int i) { return wf[i]; });
    dft16<1>(u);
    turn(IC<1>());
    st_(4);
    dft16<1>(u);
#pragma unroll
    for (int m = 0; m < 16; ++m) *(c32*)(mine + (w >> 1) * 32 + c * 2080 + j * 8 + m * 128) = u[m];
    st_(5);
    asm volatile("s_waitcnt lgkmcnt(0)\n\ts_barrier" ::: "memory");
    st_(6);
#pragma unroll
    for (int q = 0; q < 8; ++q) {
        const f32x4 v = *(const f32x4*)(e1 + c * 32 + (q >> 2) * A2W + (q & 3) * 2080);
        u[2 * q] = v.xy; u[2 * q + 1] = v.zw;
    }
    SB();
    ctwn<1, 2>(u, [&](int i) { return t1[i & 1][(i >> 1) - 1]; });
    dft8x2<1>(u);
#pragma unroll
    for (int r = 0; r < 8; ++r) {
        const u32x4 o = __builtin_bit_cast(u32x4, (f32x4){u[2 * r].x, u[2 * r].y, u[2 * r + 1].x, u[2 * r + 1].y});
        st128(r ? o : nosent4(o), rws, lane * 16u, r ? fo + r * 8192u : late ? yo : yo + 8192u, 16);
        if (r == 0) st_(7);
    }
}

struct RecArgs {
    unsigned* ctl; char* win; const u16* mem0; const u16* xin; u16* out;
    const u16* W[6];
};
template <int I> __device__ __forceinline__ char* karg(int i = 0) {
    unsigned long long p;
    asm volatile("s_load_dwordx2 %0, %1, %2\n\ts_waitcnt lgkmcnt(0)" : "=s"(p) : "s"(__builtin_amdgcn_kernarg_segment_ptr()), "s"(I * 8 + i * 8) : "memory");
    return (char*)p;
}
constexpr unsigned O_H = (unsigned)OFF_H, O_X1 = (unsigned)OFF_X1, O_Z = (unsigned)OFF_Z, O_Z2 = (unsigned)OFF_Z2, O_PT = (unsigned)OFF_PT, O_QK = (unsigned)OFF_QK;
#define MXV 0xAAAAAull
#define MXQ 0x88888000000ull
static_assert((MXV & 15) == 10 && !(MXQ & 15) && !((MXV | MXQ) >> 63), "segment sets");
constexpr int NREC = 96 + NOWN, NQT = NREC + NOWN, NXT = NQT + 32, NWRK = NXT + 32;
__device__ __forceinline__ bool spin_fail(unsigned& spins, __amdgpu_buffer_rsrc_t rws) {
    if (++spins > SPIN_CAP) { __builtin_amdgcn_raw_buffer_store_b32(1u, rws, 0u, 0u, 16); return true; }
    return (spins & 1023u) == 0u && __builtin_amdgcn_readfirstlane(__builtin_amdgcn_raw_buffer_load_b32(rws, 0u, 0u, 16));
}
#define STR2(x) #x
#define STR(x) STR2(x)
#define TPRE 0
#define QP 2
#define BQN 7
#define BQ(n) ".if " #n "==" STR(BQN) "\n\ts_barrier\n\t.endif\n\t"
#define SST(k) do {} while (0)
#define LSEL 1
#define RT(k) do {} while (0)
#define RTS() do {} while (0)
#define B3ATTR
#define MTA(k)
#define MTRT() do {} while (0)
#define MT(k) do {} while (0)
#define MTS() do {} while (0)
#define FC(x) do {} while (0)
#define DQC 1130
#define DVC 1200
#define M1 44
#define MZ 40
#define MZP 20
#define M2 224
#define LTP (LT0 + 15)
#define LTZ 340
#define PEN 280
#define KSL 1
#define SVO 64
#define DZP 46
#define LT0 330
#define SAD 1
#define PEN2 80
#define PEN1 50
#define PEN0 60
#define DDM2 0
#define DRA 700
#define TSLP 1
#define PFS 0
#define PFOFF 1
#define OPR 2
#define BSPARSE 0
#define ZLAG 7
#define TKP 0
#define DPS 12
#define RZ 63
#define RQ 63
#define R2 63
#define DXT 3000
#define SSW 99
#define LTM 18
#define XS0 10
#define XSU 8
#define XSM 32
#define LTU 4
#define LTD 1
#define QUIET 0
#define QUAD 1
#define LTS 3
#define XSS 3
#define TKS 1
#define LT1 358
#define FDL 2
#define ZLA 3
#define DPT2 3
#define NORND 0
#define LTX 28
#define TKE 200
#define DVO 0
#define ZPS 4
#define ASLP 4
#define BADG 0
#define DW1 0
#define WLA 4
#define XSLP 30
#define DOUT 150
#define DA1 145
#define DA2 100
#define DA20 120
#define DFB 150
#define DA3 185
#define DA30 230
#define YSLP 0
#define QEU 14
#define QE2 30
#define PEN00 120
#define DQ0 1020
#define DQ00 1250
#define DZ0 420
#define DZ00 430
#define DOU2 200
#define DFA 0
#define SRG 0x5A000000u
#define OBN 5
#define OB(n) ".if " #n "==" STR(OBN) "\n\ts_barrier\n\tds_read_b64 v[102:103], %[sp]\n\tds_read_b64 v[104:105], %[sp] offset:512\n\tds_read_b64 v[106:107], %[sp] offset:1024\n\t.endif\n\t"
#define VSLP 0
#define CPF 1
#define DST 4
#define XST 0
#define ODL 8
#define VTS 1
#define XF 7
#define XFS 32
#define DOU1 50
#define DR0 0
#define DFB0 120
#define TPF 21
#define DZF 160
#define DZF0 175
#define DV0 260
#define QTP 0
#define XDL 20
#define DOW 200
#define DOU3 300
#define HLA 8
#define PSLP 16
#define PFD 4
#define NPF 3
#define KSL2 1
#define KBQ 4
#define VB1 6
#define VB2 5
#define KR1 3
#define QB2 2
#define NB2 6
#define DZR 24
#define ML 470
#define MN 12
#define CF 190
#define DB 1500
#define DP 200
#define ZSL 1
#define NB1 3
#define DPT 3
#define DDMA 8
#define DZ2 800
#define QEARLY 0
#define OSLP 12
#define OSLP2 4
#define WSLP 32
#define DO0 12000
#define DW0 1200
#define DQT 9000
#define DQ4 700
#define ZEARLY 3
#define HSLP 4
#define RMAP 0
#define DIE 0
#define VMWAIT() do { asm volatile("" ::: "memory"); __builtin_amdgcn_s_waitcnt(0x0F70); asm volatile("" ::: "memory"); } while (0)
#define BAR() asm volatile("s_waitcnt lgkmcnt(0)\n\ts_barrier" ::: "memory")
#define PIN1(a) asm volatile("" : "+v"(a) :: "memory")
#define PIN2(a, b) asm volatile("" : "+v"(a), "+v"(b) :: "memory")
#define BAR0() asm volatile("s_barrier" ::: "memory")
__device__ __forceinline__ unsigned fresh_lane() { unsigned l; asm volatile("v_mbcnt_lo_u32_b32 %0, -1, 0\n\tv_mbcnt_hi_u32_b32 %0, -1, %0" : "=v"(l)); __builtin_assume(l < 64u); return l; }
__device__ __forceinline__ void wait_until(unsigned long long t) { while ((long long)(__builtin_amdgcn_s_memtime() - t) < 0) __builtin_amdgcn_s_sleep(1); }
struct Clk { unsigned ta, ma, cpq, qpc; };
__device__ __forceinline__ int sx24(unsigned v) { return (int)(v << 8) >> 8; }
#define WAIT_FINE \
        "s_memrealtime vcc\n\t" \
        "s_memtime s[100:101]\n\t" \
        "s_brev_b32 %[fi], -2\n\t" \
        "s_waitcnt lgkmcnt(0)\n\t" \
        "s_mov_b32 %[rp], vcc_lo\n\t" \
        "s_lshl_b32 vcc_hi, vcc_lo, 2\n\t" \
        "s_sub_u32 %[x], %[t], vcc_hi\n\t" \
        "s_bfe_i32 %[x], %[x], 0x180000\n\t" \
        "s_min_i32 %[x], %[x], 4000\n\t" \
        "s_max_i32 %[x], %[x], -4000\n\t" \
        "s_add_u32 %[t], vcc_hi, %[x]\n\t" \
        "s_mul_i32 %[x], %[x], %[cq]\n\t" \
        "s_ashr_i32 %[x], %[x], 8\n\t" \
        "s_sub_u32 %[tm], %[t], %[ta]\n\t" \
        "s_bfe_i32 %[tm], %[tm], 0x180000\n\t" \
        "s_min_i32 %[tm], %[tm], 1000000\n\t" \
        "s_max_i32 %[tm], %[tm], -1000000\n\t" \
        "s_mul_i32 %[tm], %[tm], %[cq]\n\t" \
        "s_ashr_i32 %[tm], %[tm], 8\n\t" \
        "s_add_u32 %[tm], %[tm], %[ma]\n\t" \
        "s_sub_u32 %[tm], %[tm], s100\n\t" \
        "s_lshr_b32 vcc_hi, %[cq], 4\n\t" \
        "s_sub_u32 vcc_hi, %[x], vcc_hi\n\t" \
        "s_max_i32 %[tm], %[tm], vcc_hi\n\t" \
        "s_min_i32 %[tm], %[tm], %[x]\n\t" \
        "s_add_u32 %[tm], %[tm], s100\n" \
        "1:\n\t" \
        "s_memrealtime vcc\n\t" \
        "s_memtime s[100:101]\n\t" \
        "s_waitcnt lgkmcnt(0)\n\t" \
        "s_cmp_eq_u32 vcc_lo, %[rp]\n\t" \
        "s_cbranch_scc1 2f\n\t" \
        "s_mov_b32 %[rp], vcc_lo\n\t" \
        "s_lshl_b32 %[x], vcc_lo, 2\n\t" \
        "s_sub_u32 %[x], %[t], %[x]\n\t" \
        "s_bfe_i32 %[x], %[x], 0x180000\n\t" \
        "s_mul_i32 %[x], %[x], %[cq]\n\t" \
        "s_ashr_i32 %[x], %[x], 8\n\t" \
        "s_add_u32 %[x], %[x], s100\n\t" \
        "s_sub_u32 s101, %[x], %[tm]\n\t" \
        "s_cmp_lt_i32 s101, %[fi]\n\t" \
        "s_cselect_b32 %[tm], %[x], %[tm]\n\t" \
        "s_mov_b32 %[fi], 0\n" \
        "2:\n\t" \
        "s_sub_u32 %[x], %[tm], s100\n\t" \
        "s_cmp_gt_i32 %[x], 260\n\t" \
        "s_cbranch_scc1 1b\n\t" \
        "s_cmp_eq_u32 %[fi], 0\n\t" \
        "s_cselect_b32 %[ta], %[t], %[ta]\n\t" \
        "s_cselect_b32 %[ma], %[tm], %[ma]\n" \
        "3:\n\t" \
        "s_memtime s[100:101]\n\t" \
        "s_waitcnt lgkmcnt(0)\n\t" \
        "s_sub_u32 %[x], %[tm], s100\n\t" \
        "s_cmp_gt_i32 %[x], 92\n\t" \
        "s_cbranch_scc1 3b\n\t" \
        "s_max_i32 %[x], %[x], 0\n\t" \
        "s_andn2_b32 %[x], %[x], 3\n\t" \
        "s_sub_u32 %[x], 104, %[x]\n\t" \
        "s_getpc_b64 vcc\n\t" \
        "s_add_u32 vcc_lo, vcc_lo, %[x]\n\t" \
        "s_addc_u32 vcc_hi, vcc_hi, 0\n\t" \
        "s_setpc_b64 vcc\n\t" \
        ".rept 23\n\t" \
        "s_nop 0\n\t" \
        ".endr\n\t"
#define WAIT_FINE_OUT [tm] "=&s"(tm), [rp] "=&s"(rp), [x] "=&s"(x), [fi] "=&s"(fi), [t] "+s"(t), [ta] "+s"(c.ta), [ma] "+s"(c.ma)
__device__ __forceinline__ void wait_fine_bar(unsigned t, Clk& c) {
    unsigned tm, rp, x, fi;
    asm volatile(WAIT_FINE "s_barrier" : WAIT_FINE_OUT : [cq] "s"(c.cpq) : "vcc", "scc", "s100", "s101", "memory");
}
__device__ __forceinline__ void wait_cyc_bar(unsigned tm) {
    unsigned x;
    asm volatile(
        "3:\n\t"
        "s_memtime s[100:101]\n\t"
        "s_waitcnt lgkmcnt(0)\n\t"
        "s_sub_u32 %0, %1, s100\n\t"
        "s_cmp_gt_i32 %0, 92\n\t"
        "s_cbranch_scc1 3b\n\t"
        "s_max_i32 %0, %0, 0\n\t"
        "s_andn2_b32 %0, %0, 3\n\t"
        "s_sub_u32 %0, 104, %0\n\t"
        "s_getpc_b64 vcc\n\t"
        "s_add_u32 vcc_lo, vcc_lo, %0\n\t"
        "s_addc_u32 vcc_hi, vcc_hi, 0\n\t"
        "s_setpc_b64 vcc\n\t"
        ".rept 23\n\t"
        "s_nop 0\n\t"
        ".endr\n\t"
        "s_barrier"
        : "=&s"(x) : "s"(tm) : "vcc", "scc", "s100", "s101", "memory");
}
__device__ __forceinline__ unsigned cyc_at(const Clk& c, int d) {
    d = min(d, 20000);
    asm volatile("" : "+s"(d));
    return c.ma + (unsigned)((max(d, -4000) * (int)c.cpq) >> 8);
}
__device__ __forceinline__ void clk_rate(Clk& c, unsigned ta0, unsigned ma0) {
    const int dq = sx24(c.ta - ta0);
    if (dq > 200) {
        const float r = fminf(fmaxf((float)(c.ma - ma0) * __builtin_amdgcn_rcpf((float)dq), 1.f), 8.f);
        asm volatile("s_nop 1\n\tv_readfirstlane_b32 %0, %2\n\tv_readfirstlane_b32 %1, %3\n\ts_nop 3" : "=s"(c.cpq), "=s"(c.qpc)
                     : "v"((unsigned)(256.f * r + 0.5f)), "v"((unsigned)(65536.f * __builtin_amdgcn_rcpf(r))));
    }
}
__device__ __forceinline__ Clk clk_init() { return Clk{4u * (unsigned)__builtin_amdgcn_s_memrealtime(), (unsigned)__builtin_amdgcn_s_memtime(), 1470u, 11413u}; }
__device__ __forceinline__ int read_sched(__amdgpu_buffer_rsrc_t rws, __amdgpu_buffer_rsrc_t rwin, int s, unsigned tg, unsigned vo, unsigned to, unsigned f, unsigned g0, int& lt, int& xs, unsigned& fc) {
    int d = (int)((f - g0) & 0xFFFFFFu);
    if (__builtin_expect(__any((f >> 24) != tg) || s == 0, 0)) {
        for (unsigned spins = 0; __any((f >> 24) != tg);) {
            FC(fc);
            if (spin_fail(spins, rws)) break;
            __builtin_amdgcn_s_sleep(TSLP);
            asm volatile("" ::: "memory");
            if ((f >> 24) != tg) f = __builtin_amdgcn_raw_buffer_load_b32(rwin, vo, to, 16);
        }
        const unsigned f0 = __builtin_amdgcn_readfirstlane(f) - 0x100000u;
        d = (int)((f - f0) & 0xFFFFFFu) + (int)((f0 - g0) & 0xFFFFFFu);
    }
    d = max(d, __builtin_amdgcn_update_dpp(0, d, 0xB1, 0xF, 0xF, true));
    d = max(d, __builtin_amdgcn_update_dpp(0, d, 0x4E, 0xF, 0xF, true));
    d = max(d, __builtin_amdgcn_update_dpp(0, d, 0x141, 0xF, 0xF, true));
    d = max(d, __builtin_amdgcn_update_dpp(0, d, 0x140, 0xF, 0xF, true));
    d = max(d, __builtin_amdgcn_update_dpp(0, d, 0x142, 0xA, 0xF, true));
    d = max(__builtin_amdgcn_readlane(d, 16), __builtin_amdgcn_readlane(d, 48));
    const int l0 = lt, x0 = xs;
    d -= 0x400000;
    const bool miss = d > l0 + min(x0 + LTM, LTX);
    if (s >= SAD) { lt = miss ? l0 : min(max(d, l0 - 1 - ((l0 - d) >> LTS)), l0 + LTU); xs = miss ? min(x0 + XSU, XSM) : max(x0 - 1 - (x0 >> XSS), s < XFS ? XF : 0); }
    if (s == 1) lt = LT0;
    return 0x400000 + (s < SAD ? d + (tg ? PEN0 : PEN00) : miss ? d + PEN : l0 + x0) + M1 + M2 + x0;
}
constexpr int QS = 272;
constexpr int TQS = 1152;
constexpr int L_Q = 10240, L_K = L_Q + 2 * TQS, L_S = L_K + 2 * TQS, L_TOK = L_S + 256, L_H = L_TOK + 49152, L_END = L_H + 2 * 40960;
typedef __attribute__((ext_vector_type(4))) short v4i16_t;
__device__ __forceinline__ u32x2 ldtr(const char* p) { return __builtin_bit_cast(u32x2, __builtin_amdgcn_ds_read_tr16_b64_v4i16((LAS v4i16_t*)p)); }
__device__ __forceinline__ u32x2 ldtr1(const LAS char* p) { return __builtin_bit_cast(u32x2, __builtin_amdgcn_ds_read_tr16_b64_v4i16((LAS v4i16_t*)p)); }
__device__ __forceinline__ u32x4 ldtr2(const LAS char* p0, const LAS char* p1) {
    const u32x2 x = __builtin_bit_cast(u32x2, __builtin_amdgcn_ds_read_tr16_b64_v4i16((LAS v4i16_t*)p0)), y = __builtin_bit_cast(u32x2, __builtin_amdgcn_ds_read_tr16_b64_v4i16((LAS v4i16_t*)p1));
    return (u32x4){x[0], x[1], y[0], y[1]};
}
__device__ __forceinline__ u32x4 ldtr2(const char* p0, const char* p1) { const u32x2 x = ldtr(p0), y = ldtr(p1); return (u32x4){x[0], x[1], y[0], y[1]}; }

__device__ __forceinline__ unsigned poll_lim(int& budget) {
    --budget;
    unsigned lim = budget > 0 ? 0xFFFFFFFEu : 0xFFFFFFFFu;
    asm volatile("" : "+s"(lim));
    return lim;
}
__device__ __forceinline__ bool mem_missing(unsigned lds, unsigned lim) {
    const LAS char* p = (const LAS char*)(size_t)(lds + fresh_lane() * 16u);
    unsigned m = 0;
#pragma unroll 1
    for (int h = 0; h < 2; ++h) {
        u32x4 v[5];
#pragma unroll
        for (int i = 0; i < 5; ++i) v[i] = *(const LAS u32x4*)(p + h * 5120 + i * 1024);
        SB();
#pragma unroll
        for (int i = 0; i < 5; ++i) m = max(max(m, max(v[i][0], v[i][1])), max(v[i][2], v[i][3]));
    }
    return __any(m > lim);
}
#define GATHER \
        "buffer_load_dwordx4 %[vo], %[rw], %[s0] offen sc1 lds\n\t" \
        "buffer_load_dwordx4 %[vo], %[rw], %[s0] offen offset:1024 sc1 lds\n\t" \
        "buffer_load_dwordx4 %[vo], %[rw], %[s0] offen offset:2048 sc1 lds\n\t" \
        "buffer_load_dwordx4 %[vo], %[rw], %[s0] offen offset:3072 sc1 lds\n\t" \
        "s_movk_i32 m0, 0x1000\n\t" \
        "s_nop 0\n\t" \
        "buffer_load_dwordx4 %[vo], %[rw], %[s4] offen sc1 lds\n\t" \
        "buffer_load_dwordx4 %[vo], %[rw], %[s4] offen offset:1024 sc1 lds\n\t" \
        "buffer_load_dwordx4 %[vo], %[rw], %[s4] offen offset:2048 sc1 lds\n\t" \
        "buffer_load_dwordx4 %[vo], %[rw], %[s4] offen offset:3072 sc1 lds\n\t" \
        "s_movk_i32 m0, 0x2000\n\t" \
        "s_nop 0\n\t" \
        "buffer_load_dwordx4 %[vo], %[rw], %[s1] offen sc1 lds\n\t" \
        "buffer_load_dwordx4 %[vo], %[rw], %[s1] offen offset:1024 sc1 lds"
#define GATHER_IN [vo] "v"(vo), [rw] "s"(rwin), [s0] "s"(so0), [s4] "s"(so0 + 4096u), [s1] "s"(so1)
__device__ __forceinline__ void gather_at(unsigned t, Clk& c, __amdgpu_buffer_rsrc_t rwin, unsigned vo, unsigned so0, unsigned so1) {
    unsigned tm, rp, x, fi;
    asm volatile("s_mov_b32 m0, 0\n\t" WAIT_FINE GATHER : WAIT_FINE_OUT : [cq] "s"(c.cpq), GATHER_IN : "vcc", "scc", "s100", "s101", "m0", "memory");
}
__device__ __forceinline__ void dma4k(__amdgpu_buffer_rsrc_t r, unsigned vo, unsigned so, unsigned lds) {
    asm volatile(
        "s_mov_b32 m0, %3\n\t"
        "s_nop 0\n\t"
        "buffer_load_dwordx4 %0, %1, %2 offen sc1 lds\n\t"
        "buffer_load_dwordx4 %0, %1, %2 offen offset:1024 sc1 lds\n\t"
        "buffer_load_dwordx4 %0, %1, %2 offen offset:2048 sc1 lds\n\t"
        "buffer_load_dwordx4 %0, %1, %2 offen offset:3072 sc1 lds"
        :: "v"(vo), "s"(r), "s"(so), "s"(lds) : "m0", "memory");
}
__device__ __forceinline__ int bs2(int c) { return (c & ~3) | ((c & 1) << 1) | ((c >> 1) & 1); }
__device__ __forceinline__ void get_mem0(u32x4 (&m)[2], int t) {
    const u16* mem0 = (const u16*)karg<2>();
#pragma unroll
    for (int i = 0; i < 2; ++i) {
        const int c = min(t + 512 * i, 639);
        const unsigned* src = (const unsigned*)(mem0 + ((c & 6) + ((c >> 3) & 1)) * D_ + (c >> 4) * 16 + (c & 1) * 2);
        m[i] = (u32x4){src[0], src[2], src[4], src[6]};
    }
    SB();
}
__device__ __forceinline__ void put_mem0(const u32x4 (&m)[2], char* memL, int t) { *(u32x4*)(memL + t * 16) = m[0]; *(u32x4*)(memL + min(t + 512, 639) * 16) = m[1]; }
__device__ __forceinline__ f32x2 hadd(f32x4 c) { return (f32x2){c[0], c[1]} + (f32x2){c[2], c[3]}; }
__device__ __forceinline__ unsigned hpack(f32x4 c) { const f32x2 y = hadd(c); return pack2(y[0], y[1]); }
__device__ __forceinline__ u32x8 cat8(u32x4 lo, u32x4 hi) { return (u32x8){lo[0], lo[1], lo[2], lo[3], hi[0], hi[1], hi[2], hi[3]}; }
__device__ __forceinline__ u32x4 half8(const u32x8& v, int u) { return u ? (u32x4){v[4], v[5], v[6], v[7]} : (u32x4){v[0], v[1], v[2], v[3]}; }
__device__ __forceinline__ void dma1k(__amdgpu_buffer_rsrc_t r, unsigned vo, unsigned so, unsigned lds) {
    asm volatile("s_mov_b32 m0, %3\n\ts_nop 0\n\tbuffer_load_dwordx4 %0, %1, %2 offen sc1 lds" :: "v"(vo), "s"(r), "s"(so), "s"(lds) : "m0", "memory");
}
template <int N, int ST> __device__ __forceinline__ void stage_h(__amdgpu_buffer_rsrc_t rws, unsigned so, unsigned lds, int lane) {
#pragma unroll
    for (int i = 0; i < N; ++i) dma1k(rws, lane * 16u, so + i * ST, lds + i * ST);
}
template <int N, int ST, int M> __device__ __forceinline__ void check_h(__amdgpu_buffer_rsrc_t rws, unsigned so, unsigned lds, int lane) {
    asm volatile("s_waitcnt vmcnt(%0)" :: "n"(M) : "memory");
    for (unsigned spins = 0; spins < SPIN_CAP; ++spins) {
        unsigned m = 0;
#pragma unroll
        for (int i = 0; i < N; ++i) m = max(m, smax(*(const LAS u32x4*)(size_t)(lds + lane * 16u + i * ST)));
        if (!__any(m == 0xFFFFFFFFu)) break;
        stage_h<N, ST>(rws, so, lds, lane);
        asm volatile("s_waitcnt vmcnt(0)" ::: "memory");
    }
}
__device__ __forceinline__ void wait_h(__amdgpu_buffer_rsrc_t rws, __amdgpu_buffer_rsrc_t rfl, unsigned fb, int seg, int& hrdy) {
    unsigned lim = hrdy ? 63u : (unsigned)min(seg + HLA, 63);
    if (__builtin_expect(hrdy <= seg, 0)) for (unsigned spins = 0;;) {
        asm volatile("" ::: "memory");
        const u32x4 f = __builtin_amdgcn_raw_buffer_load_b128(rfl, min(fresh_lane(), lim) * 128u, W_H + fb, 16);
        const unsigned long long miss = __ballot((f[0] | f[1] | f[2] | f[3]) != 0u);
        hrdy = miss ? __builtin_ctzll(miss) : (int)lim + 1;
        if (hrdy > seg) break;
        if (spin_fail(spins, rws)) break;
        lim = (unsigned)min(seg + HLA, 63);
        __builtin_amdgcn_s_sleep(HSLP);
    }
}
__device__ __forceinline__ u32x4 ask_h(__amdgpu_buffer_rsrc_t rfl, unsigned fb, unsigned lim) { return __builtin_amdgcn_raw_buffer_load_b128(rfl, min(fresh_lane(), lim) * 128u, W_H + fb, 16); }
__device__ __forceinline__ int see_h(u32x4 f) { const unsigned long long miss = __ballot((f[0] | f[1] | f[2] | f[3]) != 0u); return miss ? __builtin_ctzll(miss) : 64; }
template <int CTRL> __device__ __forceinline__ unsigned dppu(unsigned v) { return (unsigned)__builtin_amdgcn_mov_dpp((int)v, CTRL, 0xF, 0xF, true); }
template <int CTRL> __device__ __forceinline__ u32x4 ror4(u32x4 v) { return (u32x4){dppu<CTRL>(v[0]), dppu<CTRL>(v[1]), dppu<CTRL>(v[2]), dppu<CTRL>(v[3])}; }
__device__ __forceinline__ float xmax16(float v) {
    auto q = __builtin_amdgcn_permlane16_swap(__float_as_uint(v), __float_as_uint(v), false, false);
    return fmx(__uint_as_float(q[0]), __uint_as_float(q[1]));
}
__device__ __forceinline__ float xsum16(float v) {
    auto q = __builtin_amdgcn_permlane16_swap(__float_as_uint(v), __float_as_uint(v), false, false);
    return __uint_as_float(q[0]) + __uint_as_float(q[1]);
}
__device__ __forceinline__ float addswap16(float a, float b) {
    auto r = __builtin_amdgcn_permlane16_swap(__float_as_uint(a), __float_as_uint(b), false, false);
    return __uint_as_float(r[0]) + __uint_as_float(r[1]);
}
#define LANES() \
    const int lane = (int)fresh_lane(), tid = w * 64 + lane, fr = lane & 15, fq = lane >> 4; \
    const int sidx = (lane & 2) ? 0xEEEE : 0x4444; \
    const int ma0 = fq * 256 + (fr & 1) * 128 + (fr >> 1) * 16; \
    const int prow = 16 * ((fr >> 2) & 1) + 4 * (fr >> 3) + (fr & 3), hoff = fr * 128; \
    (void)tid; (void)sidx; (void)ma0; (void)prow; (void)hoff

__device__ __forceinline__ int seg_after(unsigned long long m, int j) { m = j >= 63 ? 0ull : m >> (j + 1); return m ? j + 1 + __builtin_ctzll(m) : NSEG; }
__device__ __forceinline__ int seg_done(unsigned long long m, int f0, int f1) { return min(seg_after(~m, f0), seg_after(m, f1)) - 1; }
template <int OFF> __device__ __forceinline__ void ldfrag(u32x4& d, const LAS char* p) { asm volatile("ds_read_b128 %0, %1 offset:%2" : "=v"(d) : "v"(p), "n"(OFF) : "memory"); }
template <int U, class FM, class FV> __device__ __forceinline__ void project(const u32x8 (&wt)[2][10], f32x4 (&tc)[2][2], u32x4 (&bb)[8], const LAS char* c0, const LAS char* c1, const LAS char* n0, const LAS char* n1, FM mid, FV vm) {
    constexpr int t = U >> 2, k = U & 3, v = (U + 7) % 40;
    if (U == 33) mid();
    vm(IC<U>());
    const LAS char* p = U < 33 ? (v >> 1 & 1 ? c1 : c0) : (v >> 1 & 1 ? n1 : n0);
    if (U < 2) asm volatile("s_nop 0\n\ts_waitcnt lgkmcnt(6)\n\tv_mfma_f32_16x16x32_bf16 %0, %3, %7, 0\n\tds_read_b128 %2, %5 offset:%6\n\tv_mfma_f32_16x16x32_bf16 %1, %4, %7, 0"
                            : "=&v"(tc[0][k & 1]), "=&v"(tc[1][k & 1]), "+v"(bb[(U + 7) & 7]) : "v"(half8(wt[0][t], k >> 1)), "v"(half8(wt[1][t], k >> 1)), "v"(p), "n"((v >> 2) * 4096 + (v & 1) * 2048), "v"(bb[U & 7]) : "memory");
    else asm volatile("s_waitcnt lgkmcnt(6)\n\tv_mfma_f32_16x16x32_bf16 %0, %3, %7, %0\n\tds_read_b128 %2, %5 offset:%6\n\tv_mfma_f32_16x16x32_bf16 %1, %4, %7, %1"
                      : "+v"(tc[0][k & 1]), "+v"(tc[1][k & 1]), "+v"(bb[(U + 7) & 7]) : "v"(half8(wt[0][t], k >> 1)), "v"(half8(wt[1][t], k >> 1)), "v"(p), "n"((v >> 2) * 4096 + (v & 1) * 2048), "v"(bb[U & 7]) : "memory");
    if constexpr (U < 39) project<U + 1>(wt, tc, bb, c0, c1, n0, n1, mid, vm);
    else asm volatile("s_nop 7" : "+v"(tc[0][0]), "+v"(tc[0][1]), "+v"(tc[1][0]), "+v"(tc[1][1]));
}
__device__ __forceinline__ void project_end(u32x4 (&bb)[8]) {
    asm volatile("s_waitcnt lgkmcnt(0)" : "+v"(bb[0]), "+v"(bb[1]), "+v"(bb[2]), "+v"(bb[3]), "+v"(bb[4]), "+v"(bb[5]), "+v"(bb[6]), "+v"(bb[7]) :: "memory");
}
__device__ __forceinline__ void project0(u32x4 (&bb)[8], const LAS char* c0, const LAS char* c1) {
    ldfrag<0>(bb[0], c0); ldfrag<2048>(bb[1], c0); ldfrag<0>(bb[2], c1); ldfrag<2048>(bb[3], c1);
    ldfrag<4096>(bb[4], c0); ldfrag<6144>(bb[5], c0); ldfrag<4096>(bb[6], c1); ldfrag<6144>(bb[7], c1);
    project_end(bb);
}
constexpr int WLB = 20992;
template <bool O, class F> __device__ __forceinline__ void load_w(u32x8 (&wt)[2][10], const char* W, int hh, int w, int c0, int cd, char* st, F gap) {
    const int lane = (int)fresh_lane(), fr = lane & 15, fq = lane >> 4, x = w & 3;
    const __amdgpu_buffer_rsrc_t r = mkrsrc(W, D_ * HD * 2);
    u32x4 raw[40];
    const unsigned vo = O ? (unsigned)(lane * 16) : (unsigned)(fq * 8192 + fr * 16);
    unsigned so = O ? (unsigned)(hh * 128 + 8 * x) * 1280u : (unsigned)(8 * x * 8192 + hh * 256);
    asm volatile("" : "+s"(so));
    auto fetch = [&](int i) {
        gap(-1);
        if (O) raw[i] = __builtin_amdgcn_raw_buffer_load_b128(r, vo + (i % 5 & 3) * 1024u, so + (unsigned)((32 * (i / 10) + 4 * (i / 5 & 1)) * 1280 + (i % 5 >> 2) * 4096), WAUX);
        else raw[i] = __builtin_amdgcn_raw_buffer_load_b128(r, vo, so + (unsigned)(32 * (i >> 1) + 4 * (i & 1)) * 8192u, WAUX);
    };
#pragma unroll
    for (int i = 0; i < 5 * WLA; ++i) { fetch(i); if (i % 5 == 4) { BAR0(); gap(i / 5); } }
    st += (w >> 2) * WLB;
    const int row = 4 * fq + (fr >> 2);
    int wo[5];
#pragma unroll
    for (int e = 0; e < 5; ++e) {
        const int id = 64 * e + lane, rr = id / 80, ch = id - 80 * rr;
        wo[e] = O ? (4 * x + rr) * 1312 + (ch >> 1) * 32 + (ch & 1) * 4 : (4 * x + fq) * 256 + ((fr ^ (2 * ((4 * x + fq) & 7))) * 16) + e * 4096;
    }
    const int r0 = O ? row * 1312 + x * 320 + (fr & 3) * 8 : row * 256 + (((c0 + ((fr >> 1) & 1)) ^ (2 * (row & 7))) * 16) + (fr & 1) * 8;
    const int r1 = O ? 0 : row * 256 + (((c0 + cd + ((fr >> 1) & 1)) ^ (2 * (row & 7))) * 16) + (fr & 1) * 8;
#pragma unroll
    for (int p = 0; p < 8; ++p) {
        char* b = st + (p & 1) * 2 * WLB;
#pragma unroll
        for (int e = 0; e < 5; ++e) {
            const u32x4 v = raw[5 * p + e];
            if (O) {
                unsigned* d = (unsigned*)(b + wo[e]);
                d[0] = __builtin_amdgcn_perm(v[2], v[0], 0x05040100); d[2] = __builtin_amdgcn_perm(v[3], v[1], 0x05040100);
                d[4] = __builtin_amdgcn_perm(v[2], v[0], 0x07060302); d[6] = __builtin_amdgcn_perm(v[3], v[1], 0x07060302);
            } else *(u32x4*)(b + wo[e]) = v;
        }
        BAR();
#pragma unroll
        for (int e = 0; e < 5; ++e) if (p + WLA < 8) fetch(5 * (p + WLA) + e);
        SB();
        gap(p + WLA);
#pragma unroll
        for (int e = 0; e < 10; ++e) {
            const int P = O ? p : 5 * p + (e >> 1), d = 2 * (P & 3);
            const u32x2 y = ldtr(b + (O ? r0 + e * 32 : (e & 1 ? r1 : r0) + (e >> 1) * 4096));
            u32x8& t = O ? wt[P >> 2][e] : wt[e & 1][P >> 2];
            t[d] = y[0]; t[d + 1] = y[1];
        }
    }
    BAR();
}

__global__ __launch_bounds__(512, 2) B3ATTR void b3_kernel(RecArgs a) {
    extern __shared__ __attribute__((aligned(16))) char smem[];
    char* memL = smem;
    char* tokL = smem + L_TOK;
    char* hL = smem + L_H;
    int w = __builtin_amdgcn_readfirstlane(threadIdx.x >> 6);
    __amdgpu_buffer_rsrc_t rwin = mkrsrc(a.win, W_END + 2 * PAIR);
    __amdgpu_buffer_rsrc_t rws = mkrsrc(a.ctl, (unsigned)OFF_WIN);
    asm volatile("" : "+s"(rwin), "+s"(rws));
    unsigned long long tref = 0;
    const unsigned long long tstart = __builtin_amdgcn_s_memtime();
    auto get_tag = [&]() __attribute__((always_inline)) {
        unsigned long long e;
        asm volatile("s_buffer_load_dwordx2 %0, %1, 8\n\ts_waitcnt lgkmcnt(0)" : "=s"(e) : "s"(rws) : "memory");
        e += 0x9E3779B97F4A7C15ull;
        return (u32x2){(unsigned)e, (unsigned)(e >> 32)};
    };
    auto role = [&](unsigned rot) {
        const unsigned vx = blockIdx.x + rot;
        const int j = blockIdx.x >> 3, x = vx & 3, hh = 8 * (blockIdx.x & 3) + (j & 7);
        int r;
        if (!(vx & 4)) r = j < 8 ? hh : j < 16 ? 32 + hh : j < 26 ? 80 + 10 * x + j : j < 30 ? 255 - 4 * (j - 26) - x : NREC + 2 + 2 * x + j;
        else r = j < 8 ? NQT + hh : j < 16 ? 64 + hh : j < 24 ? NREC - 16 + 8 * x + j : NXT + hh;
        return r;
    };
    int bid = role(xcc_id() - blockIdx.x + BADG * (blockIdx.x >> 3));
    unsigned nh = 0, nz = 0, mc = 0, qz = 0;
    auto hint = [&](unsigned vo, unsigned so) __attribute__((always_inline)) {
        const u32x2 tag = get_tag();
        u32x2 f;
        for (unsigned spins = 0; spins < SPIN_CAP; ++spins) {
            asm volatile("" ::: "memory");
            f = __builtin_amdgcn_raw_buffer_load_b64(rwin, vo, so, 16);
            if (!__any(f[1] != tag[1] || (f[0] ^ tag[0]) > 255u)) break;
            __builtin_amdgcn_s_sleep(4);
        }
        return f[0] & 255u;
    };
    auto tell = [&](unsigned so, unsigned v) __attribute__((always_inline)) {
        const u32x2 tag = get_tag();
        __builtin_amdgcn_raw_buffer_store_b64((u32x2){(tag[0] & ~255u) | v, tag[1]}, rwin, 0u, so, 16);
    };
    auto share = [&](unsigned v) __attribute__((always_inline)) {
        if (w == 0) *(volatile LAS unsigned*)(smem + L_S + 64) = v;
        BAR();
        v = __builtin_amdgcn_readfirstlane(*(volatile LAS unsigned*)(smem + L_S + 64));
        BAR();
        return v;
    };
    auto config = [&]() __attribute__((always_inline)) {
        unsigned v = 0;
        if (w == 0) {
            const unsigned l = fresh_lane(), f = hint(l * 8u, W_CFG), rot = __builtin_amdgcn_readfirstlane(hint(0u, W_ARM));
            const unsigned long long ok = __ballot((f & 4u) && !((l + rot) & 4u)), h = __ballot(f & 1u), q = __ballot(((f >> 1) ^ (l + rot)) & 1u);
            v = rot;
#pragma unroll
            for (int p = 0; p < 2; ++p) {
                unsigned long long m = ok & (p ? 0xFF00FF00FF00FF00ull : 0x00FF00FF00FF00FFull);
                const bool hb = 2 * __builtin_popcountll(m & h) > __builtin_popcountll(m);
                m &= hb ? h : ~h;
                v |= (hb ? 8u : 0u) << p | (2 * __builtin_popcountll(m & q) > __builtin_popcountll(m) ? 32u : 0u) << p;
            }
        }
        v = share(v);
        nh = (v >> 3 & 1u) * HALF; nz = (v >> 4 & 1u) * HALF;
        mc = ((blockIdx.x + v) & 3u) ^ (v >> 5 & 1u); qz = v >> 6 & 1u;
        return v;
    };
    auto zpair = [&](unsigned x, unsigned k) { return (((x >> 1) * 10u + k) * 2u + ((x & 1u) ^ qz)) * PAIR + nz; };
    unsigned mo, mo1, tro;
    auto two = [&]() { const unsigned l = fresh_lane(); return l < 4 ? (l >> 1) * PAIR + (l & 1) * 4096u + nh : 0x40000000u; };
    const u32x4 SENT = {0xFFFFFFFFu, 0xFFFFFFFFu, 0xFFFFFFFFu, 0xFFFFFFFFu};
    const f32x4 z4 = {0.f, 0.f, 0.f, 0.f};
    int hrdy = 0, budget = (int)SPIN_CAP;
    unsigned fc0 = 0, fc1 = 0, fc2 = 0;
    Clk ck = {0u, 0u, 1600u, 10486u};
    int lay = 0;
    unsigned fb = 0, zb = W_ZM, hb = 0;
    unsigned tpp = 0;
    int lt = LT1, xs = XS0;
    struct Tk { unsigned vo, tw, sl; const LAS char* sp; };
    auto tk_init = [&]() {
        const unsigned l = fresh_lane();
        Tk k = {l * 16u, two(), (l & 31u) * 128u + (l >> 5) * 4u, (const LAS char*)(memL + l * 64 + 4 + ((l >> 1) & 7) * 8)};
        PIN2(k.vo, k.tw); PIN2(k.sl, k.sp);
        ck = clk_init();
        return k;
    };
    auto sched2 = [&](Tk& k, int s, unsigned f) {
        const unsigned g0 = tpp - 0x400000u;
        tpp = g0 + (unsigned)read_sched(rws, rwin, s, (unsigned)(s + 64 * lay), k.sl, tro + (unsigned)(s & 3) * 2u * PAIR, f, g0, lt, xs, fc1);
        k.sl &= ~4u;
        RT(6);
    };
    auto sched = [&](Tk& k, int s) { sched2(k, s, __builtin_amdgcn_raw_buffer_load_b32(rwin, k.sl, tro + (unsigned)(s & 3) * 2u * PAIR, 16)); };
    auto fetch = [&](const Tk& k, int s, bool first) {
        const unsigned sb = (unsigned)(s & 3) * 6u * PAIR, ta0 = ck.ta, mm0 = ck.ma;
        if (__builtin_expect(s > 0 && first, 1)) {
            while ((unsigned)sx24(tpp - TKE - 4u * (unsigned)__builtin_amdgcn_s_memrealtime()) < 20000u) __builtin_amdgcn_s_sleep(1);
            gather_at(tpp, ck, rwin, k.vo, mo + sb, mo1 + sb);
        }
        __builtin_amdgcn_s_setprio(0);
        RT(1);
        clk_rate(ck, ta0, mm0);
        unsigned tb = ck.ma - DQC;
        asm volatile("s_waitcnt vmcnt(%1) lgkmcnt(0)\n\ts_barrier\n\ts_waitcnt vmcnt(%2)\n\ts_barrier" : "+s"(tb) : "n"(10 - NB1), "n"(10 - NB2) : "memory");
        return tb;
    };
    unsigned mb = 0;
    auto again = [&](bool tk, unsigned vo, unsigned& sf, int s, unsigned lim, const LAS char*& p, const LAS char*& q) __attribute__((always_inline)) {
        const unsigned nb = mb == 0x10000u ? 0x14000u : 0x10000u;
        if (tk) {
            const unsigned c = (mc + 1u + (nb >> 14 & 1u)) & 3u, sb = W_MEM + (unsigned)(s & 3) * 6u * PAIR + nh, t = sb + (4u + (c & 1u)) * PAIR + (c >> 1) * 2048u;
            dma4k(rwin, vo, sb + c * PAIR, nb); dma4k(rwin, vo, sb + c * PAIR + 4096u, nb + 4096u);
            dma1k(rwin, vo, t, nb + 8192u); dma1k(rwin, vo, t + 1024u, nb + 9216u);
            asm volatile("s_waitcnt vmcnt(10)" : "+v"(sf) :: "memory");
        }
        const bool r = mem_missing(mb, lim);
        if (tk && !r) VMWAIT();
        if (r) { p += nb - mb; q += nb - mb; mb = nb; FC(fc0); }
        return r;
    };
    auto places = [&]() {
        mo = W_MEM + mc * PAIR + nh; mo1 = W_MEM + (4u + (mc & 1u)) * PAIR + nh + (mc >> 1) * 2048u;
        tro = W_T + (mc & 1u) * PAIR + nh + (mc >> 1) * 4096u;
    };
    auto fresh = [&]() {
        asm volatile("" : "+s"(bid), "+s"(w), "+s"(nh), "+s"(nz), "+s"(mc), "+s"(qz), "+s"(lay));
        fb = lay * W_FL; hb = O_H + (1 - lay) * (4u * 40960u); zb = W_ZM + lay * (NSEG % 3 * NOWN * PAIR);
        tref = 0; hrdy = 0; budget = (int)SPIN_CAP; ck = Clk{0u, 0u, 1600u, 10486u}; tpp = 0; lt = LT1; xs = XS0;
    };
    auto filter = [&]() __attribute__((always_inline)) {
        if ((bid >= 32 && bid < 64) || (bid >= NQT && bid < NXT)) {
            const unsigned u = bid < 64 ? bid : bid - NQT, l = fresh_lane();
            filter_unit((const u16*)karg<9>(), (const u16*)karg<10>(), u * 32, rws, (u16*)tokL, w * 64 + l);
            VMWAIT();
            BAR();
            if (w == 0) __builtin_amdgcn_raw_buffer_store_b64(get_tag(), rwin, (l & 1u) * 8u, W_ST + u * 128u + 64u, 16);
        }
    };
    auto settle = [&]() __attribute__((always_inline)) {
        if (TPF && w == 1 && bid >= 32) for (unsigned i = 0; i < (unsigned)OFF_WIN; i += 64u << TPF) dma1k(rws, fresh_lane() << TPF, i, L_Q);
        const int b = role(config()); if (__builtin_expect(b != bid, 0)) { bid = b; filter(); } };
    auto warm = [&]() __attribute__((always_inline)) {
        unsigned long long pc;
        unsigned len;
        asm volatile("s_getpc_b64 %0\n1:\n\ts_mov_b32 %1, 2f-1b\n\t.subsection 1\n2:\n\t.subsection 0" : "=s"(pc), "=s"(len));
        if (CPF) dma1k(mkrsrc((const void*)pc, len), fresh_lane() * 16u, (blockIdx.x >> 3) * 1024u + w * 16384u, L_END - 8192 + w * 1024);
    };
    auto a1_done = [&]() __attribute__((always_inline)) {
        VMWAIT();
        BAR();
        if (w == 0) __builtin_amdgcn_raw_buffer_store_b64(get_tag(), rwin, 0u, W_ST + (blockIdx.x - 128u >> 1) * 128u + (blockIdx.x & 1u) * 8u, 16);
        SST(29);
    };
    auto due = [&](int i, int d) __attribute__((always_inline)) {
        __builtin_amdgcn_raw_buffer_store_b32(nosent1((unsigned)__builtin_amdgcn_s_memrealtime() + d), rwin, 0u, W_E + fb + i * 128u, 16);
    };
    auto await = [&](int i, int n, int d, unsigned f = ~0u) __attribute__((always_inline)) {
        if (__any(f == ~0u)) for (unsigned spins = 0;;) {
            asm volatile("" ::: "memory");
            f = __builtin_amdgcn_raw_buffer_load_b32(rwin, min(fresh_lane(), n - 1u) * 128u, W_E + i * 128u, 16);
            if (!__any(f == ~0u)) break;
            if (spin_fail(spins, rws)) break;
            __builtin_amdgcn_s_sleep(HSLP);
        }
        while (__any(f + d - (unsigned)__builtin_amdgcn_s_memrealtime() - 1u < 1000u)) {}
    };
    constexpr int E_OUT = 0, E_A1 = 8, E_A2 = 16, E_A3 = 24, E_ZT = 28;
    auto soon = [&](int j) __attribute__((always_inline)) {
        if (w == 0) { if (j >= 4) wait_h(rws, rwin, fb, j, hrdy); else await(E_A3 + fb / 128u, 4, 0); }
        BAR();
    };
    bool slow = false;
    auto gap = [&](int p) __attribute__((always_inline)) {
        if (slow) { if (p < 0) { if (XST) __builtin_amdgcn_s_sleep(XST); } else if (XSLP && p < 7) __builtin_amdgcn_s_sleep(XSLP); }
    };
    SST(27);
    {
        LANES();
        const unsigned u = blockIdx.x - 128u;
        if (blockIdx.x < 128) {
            const unsigned po = (blockIdx.x & 8u ? W_END : W_PROBE) + ((blockIdx.x >> 1 & 24u) | (blockIdx.x & 7u)) * 128u;
            const bool pr = bid >= 32 && bid < 64 && w == 7;
            if (pr) __builtin_amdgcn_raw_buffer_store_b32(0u, rwin, (lane & 3) * HALF + (lane >> 2 & 1) * 4096u, po, 16);
            warm();
            filter();
            if (blockIdx.x >= 64 && w == 7) tell(W_CFG + (blockIdx.x - 64u) * 8u, pr ? 4u | probe(rwin, po) : 0u);
            BAR0();
        } else a1_unit<8, 512>(mkrsrc(karg<3>(), S_ * D_ * 2), u * 16, false, rws, smem, row_tw(lane), w, lane, [&](int k) {
            if (k == 0 && u == 0 && w == 0) { tell(W_ROT, xcc_id()); __builtin_amdgcn_raw_buffer_store_b32(0u, rws, 0u, 0u, 16); }
            if (k == -1) {
                u32x4 sv = SENT;
                PIN1(sv);
#pragma unroll
                for (int i = 0; i < 4; ++i) {
                    static_assert(ARM_END - OFF_ARM <= 512 * 8192 && W_PROBE + W_END - W_ZM <= 512 * 8192, "arming");
                    const unsigned g = (i * 128u + u) * 8192u;
                    st128(sv, rwin, tid * 16u, g < W_PROBE ? g : g < W_PROBE + W_END - W_ZM ? g + W_ZM - W_PROBE : 0x40000000u, 16);
                    st128(sv, rws, tid * 16u, g < ARM_END - OFF_ARM ? (unsigned)OFF_ARM + g : 0x40000000u, 16);
                }
            }
        });
        SST(28);
        if (blockIdx.x >= 128) {
            if (w == 0) *(volatile LAS unsigned*)(smem + L_END - 16) = hint(0u, W_ROT);
            a1_done();
            const unsigned rot = __builtin_amdgcn_readfirstlane(*(volatile LAS unsigned*)(smem + L_END - 16));
            bid = role(rot);
            if (bid == 255 && w == 0) {
                const u32x2 tag = get_tag();
                for (unsigned spins = 0; spins < SPIN_CAP; ++spins) {
                    asm volatile("" ::: "memory");
                    const u32x4 g = __builtin_amdgcn_raw_buffer_load_b128(rwin, lane * 128u, W_ST, 16);
                    if (!__any(((g[0] ^ tag[0]) | (g[1] ^ tag[1]) | (g[2] ^ tag[0]) | (g[3] ^ tag[1])) != 0u)) break;
                    __builtin_amdgcn_s_sleep(4);
                }
                tell(W_ARM, rot);
            }
            SST(30);
        }
    }
    if (bid < 96 || (bid >= NQT && bid < NWRK)) {
    u32x8 wt[2][10];
#pragma nounroll
    for (; lay < 2; ++lay) {
    fresh();
    const bool xt = bid >= NXT;
    SST(0);
    if ((bid >= 32 && bid < 96) || xt) {
        const int hh = (xt ? bid - NXT : bid) & 31;
        u32x4 m0[2];
        get_mem0(m0, w * 64 + fresh_lane());
        if (lay == 0 || xt) {
            wait_until(tstart + (bid < 64 ? DVO : xt ? DXT : DW1));
            if (w < 4) load_w<false>(wt, karg<7>(), hh, w, 2 * w, 8, tokL, gap); else load_w<true>(wt, karg<8>(), hh, w, 0, 0, tokL, gap);
            SST(5);
            slow = true;
            if (lay == 0) settle();
        }
        places();
        const bool vt = bid >= 64;
        const unsigned long long mv = xt ? MXV : ~MXV;
        auto nextv = [&](int j) { return min(seg_after(mv, j), NSEG - 1); };
        const int jv1 = nextv(-1), jv2 = nextv(jv1);
        {
            LANES();
            put_mem0(m0, memL, tid);
            VMWAIT();
            if (tid < 8) *(unsigned*)(smem + L_S + tid * 4) = 0u;
            SST(1);
            if (vt) {
                soon(jv1);
                SST(2);
                const unsigned so = hb + (unsigned)(jv1 * 40960 + w * 1024), lo = L_H + w * 1024;
                stage_h<5, 8192>(rws, so, lo, lane);
                BAR0();
                if (w < 4) check_h<5, 8192, 0>(rws, so, lo, lane);
                else { stage_h<10, 4096>(rws, hb + (unsigned)(jv2 * 40960 + (w - 4) * 1024), lo + 36864u, lane); check_h<5, 8192, 10>(rws, so, lo, lane); }
            }
        }
        auto steps = [&](auto cc) {
            constexpr int C = decltype(cc)::v & 1;
            constexpr bool TK = decltype(cc)::v == 3;
            LANES();
            Tk tk = {0u, 0u, 0x40000000u, nullptr};
            if (TK) { tk = tk_init(); __builtin_amdgcn_s_setprio(TKP); }
            const unsigned zi = (unsigned)(hh * 128 + fr * 8) * 2u;
            unsigned zo0 = zpair(w & 3, fq) + zi, zo1 = zo0 + 8 * PAIR, zo2 = zpair(w & 3, 8 + (fq >> 1)) + zi + (fq & 1) * 8;
            const LAS char* va = (const LAS char*)(smem + L_Q + (fr & 1) * 544 + ((fr >> 1) & 1) * 1152 + (16 * fq + 2 * (fr >> 2)) * 4);
            LAS char* vw = (LAS char*)(smem + L_Q + (fq & 1) * 544 + (fq >> 1) * 1152 + (16 * w + bs2(fr)) * 4);
            const LAS char* mp0 = (const LAS char*)(memL + ma0);
            PIN2(zo0, zo1); PIN1(zo2);
            if (C == 0) { PIN2(mp0, vw); } else PIN1(va);
            u32x4 za = SENT, zc = SENT;
            u32x2 ze = {0u, 0u};
            auto put_z2 = [&](int s) {
                const unsigned z2b = s < NSEG ? O_Z2 + (unsigned)s * (NOWN * 8192u) + (unsigned)(10 * (w & 3) * 8192 + hh * 256) : 0x40000000u, l2 = fresh_lane();
                st128(za, rws, (l2 >> 4) * 8192u + (l2 & 15u) * 16u, z2b, 16);
                st128(zc, rws, (l2 >> 4) * 8192u + (l2 & 15u) * 16u, z2b + 4u * 8192u, 16);
                __builtin_amdgcn_raw_buffer_store_b64(ze, rws, (l2 >> 5) * 8192u + (l2 & 15u) * 16u + ((l2 >> 4) & 1u) * 8u, z2b + 8u * 8192u, 16);
            };
            for (int s = 0; s < NSEG; ++s) {
                RT(0);
                bool redo = false;
                unsigned sf = 0;
                do {
                    unsigned lim = s > 0 ? poll_lim(budget) : ~0u;
                    unsigned long long sus;
                    asm volatile("" : "+s"(lim));
                    if (C == 0) {
                        asm volatile(
                            "v_mov_b64 v[200:201], 0\n\t"
                            "v_mov_b64 v[202:203], 0\n\t"
                            "v_mov_b64 v[204:205], 0\n\t"
                            "v_mov_b64 v[206:207], 0\n\t"
                            "s_setprio 2\n\t"
                            "s_waitcnt lgkmcnt(0)\n\t"
                            "s_barrier\n\t"
                            MTA(0)
                            "ds_read_b128 v[212:215], %[mp] offset:0\n\t"
                            "ds_read_b128 v[216:219], %[mp] offset:1024\n\t"
                            "ds_read_b128 v[220:223], %[mp] offset:2048\n\t"
                            "s_barrier\n\t"
                            "ds_read_b128 v[224:227], %[mp] offset:3072\n\t"
                            "ds_read_b128 v[228:231], %[mp] offset:4096\n\t"
                            "ds_read_b128 v[232:235], %[mp] offset:5120\n\t"
                            "s_waitcnt lgkmcnt(5)\n\t"
                            "v_smfmac_f32_16x16x64_bf16 v[200:203], v[212:215], %[w00], %[ix]\n\t"
                            "v_smfmac_f32_16x16x64_bf16 v[204:207], v[212:215], %[w10], %[ix]\n\t"
                            "s_waitcnt lgkmcnt(4)\n\t"
                            "v_smfmac_f32_16x16x64_bf16 v[200:203], v[216:219], %[w01], %[ix]\n\t"
                            "v_smfmac_f32_16x16x64_bf16 v[204:207], v[216:219], %[w11], %[ix]\n\t"
                            "s_barrier\n\t"
                            MTA(1)
                            "ds_read_b128 v[236:239], %[mp] offset:6144\n\t"
                            "ds_read_b128 v[240:243], %[mp] offset:7168\n\t"
                            "s_waitcnt lgkmcnt(5)\n\t"
                            "v_smfmac_f32_16x16x64_bf16 v[200:203], v[220:223], %[w02], %[ix]\n\t"
                            "ds_read_b128 v[244:247], %[mp] offset:8192\n\t"
                            "ds_read_b128 v[248:251], %[mp] offset:9216\n\t"
                            "s_waitcnt lgkmcnt(4)\n\t"
                            "v_smfmac_f32_16x16x64_bf16 v[200:203], v[224:227], %[w03], %[ix]\n\t"
                            "v_smfmac_f32_16x16x64_bf16 v[200:203], v[228:231], %[w04], %[ix]\n\t"
                            "v_smfmac_f32_16x16x64_bf16 v[200:203], v[232:235], %[w05], %[ix]\n\t"
                            "s_waitcnt lgkmcnt(3)\n\t"
                            "v_smfmac_f32_16x16x64_bf16 v[200:203], v[236:239], %[w06], %[ix]\n\t"
                            "s_waitcnt lgkmcnt(2)\n\t"
                            "v_smfmac_f32_16x16x64_bf16 v[200:203], v[240:243], %[w07], %[ix]\n\t"
                            "s_waitcnt lgkmcnt(1)\n\t"
                            "v_smfmac_f32_16x16x64_bf16 v[200:203], v[244:247], %[w08], %[ix]\n\t"
                            "s_waitcnt lgkmcnt(0)\n\t"
                            "v_smfmac_f32_16x16x64_bf16 v[200:203], v[248:251], %[w09], %[ix]\n\t"
                            "v_smfmac_f32_16x16x64_bf16 v[204:207], v[220:223], %[w12], %[ix]\n\t"
                            "s_nop 2\n\t"
                            "v_smfmac_f32_16x16x64_bf16 v[204:207], v[224:227], %[w13], %[ix]\n\t"
                            "s_nop 2\n\t"
                            "v_add_f32 v200, v200, v202\n\t"
                            "v_add_f32 v201, v201, v203\n\t"
                            "v_smfmac_f32_16x16x64_bf16 v[204:207], v[228:231], %[w14], %[ix]\n\t"
                            "v_cvt_pk_bf16_f32 v200, v200, v201\n\t"
                            "ds_write_b32 %[vw], v200\n\t"
                            MTA(2)
                            "v_smfmac_f32_16x16x64_bf16 v[204:207], v[232:235], %[w15], %[ix]\n\t"
                            "v_smfmac_f32_16x16x64_bf16 v[204:207], v[236:239], %[w16], %[ix]\n\t"
                            "v_smfmac_f32_16x16x64_bf16 v[204:207], v[240:243], %[w17], %[ix]\n\t"
                            "v_smfmac_f32_16x16x64_bf16 v[204:207], v[244:247], %[w18], %[ix]\n\t"
                            "s_waitcnt lgkmcnt(0)\n\t"
                            "s_barrier\n\t"
                            MTA(5)
                            "v_smfmac_f32_16x16x64_bf16 v[204:207], v[248:251], %[w19], %[ix]\n\t"
                            "s_nop 7\n\t"
                            "v_pk_add_f32 v[204:205], v[204:205], v[206:207]\n\t"
                            "v_cvt_pk_bf16_f32 v200, v204, v205\n\t"
                            "ds_write_b32 %[vw], v200 offset:256\n\t"
                            MTA(3)
                            "s_waitcnt vmcnt(0) lgkmcnt(0)\n\t"
                            "s_barrier\n\t"
                            "s_setprio 0\n\t"
                            "v_cmp_u_f32 %[sus], v204, v205\n\t"
                            : [sus] "=s"(sus) : [mp] "v"(mp0), [vw] "v"(vw), [ix] "v"(sidx),
                              [w00] "v"(wt[0][0]), [w01] "v"(wt[0][1]), [w02] "v"(wt[0][2]), [w03] "v"(wt[0][3]), [w04] "v"(wt[0][4]), [w05] "v"(wt[0][5]), [w06] "v"(wt[0][6]), [w07] "v"(wt[0][7]), [w08] "v"(wt[0][8]), [w09] "v"(wt[0][9]), [w10] "v"(wt[1][0]), [w11] "v"(wt[1][1]), [w12] "v"(wt[1][2]), [w13] "v"(wt[1][3]), [w14] "v"(wt[1][4]), [w15] "v"(wt[1][5]), [w16] "v"(wt[1][6]), [w17] "v"(wt[1][7]), [w18] "v"(wt[1][8]), [w19] "v"(wt[1][9])
                            : "v200", "v201", "v202", "v203", "v204", "v205", "v206", "v207", "v208", "v209", "v212", "v213", "v214", "v215", "v216", "v217", "v218", "v219", "v220", "v221", "v222", "v223", "v224", "v225", "v226", "v227", "v228", "v229", "v230", "v231", "v232", "v233", "v234", "v235", "v236", "v237", "v238", "v239", "v240", "v241", "v242", "v243", "v244", "v245", "v246", "v247", "v248", "v249", "v250", "v251", "vcc", "memory");
                        MT(4);
                    } else {
                        f32x16 ca, cb;
                        f32x8 cc;
                        asm volatile(".irp r,212,214,216,218,220,222,224,226,228,230,232,234,236,238,240,242,244,246,248,250\n\tv_mov_b64 v[\\r:\\r+1], 0\n\t.endr"
                                     : "={v[212:227]}"(ca), "={v[228:243]}"(cb), "={v[244:251]}"(cc));
                        unsigned sr = tro + (unsigned)(s & 3) * 2u * PAIR;
                        asm volatile("" : "+s"(sr));
                        if (TK) { fetch(tk, s, !redo); asm volatile("s_waitcnt vmcnt(0)\n\ts_barrier" ::: "memory"); RT(3); }
                        else asm volatile("s_waitcnt lgkmcnt(0)\n\ts_barrier\n\ts_barrier\n\ts_barrier" ::: "memory");
                        unsigned zs = zb;
                        asm volatile(
                            "s_setprio 1\n\t"
                            "s_waitcnt vmcnt(0) lgkmcnt(0)\n\t"
                            "s_barrier\n\t"
                            "ds_read_b64_tr_b16 v[192:193], %[va]\n\t"
                            "ds_read_b64_tr_b16 v[194:195], %[va] offset:32\n\t"
                            "s_waitcnt lgkmcnt(0)\n\t"
                            "v_smfmac_f32_16x16x64_bf16 v[212:215], v[192:195], %[w0], %[ix]\n\t"
                            "v_smfmac_f32_16x16x64_bf16 v[220:223], v[192:195], %[w2], %[ix]\n\t"
                            "v_smfmac_f32_16x16x64_bf16 v[216:219], v[192:195], %[w1], %[ix]\n\t"
                            "v_smfmac_f32_16x16x64_bf16 v[224:227], v[192:195], %[w3], %[ix]\n\t"
                            "s_barrier\n\t"
                            MTA(4)
                            "ds_read_b64_tr_b16 v[196:197], %[va] offset:256\n\t"
                            "v_smfmac_f32_16x16x64_bf16 v[228:231], v[192:195], %[w4], %[ix]\n\t"
                            "ds_read_b64_tr_b16 v[198:199], %[va] offset:288\n\t"
                            "v_smfmac_f32_16x16x64_bf16 v[236:239], v[192:195], %[w6], %[ix]\n\t"
                            "v_smfmac_f32_16x16x64_bf16 v[232:235], v[192:195], %[w5], %[ix]\n\t"
                            "v_smfmac_f32_16x16x64_bf16 v[240:243], v[192:195], %[w7], %[ix]\n\t"
                            "v_cmp_u_f32 %[sus], v212, v213\n\t"
                            "v_smfmac_f32_16x16x64_bf16 v[244:247], v[192:195], %[w8], %[ix]\n\t"
                            "v_smfmac_f32_16x16x64_bf16 v[248:251], v[192:195], %[w9], %[ix]\n\t"
                            "s_waitcnt lgkmcnt(0)\n\t"
                            "v_smfmac_f32_16x16x64_bf16 v[212:215], v[196:199], %[x0], %[ix]\n\t"
                            "s_nop 0\n\t"
                            "v_smfmac_f32_16x16x64_bf16 v[220:223], v[196:199], %[x2], %[ix]\n\t"
                            "s_cmp_lg_u64 %[sus], 0\n\t"
                            "s_cselect_b32 %[zs], 2.0, %[zs]\n\t"
                            "v_smfmac_f32_16x16x64_bf16 v[216:219], v[196:199], %[x1], %[ix]\n\t"
                            "s_nop 1\n\t"
                            "v_smfmac_f32_16x16x64_bf16 v[224:227], v[196:199], %[x3], %[ix]\n\t"
                            "v_add_f32 v212, v212, v214\n\t"
                            "v_smfmac_f32_16x16x64_bf16 v[228:231], v[196:199], %[x4], %[ix]\n\t"
                            "v_add_f32 v213, v213, v215\n\t"
                            "v_smfmac_f32_16x16x64_bf16 v[236:239], v[196:199], %[x6], %[ix]\n\t"
                            "v_add_f32 v220, v220, v222\n\t"
                            "v_smfmac_f32_16x16x64_bf16 v[232:235], v[196:199], %[x5], %[ix]\n\t"
                            "v_add_f32 v221, v221, v223\n\t"
                            "v_smfmac_f32_16x16x64_bf16 v[240:243], v[196:199], %[x7], %[ix]\n\t"
                            "v_cvt_pk_bf16_f32 v200, v212, v213\n\t"
                            "v_smfmac_f32_16x16x64_bf16 v[244:247], v[196:199], %[x8], %[ix]\n\t"
                            "v_cvt_pk_bf16_f32 v201, v220, v221\n\t"
                            "v_smfmac_f32_16x16x64_bf16 v[248:251], v[196:199], %[x9], %[ix]\n\t"
                            ".if %[tk]\n\tbuffer_load_dword %[sf], %[sl], %[rw], %[sr] offen sc1\n\t.endif\n\t"
                            "v_add_f32 v216, v216, v218\n\t"
                            "v_add_f32 v217, v217, v219\n\t"
                            "v_add_f32 v224, v224, v226\n\t"
                            "v_add_f32 v225, v225, v227\n\t"
                            "v_cvt_pk_bf16_f32 v202, v216, v217\n\t"
                            "v_cvt_pk_bf16_f32 v203, v224, v225\n\t"
                            "v_permlane32_swap_b32 v200, v201\n\t"
                            "v_pk_add_f32 v[228:229], v[228:229], v[230:231]\n\t"
                            "v_permlane32_swap_b32 v202, v203\n\t"
                            "v_min_u32 v201, -2, v201\n\t"
                            "v_pk_add_f32 v[236:237], v[236:237], v[238:239]\n\t"
                            "v_min_u32 v203, -2, v203\n\t"
                            "v_permlane16_swap_b32 v200, v202\n\t"
                            "v_pk_add_f32 v[232:233], v[232:233], v[234:235]\n\t"
                            "v_permlane16_swap_b32 v201, v203\n\t"
                            "v_pk_add_f32 v[240:241], v[240:241], v[242:243]\n\t"
                            MTA(5)
                            "buffer_store_dwordx4 v[200:203], %[z0], %[rw], %[zs] offen sc1\n\t"
                            "v_cvt_pk_bf16_f32 v204, v228, v229\n\t"
                            "v_cvt_pk_bf16_f32 v205, v236, v237\n\t"
                            "v_cvt_pk_bf16_f32 v206, v232, v233\n\t"
                            "v_cvt_pk_bf16_f32 v207, v240, v241\n\t"
                            "v_permlane32_swap_b32 v204, v205\n\t"
                            "v_pk_add_f32 v[244:245], v[244:245], v[246:247]\n\t"
                            "v_permlane32_swap_b32 v206, v207\n\t"
                            "v_min_u32 v205, -2, v205\n\t"
                            "v_pk_add_f32 v[248:249], v[248:249], v[250:251]\n\t"
                            "v_min_u32 v207, -2, v207\n\t"
                            "v_permlane16_swap_b32 v204, v206\n\t"
                            "v_cvt_pk_bf16_f32 v208, v244, v245\n\t"
                            "v_permlane16_swap_b32 v205, v207\n\t"
                            "v_cvt_pk_bf16_f32 v209, v248, v249\n\t"
                            "buffer_store_dwordx4 v[204:207], %[z1], %[rw], %[zs] offen sc1\n\t"
                            "s_nop 0\n\t"
                            "v_permlane32_swap_b32 v208, v209\n\t"
                            "v_min_u32 v209, -2, v209\n\t"
                            "buffer_store_dwordx2 v[208:209], %[z2], %[rw], %[zs] offen sc1\n\t"
                            ".if %[tk]\n\ts_setprio 0\n\ts_cmp_lg_u64 %[sus], 0\n\ts_cbranch_scc1 1f\n\ts_waitcnt vmcnt(3)\n1:\n\t.endif"
                            : [sf] "+&v"(sf), [zs] "+&s"(zs), [sus] "=&s"(sus), "=&{v[200:203]}"(za), "=&{v[204:207]}"(zc), "=&{v[208:209]}"(ze), "+{v[212:227]}"(ca), "+{v[228:243]}"(cb), "+{v[244:251]}"(cc)
                            : [va] "v"(va), [ix] "v"(sidx), [sl] "v"(tk.sl), [sr] "s"(sr), [rw] "s"(rwin), [z0] "v"(zo0), [z1] "v"(zo1), [z2] "v"(zo2), [tk] "n"(TK ? 1 : 0),
                              [w0] "v"(wt[0][0]), [w1] "v"(wt[0][1]), [w2] "v"(wt[0][2]), [w3] "v"(wt[0][3]), [w4] "v"(wt[0][4]), [w5] "v"(wt[0][5]), [w6] "v"(wt[0][6]), [w7] "v"(wt[0][7]), [w8] "v"(wt[0][8]), [w9] "v"(wt[0][9]),
                              [x0] "v"(wt[1][0]), [x1] "v"(wt[1][1]), [x2] "v"(wt[1][2]), [x3] "v"(wt[1][3]), [x4] "v"(wt[1][4]), [x5] "v"(wt[1][5]), [x6] "v"(wt[1][6]), [x7] "v"(wt[1][7]), [x8] "v"(wt[1][8]), [x9] "v"(wt[1][9])
                            : "v192", "v193", "v194", "v195", "v196", "v197", "v198", "v199", "scc", "memory");
                        RT(4);
                        MT(6);
                        tref = __builtin_amdgcn_s_memtime();
                        __builtin_amdgcn_s_setprio(TKP);
                    }
                    redo = false;
                    if (__builtin_expect(sus != 0, 0)) {
                        redo = again(TK, tk.vo, sf, s, lim, mp0, tk.sp);
                        if (!redo && C == 1) { st128(za, rwin, zo0, zb, 16); st128(zc, rwin, zo1, zb, 16); __builtin_amdgcn_raw_buffer_store_b64(ze, rwin, zo2, zb, 16); }
                    }
                } while (__builtin_expect(redo, 0));
                if (__builtin_expect(mb != 0, 0)) { mp0 -= mb; mb = 0; }
                if (s < 8) SST(8 + s);
                if (C == 0) {
                    if (s + 1 < NSEG) {
                        __builtin_amdgcn_s_sleep(DZR);
                        const unsigned zq = s == 0 ? 0x40000000u : zb == W_ZM ? W_ZM + 2u * NOWN * PAIR : zb - NOWN * PAIR;
                        unsigned s1;
                        asm volatile("v_mov_b32 %0, -1" : "=v"(s1));
                        st128(((u32x4){s1, s1, s1, s1}), rwin, zo0, zq, 16);
                        st128(((u32x4){s1, s1, s1, s1}), rwin, zo1, zq, 16);
                        __builtin_amdgcn_raw_buffer_store_b64((u32x2){s1, s1}, rwin, zo2, zq, 16);
                    }
                } else {
                    if (TK && s + 1 < NSEG) sched2(tk, s, sf);
                    if (!TK) wait_until(tref + DZ2);
                    if (s + 2 < NSEG) put_z2(s);
                }
                if (budget <= 0) __builtin_amdgcn_raw_buffer_store_b32(1u, rws, 0u, 0u, 16);
                zb = zb == W_ZM + 2u * NOWN * PAIR ? W_ZM : zb + NOWN * PAIR;
                RT(5);
                RTS();
                MT(7);
                MTS();
            }
        };
        if (!vt) {
            if (w < 4) steps(IC<0>()); else if (w < 7) steps(IC<1>()); else steps(IC<3>());
        } else {
            BAR();
            SST(3);
            LANES();
            const int ni = __builtin_popcountll(mv) + 1;
            if (w < 4) {
                const LAS char* c0 = (const LAS char*)(hL + hoff + ((fq ^ (fr & 7)) << 4));
                const LAS char* c1 = (const LAS char*)(hL + hoff + (((4 + fq) ^ (fr & 7)) << 4));
                const LAS char* n0 = c0 + 40960; const LAS char* n1 = c1 + 40960;
                u32x4 bb[8];
                project0(bb, c0, c1);
                for (int i = 1; i < ni; ++i) {
                    if (vt) MT(0);
                    f32x4 tc[2][2];
                    project<0>(wt, tc, bb, c0, c1, n0, n1,
                        [&]() {
                            MT(1);
                            if (__builtin_expect(i > 1, 1)) BAR();
                            MT(2);
                        }, [](auto) {});
                    { const LAS char* x = c0; c0 = n0; n0 = x; x = c1; c1 = n1; n1 = x; }
                    char* tb = tokL + (i & 1) * 32 * QS + (16 * w + 4 * fq) * 2;
#pragma unroll
                    for (int g = 0; g < 2; ++g)
#pragma unroll
                        for (int tt = 0; tt < 2; ++tt) *(u32x2*)(tb + (prow + 8 * tt) * QS + g * 128) = (u32x2){pack2(tc[g][tt][0], tc[g][tt][1]), pack2(tc[g][tt][2], tc[g][tt][3])};
                    asm volatile("" ::: "memory");
                    *(volatile LAS int*)(smem + L_S + 16 + w * 4) = i;
                    if (__builtin_expect(i == 1, 0)) { BAR(); if (VSLP) __builtin_amdgcn_s_sleep(VSLP); project0(bb, c0, c1); }
                    if (vt) { MT(3); const int s = i & 63; MTRT(); MTS(); }
                }
                project_end(bb);
            } else {
                __builtin_amdgcn_s_setprio(OPR);
                const unsigned hv = lane * 16u, fz_ = W_FZ + fb + (unsigned)(hh * 128 + xt * 4096 + (w - 4) * 8192);
                unsigned fp = ~0u;
                u32x4 r = SENT;
                int jo = jv1, jh = jv2;
                check_h<10, 4096, 0>(rws, hb + (unsigned)(jv2 * 40960 + (w - 4) * 1024), L_H + 40960 + (w - 4) * 1024, lane);
                SST(16);
                auto rows = [&](int b) {
                    for (int t = 0; t < 10; ++t) dma1k(rws, hv, hb + (unsigned)(jh * 40960 + (w - 4) * 1024 + t * 4096), (unsigned)(L_H + ((b + 1) & 1) * 40960 + (w - 4) * 1024 + t * 4096));
                    VMWAIT();
                };
                for (int i = 1; i < ni; ++i, jo = nextv(jo)) {
                    const int b = i + (i >= ZLAG);
                    if (__builtin_expect(i == ZLAG, 0)) {
                        VMWAIT();
                        jh = nextv(jh);
                        wait_h(rws, rwin, fb, jh, hrdy);
                        BAR0();
                        rows(i);
                    }
                    jh = nextv(jh);
                    u32x4 hf;
                    asm volatile("" : "=v"(hf));
                    if (__builtin_expect(hrdy <= jh, 0)) asm volatile("buffer_load_dwordx4 %0, %1, %2, %3 offen sc1" : "+v"(hf) : "v"(min(lane, jh + 16) * 128), "s"(rwin), "s"(W_H + fb) : "memory");
                    if (b < ni) BAR();
                    if (i == 1) SST(17);
                    if (vt) MT(1);
                    for (;;) {
                        const u32x4 f = *(volatile LAS u32x4*)(smem + L_S + 16);
                        if (__builtin_amdgcn_readfirstlane(min(min(f[0], f[1]), min(f[2], f[3]))) >= (unsigned)i) break;
                        __builtin_amdgcn_s_sleep(1);
                    }
                    asm volatile("" ::: "memory");
                    u32x4 vf[2][4];
#pragma unroll
                    for (int ks = 0; ks < 4; ++ks)
#pragma unroll
                        for (int t = 0; t < 2; ++t) vf[t][ks] = *(const u32x4*)(tokL + (i & 1) * 32 * QS + (16 * t + fr) * QS + ks * 64 + fq * 16);
                    SB();
                    if (__builtin_expect(hrdy <= jh, 0)) { asm volatile("s_waitcnt vmcnt(0)" : "+v"(hf) :: "memory"); hrdy = min(see_h(hf), jh + 17); }
                    const bool ok = hrdy > jh;
                    if (__builtin_expect((i < ZLAG || !ok) && (int)fp > ZEARLY, 0)) { VMWAIT(); __builtin_amdgcn_raw_buffer_store_b32(fp, rwin, 0u, fz_, 16); }
                    if (__builtin_expect(jo == 0, 0) && w == 7) {
                        if (!(hh & 7)) due(E_ZT + (hh >> 3), lay ? DZF : DZF0);
                        __builtin_amdgcn_raw_buffer_store_b32(((4u * (unsigned)__builtin_amdgcn_s_memrealtime() + 4 * (lay ? DZF : DZF0) + DV0) & 0xFFFFFFu) | (unsigned)lay << 30, rwin, two(), W_T + hh * 128u + 4u, 16);
                    }
                    if (XDL && __builtin_expect(jo == 1, 0)) __builtin_amdgcn_s_sleep(XDL);
                    if (i == 1) SST(18);
                    if (vt) MT(0);
                    const unsigned ztb = O_Z + (unsigned)(jo * ZTSEG) * 2u + (unsigned)(10 * (w - 4) * H_ + hh) * 1024u;
                    f32x4 c[2][2];
                    unsigned hs = hb + (unsigned)(jh * 40960 + (w - 4) * 1024), hd = (unsigned)(L_H + ((b + 1) & 1) * 40960 + (w - 4) * 1024);
                    asm volatile("" : "+s"(hs), "+s"(hd));
#pragma unroll
                    for (int nt = 0; nt < 11; ++nt) {
                        auto& cc = c[nt & 1]; auto& cp = c[(nt + 1) & 1];
#pragma unroll
                        for (int ks = 0; ks < 4; ++ks) {
                            if (nt < 10) { cc[0] = mfma32(vf[0][ks], half8(wt[ks >> 1][nt], ks & 1), ks ? cc[0] : z4); PIN1(cc[0]); }
                            if (nt > 0 && ks >= 1 && ks < 3) { r[2 * ks - 2] = pack2(cp[ks - 1][0], cp[ks - 1][1]); PIN1(r); }
                            if (nt < 10) { cc[1] = mfma32(vf[1][ks], half8(wt[ks >> 1][nt], ks & 1), ks ? cc[1] : z4); PIN1(cc[1]); }
                            if (nt > 0 && ks >= 1 && ks < 3) { r[2 * ks - 1] = pack2(cp[ks - 1][2], cp[ks - 1][3]); PIN1(r); }
                            if (nt > 0 && ks == 2) st128(r, rws, lane * 16u, ztb + (unsigned)((nt - 1) * H_) * 1024u, 16);
                            if (nt < 5 && !(ks & 1) && __builtin_expect(ok, 1)) dma1k(rws, hv, hs + (2 * nt + (ks >> 1)) * 4096u, hd + (2 * nt + (ks >> 1)) * 4096u);
                            if (nt == 5 && ks == 0 && __builtin_expect(ok, 1)) {
                                asm volatile("s_waitcnt vmcnt(14)" ::: "memory");
                                __builtin_amdgcn_raw_buffer_store_b32(fp, rwin, 0u, fz_, 16);
                            }
                            SB();
                        }
                    }
                    if (i == 1) SST(19);
                    if (vt) MT(2);
                    fp = (unsigned)jo;
                    if (__builtin_expect(jo <= ZEARLY || i + 1 == ni || i + 1 == ZLAG || !ok, 0)) {
                        VMWAIT();
                        __builtin_amdgcn_raw_buffer_store_b32(fp, rwin, 0u, fz_, 16);
                    } else asm volatile("s_waitcnt vmcnt(7)" ::: "memory");
                    if (i == 1) SST(20);
                    if (__builtin_expect(!ok, 0)) { wait_h(rws, rwin, fb, jh, hrdy); rows(b); }
                    if (jo < 8) SST(8 + jo);
                    if (vt && (!BSPARSE || jo % 6 == 2)) { const int s = jo; RT(0); RTS(); }
                    if (vt) { MT(3); const int s = i & 63; MTRT(); MTS(); }
                }
            }
            __builtin_amdgcn_s_setprio(0);
            VMWAIT();
            BAR();
        }
    }
    if (bid < 32 || bid >= NQT) {
        LANES();
        const int hh = (bid < 32 ? bid : bid - NQT) & 31;
        u32x4 m0[2];
        get_mem0(m0, tid);
        if (lay == 0 || xt) {
            wait_until(tstart + (bid >= NQT ? DQT : DW1));
            load_w<false>(wt, karg<5>(w >> 2), hh, w, 4 * (w & 3), 2, tokL, gap);
            SST(5);
            if (!xt) settle();
        }
        places();
        const bool qt = bid >= NQT;
        const unsigned long long mq = xt ? MXQ : ~MXQ;
        auto nextq = [&](int j) { return min(seg_after(mq, j), NSEG - 1); };
        const int jq0 = qt ? nextq(1) : 0, jq1 = nextq(jq0);
        put_mem0(m0, memL, tid);
        if (tid < 4) *(unsigned*)(smem + L_S + tid * 4) = ~0u;
        SST(1);
        soon(jq1);
        SST(2);
        const unsigned so1 = hb + (unsigned)(jq1 * 40960 + w * 1024), lo1 = L_H + 40960 + w * 1024;
        stage_h<5, 8192>(rws, hb + (unsigned)(jq0 * 40960 + w * 1024), lo1 - 40960u, lane);
        BAR0();
        stage_h<5, 8192>(rws, so1, lo1, lane);
        check_h<5, 8192, 5>(rws, hb + (unsigned)(jq0 * 40960 + w * 1024), lo1 - 40960u, lane);
        BAR();
        SST(3);
        int qrdy = 0;
        {
            LANES();
            const unsigned to0 = (unsigned)((w ^ 4) >> 2) * 8192u + (unsigned)((w & 3) * 64 + (lane ^ ((fq & 1) * (w < 4 ? 8 : 5)))) * 16u, to1 = to0 + 4096u, hv = lane * 16u;
            const LAS char* c0 = (const LAS char*)(hL + hoff + ((fq ^ (fr & 7)) << 4));
            const LAS char* c1 = (const LAS char*)(hL + hoff + (((4 + fq) ^ (fr & 7)) << 4));
            u32x4 bb[8], g0, g1;
            asm volatile("" : "=v"(g0), "=v"(g1));
            f32x4 tc[2][2];
            auto pack = [&]() {
                g0 = (u32x4){pack2(tc[0][0][0], tc[0][0][1]), pack2(tc[0][0][2], tc[0][0][3]), pack2(tc[1][0][0], tc[1][0][1]), pack2(tc[1][0][2], tc[1][0][3])};
                g1 = (u32x4){pack2(tc[0][1][0], tc[0][1][1]), pack2(tc[0][1][2], tc[0][1][3]), pack2(tc[1][1][0], tc[1][1][1]), pack2(tc[1][1][2], tc[1][1][3])};
            };
            const LAS char* n0 = c0 + 40960; const LAS char* n1 = c1 + 40960;
            if (!qt) {
                project0(bb, c0, c1);
                for (int j = 0; j < 2; ++j) {
                    project<0>(wt, tc, bb, c0, c1, n0, n1, [&]() { if (j == 0) { check_h<5, 8192, 0>(rws, so1, lo1, lane); BAR(); } }, [](auto) {});
                    pack();
                    *(u32x4*)(tokL + j * 16384 + to0) = g0; *(u32x4*)(tokL + j * 16384 + to1) = g1;
                    c0 = n0; c1 = n1;
                    if (j == 0 && w == 7) __builtin_amdgcn_raw_buffer_store_b32(((4u * (unsigned)__builtin_amdgcn_s_memrealtime() + (lay ? DQ0 : DQ00)) & 0xFFFFFFu) | (unsigned)lay << 30, rwin, two(), W_T + hh * 128u, 16);
                }
                project_end(bb);
                VMWAIT();
            } else {
                project0(bb, c0, c1);
                unsigned hs = hb + (unsigned)(jq1 * 40960 + w * 1024), hd = (unsigned)(L_H + 40960 + w * 1024), fv = tid == 0 ? 0u : 0x40000000u;
                const unsigned fq_ = W_FQ + fb + (unsigned)(hh * 128 + xt * 64);
                const int jl = 63 - __builtin_clzll(mq);
                if (w < 4) __builtin_amdgcn_s_setprio(QTP);
                for (int jp = 1, j = jq0, jn = jq1;; jp = j, j = jn, jn = nextq(jn)) {
                    MT(0);
                    project<0>(wt, tc, bb, c0, c1, n0, n1,
                        [&]() {
                            if (__builtin_expect(hrdy <= nextq(jn), 0)) hrdy = min(see_h(g0), nextq(jn) + 17);
                            hs = hrdy > nextq(jn) ? hb + (unsigned)(nextq(jn) * 40960 + w * 1024) : 0x40000000u; hd = (unsigned)(2 * L_H + 40960 + 2 * w * 1024) - hd;
                            unsigned fj = (unsigned)jp;
                            asm volatile("" : "+s"(hs), "+s"(hd));
                            PIN1(fj);
                            MT(1);
                            if (__builtin_expect(j == jq0, 0)) check_h<5, 8192, 0>(rws, so1, lo1, lane);
                            VMWAIT();
                            BAR();
                            MT(2);
                            if (w == 0) __builtin_amdgcn_raw_buffer_store_b32(fj, rwin, fv, fq_, 16);
                        },
                        [&](auto uu) {
                            constexpr int u = decltype(uu)::v;
                            if (u == QEU && __builtin_expect(j <= QE2, 0)) {
                                asm volatile("s_waitcnt vmcnt(3)\n\ts_barrier" ::: "memory");
                                if (w == 0) __builtin_amdgcn_raw_buffer_store_b32((unsigned)jp, rwin, fv, fq_, 16);
                            }
                            if (u == 20 && __builtin_expect(hrdy <= nextq(jn), 0)) g0 = ask_h(rwin, fb, nextq(jn) + 16);
                            if (u == 34 || u == 38 || u == 2 || u == 6 || u == 10) { constexpr int t = ((u + 6) % 40) / 4; dma1k(rws, hv, hs + t * 8192u, hd + t * 8192u); }
                        });
                    pack();
                    const unsigned qs = O_QK + (unsigned)(j * H_ + hh) * 16384u;
                    st128(g0, rws, to0, qs, 16);
                    st128(g1, rws, to1, qs, 16);
                    { const LAS char* x = c0; c0 = n0; n0 = x; x = c1; c1 = n1; n1 = x; }
                    if (__builtin_expect(j <= QEARLY || j == jl, 0)) {
                        VMWAIT();
                        BAR0();
                        if (tid == 0) __builtin_amdgcn_raw_buffer_store_b32((unsigned)j, rwin, 0u, fq_, 16);
                        if (j == jl) break;
                    }
                    if (__builtin_expect(hrdy <= nextq(jn), 0)) {
                        wait_h(rws, rwin, fb, nextq(jn), hrdy);
                        hs = hb + (unsigned)(nextq(jn) * 40960 + w * 1024);
                        dma1k(rws, hv, hs, hd); dma1k(rws, hv, hs + 8192u, hd + 8192u);
                    }
                    MT(3);
                    if (j < 8) SST(8 + j);
                    if (!BSPARSE || j % 6 == 2) { const int s = j; RT(0); RTS(); MTRT(); MTS(); }
                }
                project_end(bb);
                VMWAIT();
                BAR();
            }
        }
        SST(4);
        auto steps = [&](auto cc) {
        constexpr int C = decltype(cc)::v >> 1, R = decltype(cc)::v & 1;
        LANES();
        const LAS char* ka = (const LAS char*)(smem + L_K + (fr & 1) * TQS + (4 * fq + (fr >> 2)) * 8);
        const LAS char* qa = (const LAS char*)(smem + L_Q + (fr & 1) * 544 + ((fr >> 1) & 1) * 1152 + (16 * fq + 2 * (fr >> 2)) * 4);
        LAS char* qw = (LAS char*)(C < 2 ? smem + L_Q + (fq & 1) * 544 + (fq >> 1) * 1152 + (32 * w + 8 * (fr >> 2) + bs2(fr & 3)) * 4
                                         : smem + L_K + (fq >> 1) * TQS + (32 * (w & 3) + fr) * 8 + (fq & 1) * 4);
        const LAS char* mp0 = (const LAS char*)(memL + ma0);
        const int z = 16 * (fr >> 3) + 4 * (fr & 3) + 2 * ((fr >> 2) & 1);
        const int ko = ((z >> 4) * 256 + fq * 16 + ((z & 15) ^ ((fq & 1) * 5))) * 16;
        unsigned pa0 = (4u * C + 2u * (hh >> 4)) * PAIR + (unsigned)((hh & 15) * 512 + R * 256 + lane * 4);
        unsigned pa2 = ((8u + (fr >> 3)) * PAIR + C * 4096u + (unsigned)((hh >> 1) * 256 + R * 128 + (hh & 1) * 64 + fq * 16 + (fr & 6) * 2)) | (fr & 1) << 30;
        PIN2(ka, qw); PIN1(mp0);
        constexpr bool TK = C == 2 && R == 1;
        Tk tk = {0u, 0u, 0x40000000u, nullptr};
        if (TK) { tk = tk_init(); __builtin_amdgcn_s_setprio(TKP); }
        if (C < 2) { PIN2(qa, pa0); PIN1(pa2); }
        unsigned fqv = w == 4 ? (lane & 1) * 64u : 0x40000000u;
        if (C == 2 && !TK) PIN1(fqv);
        int qf = 1, tbo = 0;
        for (int s = 0; s < NSEG; ++s) {
            RT(0);
            const char* tb = tokL + tbo;
            unsigned pb = __builtin_amdgcn_readfirstlane(s + 1 < NSEG ? W_P + (unsigned)(s & 3) * 10u * PAIR + nh : 0x40000000u);
            unsigned pb1 = pb + PAIR;
            asm volatile("" : "+s"(pb), "+s"(pb1));
            if (s == 0) BAR();
            u32x8 kx[2][2];
            auto keys = [&]() {
#pragma unroll
                for (int u = 0; u < 4; ++u) kx[u >> 1][u & 1] = cat8(*(const u32x4*)(tb + (ko ^ ((u >> 1) * 16)) + (u & 1) * 2048), *(const u32x4*)(tb + (ko ^ ((u >> 1) * 16)) + (u & 1) * 2048 + 1024));
                PIN2(kx[0][0], kx[0][1]); PIN2(kx[1][0], kx[1][1]);
            };
            if (C < 2) keys();
            bool redo = false;
            unsigned sf = 0;
            do {
                unsigned lim = s > 0 ? poll_lim(budget) : ~0u;
                unsigned long long sus;
                f32x4 sc;
                if (C < 2) {
                    f32x4 s0, s1, s2;
                    unsigned late;
                    asm volatile(
                        ".irp r,232,234,236,238,240,242,244,246,248,250\n\tv_mov_b64 v[\\r:\\r+1], 0\n\t.endr\n\t"
                        "s_cmp_lg_u32 %[p0], 2.0\n\t"
                        "s_cbranch_scc1 1f\n\t"
                        "s_mov_b32 %[lt], -1\n\ts_barrier\n\ts_barrier\n\ts_barrier\n\ts_branch 2f\n"
                        "1:\n\t"
                        "s_setprio 2\n\t"
                        "s_waitcnt lgkmcnt(0)\n\t"
                        "s_barrier\n\t"
                        MTA(0)
                        "ds_read_b128 v[208:211], %[mp] offset:0\n\t"
                        "ds_read_b128 v[212:215], %[mp] offset:1024\n\t"
                        "ds_read_b128 v[216:219], %[mp] offset:2048\n\t"
                        "s_barrier\n\t"
                        "ds_read_b128 v[220:223], %[mp] offset:3072\n\t"
                        "s_waitcnt lgkmcnt(3)\n\t"
                        "v_smfmac_f32_16x16x64_bf16 v[232:235], v[208:211], %[w00], %[ix]\n\t"
                        "v_smfmac_f32_16x16x64_bf16 v[236:239], v[208:211], %[w10], %[ix]\n\t"
                        "ds_read_b128 v[224:227], %[mp] offset:4096\n\t"
                        "s_waitcnt lgkmcnt(3)\n\t"
                        "v_smfmac_f32_16x16x64_bf16 v[232:235], v[212:215], %[w01], %[ix]\n\t"
                        "v_smfmac_f32_16x16x64_bf16 v[236:239], v[212:215], %[w11], %[ix]\n\t"
                        "s_barrier\n\t"
                        MTA(1)
                        "ds_read_b128 v[228:231], %[mp] offset:5120\n\t"
                        "s_waitcnt lgkmcnt(3)\n\t"
                        "v_smfmac_f32_16x16x64_bf16 v[232:235], v[216:219], %[w02], %[ix]\n\t"
                        "v_smfmac_f32_16x16x64_bf16 v[236:239], v[216:219], %[w12], %[ix]\n\t"
                        "ds_read_b128 v[208:211], %[mp] offset:6144\n\t"
                        "s_waitcnt lgkmcnt(3)\n\t"
                        "v_smfmac_f32_16x16x64_bf16 v[232:235], v[220:223], %[w03], %[ix]\n\t"
                        "v_smfmac_f32_16x16x64_bf16 v[236:239], v[220:223], %[w13], %[ix]\n\t"
                        "ds_read_b128 v[212:215], %[mp] offset:7168\n\t"
                        "s_waitcnt lgkmcnt(3)\n\t"
                        "v_smfmac_f32_16x16x64_bf16 v[232:235], v[224:227], %[w04], %[ix]\n\t"
                        "v_smfmac_f32_16x16x64_bf16 v[236:239], v[224:227], %[w14], %[ix]\n\t"
                        "ds_read_b128 v[216:219], %[mp] offset:8192\n\t"
                        "s_waitcnt lgkmcnt(3)\n\t"
                        "v_smfmac_f32_16x16x64_bf16 v[232:235], v[228:231], %[w05], %[ix]\n\t"
                        "v_smfmac_f32_16x16x64_bf16 v[236:239], v[228:231], %[w15], %[ix]\n\t"
                        "ds_read_b128 v[220:223], %[mp] offset:9216\n\t"
                        "s_waitcnt lgkmcnt(3)\n\t"
                        "v_smfmac_f32_16x16x64_bf16 v[232:235], v[208:211], %[w06], %[ix]\n\t"
                        "v_smfmac_f32_16x16x64_bf16 v[236:239], v[208:211], %[w16], %[ix]\n\t"
                        "ds_read_b32 v207, v250 offset:%[ls]\n\t"
                        "s_waitcnt lgkmcnt(3)\n\t"
                        "v_smfmac_f32_16x16x64_bf16 v[232:235], v[212:215], %[w07], %[ix]\n\t"
                        "v_smfmac_f32_16x16x64_bf16 v[236:239], v[212:215], %[w17], %[ix]\n\t"
                        "s_waitcnt lgkmcnt(0)\n\t"
                        "v_smfmac_f32_16x16x64_bf16 v[232:235], v[216:219], %[w08], %[ix]\n\t"
                        "v_readfirstlane_b32 %[lt], v207\n\t"
                        "v_smfmac_f32_16x16x64_bf16 v[232:235], v[220:223], %[w09], %[ix]\n\t"
                        "v_smfmac_f32_16x16x64_bf16 v[236:239], v[216:219], %[w18], %[ix]\n\t"
                        "v_smfmac_f32_16x16x64_bf16 v[236:239], v[220:223], %[w19], %[ix]\n\t"
                        "s_setprio " STR(QP) "\n\t"
                        "s_nop 5\n\t"
                        "v_add_f32 v232, v232, v234\n\t"
                        "v_add_f32 v233, v233, v235\n\t"
                        "v_add_f32 v236, v236, v238\n\t"
                        "v_add_f32 v237, v237, v239\n\t"
                        "v_cvt_pk_bf16_f32 v232, v232, v233\n\t"
                        "v_cvt_pk_bf16_f32 v236, v236, v237\n\t"
                        "ds_write2_b32 %[qw], v232, v236 offset1:4\n\t"
                        MTA(2)
                        "2:"
                        : [lt] "=&s"(late), "=&{v[240:243]}"(s0), "=&{v[244:247]}"(s1), "=&{v[248:251]}"(s2)
                        : [mp] "v"(mp0), [qw] "v"(qw), [ix] "v"(sidx), [ls] "n"(L_S), [p0] "s"(pb), [w00] "v"(wt[0][0]), [w10] "v"(wt[1][0]), [w01] "v"(wt[0][1]), [w11] "v"(wt[1][1]), [w02] "v"(wt[0][2]), [w12] "v"(wt[1][2]), [w03] "v"(wt[0][3]), [w13] "v"(wt[1][3]), [w04] "v"(wt[0][4]), [w14] "v"(wt[1][4]), [w05] "v"(wt[0][5]), [w15] "v"(wt[1][5]), [w06] "v"(wt[0][6]), [w16] "v"(wt[1][6]), [w07] "v"(wt[0][7]), [w17] "v"(wt[1][7]), [w08] "v"(wt[0][8]), [w18] "v"(wt[1][8]), [w09] "v"(wt[0][9]), [w19] "v"(wt[1][9])
                        : "v207", "v208", "v209", "v210", "v211", "v212", "v213", "v214", "v215", "v216", "v217", "v218", "v219", "v220", "v221", "v222", "v223", "v224", "v225", "v226", "v227", "v228", "v229", "v230", "v231", "v232", "v233", "v234", "v235", "v236", "v237", "v238", "v239", "scc", "memory");
                    if (__builtin_expect(late == (unsigned)s, 0)) keys();
#define TAIL(A0, A2, B0, B2, C0, C2) \
                        "s_cmp_lg_u32 %[p0], 2.0\n\ts_cbranch_scc1 1f\n\ts_barrier\n\ts_barrier\n\t" \
                        "ds_read_b64_tr_b16 v[232:233], %[ka]\n\ts_waitcnt lgkmcnt(0)\n\tv_lshlrev_b32 v232, 16, v232\n\tv_cmp_u_f32 vcc, v232, v232\n\ts_branch 3f\n1:\n\t" \
                        ".if " STR(BQN) "==0\n\t" \
                        "s_setprio 3\n\t" \
                        "s_waitcnt vmcnt(0) lgkmcnt(0)\n\t" \
                        "s_barrier\n\t" \
                        MTA(3) \
                        "ds_read_b64_tr_b16 v[208:209], %[qa]\n\t" \
                        "ds_read_b64_tr_b16 v[210:211], %[qa] offset:32\n\t" \
                        "ds_read_b64_tr_b16 v[212:213], %[qa] offset:256\n\t" \
                        "ds_read_b64_tr_b16 v[214:215], %[qa] offset:288\n\t" \
                        "ds_read_b64_tr_b16 v[216:217], %[ka] offset:0\n\t" \
                        "ds_read_b64_tr_b16 v[218:219], %[ka] offset:128\n\t" \
                        "ds_read_b64_tr_b16 v[220:221], %[ka] offset:256\n\t" \
                        "ds_read_b64_tr_b16 v[222:223], %[ka] offset:384\n\t" \
                        "ds_read_b64_tr_b16 v[224:225], %[ka] offset:512\n\t" \
                        "ds_read_b64_tr_b16 v[226:227], %[ka] offset:640\n\t" \
                        "s_waitcnt lgkmcnt(8)\n\t" \
                        "v_smfmac_f32_16x16x64_bf16 v[240:243], v[208:211], %[k00], %[ix]\n\t" \
                        "ds_read_b64_tr_b16 v[228:229], %[ka] offset:768\n\t" \
                        "s_waitcnt lgkmcnt(7)\n\t" \
                        "v_smfmac_f32_16x16x64_bf16 v[240:243], v[212:215], %[k01], %[ix]\n\t" \
                        "ds_read_b64_tr_b16 v[230:231], %[ka] offset:896\n\t" \
                        "v_smfmac_f32_16x16x64_bf16 v[244:247], v[208:211], %[k10], %[ix]\n\t" \
                        "v_smfmac_f32_16x16x64_bf16 v[244:247], v[212:215], %[k11], %[ix]\n\t" \
                        "s_nop 4\n\t" \
                        ".elseif " STR(TPRE) "==0\n\t" \
                        "s_waitcnt lgkmcnt(0)\n\t" \
                        "s_barrier\n\t" \
                        "ds_read_b64_tr_b16 v[208:209], %[qa]\n\t" \
                        "ds_read_b64_tr_b16 v[210:211], %[qa] offset:32\n\t" \
                        "ds_read_b64_tr_b16 v[212:213], %[qa] offset:256\n\t" \
                        "ds_read_b64_tr_b16 v[214:215], %[qa] offset:288\n\t" \
                        "s_setprio 3\n\t" \
                        "s_waitcnt vmcnt(0) lgkmcnt(0)\n\t" \
                        "s_barrier\n\t" \
                        MTA(3) \
                        "ds_read_b64_tr_b16 v[216:217], %[ka] offset:0\n\t" \
                        "ds_read_b64_tr_b16 v[218:219], %[ka] offset:128\n\t" \
                        "ds_read_b64_tr_b16 v[220:221], %[ka] offset:256\n\t" \
                        "ds_read_b64_tr_b16 v[222:223], %[ka] offset:384\n\t" \
                        "v_smfmac_f32_16x16x64_bf16 v[240:243], v[208:211], %[k00], %[ix]\n\t" \
                        "ds_read_b64_tr_b16 v[224:225], %[ka] offset:512\n\t" \
                        "ds_read_b64_tr_b16 v[226:227], %[ka] offset:640\n\t" \
                        "v_smfmac_f32_16x16x64_bf16 v[240:243], v[212:215], %[k01], %[ix]\n\t" \
                        "ds_read_b64_tr_b16 v[228:229], %[ka] offset:768\n\t" \
                        "ds_read_b64_tr_b16 v[230:231], %[ka] offset:896\n\t" \
                        "v_smfmac_f32_16x16x64_bf16 v[244:247], v[208:211], %[k10], %[ix]\n\t" \
                        "v_smfmac_f32_16x16x64_bf16 v[244:247], v[212:215], %[k11], %[ix]\n\t" \
                        "s_nop 3\n\t" \
                        ".else\n\t" \
                        "s_waitcnt lgkmcnt(0)\n\t" \
                        "s_barrier\n\t" \
                        "ds_read_b64_tr_b16 v[208:209], %[qa]\n\t" \
                        "ds_read_b64_tr_b16 v[210:211], %[qa] offset:32\n\t" \
                        "ds_read_b64_tr_b16 v[212:213], %[qa] offset:256\n\t" \
                        "ds_read_b64_tr_b16 v[214:215], %[qa] offset:288\n\t" \
                        "s_setprio 0\n\t" \
                        "s_waitcnt vmcnt(0) lgkmcnt(0)\n\t" \
                        "v_smfmac_f32_16x16x64_bf16 v[240:243], v[208:211], %[k00], %[ix]\n\t" \
                        "v_smfmac_f32_16x16x64_bf16 v[240:243], v[212:215], %[k01], %[ix]\n\t" \
                        "s_setprio 3\n\t" \
                        "s_barrier\n\t" \
                        MTA(3) \
                        "ds_read_b64_tr_b16 v[216:217], %[ka] offset:0\n\t" \
                        "ds_read_b64_tr_b16 v[218:219], %[ka] offset:128\n\t" \
                        "ds_read_b64_tr_b16 v[220:221], %[ka] offset:256\n\t" \
                        "ds_read_b64_tr_b16 v[222:223], %[ka] offset:384\n\t" \
                        "v_smfmac_f32_16x16x64_bf16 v[244:247], v[208:211], %[k10], %[ix]\n\t" \
                        "ds_read_b64_tr_b16 v[224:225], %[ka] offset:512\n\t" \
                        "ds_read_b64_tr_b16 v[226:227], %[ka] offset:640\n\t" \
                        "v_smfmac_f32_16x16x64_bf16 v[244:247], v[212:215], %[k11], %[ix]\n\t" \
                        "ds_read_b64_tr_b16 v[228:229], %[ka] offset:768\n\t" \
                        "ds_read_b64_tr_b16 v[230:231], %[ka] offset:896\n\t" \
                        ".endif\n\t" \
                        "v_add_f32 v232, " A0 ", " A2 "\n\t" \
                        "v_mul_f32 v236, 0x3e0293ee, v232\n\t" \
                        "v_exp_f32 v236, v236\n\t" \
                        "s_waitcnt lgkmcnt(4)\n\t" \
                        "v_smfmac_f32_16x16x64_bf16 v[248:251], v[208:211], v[216:223], %[ix]\n\t" \
                        "s_waitcnt lgkmcnt(0)\n\t" \
                        "v_smfmac_f32_16x16x64_bf16 v[248:251], v[212:215], v[224:231], %[ix]\n\t" \
                        "v_add_f32 v233, " B0 ", " B2 "\n\t" \
                        "v_mul_f32 v237, 0x3e0293ee, v233\n\t" \
                        "v_exp_f32 v237, v237\n\t" \
                        "s_nop 0\n\t" \
                        "v_add_f32 v239, v236, v237\n\t" \
                        "s_nop 1\n\t" \
                        "v_add_f32_dpp v239, v239, v239 row_ror:8 row_mask:0xf bank_mask:0xf\n\t" \
                        MTA(4) \
                        "v_add_f32 v234, " C0 ", " C2 "\n\t" \
                        "v_mul_f32 v238, 0x3e0293ee, v234\n\t" \
                        "v_exp_f32 v238, v238\n\t" \
                        "s_nop 0\n\t" \
                        "v_add_f32 v239, v239, v238\n\t" \
                        "v_mov_b32_dpp v235, v238 quad_perm:[1,1,3,3] row_mask:0xf bank_mask:0xf\n\t" \
                        "s_nop 0\n\t" \
                        "v_add_f32_dpp v239, v239, v239 row_ror:4 row_mask:0xf bank_mask:0xf\n\t" \
                        "s_nop 1\n\t" \
                        "v_add_f32_dpp v239, v239, v239 row_ror:2 row_mask:0xf bank_mask:0xf\n\t" \
                        "s_nop 1\n\t" \
                        "v_add_f32_dpp v239, v239, v239 row_ror:1 row_mask:0xf bank_mask:0xf\n\t" \
                        "v_rcp_f32 v207, v239\n\t" \
                        "v_add_u32 v239, 0xe8800000, v239\n\t" \
                        "v_mul_f32 v236, v236, v207\n\t" \
                        "v_cmp_lt_u32 vcc, %[rg], v239\n\t" \
                        "v_mul_f32 v237, v237, v207\n\t" \
                        "v_cvt_pk_bf16_f32 v236, v236, v237\n\t" \
                        "s_cbranch_vccnz 3f\n\t" \
                        MTA(5) \
                        "buffer_store_dword v236, %[pa], %[rw], %[p0] offen sc1\n\t" \
                        "v_mul_f32 v238, v238, v207\n\t" \
                        "v_mul_f32 v235, v235, v207\n\t" \
                        "buffer_store_dword v236, %[pa], %[rw], %[p1] offen sc1\n\t" \
                        "v_cvt_pk_bf16_f32 v238, v238, v235\n\t" \
                        "buffer_store_dword v238, %[pm], %[rw], %[p0] offen sc1\n\t" \
                        "3:\n\t" \
                        "s_mov_b64 %[sus], vcc\n\t" \
                        "s_setprio 0\n\t"
#define TAILIO : [sus] "=&s"(sus), "=&{v[232:235]}"(sc), "+{v[240:243]}"(s0), "+{v[244:247]}"(s1), "+{v[248:251]}"(s2) \
                        : [qa] "v"(qa), [ka] "v"(ka), [ix] "v"(sidx), [pa] "v"(pa0), [pm] "v"(pa2), [rw] "s"(rwin), [p0] "s"(pb), [p1] "s"(pb1), [rg] "s"(SRG), \
                          [k00] "v"(kx[0][0]), [k01] "v"(kx[0][1]), [k10] "v"(kx[1][0]), [k11] "v"(kx[1][1]) \
                        : "v207", "v208", "v209", "v210", "v211", "v212", "v213", "v214", "v215", "v216", "v217", "v218", "v219", "v220", "v221", "v222", "v223", "v224", "v225", "v226", "v227", "v228", "v229", "v230", "v231", "v236", "v237", "v238", "v239", "vcc", "scc", "memory"
                    if (R == 0) asm volatile(TAIL("v240", "v242", "v244", "v246", "v248", "v250") TAILIO);
                    else asm volatile(TAIL("v241", "v243", "v245", "v247", "v249", "v251") TAILIO);
                    RT(4);
                    MT(6);
                } else {
                    f32x8 a01;
                    asm volatile("v_mov_b64 v[200:201], 0\n\tv_mov_b64 v[202:203], 0\n\tv_mov_b64 v[204:205], 0\n\tv_mov_b64 v[206:207], 0" : "={v[200:207]}"(a01));
                    unsigned sr = TK ? tro + (unsigned)(s & 3) * 2u * PAIR : qrdy < min(s + 3, NSEG - 1) ? W_FQ + fb + (unsigned)hh * 128u : 0x40000000u;
                    unsigned tb = 0, tg = (unsigned)(s + 64 * lay) << 24, so = s > 0 ? W_T + (unsigned)(s & 3) * 2u * PAIR + (unsigned)hh * 128u : 0x40000000u;
                    asm volatile("" : "+s"(lim), "+s"(sr));
                    if (TK) { asm volatile("" : "+s"(tg), "+s"(so)); tb = fetch(tk, s, !redo); }
                    else asm volatile("s_waitcnt lgkmcnt(0)\n\ts_barrier\n\ts_barrier\n\ts_waitcnt vmcnt(0)\n\ts_barrier" ::: "memory");
                    asm volatile(
                        ".if %[tk]\n\t"
                        "s_waitcnt vmcnt(0)\n\t"
                        "s_memtime s[100:101]\n\t"
                        "s_barrier\n\t"
                        "s_setprio 1\n\t"
                        "s_waitcnt lgkmcnt(0)\n\t"
                        "ds_read2st64_b32 v[208:209], %[sp] offset1:16\n\t"
                        "ds_read_b32 v210, %[sp] offset:6144\n\t"
                        "ds_read_b128 v[212:215], %[mp] offset:0\n\t"
                        "ds_read_b128 v[216:219], %[mp] offset:1024\n\t"
                        "ds_read_b128 v[220:223], %[mp] offset:2048\n\t"
                        "ds_read_b128 v[224:227], %[mp] offset:3072\n\t"
                        "s_sub_u32 %[tb], s100, %[tb]\n\t"
                        "s_mul_i32 %[tb], %[tb], %[qc]\n\t"
                        "s_lshr_b32 %[tb], %[tb], 16\n\t"
                        "s_add_u32 %[tb], %[tb], %[ta]\n\t"
                        "s_and_b32 %[tb], %[tb], 0xffffff\n\t"
                        "s_or_b32 %[tb], %[tb], %[tg]\n\t"
                        "v_mov_b32 v211, %[tb]\n\t"
                        "s_waitcnt lgkmcnt(4)\n\t"
                        "v_max3_u32 v208, v208, v209, v210\n\t"
                        "v_cmp_lt_u32 vcc, %[lim], v208\n\t"
                        "ds_read_b128 v[228:231], %[mp] offset:4096\n\t"
                        "s_cmp_eq_u64 vcc, 0\n\t"
                        "s_cselect_b32 %[so], %[so], 2.0\n\t"
                        "buffer_store_dword v211, %[tw], %[rw], %[so] offen sc1\n\t"
                        "ds_read_b128 v[232:235], %[mp] offset:5120\n\t"
                        ".else\n\t"
                        "s_setprio 1\n\t"
                        "ds_read_b128 v[212:215], %[mp] offset:0\n\t"
                        "ds_read_b128 v[216:219], %[mp] offset:1024\n\t"
                        "ds_read_b128 v[220:223], %[mp] offset:2048\n\t"
                        "ds_read_b128 v[224:227], %[mp] offset:3072\n\t"
                        "ds_read_b128 v[228:231], %[mp] offset:4096\n\t"
                        "ds_read_b128 v[232:235], %[mp] offset:5120\n\t"
                        ".endif\n\t"
                        ".if !%[tk]\n\tbuffer_load_dword %[sf], %[sl], %[rw], %[sr] offen sc1\n\t.endif\n\t"
                        MTA(4)
                        "s_waitcnt lgkmcnt(5)\n\t"
                        "v_smfmac_f32_16x16x64_bf16 v[200:203], v[212:215], %[w00], %[ix]\n\t"
                        "ds_read_b128 v[236:239], %[mp] offset:6144\n\t"
                        "v_smfmac_f32_16x16x64_bf16 v[204:207], v[212:215], %[w10], %[ix]\n\t"
                        "ds_read_b128 v[240:243], %[mp] offset:7168\n\t"
                        "s_waitcnt lgkmcnt(6)\n\t"
                        "v_smfmac_f32_16x16x64_bf16 v[200:203], v[216:219], %[w01], %[ix]\n\t"
                        "ds_read_b128 v[244:247], %[mp] offset:8192\n\t"
                        "v_smfmac_f32_16x16x64_bf16 v[204:207], v[216:219], %[w11], %[ix]\n\t"
                        "ds_read_b128 v[248:251], %[mp] offset:9216\n\t"
                        "s_waitcnt lgkmcnt(4)\n\t"
                        "v_smfmac_f32_16x16x64_bf16 v[200:203], v[220:223], %[w02], %[ix]\n\t"
                        "v_smfmac_f32_16x16x64_bf16 v[204:207], v[220:223], %[w12], %[ix]\n\t"
                        "v_smfmac_f32_16x16x64_bf16 v[200:203], v[224:227], %[w03], %[ix]\n\t"
                        "v_smfmac_f32_16x16x64_bf16 v[204:207], v[224:227], %[w13], %[ix]\n\t"
                        "v_smfmac_f32_16x16x64_bf16 v[200:203], v[228:231], %[w04], %[ix]\n\t"
                        "v_smfmac_f32_16x16x64_bf16 v[204:207], v[228:231], %[w14], %[ix]\n\t"
                        "v_smfmac_f32_16x16x64_bf16 v[200:203], v[232:235], %[w05], %[ix]\n\t"
                        "v_smfmac_f32_16x16x64_bf16 v[204:207], v[232:235], %[w15], %[ix]\n\t"
                        "s_waitcnt lgkmcnt(0)\n\t"
                        "v_smfmac_f32_16x16x64_bf16 v[200:203], v[236:239], %[w06], %[ix]\n\t"
                        "v_smfmac_f32_16x16x64_bf16 v[204:207], v[236:239], %[w16], %[ix]\n\t"
                        BQ(3)
                        "v_smfmac_f32_16x16x64_bf16 v[200:203], v[240:243], %[w07], %[ix]\n\t"
                        "v_smfmac_f32_16x16x64_bf16 v[204:207], v[240:243], %[w17], %[ix]\n\t"
                        BQ(5)
                        "v_smfmac_f32_16x16x64_bf16 v[200:203], v[244:247], %[w08], %[ix]\n\t"
                        "v_smfmac_f32_16x16x64_bf16 v[200:203], v[248:251], %[w09], %[ix]\n\t"
                        BQ(7)
                        "v_smfmac_f32_16x16x64_bf16 v[204:207], v[244:247], %[w18], %[ix]\n\t"
                        "v_smfmac_f32_16x16x64_bf16 v[204:207], v[248:251], %[w19], %[ix]\n\t"
                        BQ(9)
                        "s_nop 5\n\t"
                        "v_pk_add_f32 v[200:201], v[200:201], v[202:203]\n\t"
                        "v_cvt_pk_bf16_f32 v200, v200, v201\n\t"
                        "v_pk_add_f32 v[204:205], v[204:205], v[206:207]\n\t"
                        "v_cvt_pk_bf16_f32 v201, v204, v205\n\t"
                        "ds_write2_b32 %[qw], v200, v201 offset1:32\n\t"
                        ".if %[tk]\n\tbuffer_load_dword %[sf], %[sl], %[rw], %[sr] offen sc1\n\t.endif\n\t"
                        "v_cmp_u_f32 %[sus], v204, v205\n\t"
                        MTA(2)
                        "s_waitcnt lgkmcnt(0)\n\t"
                        "s_barrier\n\t"
                        "s_setprio 0\n\t"
                        ".if %[tk]\n\ts_cmp_lg_u64 %[sus], 0\n\ts_cbranch_scc1 1f\n\t.endif\n\ts_waitcnt vmcnt(0)\n1:"
                        : [sus] "=&s"(sus), [sf] "+&v"(sf), [tb] "+&s"(tb), [so] "+&s"(so), "+{v[200:207]}"(a01)
                        : [mp] "v"(mp0), [qw] "v"(qw), [ix] "v"(sidx), [sl] "v"(TK ? tk.sl : fqv), [tk] "n"(TK ? 1 : 0), [sr] "s"(sr), [rw] "s"(rwin),
                          [sp] "v"(TK ? tk.sp : mp0), [lim] "s"(lim), [qc] "s"(ck.qpc), [ta] "s"(ck.ta), [tg] "s"(tg), [tw] "v"(TK ? tk.tw : fqv), [w00] "v"(wt[0][0]), [w10] "v"(wt[1][0]), [w01] "v"(wt[0][1]), [w11] "v"(wt[1][1]), [w02] "v"(wt[0][2]), [w12] "v"(wt[1][2]), [w03] "v"(wt[0][3]), [w13] "v"(wt[1][3]), [w04] "v"(wt[0][4]), [w14] "v"(wt[1][4]), [w05] "v"(wt[0][5]), [w15] "v"(wt[1][5]), [w06] "v"(wt[0][6]), [w16] "v"(wt[1][6]), [w07] "v"(wt[0][7]), [w17] "v"(wt[1][7]), [w08] "v"(wt[0][8]), [w18] "v"(wt[1][8]), [w09] "v"(wt[0][9]), [w19] "v"(wt[1][9])
                        : "v208", "v209", "v210", "v211", "v212", "v213", "v214", "v215", "v216", "v217", "v218", "v219", "v220", "v221", "v222", "v223", "v224", "v225", "v226", "v227", "v228", "v229", "v230", "v231", "v232", "v233", "v234", "v235", "v236", "v237", "v238", "v239", "v240", "v241", "v242", "v243", "v244", "v245", "v246", "v247", "v248", "v249", "v250", "v251", "vcc", "scc", "s100", "s101", "memory");
                    MT(3);
                }
                redo = false;
                if (__builtin_expect(sus != 0, 0)) {
                    redo = again(TK, tk.vo, sf, s, lim, mp0, tk.sp);
                    if (!redo && C < 2) {
                        const float c = 0.08838834764831845f * 1.44269504088896340736f;
                        float mx = fmx(fmx(sc[0], sc[1]), sc[2]);
                        mx = fmx(mx, dppf<0x128>(mx)); mx = fmx(mx, dppf<0x124>(mx)); mx = fmx(mx, dppf<0x122>(mx)); mx = fmx(mx, dppf<0x121>(mx));
                        const float e0 = __builtin_amdgcn_exp2f((sc[0] - mx) * c), e1 = __builtin_amdgcn_exp2f((sc[1] - mx) * c), e2 = __builtin_amdgcn_exp2f((sc[2] - mx) * c);
                        float d = e0 + e1;
                        d += dppf<0x128>(d); d += e2; d += dppf<0x124>(d); d += dppf<0x122>(d); d += dppf<0x121>(d);
                        d = __builtin_amdgcn_rcpf(d);
                        const unsigned p0 = nosent1(pack2(e0 * d, e1 * d)), p1 = nosent1(pack2(e2 * d, dppf<0xF5>(e2) * d));
                        __builtin_amdgcn_raw_buffer_store_b32(p0, rwin, pa0, pb, 16); __builtin_amdgcn_raw_buffer_store_b32(p0, rwin, pa0, pb1, 16);
                        __builtin_amdgcn_raw_buffer_store_b32(p1, rwin, pa2, pb, 16);
                    }
                }
            } while (__builtin_expect(redo, 0));
            if (__builtin_expect(mb != 0, 0)) { mp0 -= mb; tk.sp -= mb; mb = 0; }
            if (s < 8) SST(8 + s);
            if (s >= 56) SST(s - 40);
            if (C + R == 1) {
                {
                    __builtin_amdgcn_s_sleep(DDMA);
                    unsigned l2 = fresh_lane(), s1;
                    asm volatile("v_mov_b32 %0, -1" : "=v"(s1));
                    const unsigned base = W_P + (unsigned)((s - 1) & 3) * 10u * PAIR + nh;
                    asm volatile("s_waitcnt vmcnt(3)" ::: "memory");
                    st128(((u32x4){s1, s1, s1, s1}), rwin, (l2 >> 5) * PAIR + (l2 & 31u) * 16u, base + (unsigned)(R * 4 + 2 * (hh >> 4)) * PAIR + (unsigned)(hh & 15) * 512u, 16);
                    if (R) __builtin_amdgcn_raw_buffer_store_b64((u32x2){s1, s1}, rwin, (l2 >> 5) * PAIR + (l2 & 16u) * 256u + (l2 & 8u) * 16u + (l2 & 7u) * 8u, base + 8u * PAIR + (unsigned)((hh >> 1) * 256 + (hh & 1) * 64), 16);
                    else __builtin_amdgcn_raw_buffer_store_b32(s1, rwin, two(), W_T + (unsigned)((s + 2) & 3) * 2u * PAIR + (unsigned)hh * 128u, 16);
                }
            } else if (TK) {
                if (s + 1 < NSEG) sched2(tk, s, sf);
            } else if (C == 2 && w == 4) {
                qrdy = max(qrdy, seg_done(MXQ, __builtin_amdgcn_readlane(sf, 0), __builtin_amdgcn_readlane(sf, 1)));
                if (__builtin_expect(qf <= s, 0) && s + 1 < NSEG) {
                    for (unsigned spins = 0; qrdy <= s;) {
                        asm volatile("" ::: "memory");
                        const int f = (int)__builtin_amdgcn_raw_buffer_load_b32(rwin, fqv, W_FQ + fb + (unsigned)hh * 128u, 16);
                        qrdy = seg_done(MXQ, __builtin_amdgcn_readlane(f, 0), __builtin_amdgcn_readlane(f, 1));
                        if (qrdy > s) break;
                        FC(fc2);
                        if (spin_fail(spins, rws)) { qrdy = NSEG; break; }
                        __builtin_amdgcn_s_sleep(2);
                    }
                    *(volatile LAS int*)(smem + L_S) = s + 1;
                }
                while (qf < min(min(s + 2, qrdy), NSEG - 1)) {
                    ++qf;
                    const unsigned so = O_QK + (unsigned)((qf * H_ + hh) * 16384), lo = (unsigned)(L_TOK + qf % 3 * 16384), l2 = fresh_lane();
#pragma unroll
                    for (int i = 0; i < 4; ++i) dma4k(rws, l2 * 16u, so + i * 4096u, lo + i * 4096u);
                }
            } else {
                if (C == 2 && s + 1 < NSEG) __builtin_amdgcn_s_sleep(DPT);
                const float scale = 0.08838834764831845f, l2e = 1.44269504088896340736f * (NORND ? scale : 1.f);
                const int tq = (w >> 1) & 1, qh = w & 1;
                f32x4 sc[3] = {z4, z4, z4};
                u32x4 kt[2][4], qf[4], kf[4];
                const unsigned l2 = fresh_lane(), kto = ((l2 & 8u) * 512u + (l2 & 51u) * 16u + (l2 & 4u) * 32u) ^ ((l2 & 16u) * 5u), qto = ((l2 & 55u) * 16u + qh * 128u) ^ ((l2 & 16u) * 8u);
#pragma unroll
                for (int ks = 0; ks < 4; ++ks) {
                    qf[ks] = *(const u32x4*)(tb + 8192 + (tq * 4 + ks) * 1024 + qto);
                    kt[0][ks] = *(const u32x4*)(tb + ks * 1024 + kto);
                    kt[1][ks] = *(const u32x4*)(tb + ks * 1024 + (kto ^ 64u));
                    SB();
                }
#pragma unroll
                for (int ks = 0; ks < 4; ++ks) {
                    sc[0] = mfma32(kt[0][ks], qf[ks], sc[0]); PIN1(sc[0]);
                    kf[ks] = ldtr2(ka + ks * 256, ka + ks * 256 + 128);
                    SB();
                    sc[1] = mfma32(kt[1][ks], qf[ks], sc[1]); PIN1(sc[1]);
                }
#pragma unroll
                for (int ks = 0; ks < 4; ++ks) sc[2] = mfma32(kf[ks], qf[ks], sc[2]);
                const bool hi = (l2 & 8u) != 0u;
                float v[6];
#pragma unroll
                for (int r = 0; r < 4; ++r) v[r] = hi ? sc[1][r] : sc[0][r];
                v[4] = hi ? sc[2][2] : sc[2][0]; v[5] = hi ? sc[2][3] : sc[2][1];
#pragma unroll
                for (int r = 0; r < 6; r += 2) {
                    if (NORND) break;
                    const unsigned b0 = pack2(v[r], v[r + 1]);
                    const unsigned b1 = pack2(lo2f(b0) * scale, hi2f(b0) * scale);
                    v[r] = lo2f(b1); v[r + 1] = hi2f(b1);
                }
                const int lim = 8 * (w & 3) + (int)(l2 & 7u) - 8 * (int)(l2 >> 4) - (int)((l2 & 8u) >> 1);
#pragma unroll
                for (int r = 0; r < 4; ++r) if (r > lim) v[r] = NORND ? -1000000000.0f / scale : bfr(-1000000000.0f);
                float mx = fmx(fmx(fmx(fmx(fmx(v[0], v[1]), v[2]), v[3]), v[4]), v[5]);
                mx = xmax3216(fmx(mx, ror8f(mx)));
                const float mb = -mx * l2e;
#pragma unroll
                for (int r = 0; r < 6; ++r) v[r] = __builtin_amdgcn_exp2f(fmaf(v[r], l2e, mb));
                float sum = fmaf(v[4] + v[5], 0.5f, (v[0] + v[1]) + (v[2] + v[3]));
                sum = xsum3216(sum + ror8f(sum));
                const float inv = __builtin_amdgcn_rcpf(sum);
                const unsigned p0 = pack2(v[0] * inv, v[1] * inv), p1 = nosent1(pack2(v[2] * inv, v[3] * inv)), p2 = nosent1(pack2(v[4] * inv, v[5] * inv));
                const unsigned base = s + 2 < NSEG ? O_PT + (unsigned)(s * PTSEG) * 2u : W_PL + fb + (s & 1) * 5u * PAIR;
                const __amdgpu_buffer_rsrc_t rp = s + 2 < NSEG ? rws : rwin;
                const unsigned oob = (l2 & 8u) << 27;
                st128(((u32x4){p0, p1, dppu<0x128>(p0), dppu<0x128>(p1)}), rp, (l2 * 16u) | oob, base + (unsigned)((hh * 2 + tq) * 1024 + qh * 128), 16);
                __builtin_amdgcn_raw_buffer_store_b64((u32x2){p2, dppu<0x128>(p2)}, rp, ((l2 & 7u) * 16u + (l2 >> 4) * 8u) | oob | (l2 & 32u) << 25, base + 65536u + (unsigned)((tq * 32 + hh) * 256 + qh * 128), 16);
            }
            tbo = tbo == 32768 ? 0 : tbo + 16384;
            if (__builtin_expect(budget <= 0, 0)) __builtin_amdgcn_raw_buffer_store_b32(1u, rws, 0u, 0u, 16);
            RT(5);
            RTS();
            MT(7);
            MTS();
        }
        };
        if (qt) {} else if (w == 0) steps(IC<0>()); else if (w == 1) steps(IC<1>()); else if (w == 2) steps(IC<2>()); else if (w == 3) steps(IC<3>()); else if (w < 7) steps(IC<4>()); else steps(IC<5>());
        __builtin_amdgcn_s_setprio(0);
    }
    SST(31);
    }
    } else
#pragma nounroll
    for (; lay < 2; ++lay) {
    fresh();
    SST(0);
    {
        LANES();
        const int G = bid < NWRK ? bid - 96 : bid == NWRK ? 80 : -1;
        const int fi = bid >= NWRK ? 255 - bid : bid < NREC ? 16 + G / 10 + 4 * (G % 10) : 99;
        const bool first = fi < 32;
        u32x2 tag = {0u, 0u};
        if (lay == 0) tag = get_tag();
        const unsigned fv = lay ? min(lane, 6) * 1024u : (lane & 7) * 1024u + (lane & 8) * 8u, fs = (lay ? W_FA : W_ST) + w * 128u;
        auto ask = [&]() { asm volatile("" ::: "memory"); return __builtin_amdgcn_raw_buffer_load_b128(rwin, fv, fs, 16); };
        auto miss = [&](u32x4 f) { return (unsigned)__ballot(((f[0] ^ tag[0]) | (f[1] ^ tag[1]) | (f[2] ^ tag[0]) | (f[3] ^ tag[1])) != 0u); };
        if (G < 0) for (unsigned spins = 0; spins < SPIN_CAP && (miss(ask()) & 255u); ++spins) __builtin_amdgcn_s_sleep(ASLP);
        BAR();
        RowTw tw;
        if (G < 0) tw = row_tw(lane);
        else {
            unsigned m = 0xFFFFu, e = ~0u;
            u32x4 f = SENT;
            a2_unit(rws, G, smem, w, lane, lay == 0, [&](int r) {
                if (r == 5) {
                    if (lay) { f = ask(); e = __builtin_amdgcn_raw_buffer_load_b32(rwin, 0u, W_E + (E_A1 + w) * 128u, 16); }
                    SB();
                    tw = row_tw(lane);
                    return;
                }
                if (r == 6) { if (lay) m = miss(f); else if (DFA) __builtin_amdgcn_s_sleep(DFA); }
                if (r == 7 && lay) await(E_A1 + w, 1, 0, e);
                else for (unsigned spins = 0; spins < SPIN_CAP && (m & (r == 6 ? 127u : r == 7 ? 128u : 0xFF00u)); ++spins) {
                    if (spins) __builtin_amdgcn_s_sleep(ASLP);
                    m = miss(ask());
                }
                if (r == 7) SST(5);
            }, [&](int k) {
                if (k == 4 && w == 7 && blockIdx.x < 136) due(E_A2 + (blockIdx.x & 7), lay ? DA2 : DA20);
                if (k == 7 && YSLP) __builtin_amdgcn_s_sleep(YSLP);
                if (bid < NREC) SST(8 + k);
            });
            SST(6);
            VMWAIT();
            BAR0();
            if (tid == 0) __builtin_amdgcn_raw_buffer_store_b32(0u, rwin, 0u, W_FB + fb + (unsigned)((G >> 2) * 128 + (G & 3) * 4), 16);
            SST(7);
        }
        auto columns = [&](int all) {
            if (w == 7) await(E_A2 + fb / 128u, 8, all);
            if (w == 7 && all) for (unsigned spins = 0;;) {
                asm volatile("" ::: "memory");
                const u32x4 f = __builtin_amdgcn_raw_buffer_load_b128(rwin, (unsigned)min(lane, 20) * 128u, W_FB + fb, 16);
                if (!__any((f[0] | (lane < 20 ? f[1] | f[2] | f[3] : 0u)) != 0u)) break;
                if (spin_fail(spins, rws)) break;
                __builtin_amdgcn_s_sleep(ASLP);
            }
            BAR0();
            if (!all || !first) SST(3);
        };
        const __amdgpu_buffer_rsrc_t rx = mkrsrc(lay ? karg<0>() + OFF_X2 : karg<3>(), S_ * D_ * 2);
        if (first) {
            const unsigned fo = fb + (unsigned)((fi >> 2) * 128 + (fi & 3) * 4);
            a3_units<4>(rws, rx, fi * 8, 0, lay, smem, tw, w, lane, [&](int k) {
                if (k < 0) { if (G < 0) columns(0); } else if (fi < 16) SST(16 + k);
                if (k == 0 && w == 7 && fi < 4) due(E_A3 + fi, lay ? DA3 : DA30);
            }, [&](int) { if (tid == 0) __builtin_amdgcn_raw_buffer_store_b32(0u, rwin, 0u, W_H + fo, 16); });
            SST(4);
        }
                if (!first || bid >= NWRK) {
            const bool two = bid >= NREC && bid < NREC + 32;
            const int u0 = two ? 48 + (bid - NREC) / 8 + 4 * ((bid - NREC) % 8) : bid < NREC ? fi - 24 : bid < NWRK ? bid - NREC - 32 : 32 + fi;
            columns(two ? DFB - 48 * DST + u0 * DST : DFB0);
            a3_units<8>(rws, rx, (16 + u0) * 16, two ? 512 : S_, lay, smem, tw, w, lane, [](int) {}, [&](int r) {
                if (tid == 0) __builtin_amdgcn_raw_buffer_store_b64((u32x2){0u, 0u}, rwin, 0u, W_H + fb + (unsigned)((r >> 5) * 128 + (r & 16) / 2), 16);
                if (r < 1536 && two) SST(2);
            });
            VMWAIT();
        }
        if (lay == 0 && bid < NREC) config();
        SST(1);
        places();
    }
    const __amdgpu_buffer_rsrc_t rout = mkrsrc(lay ? karg<4>() : karg<0>() + OFF_X2, S_ * D_ * 2);
    if (bid < NREC) {
        LANES();
        const int o = bid - 96, hg = w & 3;
        const unsigned ztoff = (unsigned)((o * H_ + 8 * hg) * 64 + lane) * 16u;
        const unsigned zoff = zpair(o / 10, o % 10) + (unsigned)(lane * 16);
        const unsigned poff = (4u * (mc >> 1) + 2u * (hg >> 1) + (mc & 1u)) * PAIR + nh + (unsigned)((hg & 1) * 4096 + (fq >> 1) * 512 + (fr >> 2) * 128 + (fr & 1) * 64 + (fq & 1) * 32 + (fr & 2) * 8);
        const unsigned pmoff = (8u + (mc & 1u)) * PAIR + nh + (mc >> 1) * 4096u + (unsigned)(fq * 256 + (fr >> 3) * 128 + (fr & 2) * 32 + (fr & 4) * 8 + (fr & 1) * 16);
        const unsigned fzo = (unsigned)(8 * hg + (lane & 7)) * 128u + (unsigned)(o / 10) * 8192u + (lane & 8) * 512u;
        const unsigned moff = (o < 32 ? (unsigned)o * 256u : 4u * PAIR + (unsigned)(o - 32) * 256u) + nh + (unsigned)(lane * 4);
        const unsigned mc2 = o < 32 ? 2u * PAIR : 2048u;
        char* op = smem + 8192;
        SST(2);
        u32x8 zf[4];
        asm volatile("" : "={v[160:167]}"(zf[0]), "={v[168:175]}"(zf[1]), "={v[176:183]}"(zf[2]), "={v[184:191]}"(zf[3]));
        int zr = -1;
        const unsigned ra = (o < 32 ? (unsigned)fq * PAIR + (unsigned)o * 256u : (4u + (fq & 1)) * PAIR + (unsigned)(fq >> 1) * 2048u + (unsigned)(o - 32) * 256u) + nh + (unsigned)(fr * 16);
        auto zt_wait = [&](int s) {
            if (__builtin_expect(zr < s, 0)) for (unsigned spins = 0;;) {
                asm volatile("" ::: "memory");
                if (!__any((int)__builtin_amdgcn_raw_buffer_load_b32(rwin, fzo, W_FZ + fb, 16) < s && ((lane >> 3) & 1) == (int)((MXV >> s) & 1))) break;
                FC(fc2);
                if (spin_fail(spins, rws)) break;
                __builtin_amdgcn_s_sleep(ZPS);
            }
        };
        auto sent = [&](int s) {
            for (;;) {
                const u32x2 x = *(volatile LAS u32x2*)(smem + 4096);
                zr = __builtin_amdgcn_readfirstlane(x[0]);
                if (__builtin_amdgcn_readfirstlane(x[1]) == s) break;
                __builtin_amdgcn_s_sleep(2);
            }
        };
#pragma nounroll
        for (int real = w != 5 || lay; real < 2; ++real) {
        if (!real) {} else if (w < 4) {
            await(E_ZT + fb / 128u, 4, 0);
            for (int s = 0; s + 1 < NSEG; ++s) {
                const unsigned pb = W_P + (unsigned)(s & 3) * 10u * PAIR;
                zt_wait(s);
                if (s == 0) SST(1);
#define ZTL \
                    "buffer_load_dwordx4 v[160:163], %[zt], %[rs], %[so] offen sc1\n\t" \
                    "buffer_load_dwordx4 v[164:167], %[zt], %[rs], %[so] offen offset:1024 sc1\n\t" \
                    "buffer_load_dwordx4 v[168:171], %[zt], %[rs], %[so] offen offset:2048 sc1\n\t" \
                    "buffer_load_dwordx4 v[172:175], %[zt], %[rs], %[so] offen offset:3072 sc1\n\t" \
                    "buffer_load_dwordx4 v[176:179], %[zt], %[rs], %[s4] offen sc1\n\t" \
                    "buffer_load_dwordx4 v[180:183], %[zt], %[rs], %[s4] offen offset:1024 sc1\n\t" \
                    "buffer_load_dwordx4 v[184:187], %[zt], %[rs], %[s4] offen offset:2048 sc1\n\t" \
                    "buffer_load_dwordx4 v[188:191], %[zt], %[rs], %[s4] offen offset:3072 sc1\n\t"
                const unsigned zso = O_Z + (unsigned)(s * ZTSEG) * 2u;
                if (__builtin_expect(budget <= 0, 0)) __builtin_amdgcn_raw_buffer_store_b32(1u, rws, 0u, 0u, 16);
                RT(6);
                const unsigned spa = (unsigned)((s & 1) * 2048 + (w < 3 ? w * 512 : 0) + lane * 8);
#define OST(r)
                unsigned long long t0_, t1_, t2_, t3_, t4_ = 0, bad;
                int tries = budget;
                unsigned po = poff, pm = pmoff, x0 = 4u * PAIR, x1 = 4096u;
                const unsigned mb = W_MEM + (unsigned)((s + 1) & 3) * 6u * PAIR;
                if (w < 3) asm volatile("s_nop 4\n\t" ZTL
                    "s_barrier\n\t"
                    OST("%[t0]")
                    "v_mov_b64 v[96:97], 0\n\t"
                    "v_mov_b64 v[98:99], 0\n\t"
                    "s_barrier\n\t"
                    "1:\n\t"
                    "buffer_load_dwordx4 v[208:211], %[po], %[rw], %[pb] offen sc1\n\t"
                    "buffer_load_dwordx4 v[212:215], %[po], %[rw], %[pb] offen offset:1024 sc1\n\t"
                    "buffer_load_dwordx4 v[216:219], %[po], %[rw], %[pb] offen offset:2048 sc1\n\t"
                    "buffer_load_dwordx4 v[220:223], %[po], %[rw], %[pb] offen offset:3072 sc1\n\t"
                    OST("%[t1]")
                    "s_waitcnt vmcnt(3)\n\t"
                    OST("%[t3]")
                    "v_smfmac_f32_16x16x64_bf16 v[96:99], v[208:211], v[160:167], %[ix]\n\t"
                    "s_waitcnt vmcnt(2)\n\t"
                    "v_smfmac_f32_16x16x64_bf16 v[96:99], v[212:215], v[168:175], %[ix]\n\t"
                    "s_waitcnt vmcnt(1)\n\t"
                    "v_smfmac_f32_16x16x64_bf16 v[96:99], v[216:219], v[176:183], %[ix]\n\t"
                    "s_waitcnt vmcnt(0)\n\t"
                    "v_smfmac_f32_16x16x64_bf16 v[96:99], v[220:223], v[184:191], %[ix]\n\t"
                    OST("%[t2]")
                    "s_nop 7\n\t"
                    "v_pk_add_f32 v[96:97], v[96:97], v[98:99]\n\t"
                    "ds_write_b64 %[sp], v[96:97]\n\t"
                    "v_cmp_u_f32 vcc, v96, v97\n\t"
                    "s_cbranch_vccz 2f\n\t"
                    "v_max3_u32 v100, v208, v208, v209\n\t"
                    "v_max3_u32 v100, v100, v210, v211\n\t"
                    "v_max3_u32 v100, v100, v212, v213\n\t"
                    "v_max3_u32 v100, v100, v214, v215\n\t"
                    "v_max3_u32 v100, v100, v216, v217\n\t"
                    "v_max3_u32 v100, v100, v218, v219\n\t"
                    "v_max3_u32 v100, v100, v220, v221\n\t"
                    "v_max3_u32 v100, v100, v222, v223\n\t"
                    "v_cmp_eq_u32 vcc, -1, v100\n\t"
                    "s_cbranch_vccz 2f\n\t"
                    "s_sub_i32 %[tr], %[tr], 1\n\t"
                    "s_cmp_lt_i32 %[tr], 1\n\t"
                    "s_cbranch_scc1 2f\n\t"
                    "v_xor_b32 %[po], %[x0], %[po]\n\t"
                    "s_xor_b32 %[x0], %[x0], 0x14000\n\t"
                    "s_waitcnt lgkmcnt(0)\n\t"
                    "v_mov_b64 v[96:97], 0\n\t"
                    "v_mov_b64 v[98:99], 0\n\t"
                    "s_branch 1b\n\t"
                    "2:\n\t"
                    "s_waitcnt lgkmcnt(0)\n\t"
                    "s_barrier\n\t"
                    "s_sleep 3\n\t"
                    : [tr] "+&s"(tries), [po] "+&v"(po), [x0] "+&s"(x0), [t0] "=&s"(t0_), [t1] "=&s"(t1_), [t2] "=&s"(t2_), [t3] "=&s"(t3_), "=&{v[160:167]}"(zf[0]), "=&{v[168:175]}"(zf[1]), "=&{v[176:183]}"(zf[2]), "=&{v[184:191]}"(zf[3])
                    : [zt] "v"(ztoff), [rs] "s"(rws), [so] "s"(zso), [s4] "s"(zso + 4096u), [sp] "v"(spa), [ix] "v"(sidx), [rw] "s"(rwin), [pb] "s"(pb)
                    : "v96", "v97", "v98", "v99", "v100", "v208", "v209", "v210", "v211", "v212", "v213", "v214", "v215", "v216", "v217", "v218", "v219", "v220", "v221", "v222", "v223", "vcc", "scc", "memory");
                else asm volatile("s_nop 4\n\t" ZTL
                    "s_barrier\n\t"
                    "buffer_load_dwordx4 v[128:131], %[zo], %[rw], %[zb] offen sc1\n\t"
                    "buffer_load_dwordx4 v[132:135], %[zo], %[rw], %[zb] offen offset:1024 sc1\n\t"
                    "buffer_load_dwordx4 v[136:139], %[zo], %[rw], %[zb] offen offset:2048 sc1\n\t"
                    "buffer_load_dwordx4 v[140:143], %[zo], %[rw], %[zb] offen offset:3072 sc1\n\t"
                    "buffer_load_dwordx4 v[144:147], %[zo], %[rw], %[z4] offen sc1\n\t"
                    "buffer_load_dwordx4 v[148:151], %[zo], %[rw], %[z4] offen offset:1024 sc1\n\t"
                    "buffer_load_dwordx4 v[152:155], %[zo], %[rw], %[z4] offen offset:2048 sc1\n\t"
                    "buffer_load_dwordx4 v[156:159], %[zo], %[rw], %[z4] offen offset:3072 sc1\n\t"
                    OST("%[t0]")
                    "v_mov_b64 v[96:97], 0\n\t"
                    "v_mov_b64 v[98:99], 0\n\t"
                    "s_barrier\n\t"
                    "s_sleep %[fd]\n\t"
                    "buffer_load_dwordx4 v[208:211], %[po], %[rw], %[pb] offen sc1\n\t"
                    "buffer_load_dwordx4 v[212:215], %[po], %[rw], %[pb] offen offset:1024 sc1\n\t"
                    "buffer_load_dwordx4 v[216:219], %[po], %[rw], %[pb] offen offset:2048 sc1\n\t"
                    "buffer_load_dwordx4 v[220:223], %[po], %[rw], %[pb] offen offset:3072 sc1\n\t"
                    "buffer_load_dwordx4 v[224:227], %[pm], %[rw], %[pb] offen sc1\n\t"
                    "buffer_load_dwordx4 v[228:231], %[pm], %[rw], %[pb] offen offset:1024 sc1\n\t"
                    "buffer_load_dwordx4 v[232:235], %[pm], %[rw], %[pb] offen offset:2048 sc1\n\t"
                    "buffer_load_dwordx4 v[236:239], %[pm], %[rw], %[pb] offen offset:3072 sc1\n\t"
                    OST("%[t1]")
                    "s_waitcnt vmcnt(8)\n\t"
                    "v_max3_u32 v101, v129, v131, v133\n\t"
                    "v_max3_u32 v101, v101, v135, v137\n\t"
                    "v_max3_u32 v101, v101, v139, v141\n\t"
                    "v_max3_u32 v101, v101, v143, v145\n\t"
                    "v_max3_u32 v101, v101, v147, v149\n\t"
                    "v_max3_u32 v101, v101, v151, v153\n\t"
                    "v_max3_u32 v101, v101, v155, v157\n\t"
                    "v_max_u32 v101, v101, v159\n\t"
                    "v_cmp_eq_u32 vcc, -1, v101\n\t"
                    "s_cbranch_vccz 3f\n\t"
                    "buffer_load_dwordx4 v[128:131], %[zo], %[rw], %[zb] offen sc1\n\t"
                    "buffer_load_dwordx4 v[132:135], %[zo], %[rw], %[zb] offen offset:1024 sc1\n\t"
                    "buffer_load_dwordx4 v[136:139], %[zo], %[rw], %[zb] offen offset:2048 sc1\n\t"
                    "buffer_load_dwordx4 v[140:143], %[zo], %[rw], %[zb] offen offset:3072 sc1\n\t"
                    "buffer_load_dwordx4 v[144:147], %[zo], %[rw], %[z4] offen sc1\n\t"
                    "buffer_load_dwordx4 v[148:151], %[zo], %[rw], %[z4] offen offset:1024 sc1\n\t"
                    "buffer_load_dwordx4 v[152:155], %[zo], %[rw], %[z4] offen offset:2048 sc1\n\t"
                    "buffer_load_dwordx4 v[156:159], %[zo], %[rw], %[z4] offen offset:3072 sc1\n\t"
                    "s_waitcnt vmcnt(0)\n\t"
                    "v_max3_u32 v101, v129, v131, v133\n\t"
                    "v_max3_u32 v101, v101, v135, v137\n\t"
                    "v_max3_u32 v101, v101, v139, v141\n\t"
                    "v_max3_u32 v101, v101, v143, v145\n\t"
                    "v_max3_u32 v101, v101, v147, v149\n\t"
                    "v_max3_u32 v101, v101, v151, v153\n\t"
                    "v_max3_u32 v101, v101, v155, v157\n\t"
                    "v_max_u32 v101, v101, v159\n\t"
                    "3:\n\t"
                    "v_permlane16_swap_b32 v128, v129\n\t"
                    "v_permlane16_swap_b32 v130, v131\n\t"
                    "v_permlane16_swap_b32 v132, v133\n\t"
                    "v_permlane16_swap_b32 v134, v135\n\t"
                    "v_permlane16_swap_b32 v136, v137\n\t"
                    "v_permlane16_swap_b32 v138, v139\n\t"
                    "v_permlane16_swap_b32 v140, v141\n\t"
                    "v_permlane16_swap_b32 v142, v143\n\t"
                    "v_permlane16_swap_b32 v144, v145\n\t"
                    "v_permlane16_swap_b32 v146, v147\n\t"
                    "v_permlane16_swap_b32 v148, v149\n\t"
                    "v_permlane16_swap_b32 v150, v151\n\t"
                    "v_permlane16_swap_b32 v152, v153\n\t"
                    "v_permlane16_swap_b32 v154, v155\n\t"
                    "v_permlane16_swap_b32 v156, v157\n\t"
                    "v_permlane16_swap_b32 v158, v159\n\t"
                    "s_mov_b32 vcc_lo, 0x00ff00ff\n\t"
                    "s_mov_b32 vcc_hi, 0x00ff00ff\n\t"
                    "s_waitcnt vmcnt(7)\n\t"
                    OST("%[t3]")
                    "v_smfmac_f32_16x16x64_bf16 v[96:99], v[208:211], v[160:167], %[ix]\n\t"
                    "s_waitcnt vmcnt(6)\n\t"
                    "v_smfmac_f32_16x16x64_bf16 v[96:99], v[212:215], v[168:175], %[ix]\n\t"
                    "s_waitcnt vmcnt(5)\n\t"
                    "v_smfmac_f32_16x16x64_bf16 v[96:99], v[216:219], v[176:183], %[ix]\n\t"
                    "s_waitcnt vmcnt(4)\n\t"
                    "v_smfmac_f32_16x16x64_bf16 v[96:99], v[220:223], v[184:191], %[ix]\n\t"
                    OB(4)
                    "s_waitcnt vmcnt(3)\n\t"
                    "v_smfmac_f32_16x16x64_bf16 v[96:99], v[224:227], v[128:135], %[ix]\n\t"
                    OB(5)
                    "s_waitcnt vmcnt(2)\n\t"
                    "v_smfmac_f32_16x16x64_bf16 v[96:99], v[228:231], v[136:143], %[ix]\n\t"
                    OB(6)
                    "s_waitcnt vmcnt(1)\n\t"
                    "v_smfmac_f32_16x16x64_bf16 v[96:99], v[232:235], v[144:151], %[ix]\n\t"
                    "s_waitcnt vmcnt(0)\n\t"
                    "v_smfmac_f32_16x16x64_bf16 v[96:99], v[236:239], v[152:159], %[ix]\n\t"
                    OST("%[t2]")
                    "s_waitcnt lgkmcnt(0)\n\t"
                    "v_pk_add_f32 v[102:103], v[102:103], v[104:105]\n\t"
                    "v_pk_add_f32 v[102:103], v[102:103], v[106:107]\n\t"
                    "s_nop 4\n\t"
                    "6:\n\t"
                    "v_pk_add_f32 v[96:97], v[96:97], v[102:103]\n\t"
                    "v_pk_add_f32 v[96:97], v[96:97], v[98:99]\n\t"
                    "v_cmp_u_f32 %[bad], v96, v97\n\t"
                    "s_nop 0\n\t"
                    "v_cndmask_b32_dpp v98, v97, v96, vcc row_ror:8 row_mask:0xf bank_mask:0xf\n\t"
                    "s_not_b64 vcc, vcc\n\t"
                    "v_cndmask_b32_dpp v99, v96, v97, vcc row_ror:8 row_mask:0xf bank_mask:0xf\n\t"
                    "s_cmp_lg_u64 %[bad], 0\n\t"
                    "v_cvt_pk_bf16_f32 v98, v98, v99\n\t"
                    "s_cbranch_scc1 4f\n\t"
                    "2:\n\t"
                    "buffer_store_dword v98, %[mo], %[rw], %[m0] offen sc1\n\t"
                    "buffer_store_dword v98, %[mo], %[rw], %[m1] offen sc1\n\t"
                    "buffer_store_dword v98, %[mo], %[rw], %[m2] offen sc1\n\t"
                    "buffer_store_dword v98, %[mo], %[rw], %[m3] offen sc1\n\t"
                    OST("%[t4]")
                    "s_branch 5f\n\t"
                    "4:\n\t"
                    "v_min_u32 v98, -2, v98\n\t"
                    "v_max3_u32 v100, v101, v208, v209\n\t"
                    "v_max3_u32 v100, v100, v210, v211\n\t"
                    "v_max3_u32 v100, v100, v212, v213\n\t"
                    "v_max3_u32 v100, v100, v214, v215\n\t"
                    "v_max3_u32 v100, v100, v216, v217\n\t"
                    "v_max3_u32 v100, v100, v218, v219\n\t"
                    "v_max3_u32 v100, v100, v220, v221\n\t"
                    "v_max3_u32 v100, v100, v222, v223\n\t"
                    "v_max3_u32 v100, v100, v224, v225\n\t"
                    "v_max3_u32 v100, v100, v226, v227\n\t"
                    "v_max3_u32 v100, v100, v228, v229\n\t"
                    "v_max3_u32 v100, v100, v230, v231\n\t"
                    "v_max3_u32 v100, v100, v232, v233\n\t"
                    "v_max3_u32 v100, v100, v234, v235\n\t"
                    "v_max3_u32 v100, v100, v236, v237\n\t"
                    "v_max3_u32 v100, v100, v238, v239\n\t"
                    "v_cmp_eq_u32 %[bad], -1, v100\n\t"
                    "s_not_b64 vcc, vcc\n\t"
                    "s_cmp_eq_u64 %[bad], 0\n\t"
                    "s_cbranch_scc1 2b\n\t"
                    "s_sub_i32 %[tr], %[tr], 1\n\t"
                    "s_cmp_lt_i32 %[tr], 1\n\t"
                    "s_cbranch_scc1 2b\n\t"
                    "v_xor_b32 %[po], %[x0], %[po]\n\t"
                    "s_xor_b32 %[x0], %[x0], 0x14000\n\t"
                    "v_xor_b32 %[pm], %[x1], %[pm]\n\t"
                    "s_xor_b32 %[x1], %[x1], 0x5000\n\t"
                    "buffer_load_dwordx4 v[128:131], %[zo], %[rw], %[zb] offen sc1\n\t"
                    "buffer_load_dwordx4 v[132:135], %[zo], %[rw], %[zb] offen offset:1024 sc1\n\t"
                    "buffer_load_dwordx4 v[136:139], %[zo], %[rw], %[zb] offen offset:2048 sc1\n\t"
                    "buffer_load_dwordx4 v[140:143], %[zo], %[rw], %[zb] offen offset:3072 sc1\n\t"
                    "buffer_load_dwordx4 v[144:147], %[zo], %[rw], %[z4] offen sc1\n\t"
                    "buffer_load_dwordx4 v[148:151], %[zo], %[rw], %[z4] offen offset:1024 sc1\n\t"
                    "buffer_load_dwordx4 v[152:155], %[zo], %[rw], %[z4] offen offset:2048 sc1\n\t"
                    "buffer_load_dwordx4 v[156:159], %[zo], %[rw], %[z4] offen offset:3072 sc1\n\t"
                    "buffer_load_dwordx4 v[208:211], %[po], %[rw], %[pb] offen sc1\n\t"
                    "buffer_load_dwordx4 v[212:215], %[po], %[rw], %[pb] offen offset:1024 sc1\n\t"
                    "buffer_load_dwordx4 v[216:219], %[po], %[rw], %[pb] offen offset:2048 sc1\n\t"
                    "buffer_load_dwordx4 v[220:223], %[po], %[rw], %[pb] offen offset:3072 sc1\n\t"
                    "buffer_load_dwordx4 v[224:227], %[pm], %[rw], %[pb] offen sc1\n\t"
                    "buffer_load_dwordx4 v[228:231], %[pm], %[rw], %[pb] offen offset:1024 sc1\n\t"
                    "buffer_load_dwordx4 v[232:235], %[pm], %[rw], %[pb] offen offset:2048 sc1\n\t"
                    "buffer_load_dwordx4 v[236:239], %[pm], %[rw], %[pb] offen offset:3072 sc1\n\t"
                    "v_mov_b64 v[96:97], 0\n\t"
                    "v_mov_b64 v[98:99], 0\n\t"
                    "s_waitcnt vmcnt(0)\n\t"
                    "v_max3_u32 v101, v129, v131, v133\n\t"
                    "v_max3_u32 v101, v101, v135, v137\n\t"
                    "v_max3_u32 v101, v101, v139, v141\n\t"
                    "v_max3_u32 v101, v101, v143, v145\n\t"
                    "v_max3_u32 v101, v101, v147, v149\n\t"
                    "v_max3_u32 v101, v101, v151, v153\n\t"
                    "v_max3_u32 v101, v101, v155, v157\n\t"
                    "v_max_u32 v101, v101, v159\n\t"
                    "v_permlane16_swap_b32 v128, v129\n\t"
                    "v_permlane16_swap_b32 v130, v131\n\t"
                    "v_permlane16_swap_b32 v132, v133\n\t"
                    "v_permlane16_swap_b32 v134, v135\n\t"
                    "v_permlane16_swap_b32 v136, v137\n\t"
                    "v_permlane16_swap_b32 v138, v139\n\t"
                    "v_permlane16_swap_b32 v140, v141\n\t"
                    "v_permlane16_swap_b32 v142, v143\n\t"
                    "v_permlane16_swap_b32 v144, v145\n\t"
                    "v_permlane16_swap_b32 v146, v147\n\t"
                    "v_permlane16_swap_b32 v148, v149\n\t"
                    "v_permlane16_swap_b32 v150, v151\n\t"
                    "v_permlane16_swap_b32 v152, v153\n\t"
                    "v_permlane16_swap_b32 v154, v155\n\t"
                    "v_permlane16_swap_b32 v156, v157\n\t"
                    "v_permlane16_swap_b32 v158, v159\n\t"
                    "s_nop 1\n\t"
                    "v_smfmac_f32_16x16x64_bf16 v[96:99], v[208:211], v[160:167], %[ix]\n\t"
                    "v_smfmac_f32_16x16x64_bf16 v[96:99], v[212:215], v[168:175], %[ix]\n\t"
                    "v_smfmac_f32_16x16x64_bf16 v[96:99], v[216:219], v[176:183], %[ix]\n\t"
                    "v_smfmac_f32_16x16x64_bf16 v[96:99], v[220:223], v[184:191], %[ix]\n\t"
                    "v_smfmac_f32_16x16x64_bf16 v[96:99], v[224:227], v[128:135], %[ix]\n\t"
                    "v_smfmac_f32_16x16x64_bf16 v[96:99], v[228:231], v[136:143], %[ix]\n\t"
                    "v_smfmac_f32_16x16x64_bf16 v[96:99], v[232:235], v[144:151], %[ix]\n\t"
                    "v_smfmac_f32_16x16x64_bf16 v[96:99], v[236:239], v[152:159], %[ix]\n\t"
                    "s_nop 7\n\t"
                    "s_branch 6b\n\t"
                    "5:\n\t"
                    : [tr] "+&s"(tries), [po] "+&v"(po), [pm] "+&v"(pm), [x0] "+&s"(x0), [x1] "+&s"(x1), [bad] "=&s"(bad), [t0] "=&s"(t0_), [t1] "=&s"(t1_), [t2] "=&s"(t2_), [t3] "=&s"(t3_), [t4] "=&s"(t4_), "=&{v[160:167]}"(zf[0]), "=&{v[168:175]}"(zf[1]), "=&{v[176:183]}"(zf[2]), "=&{v[184:191]}"(zf[3])
                    : [zt] "v"(ztoff), [rs] "s"(rws), [so] "s"(zso), [s4] "s"(zso + 4096u), [zo] "v"(zoff), [sp] "v"(spa), [ix] "v"(sidx), [rw] "s"(rwin), [zb] "s"(zb), [z4] "s"(zb + 4096u), [pb] "s"(pb), [mo] "v"(moff), [fd] "n"(FDL),
                      [m0] "s"(mb), [m1] "s"(mb + PAIR), [m2] "s"(mb + mc2), [m3] "s"(mb + mc2 + PAIR)
                    : "v96", "v97", "v98", "v99", "v100", "v101", "v102", "v103", "v104", "v105", "v106", "v107", "v128", "v129", "v130", "v131", "v132", "v133", "v134", "v135", "v136", "v137", "v138", "v139", "v140", "v141", "v142", "v143", "v144", "v145", "v146", "v147", "v148", "v149", "v150", "v151", "v152", "v153", "v154", "v155", "v156", "v157", "v158", "v159", "v208", "v209", "v210", "v211", "v212", "v213", "v214", "v215", "v216", "v217", "v218", "v219", "v220", "v221", "v222", "v223", "v224", "v225", "v226", "v227", "v228", "v229", "v230", "v231", "v232", "v233", "v234", "v235", "v236", "v237", "v238", "v239", "vcc", "scc", "memory");
                budget = tries;
                if (w == 3) *(volatile LAS int*)(smem + 4100) = s;
                __builtin_amdgcn_s_sleep(DPS - 4);
                if (w == 3) {
                    st128(SENT, rwin, ra, W_MEM + (unsigned)(s & 3) * 6u * PAIR, 16);
                    if (lane == 0) __builtin_amdgcn_raw_buffer_store_b64((u32x2){(unsigned)s - 1u, (unsigned)max(zr, s)}, rwin, 0u, W_GO + fb + (unsigned)o * 128u, 16);
                }
                sent(s);
                zb = zb == W_ZM + 2u * NOWN * PAIR ? W_ZM : zb + NOWN * PAIR;
            }
        } else if (w == 4) {
            const unsigned fo = (unsigned)lane * 128u + (unsigned)(o / 10) * 8192u;
            int zk = -1;
            if (lane < 2) *(volatile LAS int*)(smem + 4096 + lane * 4) = -1;
            for (int s = 0; s + 1 < NSEG; ++s) {
                asm volatile("s_waitcnt lgkmcnt(0)\n\ts_barrier\n\ts_barrier" ::: "memory");
                int f = 0;
                const bool look = zk < min(s + ZLA, NSEG - 1);
                if (look) { __builtin_amdgcn_s_sleep(6); f = (int)__builtin_amdgcn_raw_buffer_load_b32(rwin, fo, W_FZ + fb, 16); }
                SB();
                BAR0();
                if (look) {
                    f = min(f, __builtin_amdgcn_update_dpp(f, f, 0xB1, 0xF, 0xF, false)); f = min(f, __builtin_amdgcn_update_dpp(f, f, 0x4E, 0xF, 0xF, false));
                    f = min(f, __builtin_amdgcn_update_dpp(f, f, 0x141, 0xF, 0xF, false)); f = min(f, __builtin_amdgcn_update_dpp(f, f, 0x140, 0xF, 0xF, false));
                    zk = seg_done(MXV, min(__builtin_amdgcn_readlane(f, 0), __builtin_amdgcn_readlane(f, 16)), min(__builtin_amdgcn_readlane(f, 32), __builtin_amdgcn_readlane(f, 48)));
                    *(volatile LAS int*)(smem + 4096) = zk;
                }
            }
        } else if (w < 7) {
            for (int s = 0; s < 3 * NSEG - 3; ++s) BAR0();
        } else {
            Tk tk = tk_init();
            __builtin_amdgcn_s_setprio(3);
            for (int s = 0; s + 1 < NSEG; ++s) {
                const unsigned ta0 = ck.ta, mm0 = ck.ma;
                unsigned tp = tpp + lt + xs + M1;
                if (s < SAD) { sched(tk, s); tp = tpp - M2 - XS0 - (lay ? PEN0 : PEN00); }
                wait_fine_bar(tp - DZP, ck);
                wait_cyc_bar(cyc_at(ck, sx24(tp - ck.ta)));
                clk_rate(ck, ta0, mm0);
                asm volatile("s_barrier ; 7" ::: "memory");
                if (s >= SAD) sched(tk, s);
                RTS();
            }
            __builtin_amdgcn_s_setprio(0);
            if (o < 2) due(E_OUT + o, (sx24(tpp + ML - 4u * (unsigned)__builtin_amdgcn_s_memrealtime()) >> 2) + DOUT);
        }
        {
            const int e = w >> 2;
            const unsigned ooff = (unsigned)((S_ - 2 * SEG + 8 * w + (lane >> 3)) * D_ + o * 16 + (lane & 7) * 2) * 2u | (real ? 0u : 0x40000000u);
            unsigned pl = real ? W_PL + fb + e * 5u * PAIR : 0x40000000u, zs = real ? W_ZM + (NSEG - 2 + e + lay * NSEG) % 3 * (NOWN * PAIR) : 0x40000000u;
            asm volatile("" : "+s"(pl), "+s"(zs));
            if (e) {
                if (real) {
                    if (w < 7) __builtin_amdgcn_s_sleep(DPS);
                    sent(NSEG - 2);
                    zt_wait(NSEG - 1);
                }
                const unsigned zl = real ? O_Z + (unsigned)((NSEG - 1) * ZTSEG) * 2u : 0x40000000u;
#pragma unroll
                for (int q = 0; q < 4; ++q) zf[q] = cat8(__builtin_amdgcn_raw_buffer_load_b128(rws, ztoff + q * 2048u, zl, 16), __builtin_amdgcn_raw_buffer_load_b128(rws, ztoff + q * 2048u + 1024u, zl, 16));
            }
            if (DR0 && !e) __builtin_amdgcn_s_sleep(DR0);
            const unsigned xr = __builtin_amdgcn_raw_buffer_load_b32(rws, ooff, O_X1, 16);
            SB();
            if (w == 7) wait_fine_bar(tpp + ML, ck); else if (e && real) BAR0();
            f32x4 c[2];
            for (unsigned spins = 0;;) {
                asm volatile("" ::: "memory");
                u32x4 pt[16], ptm[2][2], zm[2];
#pragma unroll
                for (int q = 0; q < 16; ++q) pt[q] = __builtin_amdgcn_raw_buffer_load_b128(rwin, (unsigned)(16 * hg) * 1024u + lane * 16u + (q & 3) * 1024u, pl + (q >> 2) * 4096u, 16);
#pragma unroll
                for (int q = 0; q < 2; ++q) zm[q] = __builtin_amdgcn_raw_buffer_load_b128(rwin, zoff + hg * 2048u + q * 1024u, zs, 16);
#pragma unroll
                for (int q = 0; q < 4; ++q) ptm[q >> 1][q & 1] = __builtin_amdgcn_raw_buffer_load_b128(rwin, (unsigned)(hg * 2048 + lane * 16) + (q & 1) * 1024u, pl + 65536u + (q >> 1) * 8192u, 16);
                SB();
                if (!e && !spins) BAR0();
                unsigned m = max(smax(zm[0]), smax(zm[1]));
                c[0] = z4; c[1] = z4;
#pragma unroll
                for (int q = 0; q < 16; ++q) { c[q & 1] = mfma32(half8(zf[q >> 2], q >> 1 & 1), pt[q], c[q & 1]); m = max(m, smax(pt[q])); }
#pragma unroll
                for (int q = 0; q < 4; ++q) { c[q >> 1] = mfma32((u32x4){zm[q & 1][0], zm[q & 1][2], zm[q & 1][1], zm[q & 1][3]}, ptm[q >> 1][q & 1], c[q >> 1]); m = max(m, smax(ptm[q >> 1][q & 1])); }
                if (!__any(m == 0xFFFFFFFFu)) break;
                if (spin_fail(spins, rws)) break;
            }
#pragma unroll
            for (int t = 0; t < 2; ++t)
#pragma unroll
                for (int r = 0; r < 4; ++r) *(float*)(op + w * 2048 + (16 * t + fr) * 64 + pi16(4 * fq + r) * 4) = c[t][r];
            if (real) BAR();
            const char* src = op + e * 8192 + hg * 512 + lane * 8;
            const f32x2 y = (*(const f32x2*)src + *(const f32x2*)(src + 2048)) + (*(const f32x2*)(src + 4096) + *(const f32x2*)(src + 6144));
            __builtin_amdgcn_raw_buffer_store_b32(nosent(pack2(lo2f(xr) + bfr(y[0]), hi2f(xr) + bfr(y[1]))), rout, ooff, 0u, 16);
            __builtin_amdgcn_s_sleep(4);
            u32x4 sv = SENT;
            PIN1(sv);
            st128(sv, rwin, zoff + hg * 2048u, zs, 16); st128(sv, rwin, zoff + hg * 2048u + 1024u, zs, 16);
            if (w == 3) st128(sv, rwin, ra, W_MEM + 18u * PAIR, 16);
            if (lay && o == 0 && w == 0) __builtin_amdgcn_raw_buffer_store_b64(get_tag(), rws, 0u, 8u, 16);
        }
        }
        SST(23);
    } else if (bid < NREC + 30) {
        LANES();
        const int cg = (bid - NREC) % 10;
        const unsigned ztoff = (unsigned)((4 * cg * H_ + 4 * w) * 64 + lane) * 16u;
        const unsigned ptoff = (unsigned)(8 * w * 64 + lane) * 16u, ptmoff = 65536u + (unsigned)(4 * w * 16 + lane) * 16u;
        const int tk = (w & 3) * 8 + (lane >> 3);
        const unsigned ooff = (unsigned)(tk * D_ + cg * 64 + (lane & 7) * 8) * 2u;
        const unsigned gooff = W_GO + (unsigned)((4 * cg + (lane & 2 ? 3 : 0)) * 128 + (lane & 1) * 4);
        int rdy = -1, go = -1;
        auto ask = [&]() {
            asm volatile("" ::: "memory");
            return (int)__builtin_amdgcn_raw_buffer_load_b32(rwin, gooff, fb, 16);
        };
        auto look = [&](int f) {
            go = min(__builtin_amdgcn_readlane(f, 33), __builtin_amdgcn_readlane(f, 35));
            rdy = max(rdy, max(__builtin_amdgcn_readlane(f, 32), __builtin_amdgcn_readlane(f, 34)));
        };
        auto wait_rdy = [&](int& x, int v, int s) {
            for (unsigned spins = 0; x < v;) {
                look(ask());
                if (x >= v) break;
                if (spin_fail(spins, rws)) break;
                if (s >= NSEG - 5) __builtin_amdgcn_s_sleep(OSLP2); else __builtin_amdgcn_s_sleep(OSLP);
            }
        };
        if (w >= 4) wait_h(rws, rwin, fb, 3, hrdy);
        u32x4 zf[4][4];
        auto load_z = [&](int s) {
#pragma unroll
            for (int q = 0; q < 16; ++q) zf[q >> 2][q & 3] = __builtin_amdgcn_raw_buffer_load_b128(rws, ztoff + (q & 3) * 1024u, O_Z + (unsigned)(s * ZTSEG) * 2u + (q >> 2) * (H_ * 1024u), 16);
            SB();
        };
        wait_rdy(go, (bid - NREC) / 10, 0);
        load_z((bid - NREC) / 10);
        for (int s = (bid - NREC) / 10;; s += 3) {
            u32x4 xr, zx[4], pt[8], ptm[2];
            asm volatile("" : "=v"(xr));
            RT(0);
            wait_rdy(rdy, s, s);
            RT(1);
            if (s >= NSEG - 8 && cg == 0 && w == 0) due(E_OUT + NSEG - 1 - s, s + 3 < NSEG ? DOU3 : DOU2);
            const unsigned ptb = O_PT + (unsigned)(s * PTSEG) * 2u;
#pragma unroll
            for (int q = 0; q < 8; ++q) pt[q] = __builtin_amdgcn_raw_buffer_load_b128(rws, ptoff + q * 1024u, ptb, 16);
#pragma unroll
            for (int q = 0; q < 2; ++q) ptm[q] = __builtin_amdgcn_raw_buffer_load_b128(rws, ptmoff + q * 8192u, ptb, 16);
#pragma unroll
            for (int q = 0; q < 4; ++q) zx[q] = __builtin_amdgcn_raw_buffer_load_b128(rws, tid * 16u, O_Z2 + (unsigned)(s * NOWN + 4 * cg + q) * 8192u, 16);
            if (w >= 4) xr = __builtin_amdgcn_raw_buffer_load_b128(rws, ooff, O_X1 + (unsigned)(s * SEG * D_ * 2), 16);
            const int f = ask();
            SB();
            char* op = smem + (s & 1) * 65536;
            f32x4 c[8];
#pragma unroll
            for (int q = 0; q < 4; ++q)
#pragma unroll
                for (int u = 0; u < 8; ++u) c[u] = mfma32(zf[u >> 1][q], pt[2 * q + (u & 1)], q ? c[u] : z4);
#pragma unroll
            for (int u = 0; u < 8; ++u) {
                const int tq = u & 1, t = u >> 1;
                *(f32x4*)(op + w * 8192 + (16 * tq + fr) * 256 + (((t * 4 + fq) ^ (fr & 7)) << 4)) = mfma32((u32x4){zx[t][0], zx[t][2], zx[t][1], zx[t][3]}, ptm[tq], c[u]);
            }
            look(f);
            const bool pre = go >= s + 3 && s < NSEG - 14;
            if (pre) load_z(s + 3);
            BAR();
            if (lay == 0 && s >= 3 && s < NSEG - 11 && tid == 0) __builtin_amdgcn_raw_buffer_store_b32(0u, rwin, 0u, W_OUT + (unsigned)((s - 3) * 128 + cg * 4), 16);
            if (w >= 4) {
                f32x2 x[4];
#pragma unroll
                for (int q = 0; q < 4; ++q) {
                    const char* sp = op + tk * 256 + ((((lane & 6) * 2 + q) ^ (tk & 7)) << 4) + (lane & 1) * 8;
                    x[q] = *(const f32x2*)sp;
#pragma unroll
                    for (int ww = 1; ww < 8; ++ww) x[q] += *(const f32x2*)(sp + ww * 8192);
                }
                const u32x4 r = {pack2(lo2f(xr[0]) + bfr(x[0][0]), hi2f(xr[0]) + bfr(x[2][0])), pack2(lo2f(xr[1]) + bfr(x[1][0]), hi2f(xr[1]) + bfr(x[3][0])),
                                 pack2(lo2f(xr[2]) + bfr(x[0][1]), hi2f(xr[2]) + bfr(x[2][1])), pack2(lo2f(xr[3]) + bfr(x[1][1]), hi2f(xr[3]) + bfr(x[3][1]))};
                st128(((u32x4){nosent(r[0]), nosent(r[1]), nosent(r[2]), nosent(r[3])}), rout, ooff, (unsigned)(s * SEG * D_ * 2), 16);
            } else if (!pre) __builtin_amdgcn_s_sleep(ODL);
            if (lay == 0 && s >= NSEG - 14 && s < NSEG - 8) {
                VMWAIT();
                BAR0();
                if (tid == 0) __builtin_amdgcn_raw_buffer_store_b32(0u, rwin, 0u, W_OUT + (unsigned)(s * 128 + cg * 4), 16);
            }
            RT(2);
            RTS();
            if (s < 8) SST(8 + s);
            if (s >= 56) SST(s - 40);
            if (s + 3 >= NSEG - 2) break;
            if (!pre) { wait_rdy(go, s + 3, 0); load_z(s + 3); }
        }
    } else if (lay == 0 && bid < NWRK) {
        if (w == 0) for (unsigned spins = 0; (int)__builtin_amdgcn_raw_buffer_load_b32(rwin, 0u, W_E + (E_OUT + 3) * 128u, 16) == -1 && !spin_fail(spins, rws);) {
            __builtin_amdgcn_s_sleep(64);
            asm volatile("" ::: "memory");
        }
    } else if (lay == 0) {
        LANES();
        const RowTw tw = row_tw(lane);
        for (int u = (256 - bid) & 15; u < 256; u += 16) {
            if (w == 0) {
                if (u >= 224) await(E_OUT + (255 - u >> 2), 1, 0);
                else for (unsigned spins = 0;;) {
                    asm volatile("" ::: "memory");
                    const unsigned f = __builtin_amdgcn_raw_buffer_load_b32(rwin, (unsigned)min(lane, 9) * 4u, W_OUT + (unsigned)(u >> 2) * 128u, 16);
                    if (!__any(f != 0u)) break;
                    if (spin_fail(spins, rws)) break;
                    __builtin_amdgcn_s_sleep(WSLP);
                }
            }
            BAR0();
            SST(24);
            const unsigned fo = W_FA + (unsigned)((u >> 2) * 128 + (u & 3) * 4);
            a1_unit<4, 512>(rout, u * 8, u >= 224, rws, smem, tw, w, lane, [&](int k) {
                if (k == 0 && w == 0 && u >= 224 && !(u & 3)) due(E_A1 + (u - 224 >> 2), DA1);
                if (k >= 0) SST(8 + k);
            });
            SST(25);
            if (u >= 224) continue;
            VMWAIT();
            BAR0();
            if (tid == 0) __builtin_amdgcn_raw_buffer_store_b32(0u, rwin, 0u, fo, 16);
        }
    }
    if (lay == 0 && w == 0 && bid >= NREC && bid <= NWRK) await(E_OUT + 1, 1, bid < NREC + 32 ? -DOW : -60);
    SST(31);
    }
}

constexpr int L_DBG = 0;

extern "C" void kernel_launch(void* const* d_in, const int* in_sizes, int n_in, void* d_out, int out_size, void* d_ws, size_t ws_size,
                              hipStream_t stream) {
    char* ws = (char*)d_ws;
    hipFuncSetAttribute((const void*)b3_kernel, hipFuncAttributeMaxDynamicSharedMemorySize, L_END + L_DBG);
    char* win = (char*)(((uintptr_t)(ws + OFF_WIN) + 0x1FFFFFu) & ~(uintptr_t)0x1FFFFFu);
    RecArgs ra{(unsigned*)ws, win, (const u16*)d_in[4], (const u16*)d_in[0], (u16*)d_out, {(const u16*)d_in[5], (const u16*)d_in[6], (const u16*)d_in[7], (const u16*)d_in[8], (const u16*)d_in[1], (const u16*)d_in[2]}};
    b3_kernel<<<256, 512, L_END + L_DBG, stream>>>(ra);
}
```

```cpp
#include <hip/hip_runtime.h>
#include <hip/hip_bf16.h>
#include <stdint.h>

typedef unsigned short u16;
typedef __attribute__((ext_vector_type(8))) __bf16 bf16x8;
typedef __attribute__((ext_vector_type(4))) short s16x4;
typedef __attribute__((ext_vector_type(4))) float f32x4;
typedef __attribute__((ext_vector_type(8))) float f32x8;
typedef __attribute__((ext_vector_type(16))) float f32x16;
typedef __attribute__((ext_vector_type(4))) unsigned u32x4;
typedef __attribute__((ext_vector_type(2))) unsigned u32x2;
typedef __attribute__((ext_vector_type(8))) unsigned u32x8;
typedef __attribute__((ext_vector_type(16))) __bf16 bf16x16;
typedef __attribute__((ext_vector_type(2))) unsigned short u16x2;
#define LAS __attribute__((address_space(3)))
template <int N> struct IC { static constexpr int v = N; };

constexpr int S_ = 2048, D_ = 640, DF = 321, H_ = 32, DH = 128, HD = 4096, NM = 8, SEG = 32, NSEG = 64;
constexpr int NQKV = 3 * HD;
constexpr int NOWN = 40;
constexpr int ZTR = NSEG;
constexpr size_t ZTSEG = (size_t)NOWN * H_ * 512;
constexpr unsigned PAIR = 16384, HALF = 8192;
constexpr unsigned W_MEM = 0, W_P = W_MEM + 24 * PAIR, W_T = W_P + 40 * PAIR, W_GO = W_T + 8 * PAIR, W_OUT = W_GO + PAIR, W_FQ = W_OUT + 8192, W_FZ = W_OUT + PAIR,
                   W_H = W_FZ + 2 * PAIR, W_FA = W_H + 8192, W_FB = W_GO + 8192, W_E = W_FB + 4096, W_PL = W_H + PAIR, W_FL = W_PL + 10 * PAIR - W_GO, W_PROBE = W_GO + 2 * W_FL,
                   W_ZM = 2u * 1024 * 1024, W_END = W_ZM + 120 * PAIR;
constexpr unsigned W_ST = W_PROBE + 2 * PAIR, W_ROT = W_ST + 8192, W_ARM = W_ROT + 128, W_CFG = W_ARM + 128;
static_assert(W_ROT + PAIR <= 2u * 1024 * 1024 && W_PROBE % (2 * PAIR) == 0, "window");
constexpr int PTSEG = 32768 + 8192;

#define WAUX 0
#define FPAD 512
constexpr int GF = 32768 + FPAD, YS = 12288 + FPAD;
constexpr int NG = 81;
constexpr size_t al(size_t x) { return (x + 255) & ~(size_t)255; }
constexpr size_t OFF_CTL  = 0;
constexpr size_t OFF_WFT  = 4096 + 4 * 1024 * 1024;
constexpr size_t OFF_F    = OFF_WFT + al((size_t)4 * NG * S_ * 4);
constexpr size_t OFF_X1   = OFF_F + (size_t)NG * GF;
constexpr size_t OFF_X2   = OFF_X1 + al((size_t)S_ * D_ * 2);
constexpr size_t OFF_Y0   = OFF_X2 + al((size_t)S_ * D_ * 2);
constexpr size_t OFF_H    = OFF_Y0 + (size_t)NG * YS;
constexpr size_t OFF_ARM  = OFF_Y0 - 256 * D_ * 2, ARM_END = OFF_H + 8 * 40960;
constexpr size_t OFF_Z    = OFF_H + (size_t)(NSEG + 4) * 40960;
constexpr size_t OFF_Z2   = OFF_Z + al(ZTSEG * ZTR * 2);
constexpr size_t OFF_PT   = OFF_Z2 + al((size_t)NSEG * NOWN * 8192);
constexpr size_t OFF_QK   = OFF_PT + al((size_t)NSEG * PTSEG * 2);
constexpr size_t OFF_WIN  = OFF_QK + (size_t)NSEG * H_ * 16384;
constexpr size_t OFF_END  = OFF_WIN + (size_t)6 * 1024 * 1024;
static_assert(OFF_END <= (size_t)256 * 1024 * 1024, "workspace");

#define st128(v, r, vo, so, aux) do { const u32x4 d_ = (v); __builtin_amdgcn_raw_buffer_store_b128(d_, r, vo, so, aux); asm volatile("s_nop 1" :: "v"(d_)); } while (0)
__device__ __forceinline__ float bf2f(u16 v) { return __uint_as_float((unsigned)v << 16); }
__device__ __forceinline__ u16 f2bf(float f) { __bf16 b = (__bf16)f; return __builtin_bit_cast(u16, b); }
__device__ __forceinline__ float bfr(float f) { return (float)(__bf16)f; }
typedef __attribute__((ext_vector_type(2))) float f32x2;
typedef __attribute__((ext_vector_type(2))) __bf16 bf16x2;
__device__ __forceinline__ unsigned pack2(float a, float b) { return __builtin_bit_cast(unsigned, __builtin_convertvector((f32x2){a, b}, bf16x2)); }
typedef __attribute__((ext_vector_type(2))) _Float16 f16x2;
typedef __attribute__((ext_vector_type(8))) _Float16 f16x8;
__device__ __forceinline__ unsigned packh(float a, float b) { return __builtin_bit_cast(unsigned, __builtin_convertvector((f32x2){a, b}, f16x2)); }
__device__ __forceinline__ f32x4 mfmah(u32x4 a, u32x4 b) { return __builtin_amdgcn_mfma_f32_16x16x32_f16(__builtin_bit_cast(f16x8, a), __builtin_bit_cast(f16x8, b), (f32x4){0.f, 0.f, 0.f, 0.f}, 0, 0, 0); }
__device__ __forceinline__ float lo2f(unsigned v) { return __uint_as_float(v << 16); }
__device__ __forceinline__ float hi2f(unsigned v) { return __uint_as_float(v & 0xFFFF0000u); }
__device__ __forceinline__ float fmx(float a, float b) { return __builtin_elementwise_maximum(a, b); }
__device__ __forceinline__ f32x4 mfma32(u32x4 a, u32x4 b, f32x4 c) {
    return __builtin_amdgcn_mfma_f32_16x16x32_bf16(__builtin_bit_cast(bf16x8, a), __builtin_bit_cast(bf16x8, b), c, 0, 0, 0);
}
__device__ __forceinline__ f32x4 mfma16(u32x2 a, u32x2 b, f32x4 c) {
    return __builtin_amdgcn_mfma_f32_16x16x16bf16_1k(__builtin_bit_cast(s16x4, a), __builtin_bit_cast(s16x4, b), c, 0, 0, 0);
}
__device__ __forceinline__ f32x4 smfmac(u32x4 a, u32x8 b, f32x4 c, int idx) {
    return __builtin_amdgcn_smfmac_f32_16x16x64_bf16(__builtin_bit_cast(bf16x8, a), __builtin_bit_cast(bf16x16, b), c, idx, 0, 0);
}
__device__ __forceinline__ int pi16(int k) { return ((k & 3) << 2) | ((k >> 1) & 2) | (k >> 3); }
__device__ __forceinline__ float addswap32(float a, float b) {
    auto r = __builtin_amdgcn_permlane32_swap(__float_as_uint(a), __float_as_uint(b), false, false);
    return __uint_as_float(r[0]) + __uint_as_float(r[1]);
}
__device__ __forceinline__ float xmax3216(float v) {
    auto r = __builtin_amdgcn_permlane32_swap(__float_as_uint(v), __float_as_uint(v), false, false);
    v = fmx(__uint_as_float(r[0]), __uint_as_float(r[1]));
    auto q = __builtin_amdgcn_permlane16_swap(__float_as_uint(v), __float_as_uint(v), false, false);
    return fmx(__uint_as_float(q[0]), __uint_as_float(q[1]));
}
__device__ __forceinline__ float xsum3216(float v) {
    auto r = __builtin_amdgcn_permlane32_swap(__float_as_uint(v), __float_as_uint(v), false, false);
    v = __uint_as_float(r[0]) + __uint_as_float(r[1]);
    auto q = __builtin_amdgcn_permlane16_swap(__float_as_uint(v), __float_as_uint(v), false, false);
    return __uint_as_float(q[0]) + __uint_as_float(q[1]);
}
template <int CTRL> __device__ __forceinline__ float dppf(float v) { return __uint_as_float((unsigned)__builtin_amdgcn_update_dpp(0, (int)__float_as_uint(v), CTRL, 0xF, 0xF, false)); }
__device__ __forceinline__ float wsum(float v) {
    v += dppf<0xB1>(v); v += dppf<0x4E>(v); v += dppf<0x141>(v); v += dppf<0x140>(v);
    return xsum3216(v);
}
__device__ __forceinline__ unsigned ror8(unsigned v) { return (unsigned)__builtin_amdgcn_update_dpp(0, (int)v, 0x128, 0xF, 0xF, false); }
__device__ __forceinline__ float ror8f(float v) { return __uint_as_float(ror8(__float_as_uint(v))); }
__device__ __forceinline__ unsigned nosent(unsigned v) {
    u16x2 lim = {0xFFFE, 0xFFFE};
    return __builtin_bit_cast(unsigned, __builtin_elementwise_min(__builtin_bit_cast(u16x2, v), lim));
}
__device__ __forceinline__ unsigned pkmax(unsigned a, unsigned b) {
    return __builtin_bit_cast(unsigned, __builtin_elementwise_max(__builtin_bit_cast(u16x2, a), __builtin_bit_cast(u16x2, b)));
}
__device__ __forceinline__ bool has_sent(u32x4 v) {
    unsigned m = pkmax(pkmax(v.x, v.y), pkmax(v.z, v.w));
    return (m & 0xFFFFu) == 0xFFFFu || (m >> 16) == 0xFFFFu;
}

typedef f32x2 c32;
__device__ __forceinline__ c32 fma2(c32 a, c32 b, c32 c) { return __builtin_elementwise_fma(a, b, c); }
__device__ __forceinline__ c32 dup(float v) { return (c32){v, v}; }
template <int SG> __device__ __forceinline__ c32 addi(c32 a, c32 b) {
    c32 d;
    if (SG > 0) asm("v_pk_add_f32 %0, %1, %2 op_sel:[0,1] op_sel_hi:[1,0] neg_lo:[0,1]" : "=v"(d) : "v"(a), "v"(b));
    else asm("v_pk_add_f32 %0, %1, %2 op_sel:[0,1] op_sel_hi:[1,0] neg_hi:[0,1]" : "=v"(d) : "v"(a), "v"(b));
    return d;
}
template <int SG> __device__ __forceinline__ c32 ctw(c32 a, c32 w) {
    c32 d = a * w.xx;
    if (SG < 0) asm("v_pk_fma_f32 %0, %1, %2, %0 op_sel:[1,1,0] op_sel_hi:[0,1,1] neg_lo:[0,1,0]" : "+v"(d) : "v"(a), "v"(w));
    else asm("v_pk_fma_f32 %0, %1, %2, %0 op_sel:[1,1,0] op_sel_hi:[0,1,1] neg_hi:[0,1,0]" : "+v"(d) : "v"(a), "v"(w));
    return d;
}
__device__ __forceinline__ c32 cis(float rev) { return (c32){__builtin_amdgcn_cosf(rev), __builtin_amdgcn_sinf(rev)}; }
template <int N> __device__ __forceinline__ void cpow(c32 (&t)[N], float rev) {
    t[0] = cis(rev);
    asm volatile("s_nop 0" : "+v"(t[0]));
#pragma unroll
    for (int i = 1; i < N; ++i) { t[i] = ctw<-1>(t[(i - 1) / 2], t[i / 2]); asm volatile("" : "+v"(t[i])); }
}
#define SB() __builtin_amdgcn_sched_barrier(0)
#define SA() do { SB(); asm volatile(".p2align 3"); SB(); } while (0)
template <int SG, int I0, int N, class F> __device__ __forceinline__ void ctwn(c32 (&u)[N], F w) {
    c32 d[N];
    SA();
#pragma unroll
    for (int i = I0; i < N; ++i) asm("v_pk_mul_f32 %0, %1, %2 op_sel_hi:[1,0]" : "=v"(d[i]) : "v"(u[i]), "v"(w(i)));
    SB();
#pragma unroll
    for (int i = I0; i < N; ++i) {
        if (SG < 0) asm("v_pk_fma_f32 %0, %1, %2, %0 op_sel:[1,1,0] op_sel_hi:[0,1,1] neg_lo:[0,1,0]" : "+v"(d[i]) : "v"(u[i]), "v"(w(i)));
        else asm("v_pk_fma_f32 %0, %1, %2, %0 op_sel:[1,1,0] op_sel_hi:[0,1,1] neg_hi:[0,1,0]" : "+v"(d[i]) : "v"(u[i]), "v"(w(i)));
        u[i] = d[i];
    }
    SB();
}
template <int SG, int M, int A, int B, int CI, int N> __device__ __forceinline__ void dft4n(c32 (&u)[N]) {
    c32 t[M][4];
    SA();
#pragma unroll
    for (int i = 0; i < M; ++i) {
        const c32 a = u[A * i], b = u[A * i + B], c = u[A * i + 2 * B], d = u[A * i + 3 * B];
        t[i][0] = i == CI ? addi<SG>(a, c) : a + c; t[i][1] = i == CI ? addi<-SG>(a, c) : a - c; t[i][2] = b + d; t[i][3] = b - d;
        if (B == 4) SB();
    }
    SB();
#pragma unroll
    for (int i = 0; i < M; ++i) { u[A * i] = t[i][0] + t[i][2]; u[A * i + 2 * B] = t[i][0] - t[i][2]; u[A * i + B] = addi<SG>(t[i][1], t[i][3]); u[A * i + 3 * B] = addi<-SG>(t[i][1], t[i][3]); }
    SB();
}
template <int SG> __device__ __forceinline__ void dft8x2(c32 (&u)[16]) {
    dft4n<SG, 4, 1, 4, -1>(u);
    const float r = 0.70710678118654752440f;
    c32 o[16];
#pragma unroll
    for (int e = 0; e < 2; ++e) { u[6 + e] = addi<SG>(u[6 + e], u[6 + e]); u[14 + e] = addi<-SG>(u[14 + e], u[14 + e]); }
#pragma unroll
    for (int e = 0; e < 2; ++e) { o[e] = u[e] + u[2 + e]; o[8 + e] = u[e] - u[2 + e]; o[4 + e] = addi<SG>(u[8 + e], u[10 + e]); o[12 + e] = addi<-SG>(u[8 + e], u[10 + e]); }
    SB();
#pragma unroll
    for (int e = 0; e < 2; ++e) { o[2 + e] = fma2(u[6 + e], dup(r), u[4 + e]); o[10 + e] = fma2(u[6 + e], dup(-r), u[4 + e]); o[6 + e] = fma2(u[14 + e], dup(-r), u[12 + e]); o[14 + e] = fma2(u[14 + e], dup(r), u[12 + e]); }
    SB();
#pragma unroll
    for (int i = 0; i < 16; ++i) u[i] = o[i];
}
template <int SG> __device__ __forceinline__ void dft16(c32 (&u)[16]) {
    const float c1 = 0.92387953251128675613f, s1 = 0.38268343236508977173f, r = 0.70710678118654752440f;
    dft4n<SG, 4, 1, 4, -1>(u);
    const int ix[8] = {5, 9, 13, 6, 14, 7, 11, 15};
    const float wx[8] = {c1, r, s1, r, -r, s1, -r, -c1}, wy[8] = {s1, r, c1, r, r, c1, r, -s1};
    c32 d[8];
#pragma unroll
    for (int i = 0; i < 8; ++i) d[i] = u[ix[i]] * dup(wx[i]);
    SB();
#pragma unroll
    for (int i = 0; i < 8; ++i) u[ix[i]] = fma2(u[ix[i]].yx, (c32){-SG * wy[i], SG * wy[i]}, d[i]);
    SB();
    dft4n<SG, 4, 4, 1, 2>(u);
    c32 o[16];
#pragma unroll
    for (int i = 0; i < 16; ++i) o[(i >> 2) + 4 * (i & 3)] = u[i];
#pragma unroll
    for (int i = 0; i < 16; ++i) u[i] = o[i];
}
template <int SG> __device__ __forceinline__ void dft10(c32 (&u)[10]) {
    const float c1 = 0.30901699437494742410f, c2 = -0.80901699437494742410f, s1 = 0.95105651629515357212f, s2 = 0.58778525229247312917f;
    c32 x[2][5], t[2][4], a[2][5];
    SA();
#pragma unroll
    for (int i = 0; i < 5; ++i) { x[0][i] = u[2 * i] + u[(2 * i + 5) % 10]; x[1][i] = u[2 * i] - u[(2 * i + 5) % 10]; }
    SB();
#pragma unroll
    for (int e = 0; e < 2; ++e) { t[e][0] = x[e][1] + x[e][4]; t[e][1] = x[e][2] + x[e][3]; t[e][2] = x[e][1] - x[e][4]; t[e][3] = x[e][2] - x[e][3]; }
    SB();
#pragma unroll
    for (int e = 0; e < 2; ++e) { a[e][0] = t[e][0] + t[e][1]; a[e][1] = fma2(t[e][0], dup(c1), x[e][0]); a[e][2] = fma2(t[e][0], dup(c2), x[e][0]); a[e][3] = t[e][2] * dup(s1); a[e][4] = t[e][2] * dup(s2); }
    SB();
#pragma unroll
    for (int e = 0; e < 2; ++e) { a[e][0] += x[e][0]; a[e][1] = fma2(t[e][1], dup(c2), a[e][1]); a[e][2] = fma2(t[e][1], dup(c1), a[e][2]); a[e][3] = fma2(t[e][3], dup(s2), a[e][3]); a[e][4] = fma2(t[e][3], dup(-s1), a[e][4]); }
    SB();
#pragma unroll
    for (int e = 0; e < 2; ++e) {
        u[5 * e] = a[e][0];
        u[(6 + 5 * e) % 10] = addi<SG>(a[e][1], a[e][3]); u[(24 + 5 * e) % 10] = addi<-SG>(a[e][1], a[e][3]);
        u[(12 + 5 * e) % 10] = addi<SG>(a[e][2], a[e][4]); u[(18 + 5 * e) % 10] = addi<-SG>(a[e][2], a[e][4]);
    }
    SB();
}
template <int ST> __device__ __forceinline__ void lds16(c32 (&u)[16], const char* p) {
    const LAS char* q = (const LAS char*)p;
#pragma unroll
    for (int i = 0; i < 16; ++i) asm volatile("ds_read_b64 %0, %1 offset:%2" : "=v"(u[(i >> 2) + 4 * (i & 3)]) : "v"(q), "n"(((i >> 2) + 4 * (i & 3)) * ST) : "memory");
#pragma unroll
    for (int i = 0; i < 4; ++i) asm volatile("s_waitcnt lgkmcnt(%4)" : "+v"(u[i]), "+v"(u[i + 4]), "+v"(u[i + 8]), "+v"(u[i + 12]) : "n"(12 - 4 * i) : "memory");
}
__device__ __forceinline__ c32 unpack2(unsigned v) { return (c32){lo2f(v), hi2f(v)}; }
__device__ __forceinline__ float wsum2(c32 v) {
    float t = addswap32(v.x, v.y);
    auto q = __builtin_amdgcn_permlane16_swap(__float_as_uint(t), __float_as_uint(t), false, false);
    t = __uint_as_float(q[0]) + __uint_as_float(q[1]);
    t += dppf<0xB1>(t); t += dppf<0x4E>(t); t += dppf<0x141>(t); t += dppf<0x140>(t);
    return t;
}
__device__ __forceinline__ c32 halves(float t) { return (c32){__uint_as_float(__builtin_amdgcn_readlane(__float_as_uint(t), 0)), __uint_as_float(__builtin_amdgcn_readlane(__float_as_uint(t), 32))}; }
__device__ __forceinline__ void ln_rows(const c32 (&v)[10], unsigned (&o)[10]) {
    const float mean = wsum2(((v[0] + v[1]) + (v[2] + v[3])) + ((v[4] + v[5]) + (v[6] + v[7])) + (v[8] + v[9])) * (1.f / 640.f);
    const c32 m = halves(mean), mb = halves(bfr(mean));
    c32 d[10], e[10];
#pragma unroll
    for (int r = 0; r < 10; ++r) d[r] = v[r] - m;
    SB();
#pragma unroll
    for (int r = 0; r < 5; ++r) e[r] = d[r] * d[r];
#pragma unroll
    for (int r = 0; r < 5; ++r) d[r] = fma2(d[r + 5], d[r + 5], e[r]);
    SB();
#pragma unroll
    for (int r = 0; r < 10; ++r) { const c32 y = v[r] - mb; e[r] = unpack2(pack2(y.x, y.y)); }
    const float var = wsum2((d[0] + d[1]) + (d[2] + d[3]) + d[4]) * (1.f / 640.f);
    const c32 rs = halves(__builtin_amdgcn_rcpf(bfr(__builtin_amdgcn_sqrtf(bfr(bfr(var) + 1e-5f)))));
    SB();
#pragma unroll
    for (int r = 0; r < 10; ++r) e[r] *= rs;
    SB();
#pragma unroll
    for (int r = 0; r < 10; ++r) o[r] = pack2(e[r].x, e[r].y);
    SB();
}

__device__ __forceinline__ __amdgpu_buffer_rsrc_t mkrsrc(const void* p, unsigned bytes) {
    return __builtin_amdgcn_make_buffer_rsrc((void*)p, 0, bytes, 0x00020000);
}

constexpr unsigned O_F = (unsigned)OFF_F, O_Y0 = (unsigned)OFF_Y0;
__device__ __forceinline__ unsigned nosent1(unsigned v) { return min(v, 0xFFFFFFFEu); }
__device__ __forceinline__ u32x4 nosent4(u32x4 v) { v[1] = nosent1(v[1]); v[3] = nosent1(v[3]); return v; }
__device__ __forceinline__ unsigned smax(u32x4 v) { return max(v[1], v[3]); }
constexpr unsigned SPIN_CAP = 400000u;
constexpr int RW = 5920;
struct RowTw { c32 a[9], b, c; };
__device__ __forceinline__ RowTw row_tw(int lane) {
    RowTw t;
    t.b = cis((float)(lane & 31) * (-1.f / 64.f)); t.c = cis((float)(lane & 15) * (-1.f / 32.f));
    asm volatile("" : "+v"(t.b), "+v"(t.c));
    cpow(t.a, (float)lane * (-1.f / 640.f));
    return t;
}
template <int B> __device__ __forceinline__ void cswap(c32& x, c32& y) {
#pragma unroll
    for (int e = 0; e < 2; ++e) {
        auto r = B == 32 ? __builtin_amdgcn_permlane32_swap(__float_as_uint(x[e]), __float_as_uint(y[e]), false, false) : __builtin_amdgcn_permlane16_swap(__float_as_uint(x[e]), __float_as_uint(y[e]), false, false);
        x[e] = __uint_as_float(r[0]); y[e] = __uint_as_float(r[1]);
    }
}
template <int B> __device__ __forceinline__ void cswap5(c32 (&p)[5], c32 (&q)[5]) {
#pragma unroll
    for (int i = 0; i < 5; ++i) cswap<B>(p[i], q[i]);
    SB();
}
__device__ __forceinline__ void bfly5(c32 (&p)[5], c32 (&q)[5]) {
#pragma unroll
    for (int i = 0; i < 5; ++i) { const c32 t = p[i] - q[i]; p[i] += q[i]; q[i] = t; }
    SB();
}
constexpr unsigned L_X = 16 * RW;
__device__ __forceinline__ void row_x(__amdgpu_buffer_rsrc_t rx, int row, int w, int lane) {
    asm volatile("s_mov_b32 m0, %3\n\ts_nop 0\n\tbuffer_load_dwordx4 %0, %1, %2 offen sc1 lds\n\tbuffer_load_dwordx4 %0, %1, %2 offen offset:1024 sc1 lds\n\tbuffer_load_dwordx4 %0, %1, %2 offen offset:1536 sc1 lds"
                 :: "v"(lane * 16), "s"(rx), "s"(row * (D_ * 2)), "s"(L_X + w * 2560u) : "m0", "memory");
}
template <int M> __device__ __forceinline__ void row_get(u16 (&v)[20], int w, int lane) {
    asm volatile("s_waitcnt vmcnt(%0)" :: "n"(M) : "memory");
#pragma unroll
    for (int r = 0; r < 20; ++r) v[r] = *(const LAS u16*)(size_t)(L_X + w * 2560u + lane * 2u + 128u * (r >> 1) + 1280u * (r & 1));
    SB();
}
template <int NW, int NT, class FS> __device__ __forceinline__ void a1_unit(__amdgpu_buffer_rsrc_t rx, int row0, bool tail, __amdgpu_buffer_rsrc_t rws, char* sm, const RowTw& tw, int w, int lane, FS st_) {
    const unsigned fo = tail ? O_Y0 + 8192u + (unsigned)(row0 & 255) * 16u : O_F + (unsigned)row0 * 16u, gs = tail ? YS : GF;
    if (w < NW) {
        c32 u[10];
        unsigned h[10];
        u16 x[20];
        row_x(rx, row0 + 2 * w, w, lane);
        st_(-1);
        row_get<NW == 8 ? 8 : 0>(x, w, lane);
        if (tail) for (unsigned spins = 0; spins < SPIN_CAP; ++spins) {
            unsigned m = 0;
#pragma unroll
            for (int r = 0; r < 20; ++r) m = max(m, (unsigned)x[r]);
            if (!__any(m == 0xFFFFu)) break;
            row_x(rx, row0 + 2 * w, w, lane);
            row_get<0>(x, w, lane);
        }
#pragma unroll
        for (int r = 0; r < 10; ++r) u[r] = (c32){bf2f(x[2 * r]), bf2f(x[2 * r + 1])};
        st_(0);
        ln_rows(u, h);
        st_(1);
#pragma unroll
        for (int r = 0; r < 10; ++r) u[r] = unpack2(h[r]);
        dft10<-1>(u);
        char* mine = sm + w * RW;
        char* ex = mine + ((lane >> 5) + 10 * ((lane >> 4) & 1)) * 144 + (lane & 15) * 8;
        ctwn<-1, 1>(u, [&](int i) { return tw.a[i - 1]; });
        c32 p[5], q[5];
#pragma unroll
        for (int i = 0; i < 5; ++i) { p[i] = u[2 * i]; q[i] = u[2 * i + 1]; }
        cswap5<32>(p, q); bfly5(p, q); ctwn<-1, 0>(q, [&](int) { return tw.b; });
        cswap5<16>(p, q); bfly5(p, q);
#pragma unroll
        for (int i = 0; i < 5; ++i) *(c32*)(ex + i * 288) = p[i];
        ctwn<-1, 0>(q, [&](int) { return tw.c; });
#pragma unroll
        for (int i = 0; i < 5; ++i) *(c32*)(ex + i * 288 + 2880) = q[i];
        asm volatile("" ::: "memory");
        st_(2);
        if (lane < 40) {
            c32 v[16];
#pragma unroll
            for (int m = 0; m < 8; ++m) { const f32x4 t = *(const f32x4*)(mine + lane * 144 + m * 16); v[2 * m] = t.xy; v[2 * m + 1] = t.zw; }
            dft16<-1>(v);
            asm volatile("" ::: "memory");
#pragma unroll
            for (int m = 0; m < 16; ++m) *(c32*)(mine + lane * 8 + m * 320) = v[m];
        }
        st_(3);
    }
    asm volatile("s_waitcnt lgkmcnt(0)\n\ts_barrier" ::: "memory");
    st_(4);
    const int tid = w * 64 + lane, k0 = tid / (2 * NW) * 4 + (tid & 3), id = tid % (2 * NW);
    const char* X = sm + (id >> 2) * (2 * RW);
#pragma unroll
    for (int c = 0; c * NT < NG * 2 * NW; ++c) {
        const int k4 = k0 + c * (2 * NT / NW), k = min(k4, 320);
        if ((c + 1) * NT <= NG * 2 * NW || k4 < 4 * NG) {
            u32x4 o;
#pragma unroll
            for (int e = 0; e < 2; ++e) {
                const c32 zk = *(const c32*)(X + e * RW + k * 8) * dup(1.f / 32.f), zn = *(const c32*)(X + e * RW + (k == 0 ? 0 : 640 - k) * 8) * dup(1.f / 32.f);
                o[2 * e] = packh(zk.x + zn.x, zk.y - zn.y); o[2 * e + 1] = packh(zk.y + zn.y, zn.x - zk.x);
            }
            st128(nosent4(o), rws, (unsigned)(k4 >> 2) * gs + id * 16u, fo, 16);
        }
    }
}
template <int NW, class FS, class FD> __device__ __forceinline__ void a3_units(__amdgpu_buffer_rsrc_t rws, __amdgpu_buffer_rsrc_t rx, int row0, int step, int lay, char* sm, const RowTw& tw, int w, int lane, FS st_, FD done) {
    constexpr int NT = 512, NC = (NG * 2 * NW + NT - 1) / NT, GS = NW == 4 ? YS : GF;
    const int tid = w * 64 + lane, k0 = tid / (2 * NW) * 4 + (tid & 3), id = tid % (2 * NW);
    u32x4 fv[NC];
    u16 x[20];
    auto get = [&](int row) {
        const unsigned fo = (NW == 4 ? O_Y0 + lay * 4096u : O_F) + (unsigned)row * 16u;
#pragma unroll
        for (int c = 0; c < NC; ++c) fv[c] = __builtin_amdgcn_raw_buffer_load_b128(rws, (unsigned)((min(k0 + c * (2 * NT / NW), 320) >> 2) * GS + id * 16), fo, 16);
        __builtin_amdgcn_sched_barrier(0);
    };
    if (w < NW) row_x(rx, row0 + 2 * w, w, lane);
    st_(-1);
    for (unsigned spins = 0; spins < SPIN_CAP; ++spins) {
        asm volatile("" ::: "memory");
        get(row0);
        if (NW == 8 || !__any(max(smax(fv[0]), smax(fv[1])) == 0xFFFFFFFFu)) break;
    }
    for (int n = 0;; ++n, row0 += step, sm += n & 1 ? 8 * RW : -8 * RW) {
    char* X = sm + (id >> 2) * (2 * RW);
#pragma unroll
    for (int c = 0; c < NC; ++c) {
        const int k = k0 + c * (2 * NT / NW);
        const unsigned re = k == 0 || k == 320 ? 0u : 0xFFFF0000u;
        if (c + 1 < NC || k <= 320)
#pragma unroll
            for (int e = 0; e < 2; ++e) {
                const c32 p = {lo2f(fv[c][2 * e]), __uint_as_float(fv[c][2 * e] & re)}, q = {lo2f(fv[c][2 * e + 1]), __uint_as_float(fv[c][2 * e + 1] & re)};
                *(c32*)(X + e * RW + k * 8) = addi<1>(p, q); *(c32*)(X + e * RW + (k == 0 ? 0 : 640 - k) * 8) = (c32){p.x + q.y, q.x - p.y};
            }
    }
    asm volatile("s_waitcnt vmcnt(0) lgkmcnt(0)\n\ts_barrier" ::: "memory");
    st_(0);
    if (NW == 8) {
        if (n) done(row0 - step);
        row_get<63>(x, w, lane);
        asm volatile("s_waitcnt lgkmcnt(0)" ::: "memory");
        if (row0 + step < S_) { row_x(rx, row0 + step + 2 * w, w, lane); get(row0 + step); }
    }
    if (w < NW) {
        char* mine = sm + w * RW;
        if (lane < 40) {
            c32 v[16];
            lds16<320>(v, mine + lane * 8);
            dft16<1>(v);
            asm volatile("" ::: "memory");
#pragma unroll
            for (int m = 0; m < 8; ++m) *(f32x4*)(mine + lane * 144 + m * 16) = (f32x4){v[2 * m].x, v[2 * m].y, v[2 * m + 1].x, v[2 * m + 1].y};
        }
        asm volatile("" ::: "memory");
        st_(1);
        const char* ex = mine + ((lane >> 5) + 10 * ((lane >> 4) & 1)) * 144 + (lane & 15) * 8;
        c32 u[10];
        c32 p[5], q[5];
#pragma unroll
        for (int i = 0; i < 5; ++i) q[i] = *(const c32*)(ex + i * 288 + 2880);
#pragma unroll
        for (int i = 0; i < 5; ++i) p[i] = *(const c32*)(ex + i * 288);
        SB();
        ctwn<1, 0>(q, [&](int) { return tw.c; }); bfly5(p, q); cswap5<16>(p, q);
        ctwn<1, 0>(q, [&](int) { return tw.b; }); bfly5(p, q); cswap5<32>(p, q);
#pragma unroll
        for (int i = 0; i < 5; ++i) { u[2 * i] = p[i]; u[2 * i + 1] = q[i]; }
        ctwn<1, 1>(u, [&](int i) { return tw.a[i - 1]; });
        if (NW == 4) row_get<63>(x, w, lane);
        dft10<1>(u);
        st_(2);
        asm volatile("" ::: "memory");
        char* st = mine + lane * 2;
        unsigned h[10];
#pragma unroll
        for (int r = 0; r < 10; ++r) {
            const c32 y = (c32){bf2f(x[2 * r]), bf2f(x[2 * r + 1])} + unpack2(pack2(u[r].x, u[r].y));
            const unsigned p = pack2(y.x, y.y);
            u[r] = unpack2(p);
            *(u16*)(st + r * 128) = (u16)p; *(u16*)(st + 1280 + r * 128) = (u16)(p >> 16);
        }
        st_(3);
        ln_rows(u, h);
        st_(4);
#pragma unroll
        for (int r = 0; r < 10; ++r) { *(u16*)(st + 2560 + r * 128) = (u16)h[r]; *(u16*)(st + 3840 + r * 128) = (u16)(h[r] >> 16); }
        asm volatile("" ::: "memory");
        u32x4 o[6];
#pragma unroll
        for (int c = 0; c < 6; ++c) o[c] = *(const u32x4*)(mine + (c < 3 ? 2560 : 0) + min(c % 3 * 64 + lane, 159) * 16);
#pragma unroll
        for (int c = 0; c < 3; ++c) {
            const int i = min(c * 64 + lane, 159), rr = i >= 80, ch = i - 80 * rr, row = (row0 + 2 * w + rr) & 31;
            st128(nosent4(o[c]), rws, (unsigned)((ch >> 3) * 4096 + row * 128 + (((ch & 7) ^ (row & 7)) << 4)) | (c == 2 ? (lane & 32u) << 25 : 0u), (unsigned)OFF_H + (unsigned)((row0 >> 5) + 4 - 4 * lay) * 40960u, 16);
        }
#pragma unroll
        for (int c = 0; c < 3; ++c) st128(o[3 + c], rws, (unsigned)(c * 64 + lane) * 16u | (c == 2 ? (lane & 32u) << 25 : 0u), (unsigned)OFF_X1 + (unsigned)(row0 + 2 * w) * (D_ * 2), 16);
    }
    if (NW == 4 || row0 + step >= S_) break;
    }
    st_(5);
    asm volatile("s_waitcnt vmcnt(0)\n\ts_barrier" ::: "memory");
    done(row0);
}

__device__ __forceinline__ unsigned xcc_id() { unsigned x; asm volatile("s_getreg_b32 %0, hwreg(HW_REG_XCC_ID)" : "=s"(x)); return x & 7u; }
__device__ __forceinline__ unsigned probe(__amdgpu_buffer_rsrc_t rw, unsigned base) {
    unsigned mn[4] = {~0u, ~0u, ~0u, ~0u};
#pragma unroll 1
    for (int r = 0; r < 2; ++r) {
#pragma unroll
        for (int i = 0; i < 4; ++i) {
            const unsigned long long t1 = __builtin_amdgcn_s_memtime();
            asm volatile("s_waitcnt lgkmcnt(0)" ::: "memory");
            unsigned v = __builtin_amdgcn_raw_buffer_load_b32(rw, i * HALF, base + r * 4096u, 16);
            asm volatile("s_waitcnt vmcnt(0)" : "+v"(v) :: "memory");
            const unsigned long long t2 = __builtin_amdgcn_s_memtime();
            mn[i] = min(mn[i], (unsigned)(t2 - t1) + v);
        }
    }
    return mn[1] + mn[3] < mn[0] + mn[2] ? (mn[3] < mn[1] ? 3u : 1u) : (mn[2] < mn[0] ? 2u : 0u);
}
__device__ __forceinline__ void filter_unit(const u16* wr, const u16* wi, int s0, __amdgpu_buffer_rsrc_t rws, u16* tf, int tid) {
    u32x4 v[6];
#pragma unroll
    for (int i = 0; i < 6; ++i) v[i] = ((const u32x4*)((i < 3 ? wr : wi) + (size_t)s0 * DF))[min(tid + 512 * (i % 3), 1283)];
    __builtin_amdgcn_sched_barrier(0);
#pragma unroll
    for (int i = 0; i < 6; ++i) ((u32x4*)tf)[min(tid + 512 * (i % 3), 1283) + i / 3 * 1284] = v[i];
    __syncthreads();
    const int k = tid >> 3, sq = (tid & 7) * 4;
    u16 t[6][8];
#pragma unroll
    for (int i = 0; i < 6; ++i)
#pragma unroll
        for (int j = 0; j < 8; ++j) t[i][j] = tf[((j & 1) * 32 + sq + (j >> 1)) * DF + k + 64 * i];
    __builtin_amdgcn_sched_barrier(0);
#pragma unroll
    for (int i = 0; i < 6; ++i) {
        u32x4 o;
#pragma unroll
        for (int j = 0; j < 4; ++j) o[j] = (unsigned)t[i][2 * j] | (unsigned)t[i][2 * j + 1] << 16;
        if (i < 5 || tid < 8) st128(o, rws, (unsigned)(k * S_ + sq) * 4u, (unsigned)OFF_WFT + (unsigned)(s0 * 4 + i * 64 * S_ * 4), 16);
    }
}

__device__ __forceinline__ void dma1k(__amdgpu_buffer_rsrc_t r, unsigned vo, unsigned so, unsigned lds) {
    asm volatile("s_mov_b32 m0, %3\n\ts_nop 0\n\tbuffer_load_dwordx4 %0, %1, %2 offen sc1 lds" :: "v"(vo), "s"(r), "s"(so), "s"(lds) : "m0", "memory");
}
template <class FW, class FS> __device__ __forceinline__ void a2_unit(__amdgpu_buffer_rsrc_t rws, int G, int w, int lane, bool late, FW rdy, FS st_) {
    const int fr = lane & 15, g = lane >> 4;
    const unsigned fo = O_F + (unsigned)(G * GF), yo = O_Y0 + (unsigned)(G * YS);
    const unsigned sw = (unsigned)(lane ^ (4 * (w & 1) + g)) * 16u;
    if (w >= 4) {
        const unsigned v = (w - 4) * 1024u, t = late ? fo + 28672u + v : yo + 8192u + v;
        rdy(6);
#pragma unroll
        for (int i = 0; i < 7; ++i) dma1k(rws, sw, fo + v + i * 4096u, v + i * 4096u);
        rdy(7);
        for (unsigned spins = 0; spins < SPIN_CAP; ++spins) {
            dma1k(rws, sw, t, v + 28672u);
            asm volatile("s_waitcnt vmcnt(0)" ::: "memory");
            if (late || !__any(smax(*(const LAS u32x4*)(size_t)(v + 28672u + lane * 16u)) == 0xFFFFFFFFu)) break;
        }
        asm volatile("s_barrier" ::: "memory");
    } else {
        const unsigned wl = 32768u + w * 8192u;
        c32 wf[4][8], t2[4][7];
        f32x4 tr, ti, tn;
        u32x4 mb[2], ma[2];
        float back;
        auto ldw = [&]() {
#pragma unroll
            for (int i = 0; i < 8; ++i) dma1k(rws, lane * 16u, (unsigned)OFF_WFT + (unsigned)(min(4 * G + w, 320) * 8192 + i * 1024), wl + i * 1024u);
        };
        auto cvw = [&]() {
            unsigned wv[4][8], m = 0;
            asm volatile("s_waitcnt vmcnt(0)" ::: "memory");
#pragma unroll
            for (int i = 0; i < 32; ++i) wv[i >> 3][i & 7] = *(const LAS unsigned*)(size_t)(wl + (fr + 32 * g) * 4u + ((i >> 3 & 1) * 16 + (i >> 4) * 128 + (i & 7) * 256) * 4u);
            SB();
#pragma unroll
            for (int i = 0; i < 32; ++i) m = pkmax(m, wv[i >> 3][i & 7] & 0x7F807F80u);
            float x = __uint_as_float(max(m << 16, m & 0xFFFF0000u));
            x = fmaxf(x, dppf<0xB1>(x)); x = fmaxf(x, dppf<0x4E>(x)); x = fmaxf(x, dppf<0x141>(x)); x = fmaxf(x, dppf<0x140>(x));
            const unsigned e = min(max(__float_as_uint(xmax3216(x)), 8u << 23), 246u << 23);
            back = __uint_as_float(e) * (1.f / 40.f);
#pragma unroll
            for (int i = 0; i < 32; ++i) { wf[i >> 3][i & 7] = unpack2(wv[i >> 3][i & 7]) * dup(__uint_as_float((251u << 23) - e)); asm volatile("" : "+v"(wf[i >> 3][i & 7])); }
        };
        rdy(8);
        ldw();
#pragma unroll
        for (int i = 0; i < 4; ++i) {
            const int s = 2 * g + (i & 1) + (i >> 1) * 8;
            cpow(t2[i], (float)(fr + 16 * s) * (-1.f / 2048.f));
            const c32 t = cis((float)((4 * g + i) * fr) * (-1.f / 256.f)), p = cis((float)(s * fr) * (1.f / 16.f)) * dup(0.25f);
            tr[i] = t.x; ti[i] = t.y;
            mb[0][i] = packh(p.x, p.y); mb[1][i] = packh(-p.y, p.x);
#pragma unroll
            for (int h = 0; h < 2; ++h) { const c32 q = cis((float)((4 * g + i) * (8 * h + (fr >> 1)) + 4 * (fr & 1)) * (1.f / 16.f)) * dup(0.25f); ma[h][i] = packh(q.x, q.y); }
        }
        tn = -ti;
        asm volatile("" : "+v"(tn), "+v"(tr), "+v"(ti), "+v"(mb[0]), "+v"(mb[1]), "+v"(ma[0]), "+v"(ma[1]));
        cvw();
        LAS u32x4* im = (LAS u32x4*)(size_t)((fr + 32 * g) * 128u + (unsigned)(w ^ (fr >> 1)) * 16u);
        LAS u32x4* im1 = (LAS u32x4*)((size_t)im ^ 64u);
        asm volatile("s_barrier" ::: "memory");
        st_(2);
        u32x4 x[4][2];
#pragma unroll
        for (int h = 0; h < 2; ++h)
#pragma unroll
            for (int i = 0; i < 4; ++i) x[i][h] = (h ? im1 : im)[(i & 1) * 128 + (i >> 1) * 1024];
        SB();
        f32x4 c[8][2];
        auto cvt = [](float a, float b) { unsigned r; asm("v_cvt_pk_f16_f32 %0, %1, %2" : "=v"(r) : "v"(a), "v"(b)); return r; };
        auto two = [&](auto in) {
            u32x4 d = in(0), n;
            SA();
#pragma unroll
            for (int e = 0; e < 8; ++e) {
                asm volatile("s_nop 1" : "+v"(d));
                c[e][0] = mfmah(d, mb[0]);
                if (e < 7) n = in(e + 1);
                c[e][1] = mfmah(d, mb[1]);
                d = n;
                SB();
            }
            SA();
#pragma unroll
            for (int e = 0; e < 8; e += 2) {
                const f32x4 p0 = c[e][0] * tr, p1 = c[e][0] * ti, p2 = c[e + 1][0] * tr, p3 = c[e + 1][0] * ti;
                SB();
                c[e][0] = __builtin_elementwise_fma(c[e][1], tn, p0); c[e][1] = __builtin_elementwise_fma(c[e][1], tr, p1);
                c[e + 1][0] = __builtin_elementwise_fma(c[e + 1][1], tn, p2); c[e + 1][1] = __builtin_elementwise_fma(c[e + 1][1], tr, p3);
                SB();
            }
            auto mid = [&](int e) { return (u32x4){packh(c[e][0][0], c[e][1][0]), packh(c[e][0][1], c[e][1][1]), packh(c[e][0][2], c[e][1][2]), packh(c[e][0][3], c[e][1][3])}; };
            d = mid(0);
#pragma unroll
            for (int e = 0; e < 8; ++e) {
                SB();
                c[e][0] = mfmah(ma[0], d);
                if (e < 7) n = mid(e + 1);
                c[e][1] = mfmah(ma[1], d);
                d = n;
            }
            SB();
        };
        two([&](int e) { return (u32x4){x[0][e >> 2][e & 3], x[1][e >> 2][e & 3], x[2][e >> 2][e & 3], x[3][e >> 2][e & 3]}; });
        asm volatile("s_nop 7" : "+v"(c[0][0]), "+v"(c[0][1]), "+v"(c[1][0]), "+v"(c[1][1]), "+v"(c[2][0]), "+v"(c[2][1]), "+v"(c[3][0]), "+v"(c[3][1]), "+v"(c[4][0]), "+v"(c[4][1]), "+v"(c[5][0]), "+v"(c[5][1]), "+v"(c[6][0]), "+v"(c[6][1]), "+v"(c[7][0]), "+v"(c[7][1]));
        st_(3);
        c32 u[2][16];
#pragma unroll
        for (int h = 0; h < 2; ++h) {
#pragma unroll
            for (int e = 0; e < 8; ++e) { u[h][2 * e] = c[e][h].xy; u[h][2 * e + 1] = c[e][h].zw; }
            ctwn<-1, 2>(u[h], [&](int i) { return t2[2 * h + (i & 1)][(i >> 1) - 1]; });
            dft8x2<-1>(u[h]);
            ctwn<-1, 0>(u[h], [&](int i) { return wf[2 * h + (i & 1)][i >> 1]; });
            dft8x2<1>(u[h]);
            ctwn<1, 2>(u[h], [&](int i) { return t2[2 * h + (i & 1)][(i >> 1) - 1]; });
            if (h == 0) st_(4);
        }
        two([&](int e) { return (u32x4){cvt(u[0][2 * e].y, u[0][2 * e].x), cvt(u[0][2 * e + 1].y, u[0][2 * e + 1].x), cvt(u[1][2 * e].y, u[1][2 * e].x), cvt(u[1][2 * e + 1].y, u[1][2 * e + 1].x)}; });
#pragma unroll
        for (int h = 0; h < 2; ++h) {
            SA();
#pragma unroll
            for (int j = 0; j < 8; ++j) c[4 * h + (j >> 1)][j & 1] *= back;
            SB();
#pragma unroll
            for (int i = 0; i < 4; ++i) {
                u32x4 o;
#pragma unroll
                for (int j = 0; j < 4; ++j) { const f32x4 y = c[4 * h + j][i >> 1]; asm("v_cvt_pk_bf16_f32 %0, %1, %2" : "=v"(o[j]) : "v"(y[2 * (i & 1) + 1]), "v"(y[2 * (i & 1)])); }
                (h ? im1 : im)[(i & 1) * 128 + (i >> 1) * 1024] = o;
            }
        }
        st_(5);
    }
    asm volatile("s_waitcnt lgkmcnt(0)\n\ts_barrier" ::: "memory");
    st_(6);
    u32x4 o[4];
#pragma unroll
    for (int j = 0; j < 4; ++j) o[j] = *(const LAS u32x4*)(size_t)(w * 1024u + j * 8192u + sw);
    SB();
#pragma unroll
    for (int j = 0; j < 4; ++j) {
        st128(j ? o[j] : nosent4(o[j]), rws, lane * 16u, (j || w >= 4 ? fo + j * 8192u : late ? yo : yo + 4096u) + w * 1024u, 16);
        if (j == 0) st_(7);
    }
}

struct RecArgs {
    unsigned* ctl; char* win; const u16* mem0; const u16* xin; u16* out;
    const u16* W[6];
};
template <int I> __device__ __forceinline__ char* karg(int i = 0) {
    unsigned long long p;
    asm volatile("s_load_dwordx2 %0, %1, %2\n\ts_waitcnt lgkmcnt(0)" : "=s"(p) : "s"(__builtin_amdgcn_kernarg_segment_ptr()), "s"(I * 8 + i * 8) : "memory");
    return (char*)p;
}
constexpr unsigned O_H = (unsigned)OFF_H, O_X1 = (unsigned)OFF_X1, O_Z = (unsigned)OFF_Z, O_Z2 = (unsigned)OFF_Z2, O_PT = (unsigned)OFF_PT, O_QK = (unsigned)OFF_QK;
#define MXV 0xAAAAAull
#define MXQ 0x88888000000ull
static_assert((MXV & 15) == 10 && !(MXQ & 15) && !((MXV | MXQ) >> 63), "segment sets");
constexpr int NREC = 96 + NOWN, NQT = NREC + NOWN, NXT = NQT + 32, NWRK = NXT + 32;
__device__ __forceinline__ bool spin_fail(unsigned& spins, __amdgpu_buffer_rsrc_t rws) {
    if (++spins > SPIN_CAP) { __builtin_amdgcn_raw_buffer_store_b32(1u, rws, 0u, 0u, 16); return true; }
    return (spins & 1023u) == 0u && __builtin_amdgcn_readfirstlane(__builtin_amdgcn_raw_buffer_load_b32(rws, 0u, 0u, 16));
}
#define ALN 6
#define AL ".p2align " STR(ALN) "\n\t"
#define STR2(x) #x
#define STR(x) STR2(x)
#define TPRE 0
#define QP 2
#define BQN 7
#define BQ(n) ".if " #n "==" STR(BQN) "\n\ts_barrier\n\t.endif\n\t"
#define SST(k) do {} while (0)
#define LSEL 1
#define RT(k) do {} while (0)
#define RTS() do {} while (0)
#define B3ATTR
#define MTA(k)
#define MTRT() do {} while (0)
#define MT(k) do {} while (0)
#define MTS() do {} while (0)
#define FC(x) do {} while (0)
#define DQC 1130
#define DVC 1200
#define M1 44
#define MZ 40
#define MZP 20
#define M2 224
#define LTP (LT0 + 15)
#define LTZ 340
#define PEN 280
#define KSL 1
#define SVO 64
#define DZP 46
#define LT0 330
#define SAD 1
#define PEN2 80
#define PEN1 50
#define PEN0 60
#define DDM2 0
#define DRA 700
#define TSLP 1
#define PFS 0
#define PFOFF 1
#define OPR 2
#define BSPARSE 0
#define ZLAG 7
#define TKP 0
#define DPS 12
#define RZ 63
#define RQ 63
#define R2 63
#define DXT 3000
#define SSW 99
#define LTM 18
#define XS0 10
#define XSU 8
#define XSM 32
#define LTU 4
#define LTD 1
#define QUIET 0
#define QUAD 1
#define LTS 3
#define XSS 3
#define TKS 1
#define LT1 358
#define FDL 2
#define ZLA 3
#define DPT2 3
#define NORND 0
#define LTX 28
#define TKE 200
#define DVO 0
#define ZPS 4
#define ASLP 4
#define BADG 0
#define DW1 0
#define WLA 3
#define XSLP 30
#define DOUT 200
#define DA1 145
#define DA2 215
#define DA20 270
#define DFB 150
#define DA3 185
#define DA30 230
#define YSLP 0
#define QEU 14
#define QE2 30
#define PEN00 120
#define DQ0 1080
#define DQ00 1150
#define DZ0 420
#define DZ00 430
#define DOU2 200
#define DFA 0
#define SRG 0x5A000000u
#define OBN 5
#define OB(n) ".if " #n "==" STR(OBN) "\n\ts_barrier\n\tds_read_b64 v[102:103], %[sp]\n\tds_read_b64 v[104:105], %[sp] offset:512\n\tds_read_b64 v[106:107], %[sp] offset:1024\n\t.endif\n\t"
#define VSLP 0
#define CPF 1
#define DST 4
#define XST 0
#define ODL 8
#define VTS 1
#define XF 5
#define XFS 32
#define DOU1 50
#define DR0 0
#define DFB0 120
#define TPF 21
#define DZF 160
#define DZF0 175
#define DV0 260
#define QTP 0
#define XDL 10
#define DOW 200
#define DOU3 300
#define NTM 31
#define HLA 8
#define PSLP 16
#define PFD 4
#define NPF 3
#define KSL2 1
#define KBQ 4
#define VB1 6
#define VB2 5
#define KR1 3
#define QB2 2
#define NB2 6
#define DZR 24
#define ML 470
#define MN 12
#define CF 190
#define DB 1500
#define DP 200
#define ZSL 1
#define NB1 3
#define DPT 3
#define DDMA 8
#define DZ2 800
#define QEARLY 0
#define OSLP 12
#define OSLP2 4
#define WSLP 32
#define DO0 12000
#define DW0 1200
#define DQT 9000
#define DQ4 700
#define ZEARLY 3
#define HSLP 4
#define RMAP 0
#define DIE 0
#define VMWAIT() do { asm volatile("" ::: "memory"); __builtin_amdgcn_s_waitcnt(0x0F70); asm volatile("" ::: "memory"); } while (0)
#define BAR() asm volatile("s_waitcnt lgkmcnt(0)\n\ts_barrier" ::: "memory")
#define PIN1(a) asm volatile("" : "+v"(a) :: "memory")
#define PIN2(a, b) asm volatile("" : "+v"(a), "+v"(b) :: "memory")
#define BAR0() asm volatile("s_barrier" ::: "memory")
__device__ __forceinline__ unsigned fresh_lane() { unsigned l; asm volatile("v_mbcnt_lo_u32_b32 %0, -1, 0\n\tv_mbcnt_hi_u32_b32 %0, -1, %0" : "=v"(l)); __builtin_assume(l < 64u); return l; }
__device__ __forceinline__ u32x4 sent4() { u32x2 a, b; asm volatile("v_mov_b64 %0, -1\n\tv_mov_b64 %1, -1" : "=v"(a), "=v"(b)); return (u32x4){a[0], a[1], b[0], b[1]}; }
__device__ __forceinline__ void wait_until(unsigned long long t) { while ((long long)(__builtin_amdgcn_s_memtime() - t) < 0) __builtin_amdgcn_s_sleep(1); }
struct Clk { unsigned ta, ma, cpq, qpc; };
__device__ __forceinline__ int sx24(unsigned v) { return (int)(v << 8) >> 8; }
#define WAIT_FINE AL \
        "s_memrealtime vcc\n\t" \
        "s_memtime s[100:101]\n\t" \
        "s_brev_b32 %[fi], -2\n\t" \
        "s_waitcnt lgkmcnt(0)\n\t" \
        "s_mov_b32 %[rp], vcc_lo\n\t" \
        "s_lshl_b32 vcc_hi, vcc_lo, 2\n\t" \
        "s_sub_u32 %[x], %[t], vcc_hi\n\t" \
        "s_bfe_i32 %[x], %[x], 0x180000\n\t" \
        "s_min_i32 %[x], %[x], 4000\n\t" \
        "s_max_i32 %[x], %[x], -4000\n\t" \
        "s_add_u32 %[t], vcc_hi, %[x]\n\t" \
        "s_mul_i32 %[x], %[x], %[cq]\n\t" \
        "s_ashr_i32 %[x], %[x], 8\n\t" \
        "s_sub_u32 %[tm], %[t], %[ta]\n\t" \
        "s_bfe_i32 %[tm], %[tm], 0x180000\n\t" \
        "s_min_i32 %[tm], %[tm], 1000000\n\t" \
        "s_max_i32 %[tm], %[tm], -1000000\n\t" \
        "s_mul_i32 %[tm], %[tm], %[cq]\n\t" \
        "s_ashr_i32 %[tm], %[tm], 8\n\t" \
        "s_add_u32 %[tm], %[tm], %[ma]\n\t" \
        "s_sub_u32 %[tm], %[tm], s100\n\t" \
        "s_lshr_b32 vcc_hi, %[cq], 4\n\t" \
        "s_sub_u32 vcc_hi, %[x], vcc_hi\n\t" \
        "s_max_i32 %[tm], %[tm], vcc_hi\n\t" \
        "s_min_i32 %[tm], %[tm], %[x]\n\t" \
        "s_add_u32 %[tm], %[tm], s100\n" \
        "1:\n\t" \
        "s_memrealtime vcc\n\t" \
        "s_memtime s[100:101]\n\t" \
        "s_waitcnt lgkmcnt(0)\n\t" \
        "s_cmp_eq_u32 vcc_lo, %[rp]\n\t" \
        "s_cbranch_scc1 2f\n\t" \
        "s_mov_b32 %[rp], vcc_lo\n\t" \
        "s_lshl_b32 %[x], vcc_lo, 2\n\t" \
        "s_sub_u32 %[x], %[t], %[x]\n\t" \
        "s_bfe_i32 %[x], %[x], 0x180000\n\t" \
        "s_mul_i32 %[x], %[x], %[cq]\n\t" \
        "s_ashr_i32 %[x], %[x], 8\n\t" \
        "s_add_u32 %[x], %[x], s100\n\t" \
        "s_sub_u32 s101, %[x], %[tm]\n\t" \
        "s_cmp_lt_i32 s101, %[fi]\n\t" \
        "s_cselect_b32 %[tm], %[x], %[tm]\n\t" \
        "s_mov_b32 %[fi], 0\n" \
        "2:\n\t" \
        "s_sub_u32 %[x], %[tm], s100\n\t" \
        "s_cmp_gt_i32 %[x], 260\n\t" \
        "s_cbranch_scc1 1b\n\t" \
        "s_cmp_eq_u32 %[fi], 0\n\t" \
        "s_cselect_b32 %[ta], %[t], %[ta]\n\t" \
        "s_cselect_b32 %[ma], %[tm], %[ma]\n" \
        "3:\n\t" \
        "s_memtime s[100:101]\n\t" \
        "s_waitcnt lgkmcnt(0)\n\t" \
        "s_sub_u32 %[x], %[tm], s100\n\t" \
        "s_cmp_gt_i32 %[x], 92\n\t" \
        "s_cbranch_scc1 3b\n\t" \
        "s_max_i32 %[x], %[x], 0\n\t" \
        "s_andn2_b32 %[x], %[x], 3\n\t" \
        "s_sub_u32 %[x], 104, %[x]\n\t" \
        "s_getpc_b64 vcc\n\t" \
        "s_add_u32 vcc_lo, vcc_lo, %[x]\n\t" \
        "s_addc_u32 vcc_hi, vcc_hi, 0\n\t" \
        "s_setpc_b64 vcc\n\t" \
        ".rept 23\n\t" \
        "s_nop 0\n\t" \
        ".endr\n\t"
#define WAIT_FINE_OUT [tm] "=&s"(tm), [rp] "=&s"(rp), [x] "=&s"(x), [fi] "=&s"(fi), [t] "+s"(t), [ta] "+s"(c.ta), [ma] "+s"(c.ma)
__device__ __forceinline__ void wait_fine_bar(unsigned t, Clk& c) {
    unsigned tm, rp, x, fi;
    asm volatile(WAIT_FINE "s_barrier" : WAIT_FINE_OUT : [cq] "s"(c.cpq) : "vcc", "scc", "s100", "s101", "memory");
}
__device__ __forceinline__ void wait_cyc_bar(unsigned tm) {
    unsigned x;
    asm volatile(AL
        "3:\n\t"
        "s_memtime s[100:101]\n\t"
        "s_waitcnt lgkmcnt(0)\n\t"
        "s_sub_u32 %0, %1, s100\n\t"
        "s_cmp_gt_i32 %0, 92\n\t"
        "s_cbranch_scc1 3b\n\t"
        "s_max_i32 %0, %0, 0\n\t"
        "s_andn2_b32 %0, %0, 3\n\t"
        "s_sub_u32 %0, 104, %0\n\t"
        "s_getpc_b64 vcc\n\t"
        "s_add_u32 vcc_lo, vcc_lo, %0\n\t"
        "s_addc_u32 vcc_hi, vcc_hi, 0\n\t"
        "s_setpc_b64 vcc\n\t"
        ".rept 23\n\t"
        "s_nop 0\n\t"
        ".endr\n\t"
        "s_barrier"
        : "=&s"(x) : "s"(tm) : "vcc", "scc", "s100", "s101", "memory");
}
__device__ __forceinline__ unsigned cyc_at(const Clk& c, int d) {
    d = min(d, 20000);
    asm volatile("" : "+s"(d));
    return c.ma + (unsigned)((max(d, -4000) * (int)c.cpq) >> 8);
}
__device__ __forceinline__ void clk_rate(Clk& c, unsigned ta0, unsigned ma0) {
    const int dq = sx24(c.ta - ta0);
    const float r = fminf(fmaxf((float)(c.ma - ma0) * __builtin_amdgcn_rcpf((float)dq), 1.f), 8.f);
    unsigned x, y;
    asm volatile("s_nop 1\n\tv_readfirstlane_b32 %0, %2\n\tv_readfirstlane_b32 %1, %3\n\ts_nop 3" : "=s"(x), "=s"(y)
                 : "v"((unsigned)(256.f * r + 0.5f)), "v"((unsigned)(65536.f * __builtin_amdgcn_rcpf(r))));
    c.cpq = dq > 200 ? x : c.cpq; c.qpc = dq > 200 ? y : c.qpc;
}
__device__ __forceinline__ Clk clk_init() { return Clk{4u * (unsigned)__builtin_amdgcn_s_memrealtime(), (unsigned)__builtin_amdgcn_s_memtime(), 1470u, 11413u}; }
__device__ __forceinline__ int read_sched(__amdgpu_buffer_rsrc_t rws, __amdgpu_buffer_rsrc_t rwin, int s, unsigned tg, unsigned vo, unsigned to, unsigned f, unsigned g0, int& lt, int& xs, unsigned& fc) {
    if (__builtin_expect(__any((f >> 24) != tg), 0)) {
        for (unsigned spins = 0; __any((f >> 24) != tg);) {
            FC(fc);
            if (spin_fail(spins, rws)) break;
            __builtin_amdgcn_s_sleep(TSLP);
            asm volatile("" ::: "memory");
            if ((f >> 24) != tg) f = __builtin_amdgcn_raw_buffer_load_b32(rwin, vo, to, 16);
        }
    }
    int d = (int)((f - g0) & 0xFFFFFFu);
    d = max(d, __builtin_amdgcn_update_dpp(0, d, 0xB1, 0xF, 0xF, true));
    d = max(d, __builtin_amdgcn_update_dpp(0, d, 0x4E, 0xF, 0xF, true));
    d = max(d, __builtin_amdgcn_update_dpp(0, d, 0x141, 0xF, 0xF, true));
    d = max(d, __builtin_amdgcn_update_dpp(0, d, 0x140, 0xF, 0xF, true));
    d = max(d, __builtin_amdgcn_update_dpp(0, d, 0x142, 0xA, 0xF, true));
    d = max(__builtin_amdgcn_readlane(d, 16), __builtin_amdgcn_readlane(d, 48));
    const int l0 = lt, x0 = xs;
    d -= 0x400000;
    auto sel = [](int c, int x, int y) { int r; asm("s_cmp_lt_i32 %1, 0\n\ts_cselect_b32 %0, %2, %3" : "=s"(r) : "s"(c), "s"(x), "s"(y) : "scc"); return r; };
    const int miss = l0 + min(x0 + LTM, LTX) - d, first = s - 1;
    lt = sel((s ^ 1) - 1, LT0, sel(miss | first, l0, min(max(d, l0 - 1 - ((l0 - d) >> LTS)), l0 + LTU)));
    xs = sel(first, x0, sel(miss, min(x0 + XSU, XSM), max(x0 - 1 - (x0 >> XSS), sel(s - XFS, XF, 0))));
    return 0x400000 + sel(miss | first, d + sel(first, sel((int)tg - 1, PEN00, PEN0), PEN), l0 + x0) + M1 + M2 + x0;
}
constexpr int QS = 272;
constexpr int TQS = 1152;
constexpr int L_Q = 10240, L_K = L_Q + 2 * TQS, L_S = L_K + 2 * TQS, L_TOK = L_S + 256, L_H = L_TOK + 49152, L_END = L_H + 2 * 40960;
typedef __attribute__((ext_vector_type(4))) short v4i16_t;
__device__ __forceinline__ u32x2 ldtr(const char* p) { return __builtin_bit_cast(u32x2, __builtin_amdgcn_ds_read_tr16_b64_v4i16((LAS v4i16_t*)p)); }
__device__ __forceinline__ u32x2 ldtr1(const LAS char* p) { return __builtin_bit_cast(u32x2, __builtin_amdgcn_ds_read_tr16_b64_v4i16((LAS v4i16_t*)p)); }
__device__ __forceinline__ u32x4 ldtr2(const LAS char* p0, const LAS char* p1) {
    const u32x2 x = __builtin_bit_cast(u32x2, __builtin_amdgcn_ds_read_tr16_b64_v4i16((LAS v4i16_t*)p0)), y = __builtin_bit_cast(u32x2, __builtin_amdgcn_ds_read_tr16_b64_v4i16((LAS v4i16_t*)p1));
    return (u32x4){x[0], x[1], y[0], y[1]};
}
__device__ __forceinline__ u32x4 ldtr2(const char* p0, const char* p1) { const u32x2 x = ldtr(p0), y = ldtr(p1); return (u32x4){x[0], x[1], y[0], y[1]}; }

__device__ __forceinline__ unsigned poll_lim(int& budget) {
    --budget;
    unsigned lim = budget > 0 ? 0xFFFFFFFEu : 0xFFFFFFFFu;
    asm volatile("" : "+s"(lim));
    return lim;
}
__device__ __forceinline__ bool mem_missing(unsigned lds, unsigned lim) {
    const LAS char* p = (const LAS char*)(size_t)(lds + fresh_lane() * 16u);
    unsigned m = 0;
#pragma unroll 1
    for (int h = 0; h < 2; ++h) {
        u32x4 v[5];
#pragma unroll
        for (int i = 0; i < 5; ++i) v[i] = *(const LAS u32x4*)(p + h * 5120 + i * 1024);
        SB();
#pragma unroll
        for (int i = 0; i < 5; ++i) m = max(max(m, max(v[i][0], v[i][1])), max(v[i][2], v[i][3]));
    }
    return __any(m > lim);
}
#define GATHER \
        "buffer_load_dwordx4 %[vo], %[rw], %[s0] offen sc1 lds\n\t" \
        "buffer_load_dwordx4 %[vo], %[rw], %[s0] offen offset:1024 sc1 lds\n\t" \
        "buffer_load_dwordx4 %[vo], %[rw], %[s0] offen offset:2048 sc1 lds\n\t" \
        "buffer_load_dwordx4 %[vo], %[rw], %[s0] offen offset:3072 sc1 lds\n\t" \
        "s_movk_i32 m0, 0x1000\n\t" \
        "s_nop 0\n\t" \
        "buffer_load_dwordx4 %[vo], %[rw], %[s4] offen sc1 lds\n\t" \
        "buffer_load_dwordx4 %[vo], %[rw], %[s4] offen offset:1024 sc1 lds\n\t" \
        "buffer_load_dwordx4 %[vo], %[rw], %[s4] offen offset:2048 sc1 lds\n\t" \
        "buffer_load_dwordx4 %[vo], %[rw], %[s4] offen offset:3072 sc1 lds\n\t" \
        "s_movk_i32 m0, 0x2000\n\t" \
        "s_nop 0\n\t" \
        "buffer_load_dwordx4 %[vo], %[rw], %[s1] offen sc1 lds\n\t" \
        "buffer_load_dwordx4 %[vo], %[rw], %[s1] offen offset:1024 sc1 lds"
#define GATHER_IN [vo] "v"(vo), [rw] "s"(rwin), [s0] "s"(so0), [s4] "s"(so0 + 4096u), [s1] "s"(so1)
__device__ __forceinline__ void gather_at(unsigned t, Clk& c, __amdgpu_buffer_rsrc_t rwin, unsigned vo, unsigned so0, unsigned so1) {
    unsigned tm, rp, x, fi;
    asm volatile("s_mov_b32 m0, 0\n\t" WAIT_FINE GATHER : WAIT_FINE_OUT : [cq] "s"(c.cpq), GATHER_IN : "vcc", "scc", "s100", "s101", "m0", "memory");
}
__device__ __forceinline__ void dma4k(__amdgpu_buffer_rsrc_t r, unsigned vo, unsigned so, unsigned lds) {
    asm volatile(
        "s_mov_b32 m0, %3\n\t"
        "s_nop 0\n\t"
        "buffer_load_dwordx4 %0, %1, %2 offen sc1 lds\n\t"
        "buffer_load_dwordx4 %0, %1, %2 offen offset:1024 sc1 lds\n\t"
        "buffer_load_dwordx4 %0, %1, %2 offen offset:2048 sc1 lds\n\t"
        "buffer_load_dwordx4 %0, %1, %2 offen offset:3072 sc1 lds"
        :: "v"(vo), "s"(r), "s"(so), "s"(lds) : "m0", "memory");
}
__device__ __forceinline__ int bs2(int c) { return (c & ~3) | ((c & 1) << 1) | ((c >> 1) & 1); }
__device__ __forceinline__ void get_mem0(u32x4 (&m)[2], int t) {
    const u16* mem0 = (const u16*)karg<2>();
#pragma unroll
    for (int i = 0; i < 2; ++i) {
        const int c = min(t + 512 * i, 639);
        const unsigned* src = (const unsigned*)(mem0 + ((c & 6) + ((c >> 3) & 1)) * D_ + (c >> 4) * 16 + (c & 1) * 2);
        m[i] = (u32x4){src[0], src[2], src[4], src[6]};
    }
    SB();
}
__device__ __forceinline__ void put_mem0(const u32x4 (&m)[2], __amdgpu_buffer_rsrc_t rwin, unsigned so0, unsigned so1, int t) { st128(m[0], rwin, t * 16u, so0, 16); st128(m[1], rwin, min(t, 127) * 16u, so1, 16); }
__device__ __forceinline__ f32x2 hadd(f32x4 c) { return (f32x2){c[0], c[1]} + (f32x2){c[2], c[3]}; }
__device__ __forceinline__ unsigned hpack(f32x4 c) { const f32x2 y = hadd(c); return pack2(y[0], y[1]); }
__device__ __forceinline__ u32x8 cat8(u32x4 lo, u32x4 hi) { return (u32x8){lo[0], lo[1], lo[2], lo[3], hi[0], hi[1], hi[2], hi[3]}; }
__device__ __forceinline__ u32x4 half8(const u32x8& v, int u) { return u ? (u32x4){v[4], v[5], v[6], v[7]} : (u32x4){v[0], v[1], v[2], v[3]}; }
template <int N, int ST> __device__ __forceinline__ void stage_h(__amdgpu_buffer_rsrc_t rws, unsigned so, unsigned lds, int lane) {
#pragma unroll
    for (int i = 0; i < N; ++i) dma1k(rws, lane * 16u, so + i * ST, lds + i * ST);
}
template <int N, int ST, int M> __device__ __forceinline__ void check_h(__amdgpu_buffer_rsrc_t rws, unsigned so, unsigned lds, int lane) {
    asm volatile("s_waitcnt vmcnt(%0)" :: "n"(M) : "memory");
    for (unsigned spins = 0; spins < SPIN_CAP; ++spins) {
        unsigned m = 0;
#pragma unroll
        for (int i = 0; i < N; ++i) m = max(m, smax(*(const LAS u32x4*)(size_t)(lds + lane * 16u + i * ST)));
        if (!__any(m == 0xFFFFFFFFu)) break;
        stage_h<N, ST>(rws, so, lds, lane);
        asm volatile("s_waitcnt vmcnt(0)" ::: "memory");
    }
}
__device__ __forceinline__ void wait_h(__amdgpu_buffer_rsrc_t rws, __amdgpu_buffer_rsrc_t rfl, unsigned fb, int seg, int& hrdy) {
    unsigned lim = hrdy ? 63u : (unsigned)min(seg + HLA, 63);
    if (__builtin_expect(hrdy <= seg, 0)) for (unsigned spins = 0;;) {
        asm volatile("" ::: "memory");
        const u32x4 f = __builtin_amdgcn_raw_buffer_load_b128(rfl, min(fresh_lane(), lim) * 128u, W_H + fb, 16);
        const unsigned long long miss = __ballot((f[0] | f[1] | f[2] | f[3]) != 0u);
        hrdy = miss ? __builtin_ctzll(miss) : (int)lim + 1;
        if (hrdy > seg) break;
        if (spin_fail(spins, rws)) break;
        lim = (unsigned)min(seg + HLA, 63);
        __builtin_amdgcn_s_sleep(HSLP);
    }
}
__device__ __forceinline__ u32x4 ask_h(__amdgpu_buffer_rsrc_t rfl, unsigned fb, unsigned lim) { return __builtin_amdgcn_raw_buffer_load_b128(rfl, min(fresh_lane(), lim) * 128u, W_H + fb, 16); }
__device__ __forceinline__ int see_h(u32x4 f) { const unsigned long long miss = __ballot((f[0] | f[1] | f[2] | f[3]) != 0u); return miss ? __builtin_ctzll(miss) : 64; }
template <int CTRL> __device__ __forceinline__ unsigned dppu(unsigned v) { return (unsigned)__builtin_amdgcn_mov_dpp((int)v, CTRL, 0xF, 0xF, true); }
template <int CTRL> __device__ __forceinline__ u32x4 ror4(u32x4 v) { return (u32x4){dppu<CTRL>(v[0]), dppu<CTRL>(v[1]), dppu<CTRL>(v[2]), dppu<CTRL>(v[3])}; }
__device__ __forceinline__ float xmax16(float v) {
    auto q = __builtin_amdgcn_permlane16_swap(__float_as_uint(v), __float_as_uint(v), false, false);
    return fmx(__uint_as_float(q[0]), __uint_as_float(q[1]));
}
__device__ __forceinline__ float xsum16(float v) {
    auto q = __builtin_amdgcn_permlane16_swap(__float_as_uint(v), __float_as_uint(v), false, false);
    return __uint_as_float(q[0]) + __uint_as_float(q[1]);
}
__device__ __forceinline__ float addswap16(float a, float b) {
    auto r = __builtin_amdgcn_permlane16_swap(__float_as_uint(a), __float_as_uint(b), false, false);
    return __uint_as_float(r[0]) + __uint_as_float(r[1]);
}
#define LANES() \
    const int lane = (int)fresh_lane(), tid = w * 64 + lane, fr = lane & 15, fq = lane >> 4; \
    const int sidx = (lane & 2) ? 0xEEEE : 0x4444; \
    const int ma0 = fq * 256 + (fr & 1) * 128 + (fr >> 1) * 16; \
    const int prow = 16 * ((fr >> 2) & 1) + 4 * (fr >> 3) + (fr & 3), hoff = fr * 128; \
    (void)tid; (void)sidx; (void)ma0; (void)prow; (void)hoff

__device__ __forceinline__ int seg_after(unsigned long long m, int j) { m = j >= 63 ? 0ull : m >> (j + 1); return m ? j + 1 + __builtin_ctzll(m) : NSEG; }
__device__ __forceinline__ int seg_done(unsigned long long m, int f0, int f1) { return min(seg_after(~m, f0), seg_after(m, f1)) - 1; }
template <int OFF> __device__ __forceinline__ void ldfrag(u32x4& d, const LAS char* p) { asm volatile("ds_read_b128 %0, %1 offset:%2" : "=v"(d) : "v"(p), "n"(OFF) : "memory"); }
template <int U, class FM, class FV> __device__ __forceinline__ void project(const u32x8 (&wt)[2][10], f32x4 (&tc)[2][2], u32x4 (&bb)[8], const LAS char* c0, const LAS char* c1, const LAS char* n0, const LAS char* n1, FM mid, FV vm) {
    constexpr int t = U >> 2, k = U & 3, v = (U + 7) % 40;
    if (U == 33) mid();
    vm(IC<U>());
    const LAS char* p = U < 33 ? (v >> 1 & 1 ? c1 : c0) : (v >> 1 & 1 ? n1 : n0);
    if (U < 2) asm volatile("s_nop 0\n\ts_waitcnt lgkmcnt(6)\n\tv_mfma_f32_16x16x32_bf16 %0, %3, %7, 0\n\tds_read_b128 %2, %5 offset:%6\n\tv_mfma_f32_16x16x32_bf16 %1, %4, %7, 0"
                            : "=&v"(tc[0][k & 1]), "=&v"(tc[1][k & 1]), "+v"(bb[(U + 7) & 7]) : "v"(half8(wt[0][t], k >> 1)), "v"(half8(wt[1][t], k >> 1)), "v"(p), "n"((v >> 2) * 4096 + (v & 1) * 2048), "v"(bb[U & 7]) : "memory");
    else asm volatile("s_waitcnt lgkmcnt(6)\n\tv_mfma_f32_16x16x32_bf16 %0, %3, %7, %0\n\tds_read_b128 %2, %5 offset:%6\n\tv_mfma_f32_16x16x32_bf16 %1, %4, %7, %1"
                      : "+v"(tc[0][k & 1]), "+v"(tc[1][k & 1]), "+v"(bb[(U + 7) & 7]) : "v"(half8(wt[0][t], k >> 1)), "v"(half8(wt[1][t], k >> 1)), "v"(p), "n"((v >> 2) * 4096 + (v & 1) * 2048), "v"(bb[U & 7]) : "memory");
    if constexpr (U < 39) project<U + 1>(wt, tc, bb, c0, c1, n0, n1, mid, vm);
    else asm volatile("s_nop 7" : "+v"(tc[0][0]), "+v"(tc[0][1]), "+v"(tc[1][0]), "+v"(tc[1][1]));
}
__device__ __forceinline__ void project_end(u32x4 (&bb)[8]) {
    asm volatile("s_waitcnt lgkmcnt(0)" : "+v"(bb[0]), "+v"(bb[1]), "+v"(bb[2]), "+v"(bb[3]), "+v"(bb[4]), "+v"(bb[5]), "+v"(bb[6]), "+v"(bb[7]) :: "memory");
}
__device__ __forceinline__ void project0(u32x4 (&bb)[8], const LAS char* c0, const LAS char* c1) {
    ldfrag<0>(bb[0], c0); ldfrag<2048>(bb[1], c0); ldfrag<0>(bb[2], c1); ldfrag<2048>(bb[3], c1);
    ldfrag<4096>(bb[4], c0); ldfrag<6144>(bb[5], c0); ldfrag<4096>(bb[6], c1); ldfrag<6144>(bb[7], c1);
    project_end(bb);
}
constexpr int WLB = 20992;
template <bool O, class F> __device__ __forceinline__ void load_w(u32x8 (&wt)[2][10], const char* W, int hh, int w, int c0, int cd, char* st, bool once, F gap) {
    const int lane = (int)fresh_lane(), fr = lane & 15, fq = lane >> 4, x = w & 3;
    const __amdgpu_buffer_rsrc_t r = mkrsrc(W, D_ * HD * 2);
    u32x4 raw[40];
    const unsigned vo = O ? (unsigned)(lane * 16) : (unsigned)(fq * 8192 + fr * 16);
    unsigned so = O ? (unsigned)(hh * 128 + 8 * x) * 1280u : (unsigned)(8 * x * 8192 + hh * 256);
    asm volatile("" : "+s"(so));
    auto fetch1 = [&](int p, auto aux) {
#pragma unroll
        for (int i = 5 * p; i < 5 * p + 5; ++i) {
            if (O) raw[i] = __builtin_amdgcn_raw_buffer_load_b128(r, vo + (i % 5 & 3) * 1024u, so + (unsigned)((32 * (i / 10) + 4 * (i / 5 & 1)) * 1280 + (i % 5 >> 2) * 4096), decltype(aux)::v);
            else raw[i] = __builtin_amdgcn_raw_buffer_load_b128(r, vo, so + (unsigned)(32 * (i >> 1) + 4 * (i & 1)) * 8192u, decltype(aux)::v);
        }
    };
    auto fetch = [&](int p) { if (WAUX && once) fetch1(p, IC<WAUX>()); else fetch1(p, IC<0>()); };
#pragma unroll
    for (int p = 0; p < WLA; ++p) { fetch(p); BAR0(); gap(p); }
    st += (w >> 2) * WLB;
    const int row = 4 * fq + (fr >> 2);
    int wo[5];
#pragma unroll
    for (int e = 0; e < 5; ++e) {
        const int id = 64 * e + lane, rr = id / 80, ch = id - 80 * rr;
        wo[e] = O ? (4 * x + rr) * 1312 + (ch >> 1) * 32 + (ch & 1) * 4 : (4 * x + fq) * 256 + ((fr ^ (2 * ((4 * x + fq) & 7))) * 16) + e * 4096;
    }
    const int r0 = O ? row * 1312 + x * 320 + (fr & 3) * 8 : row * 256 + (((c0 + ((fr >> 1) & 1)) ^ (2 * (row & 7))) * 16) + (fr & 1) * 8;
    const int r1 = O ? 0 : row * 256 + (((c0 + cd + ((fr >> 1) & 1)) ^ (2 * (row & 7))) * 16) + (fr & 1) * 8;
#pragma unroll
    for (int p = 0; p < 8; ++p) {
        char* b = st + (p & 1) * 2 * WLB;
#pragma unroll
        for (int e = 0; e < 5; ++e) {
            const u32x4 v = raw[5 * p + e];
            if (O) {
                unsigned* d = (unsigned*)(b + wo[e]);
                d[0] = __builtin_amdgcn_perm(v[2], v[0], 0x05040100); d[2] = __builtin_amdgcn_perm(v[3], v[1], 0x05040100);
                d[4] = __builtin_amdgcn_perm(v[2], v[0], 0x07060302); d[6] = __builtin_amdgcn_perm(v[3], v[1], 0x07060302);
            } else *(u32x4*)(b + wo[e]) = v;
        }
        BAR();
        if (p + WLA < 8) fetch(p + WLA);
        SB();
        gap(p + WLA);
#pragma unroll
        for (int e = 0; e < 10; ++e) {
            const int P = O ? p : 5 * p + (e >> 1), d = 2 * (P & 3);
            const u32x2 y = ldtr(b + (O ? r0 + e * 32 : (e & 1 ? r1 : r0) + (e >> 1) * 4096));
            u32x8& t = O ? wt[P >> 2][e] : wt[e & 1][P >> 2];
            t[d] = y[0]; t[d + 1] = y[1];
        }
    }
    BAR();
}

__global__ __launch_bounds__(512, 2) B3ATTR void b3_kernel(RecArgs a) {
    extern __shared__ __attribute__((aligned(16))) char smem[];
    char* memL = smem;
    char* tokL = smem + L_TOK;
    char* hL = smem + L_H;
    int w = __builtin_amdgcn_readfirstlane(threadIdx.x >> 6);
    __amdgpu_buffer_rsrc_t rwin = mkrsrc(a.win, W_END + 2 * PAIR);
    __amdgpu_buffer_rsrc_t rws = mkrsrc(a.ctl, (unsigned)OFF_WIN);
    asm volatile("" : "+s"(rwin), "+s"(rws));
    unsigned long long tref = 0;
    const unsigned long long tstart = __builtin_amdgcn_s_memtime();
    auto get_tag = [&]() __attribute__((always_inline)) {
        unsigned long long e;
        asm volatile("s_buffer_load_dwordx2 %0, %1, 8\n\ts_waitcnt lgkmcnt(0)" : "=s"(e) : "s"(rws) : "memory");
        e += 0x9E3779B97F4A7C15ull;
        return (u32x2){(unsigned)e, (unsigned)(e >> 32)};
    };
    auto role = [&](unsigned rot) {
        const unsigned vx = blockIdx.x + rot;
        const int j = blockIdx.x >> 3, x = vx & 3, hh = 8 * (blockIdx.x & 3) + (j & 7);
        int r;
        if (!(vx & 4)) r = j < 8 ? hh : j < 16 ? 32 + hh : j < 26 ? 80 + 10 * x + j : j < 30 ? 255 - 4 * (j - 26) - x : NREC + 2 + 2 * x + j;
        else r = j < 8 ? NQT + hh : j < 16 ? 64 + hh : j < 24 ? NREC - 16 + 8 * x + j : NXT + hh;
        return r;
    };
    int bid = role(xcc_id() - blockIdx.x + BADG * (blockIdx.x >> 3));
    unsigned nh = 0, nz = 0, mc = 0, qz = 0;
    auto hint = [&](unsigned vo, unsigned so, u32x2 f) __attribute__((always_inline)) {
        const u32x2 tag = get_tag();
        for (unsigned spins = 0; spins < SPIN_CAP && __any(f[1] != tag[1] || (f[0] ^ tag[0]) > 255u); ++spins) {
            __builtin_amdgcn_s_sleep(4);
            asm volatile("" ::: "memory");
            f = __builtin_amdgcn_raw_buffer_load_b64(rwin, vo, so, 16);
        }
        return f[0] & 255u;
    };
    auto tell = [&](unsigned so, unsigned v) __attribute__((always_inline)) {
        const u32x2 tag = get_tag();
        __builtin_amdgcn_raw_buffer_store_b64((u32x2){(tag[0] & ~255u) | v, tag[1]}, rwin, 0u, so, 16);
    };
    auto share = [&](unsigned v) __attribute__((always_inline)) {
        if (w == 0) *(volatile LAS unsigned*)(smem + L_S + 64) = v;
        BAR();
        v = __builtin_amdgcn_readfirstlane(*(volatile LAS unsigned*)(smem + L_S + 64));
        BAR();
        return v;
    };
    u32x2 cf0, cf1;
    auto config0 = [&]() __attribute__((always_inline)) { cf0 = __builtin_amdgcn_raw_buffer_load_b64(rwin, fresh_lane() * 8u, W_CFG, 16); cf1 = __builtin_amdgcn_raw_buffer_load_b64(rwin, 0u, W_ARM, 16); };
    auto config = [&]() __attribute__((always_inline)) {
        unsigned v = 0;
        if (w == 0) {
            const unsigned l = fresh_lane(), f = hint(l * 8u, W_CFG, cf0), rot = __builtin_amdgcn_readfirstlane(hint(0u, W_ARM, cf1));
            const unsigned long long ok = __ballot((f & 4u) && !((l + rot) & 4u)), h = __ballot(f & 1u), q = __ballot(((f >> 1) ^ (l + rot)) & 1u);
            v = rot;
#pragma unroll
            for (int p = 0; p < 2; ++p) {
                unsigned long long m = ok & (p ? 0xFF00FF00FF00FF00ull : 0x00FF00FF00FF00FFull);
                const bool hb = 2 * __builtin_popcountll(m & h) > __builtin_popcountll(m);
                m &= hb ? h : ~h;
                v |= (hb ? 8u : 0u) << p | (2 * __builtin_popcountll(m & q) > __builtin_popcountll(m) ? 32u : 0u) << p;
            }
        }
        v = share(v);
        nh = (v >> 3 & 1u) * HALF; nz = (v >> 4 & 1u) * HALF;
        mc = ((blockIdx.x + v) & 3u) ^ (v >> 5 & 1u); qz = v >> 6 & 1u;
        return v;
    };
    auto zpair = [&](unsigned x, unsigned k) { return (((x >> 1) * 10u + k) * 2u + ((x & 1u) ^ qz)) * PAIR + nz; };
    unsigned mo, mo1, tro;
    auto two = [&]() { const unsigned l = fresh_lane(); return l < 4 ? (l >> 1) * PAIR + (l & 1) * 4096u + nh : 0x40000000u; };
    const u32x4 SENT = {0xFFFFFFFFu, 0xFFFFFFFFu, 0xFFFFFFFFu, 0xFFFFFFFFu};
    const f32x4 z4 = {0.f, 0.f, 0.f, 0.f};
    int hrdy = 0, budget = (int)SPIN_CAP;
    unsigned fc0 = 0, fc1 = 0, fc2 = 0;
    Clk ck = {0u, 0u, 1600u, 10486u};
    int lay = 0;
    unsigned fb = 0, zb = W_ZM, hb = 0;
    unsigned tpp = 0;
    int lt = LT1, xs = XS0;
    struct Tk { unsigned vo, tw, sl; const LAS char* sp; };
    auto tk_init = [&]() {
        const unsigned l = fresh_lane();
        Tk k = {l * 16u, two(), (l & 31u) * 128u + (l >> 5) * 4u, (const LAS char*)(memL + l * 64 + 4 + ((l >> 1) & 7) * 8)};
        PIN2(k.vo, k.tw); PIN2(k.sl, k.sp);
        ck = clk_init();
        tpp = ck.ta;
        return k;
    };
    auto sched2 = [&](Tk& k, int s, unsigned f) {
        const unsigned g0 = tpp - 0x400000u;
        tpp = g0 + (unsigned)read_sched(rws, rwin, s, (unsigned)(s + 64 * lay), k.sl, tro + (unsigned)(s & 3) * 2u * PAIR, f, g0, lt, xs, fc1);
        k.sl &= ~4u;
        RT(6);
    };
    auto sched = [&](Tk& k, int s) { sched2(k, s, __builtin_amdgcn_raw_buffer_load_b32(rwin, k.sl, tro + (unsigned)(s & 3) * 2u * PAIR, 16)); };
    auto fetch = [&](const Tk& k, int s, bool first) {
        const unsigned sb = (unsigned)(s & 3) * 6u * PAIR, ta0 = ck.ta, mm0 = ck.ma;
        if (__builtin_expect(first, 1)) {
            while ((unsigned)sx24(tpp - TKE - 4u * (unsigned)__builtin_amdgcn_s_memrealtime()) < 20000u) __builtin_amdgcn_s_sleep(1);
            gather_at(tpp, ck, rwin, k.vo, mo + sb, mo1 + sb);
        }
        __builtin_amdgcn_s_setprio(0);
        RT(1);
        clk_rate(ck, ta0, mm0);
        unsigned tb = ck.ma - DQC;
        asm volatile(AL "s_waitcnt vmcnt(%1) lgkmcnt(0)\n\ts_barrier\n\ts_waitcnt vmcnt(%2)\n\ts_barrier" : "+s"(tb) : "n"(10 - NB1), "n"(10 - NB2) : "memory");
        return tb;
    };
    unsigned mb = 0;
    auto again = [&](bool tk, unsigned vo, unsigned& sf, int s, unsigned lim, const LAS char*& p, const LAS char*& q) __attribute__((always_inline)) {
        const unsigned nb = mb == 0x10000u ? 0x14000u : 0x10000u;
        if (tk) {
            const unsigned c = (mc + 1u + (nb >> 14 & 1u)) & 3u, sb = W_MEM + (unsigned)(s & 3) * 6u * PAIR + nh, t = sb + (4u + (c & 1u)) * PAIR + (c >> 1) * 2048u;
            dma4k(rwin, vo, sb + c * PAIR, nb); dma4k(rwin, vo, sb + c * PAIR + 4096u, nb + 4096u);
            dma1k(rwin, vo, t, nb + 8192u); dma1k(rwin, vo, t + 1024u, nb + 9216u);
            asm volatile("s_waitcnt vmcnt(10)" : "+v"(sf) :: "memory");
        }
        const bool r = mem_missing(mb, lim);
        if (tk && !r) VMWAIT();
        if (r) { p += nb - mb; q += nb - mb; mb = nb; FC(fc0); }
        return r;
    };
    auto places = [&]() {
        mo = W_MEM + mc * PAIR + nh; mo1 = W_MEM + (4u + (mc & 1u)) * PAIR + nh + (mc >> 1) * 2048u;
        tro = W_T + (mc & 1u) * PAIR + nh + (mc >> 1) * 4096u;
    };
    auto fresh = [&]() {
        asm volatile("" : "+s"(bid), "+s"(w), "+s"(nh), "+s"(nz), "+s"(mc), "+s"(qz), "+s"(lay));
        fb = lay * W_FL; hb = O_H + (1 - lay) * (4u * 40960u); zb = W_ZM + lay * (NSEG % 3 * NOWN * PAIR);
        tref = 0; hrdy = 0; budget = (int)SPIN_CAP; ck = Clk{0u, 0u, 1600u, 10486u}; tpp = 0; lt = LT1; xs = XS0;
    };
    auto filter = [&]() __attribute__((always_inline)) {
        if ((bid >= 32 && bid < 64) || (bid >= NQT && bid < NXT)) {
            const unsigned u = bid < 64 ? bid : bid - NQT, l = fresh_lane();
            filter_unit((const u16*)karg<9>(), (const u16*)karg<10>(), u * 32, rws, (u16*)tokL, w * 64 + l);
            VMWAIT();
            BAR();
            if (w == 0) __builtin_amdgcn_raw_buffer_store_b64(get_tag(), rwin, (l & 1u) * 8u, W_ST + u * 128u + 64u, 16);
        }
    };
    auto settle = [&]() __attribute__((always_inline)) {
        if (TPF && w == 1 && (bid >> 5 == 1 || bid >= NQT)) for (unsigned i = 0; i < (unsigned)OFF_WIN; i += 64u << TPF) dma1k(rws, fresh_lane() << TPF, i, L_Q);
        const int b = role(config()); if (__builtin_expect(b != bid, 0)) { bid = b; filter(); } };
    auto warm = [&]() __attribute__((always_inline)) {
        unsigned long long pc;
        unsigned len;
        asm volatile("s_getpc_b64 %0\n1:\n\ts_mov_b32 %1, 2f-1b\n\t.subsection 1\n2:\n\t.subsection 0" : "=s"(pc), "=s"(len));
        if (CPF) dma1k(mkrsrc((const void*)pc, len), fresh_lane() * 16u, (blockIdx.x >> 3) * 1024u + w * 16384u, L_END - 8192 + w * 1024);
    };
    auto a1_done = [&]() __attribute__((always_inline)) {
        VMWAIT();
        BAR();
        if (w == 0) __builtin_amdgcn_raw_buffer_store_b64(get_tag(), rwin, 0u, W_ST + (blockIdx.x - 128u >> 1) * 128u + (blockIdx.x & 1u) * 8u, 16);
        SST(29);
    };
    auto due = [&](int i, int d) __attribute__((always_inline)) {
        __builtin_amdgcn_raw_buffer_store_b32(nosent1((unsigned)__builtin_amdgcn_s_memrealtime() + d), rwin, 0u, W_E + fb + i * 128u, 16);
    };
    auto await = [&](int i, int n, int d, unsigned f = ~0u) __attribute__((always_inline)) {
        if (__any(f == ~0u)) for (unsigned spins = 0;;) {
            asm volatile("" ::: "memory");
            f = __builtin_amdgcn_raw_buffer_load_b32(rwin, min(fresh_lane(), n - 1u) * 128u, W_E + i * 128u, 16);
            if (!__any(f == ~0u)) break;
            if (spin_fail(spins, rws)) break;
            __builtin_amdgcn_s_sleep(HSLP);
        }
        while (__any(f + d - (unsigned)__builtin_amdgcn_s_memrealtime() - 1u < 1000u)) {}
    };
    constexpr int E_OUT = 0, E_A1 = 8, E_A2 = 16, E_A3 = 24, E_ZT = 28;
    auto soon = [&](int j) __attribute__((always_inline)) {
        if (w == 0) { if (j >= 4) wait_h(rws, rwin, fb, j, hrdy); else await(E_A3 + fb / 128u, 4, 0); }
        BAR();
    };
    bool slow = false;
    auto gap = [&](int p) __attribute__((always_inline)) {
        if (p == 7) config0();
        if (slow && p < 7) __builtin_amdgcn_s_sleep(XSLP);
    };
    SST(27);
    {
        LANES();
        const unsigned u = blockIdx.x - 128u;
        if (blockIdx.x < 128) {
            const unsigned po = (blockIdx.x & 8u ? W_END : W_PROBE) + ((blockIdx.x >> 1 & 24u) | (blockIdx.x & 7u)) * 128u;
            const bool pr = bid >= 32 && bid < 64 && w == 7;
            if (pr) __builtin_amdgcn_raw_buffer_store_b32(0u, rwin, (lane & 3) * HALF + (lane >> 2 & 1) * 4096u, po, 16);
            warm();
            filter();
            if (blockIdx.x >= 64 && w == 7) tell(W_CFG + (blockIdx.x - 64u) * 8u, pr ? 4u | probe(rwin, po) : 0u);
            BAR0();
        } else a1_unit<8, 512>(mkrsrc(karg<3>(), S_ * D_ * 2), u * 16, false, rws, smem, row_tw(lane), w, lane, [&](int k) {
            if (k == 0 && u == 0 && w == 0) { tell(W_ROT, xcc_id()); __builtin_amdgcn_raw_buffer_store_b32(0u, rws, 0u, 0u, 16); }
            if (k == -1) {
                u32x4 sv = SENT;
                PIN1(sv);
#pragma unroll
                for (int i = 0; i < 4; ++i) {
                    static_assert(ARM_END - OFF_ARM <= 512 * 8192 && W_PROBE + W_END - W_ZM <= 512 * 8192, "arming");
                    const unsigned g = (i * 128u + u) * 8192u;
                    st128(sv, rwin, tid * 16u, g < W_PROBE ? g : g < W_PROBE + W_END - W_ZM ? g + W_ZM - W_PROBE : 0x40000000u, 16);
                    st128(sv, rws, tid * 16u, g < ARM_END - OFF_ARM ? (unsigned)OFF_ARM + g : 0x40000000u, 16);
                }
            }
        });
        SST(28);
        if (blockIdx.x >= 128) {
            if (w == 0) *(volatile LAS unsigned*)(smem + L_END - 16) = hint(0u, W_ROT, __builtin_amdgcn_raw_buffer_load_b64(rwin, 0u, W_ROT, 16));
            a1_done();
            const unsigned rot = __builtin_amdgcn_readfirstlane(*(volatile LAS unsigned*)(smem + L_END - 16));
            bid = role(rot);
            if (bid == 255 && w == 0) {
                const u32x2 tag = get_tag();
                for (unsigned spins = 0; spins < SPIN_CAP; ++spins) {
                    asm volatile("" ::: "memory");
                    const u32x4 g = __builtin_amdgcn_raw_buffer_load_b128(rwin, lane * 128u, W_ST, 16);
                    if (!__any(((g[0] ^ tag[0]) | (g[1] ^ tag[1]) | (g[2] ^ tag[0]) | (g[3] ^ tag[1])) != 0u)) break;
                    __builtin_amdgcn_s_sleep(4);
                }
                tell(W_ARM, rot);
            }
            SST(30);
        }
    }
    if (bid < 96 || (bid >= NQT && bid < NWRK)) {
    u32x8 wt[2][10];
#pragma nounroll
    for (; lay < 2; ++lay) {
    fresh();
    const bool xt = bid >= NXT;
    SST(0);
    if ((bid >= 32 && bid < 96) || xt) {
        const int hh = (xt ? bid - NXT : bid) & 31;
        u32x4 m0[2];
        get_mem0(m0, w * 64 + fresh_lane());
        if (lay == 0 || xt) {
            wait_until(tstart + (bid < 64 ? DVO : xt ? DXT : DW1));
            const bool once = !slow && (NTM >> (xt ? 4 : bid >> 5) & 1);
            if (w < 4) load_w<false>(wt, karg<7>(), hh, w, 2 * w, 8, tokL, once, gap); else load_w<true>(wt, karg<8>(), hh, w, 0, 0, tokL, once, gap);
            SST(5);
            slow = true;
            if (lay == 0) settle();
        }
        places();
        const bool vt = bid >= 64;
        const unsigned long long mv = xt ? MXV : ~MXV;
        auto nextv = [&](int j) { return min(seg_after(mv, j), NSEG - 1); };
        const int jv1 = nextv(-1), jv2 = nextv(jv1);
        {
            LANES();
            if (!vt) put_mem0(m0, rwin, mo, mo1, tid);
            VMWAIT();
            BAR0();
            if (tid < 8) *(unsigned*)(smem + L_S + tid * 4) = 0u;
            SST(1);
            if (vt) {
                soon(jv1);
                SST(2);
                const unsigned so = hb + (unsigned)(jv1 * 40960 + w * 1024), lo = L_H + w * 1024;
                stage_h<5, 8192>(rws, so, lo, lane);
                BAR0();
                if (w < 4) check_h<5, 8192, 0>(rws, so, lo, lane);
                else { stage_h<10, 4096>(rws, hb + (unsigned)(jv2 * 40960 + (w - 4) * 1024), lo + 36864u, lane); check_h<5, 8192, 10>(rws, so, lo, lane); }
            }
        }
        auto steps = [&](auto cc) {
            constexpr int C = decltype(cc)::v & 1;
            constexpr bool TK = decltype(cc)::v == 3;
            LANES();
            Tk tk = {0u, 0u, 0x40000000u, nullptr};
            if (TK) { tk = tk_init(); __builtin_amdgcn_s_setprio(TKP); }
            const unsigned zi = (unsigned)(hh * 128 + fr * 8) * 2u;
            unsigned zo0 = zpair(w & 3, fq) + zi, zo1 = zo0 + 8 * PAIR, zo2 = zpair(w & 3, 8 + (fq >> 1)) + zi + (fq & 1) * 8;
            const LAS char* va = (const LAS char*)(smem + L_Q + (fr & 1) * 544 + ((fr >> 1) & 1) * 1152 + (16 * fq + 2 * (fr >> 2)) * 4);
            LAS char* vw = (LAS char*)(smem + L_Q + (fq & 1) * 544 + (fq >> 1) * 1152 + (16 * w + bs2(fr)) * 4);
            const LAS char* mp0 = (const LAS char*)(memL + ma0);
            PIN2(zo0, zo1); PIN1(zo2);
            if (C == 0) { PIN2(mp0, vw); } else PIN1(va);
            u32x4 za = SENT, zc = SENT;
            u32x2 ze = {0u, 0u};
            auto put_z2 = [&](int s) {
                const unsigned z2b = s < NSEG ? O_Z2 + (unsigned)s * (NOWN * 8192u) + (unsigned)(10 * (w & 3) * 8192 + hh * 256) : 0x40000000u, l2 = fresh_lane();
                st128(za, rws, (l2 >> 4) * 8192u + (l2 & 15u) * 16u, z2b, 16);
                st128(zc, rws, (l2 >> 4) * 8192u + (l2 & 15u) * 16u, z2b + 4u * 8192u, 16);
                __builtin_amdgcn_raw_buffer_store_b64(ze, rws, (l2 >> 5) * 8192u + (l2 & 15u) * 16u + ((l2 >> 4) & 1u) * 8u, z2b + 8u * 8192u, 16);
            };
            for (int s = 0; s < NSEG; ++s) {
                RT(0);
                bool redo = false;
                unsigned sf = 0;
                do {
                    unsigned lim = s > 0 ? poll_lim(budget) : ~0u;
                    unsigned long long sus;
                    asm volatile("" : "+s"(lim));
                    if (C == 0) {
                        asm volatile(
                            "v_mov_b64 v[200:201], 0\n\t"
                            "v_mov_b64 v[202:203], 0\n\t"
                            "v_mov_b64 v[204:205], 0\n\t"
                            "v_mov_b64 v[206:207], 0\n\t"
                            AL "s_setprio 2\n\t"
                            "s_waitcnt lgkmcnt(0)\n\t"
                            "s_barrier\n\t"
                            MTA(0)
                            "ds_read_b128 v[212:215], %[mp] offset:0\n\t"
                            "ds_read_b128 v[216:219], %[mp] offset:1024\n\t"
                            "ds_read_b128 v[220:223], %[mp] offset:2048\n\t"
                            "s_barrier\n\t"
                            "ds_read_b128 v[224:227], %[mp] offset:3072\n\t"
                            "ds_read_b128 v[228:231], %[mp] offset:4096\n\t"
                            "ds_read_b128 v[232:235], %[mp] offset:5120\n\t"
                            "s_waitcnt lgkmcnt(5)\n\t"
                            "v_smfmac_f32_16x16x64_bf16 v[200:203], v[212:215], %[w00], %[ix]\n\t"
                            "v_smfmac_f32_16x16x64_bf16 v[204:207], v[212:215], %[w10], %[ix]\n\t"
                            "s_waitcnt lgkmcnt(4)\n\t"
                            "v_smfmac_f32_16x16x64_bf16 v[200:203], v[216:219], %[w01], %[ix]\n\t"
                            "v_smfmac_f32_16x16x64_bf16 v[204:207], v[216:219], %[w11], %[ix]\n\t"
                            "s_barrier\n\t"
                            MTA(1)
                            "ds_read_b128 v[236:239], %[mp] offset:6144\n\t"
                            "ds_read_b128 v[240:243], %[mp] offset:7168\n\t"
                            "s_waitcnt lgkmcnt(5)\n\t"
                            "v_smfmac_f32_16x16x64_bf16 v[200:203], v[220:223], %[w02], %[ix]\n\t"
                            "ds_read_b128 v[244:247], %[mp] offset:8192\n\t"
                            "ds_read_b128 v[248:251], %[mp] offset:9216\n\t"
                            "s_waitcnt lgkmcnt(4)\n\t"
                            "v_smfmac_f32_16x16x64_bf16 v[200:203], v[224:227], %[w03], %[ix]\n\t"
                            "v_smfmac_f32_16x16x64_bf16 v[200:203], v[228:231], %[w04], %[ix]\n\t"
                            "v_smfmac_f32_16x16x64_bf16 v[200:203], v[232:235], %[w05], %[ix]\n\t"
                            "s_waitcnt lgkmcnt(3)\n\t"
                            "v_smfmac_f32_16x16x64_bf16 v[200:203], v[236:239], %[w06], %[ix]\n\t"
                            "s_waitcnt lgkmcnt(2)\n\t"
                            "v_smfmac_f32_16x16x64_bf16 v[200:203], v[240:243], %[w07], %[ix]\n\t"
                            "s_waitcnt lgkmcnt(1)\n\t"
                            "v_smfmac_f32_16x16x64_bf16 v[200:203], v[244:247], %[w08], %[ix]\n\t"
                            "s_waitcnt lgkmcnt(0)\n\t"
                            "v_smfmac_f32_16x16x64_bf16 v[200:203], v[248:251], %[w09], %[ix]\n\t"
                            "v_smfmac_f32_16x16x64_bf16 v[204:207], v[220:223], %[w12], %[ix]\n\t"
                            "s_nop 2\n\t"
                            "v_smfmac_f32_16x16x64_bf16 v[204:207], v[224:227], %[w13], %[ix]\n\t"
                            "s_nop 2\n\t"
                            "v_add_f32 v200, v200, v202\n\t"
                            "v_add_f32 v201, v201, v203\n\t"
                            "v_smfmac_f32_16x16x64_bf16 v[204:207], v[228:231], %[w14], %[ix]\n\t"
                            "v_cvt_pk_bf16_f32 v200, v200, v201\n\t"
                            "ds_write_b32 %[vw], v200\n\t"
                            MTA(2)
                            "v_smfmac_f32_16x16x64_bf16 v[204:207], v[232:235], %[w15], %[ix]\n\t"
                            "v_smfmac_f32_16x16x64_bf16 v[204:207], v[236:239], %[w16], %[ix]\n\t"
                            "v_smfmac_f32_16x16x64_bf16 v[204:207], v[240:243], %[w17], %[ix]\n\t"
                            "v_smfmac_f32_16x16x64_bf16 v[204:207], v[244:247], %[w18], %[ix]\n\t"
                            "s_waitcnt lgkmcnt(0)\n\t"
                            "s_barrier\n\t"
                            MTA(5)
                            "v_smfmac_f32_16x16x64_bf16 v[204:207], v[248:251], %[w19], %[ix]\n\t"
                            "s_nop 7\n\t"
                            "v_pk_add_f32 v[204:205], v[204:205], v[206:207]\n\t"
                            "v_cvt_pk_bf16_f32 v200, v204, v205\n\t"
                            "ds_write_b32 %[vw], v200 offset:256\n\t"
                            MTA(3)
                            "s_waitcnt vmcnt(0) lgkmcnt(0)\n\t"
                            "s_barrier\n\t"
                            "s_setprio 0\n\t"
                            "v_cmp_u_f32 %[sus], v204, v205\n\t"
                            : [sus] "=s"(sus) : [mp] "v"(mp0), [vw] "v"(vw), [ix] "v"(sidx),
                              [w00] "v"(wt[0][0]), [w01] "v"(wt[0][1]), [w02] "v"(wt[0][2]), [w03] "v"(wt[0][3]), [w04] "v"(wt[0][4]), [w05] "v"(wt[0][5]), [w06] "v"(wt[0][6]), [w07] "v"(wt[0][7]), [w08] "v"(wt[0][8]), [w09] "v"(wt[0][9]), [w10] "v"(wt[1][0]), [w11] "v"(wt[1][1]), [w12] "v"(wt[1][2]), [w13] "v"(wt[1][3]), [w14] "v"(wt[1][4]), [w15] "v"(wt[1][5]), [w16] "v"(wt[1][6]), [w17] "v"(wt[1][7]), [w18] "v"(wt[1][8]), [w19] "v"(wt[1][9])
                            : "v200", "v201", "v202", "v203", "v204", "v205", "v206", "v207", "v208", "v209", "v212", "v213", "v214", "v215", "v216", "v217", "v218", "v219", "v220", "v221", "v222", "v223", "v224", "v225", "v226", "v227", "v228", "v229", "v230", "v231", "v232", "v233", "v234", "v235", "v236", "v237", "v238", "v239", "v240", "v241", "v242", "v243", "v244", "v245", "v246", "v247", "v248", "v249", "v250", "v251", "vcc", "memory");
                        MT(4);
                    } else {
                        f32x16 ca, cb;
                        f32x8 cc;
                        asm volatile(".irp r,212,214,216,218,220,222,224,226,228,230,232,234,236,238,240,242,244,246,248,250\n\tv_mov_b64 v[\\r:\\r+1], 0\n\t.endr"
                                     : "={v[212:227]}"(ca), "={v[228:243]}"(cb), "={v[244:251]}"(cc));
                        unsigned sr = tro + (unsigned)(s & 3) * 2u * PAIR;
                        asm volatile("" : "+s"(sr));
                        if (TK) { fetch(tk, s, !redo); asm volatile("s_waitcnt vmcnt(0)\n\ts_barrier" ::: "memory"); RT(3); }
                        else asm volatile("s_waitcnt lgkmcnt(0)\n\ts_barrier\n\ts_barrier\n\ts_barrier" ::: "memory");
                        unsigned zs = zb;
                        asm volatile(
                            AL "s_setprio 1\n\t"
                            "s_waitcnt vmcnt(0) lgkmcnt(0)\n\t"
                            "s_barrier\n\t"
                            "ds_read_b64_tr_b16 v[192:193], %[va]\n\t"
                            "ds_read_b64_tr_b16 v[194:195], %[va] offset:32\n\t"
                            "s_waitcnt lgkmcnt(0)\n\t"
                            "v_smfmac_f32_16x16x64_bf16 v[212:215], v[192:195], %[w0], %[ix]\n\t"
                            "v_smfmac_f32_16x16x64_bf16 v[220:223], v[192:195], %[w2], %[ix]\n\t"
                            "v_smfmac_f32_16x16x64_bf16 v[216:219], v[192:195], %[w1], %[ix]\n\t"
                            "v_smfmac_f32_16x16x64_bf16 v[224:227], v[192:195], %[w3], %[ix]\n\t"
                            "s_barrier\n\t"
                            MTA(4)
                            "ds_read_b64_tr_b16 v[196:197], %[va] offset:256\n\t"
                            "v_smfmac_f32_16x16x64_bf16 v[228:231], v[192:195], %[w4], %[ix]\n\t"
                            "ds_read_b64_tr_b16 v[198:199], %[va] offset:288\n\t"
                            "v_smfmac_f32_16x16x64_bf16 v[236:239], v[192:195], %[w6], %[ix]\n\t"
                            "v_smfmac_f32_16x16x64_bf16 v[232:235], v[192:195], %[w5], %[ix]\n\t"
                            "v_smfmac_f32_16x16x64_bf16 v[240:243], v[192:195], %[w7], %[ix]\n\t"
                            "v_cmp_u_f32 %[sus], v212, v213\n\t"
                            "v_smfmac_f32_16x16x64_bf16 v[244:247], v[192:195], %[w8], %[ix]\n\t"
                            "v_smfmac_f32_16x16x64_bf16 v[248:251], v[192:195], %[w9], %[ix]\n\t"
                            "s_waitcnt lgkmcnt(0)\n\t"
                            "v_smfmac_f32_16x16x64_bf16 v[212:215], v[196:199], %[x0], %[ix]\n\t"
                            "s_nop 0\n\t"
                            "v_smfmac_f32_16x16x64_bf16 v[220:223], v[196:199], %[x2], %[ix]\n\t"
                            "s_cmp_lg_u64 %[sus], 0\n\t"
                            "s_cselect_b32 %[zs], 2.0, %[zs]\n\t"
                            "v_smfmac_f32_16x16x64_bf16 v[216:219], v[196:199], %[x1], %[ix]\n\t"
                            "s_nop 1\n\t"
                            "v_smfmac_f32_16x16x64_bf16 v[224:227], v[196:199], %[x3], %[ix]\n\t"
                            "v_add_f32 v212, v212, v214\n\t"
                            "v_smfmac_f32_16x16x64_bf16 v[228:231], v[196:199], %[x4], %[ix]\n\t"
                            "v_add_f32 v213, v213, v215\n\t"
                            "v_smfmac_f32_16x16x64_bf16 v[236:239], v[196:199], %[x6], %[ix]\n\t"
                            "v_add_f32 v220, v220, v222\n\t"
                            "v_smfmac_f32_16x16x64_bf16 v[232:235], v[196:199], %[x5], %[ix]\n\t"
                            "v_add_f32 v221, v221, v223\n\t"
                            "v_smfmac_f32_16x16x64_bf16 v[240:243], v[196:199], %[x7], %[ix]\n\t"
                            "v_cvt_pk_bf16_f32 v200, v212, v213\n\t"
                            "v_smfmac_f32_16x16x64_bf16 v[244:247], v[196:199], %[x8], %[ix]\n\t"
                            "v_cvt_pk_bf16_f32 v201, v220, v221\n\t"
                            "v_smfmac_f32_16x16x64_bf16 v[248:251], v[196:199], %[x9], %[ix]\n\t"
                            ".if %[tk]\n\tbuffer_load_dword %[sf], %[sl], %[rw], %[sr] offen sc1\n\t.endif\n\t"
                            "v_add_f32 v216, v216, v218\n\t"
                            "v_add_f32 v217, v217, v219\n\t"
                            "v_add_f32 v224, v224, v226\n\t"
                            "v_add_f32 v225, v225, v227\n\t"
                            "v_cvt_pk_bf16_f32 v202, v216, v217\n\t"
                            "v_cvt_pk_bf16_f32 v203, v224, v225\n\t"
                            "v_permlane32_swap_b32 v200, v201\n\t"
                            "v_pk_add_f32 v[228:229], v[228:229], v[230:231]\n\t"
                            "v_permlane32_swap_b32 v202, v203\n\t"
                            "v_min_u32 v201, -2, v201\n\t"
                            "v_pk_add_f32 v[236:237], v[236:237], v[238:239]\n\t"
                            "v_min_u32 v203, -2, v203\n\t"
                            "v_permlane16_swap_b32 v200, v202\n\t"
                            "v_pk_add_f32 v[232:233], v[232:233], v[234:235]\n\t"
                            "v_permlane16_swap_b32 v201, v203\n\t"
                            "v_pk_add_f32 v[240:241], v[240:241], v[242:243]\n\t"
                            MTA(5)
                            "buffer_store_dwordx4 v[200:203], %[z0], %[rw], %[zs] offen sc1\n\t"
                            "v_cvt_pk_bf16_f32 v204, v228, v229\n\t"
                            "v_cvt_pk_bf16_f32 v205, v236, v237\n\t"
                            "v_cvt_pk_bf16_f32 v206, v232, v233\n\t"
                            "v_cvt_pk_bf16_f32 v207, v240, v241\n\t"
                            "v_permlane32_swap_b32 v204, v205\n\t"
                            "v_pk_add_f32 v[244:245], v[244:245], v[246:247]\n\t"
                            "v_permlane32_swap_b32 v206, v207\n\t"
                            "v_min_u32 v205, -2, v205\n\t"
                            "v_pk_add_f32 v[248:249], v[248:249], v[250:251]\n\t"
                            "v_min_u32 v207, -2, v207\n\t"
                            "v_permlane16_swap_b32 v204, v206\n\t"
                            "v_cvt_pk_bf16_f32 v208, v244, v245\n\t"
                            "v_permlane16_swap_b32 v205, v207\n\t"
                            "v_cvt_pk_bf16_f32 v209, v248, v249\n\t"
                            "buffer_store_dwordx4 v[204:207], %[z1], %[rw], %[zs] offen sc1\n\t"
                            "s_nop 0\n\t"
                            "v_permlane32_swap_b32 v208, v209\n\t"
                            "v_min_u32 v209, -2, v209\n\t"
                            "buffer_store_dwordx2 v[208:209], %[z2], %[rw], %[zs] offen sc1\n\t"
                            ".if %[tk]\n\ts_setprio 0\n\ts_cmp_lg_u64 %[sus], 0\n\ts_cbranch_scc1 1f\n\ts_waitcnt vmcnt(3)\n1:\n\t.endif"
                            : [sf] "+&v"(sf), [zs] "+&s"(zs), [sus] "=&s"(sus), "=&{v[200:203]}"(za), "=&{v[204:207]}"(zc), "=&{v[208:209]}"(ze), "+{v[212:227]}"(ca), "+{v[228:243]}"(cb), "+{v[244:251]}"(cc)
                            : [va] "v"(va), [ix] "v"(sidx), [sl] "v"(tk.sl), [sr] "s"(sr), [rw] "s"(rwin), [z0] "v"(zo0), [z1] "v"(zo1), [z2] "v"(zo2), [tk] "n"(TK ? 1 : 0),
                              [w0] "v"(wt[0][0]), [w1] "v"(wt[0][1]), [w2] "v"(wt[0][2]), [w3] "v"(wt[0][3]), [w4] "v"(wt[0][4]), [w5] "v"(wt[0][5]), [w6] "v"(wt[0][6]), [w7] "v"(wt[0][7]), [w8] "v"(wt[0][8]), [w9] "v"(wt[0][9]),
                              [x0] "v"(wt[1][0]), [x1] "v"(wt[1][1]), [x2] "v"(wt[1][2]), [x3] "v"(wt[1][3]), [x4] "v"(wt[1][4]), [x5] "v"(wt[1][5]), [x6] "v"(wt[1][6]), [x7] "v"(wt[1][7]), [x8] "v"(wt[1][8]), [x9] "v"(wt[1][9])
                            : "v192", "v193", "v194", "v195", "v196", "v197", "v198", "v199", "scc", "memory");
                        RT(4);
                        MT(6);
                        tref = __builtin_amdgcn_s_memtime();
                        __builtin_amdgcn_s_setprio(TKP);
                    }
                    redo = false;
                    if (__builtin_expect(sus != 0, 0)) {
                        redo = again(TK, tk.vo, sf, s, lim, mp0, tk.sp);
                        if (!redo && C == 1) { st128(za, rwin, zo0, zb, 16); st128(zc, rwin, zo1, zb, 16); __builtin_amdgcn_raw_buffer_store_b64(ze, rwin, zo2, zb, 16); }
                    }
                } while (__builtin_expect(redo, 0));
                if (__builtin_expect(mb != 0, 0)) { mp0 -= mb; mb = 0; }
                if (s < 8) SST(8 + s);
                if (C == 0) {
                    if (s + 1 < NSEG) {
                        __builtin_amdgcn_s_sleep(DZR);
                        const unsigned zq = s == 0 ? 0x40000000u : zb == W_ZM ? W_ZM + 2u * NOWN * PAIR : zb - NOWN * PAIR;
                        unsigned s1;
                        asm volatile("v_mov_b32 %0, -1" : "=v"(s1));
                        st128(((u32x4){s1, s1, s1, s1}), rwin, zo0, zq, 16);
                        st128(((u32x4){s1, s1, s1, s1}), rwin, zo1, zq, 16);
                        __builtin_amdgcn_raw_buffer_store_b64((u32x2){s1, s1}, rwin, zo2, zq, 16);
                    }
                } else {
                    if (TK && s + 1 < NSEG) sched2(tk, s, sf);
                    if (!TK) wait_until(tref + DZ2);
                    if (s + 2 < NSEG) put_z2(s);
                }
                if (budget <= 0) __builtin_amdgcn_raw_buffer_store_b32(1u, rws, 0u, 0u, 16);
                zb = zb == W_ZM + 2u * NOWN * PAIR ? W_ZM : zb + NOWN * PAIR;
                RT(5);
                RTS();
                MT(7);
                MTS();
            }
        };
        if (!vt) {
            if (w < 4) steps(IC<0>()); else if (w < 7) steps(IC<1>()); else steps(IC<3>());
        } else {
            BAR();
            SST(3);
            LANES();
            const int ni = __builtin_popcountll(mv) + 1;
            if (w < 4) {
                const LAS char* c0 = (const LAS char*)(hL + hoff + ((fq ^ (fr & 7)) << 4));
                const LAS char* c1 = (const LAS char*)(hL + hoff + (((4 + fq) ^ (fr & 7)) << 4));
                const LAS char* n0 = c0 + 40960; const LAS char* n1 = c1 + 40960;
                u32x4 bb[8];
                project0(bb, c0, c1);
                for (int i = 1; i < ni; ++i) {
                    if (vt) MT(0);
                    f32x4 tc[2][2];
                    project<0>(wt, tc, bb, c0, c1, n0, n1,
                        [&]() {
                            MT(1);
                            if (__builtin_expect(i > 1, 1)) BAR();
                            MT(2);
                        }, [](auto) {});
                    { const LAS char* x = c0; c0 = n0; n0 = x; x = c1; c1 = n1; n1 = x; }
                    char* tb = tokL + (i & 1) * 32 * QS + (16 * w + 4 * fq) * 2;
#pragma unroll
                    for (int g = 0; g < 2; ++g)
#pragma unroll
                        for (int tt = 0; tt < 2; ++tt) *(u32x2*)(tb + (prow + 8 * tt) * QS + g * 128) = (u32x2){pack2(tc[g][tt][0], tc[g][tt][1]), pack2(tc[g][tt][2], tc[g][tt][3])};
                    asm volatile("" ::: "memory");
                    *(volatile LAS int*)(smem + L_S + 16 + w * 4) = i;
                    if (__builtin_expect(i == 1, 0)) { BAR(); if (VSLP) __builtin_amdgcn_s_sleep(VSLP); project0(bb, c0, c1); }
                    if (vt) { MT(3); const int s = i & 63; MTRT(); MTS(); }
                }
                project_end(bb);
            } else {
                __builtin_amdgcn_s_setprio(OPR);
                const unsigned hv = lane * 16u, fz_ = W_FZ + fb + (unsigned)(hh * 128 + xt * 4096 + (w - 4) * 8192);
                unsigned fp = ~0u;
                u32x4 r = SENT;
                int jo = jv1, jh = jv2;
                check_h<10, 4096, 0>(rws, hb + (unsigned)(jv2 * 40960 + (w - 4) * 1024), L_H + 40960 + (w - 4) * 1024, lane);
                SST(16);
                auto rows = [&](int b) {
                    for (int t = 0; t < 10; ++t) dma1k(rws, hv, hb + (unsigned)(jh * 40960 + (w - 4) * 1024 + t * 4096), (unsigned)(L_H + ((b + 1) & 1) * 40960 + (w - 4) * 1024 + t * 4096));
                    VMWAIT();
                };
                for (int i = 1; i < ni; ++i, jo = nextv(jo)) {
                    const int b = i + (i >= ZLAG);
                    if (__builtin_expect(i == ZLAG, 0)) {
                        VMWAIT();
                        jh = nextv(jh);
                        wait_h(rws, rwin, fb, jh, hrdy);
                        BAR0();
                        rows(i);
                    }
                    jh = nextv(jh);
                    u32x4 hf;
                    asm volatile("" : "=v"(hf));
                    if (__builtin_expect(hrdy <= jh, 0)) asm volatile("buffer_load_dwordx4 %0, %1, %2, %3 offen sc1" : "+v"(hf) : "v"(min(lane, jh + 16) * 128), "s"(rwin), "s"(W_H + fb) : "memory");
                    if (b < ni) BAR();
                    if (i == 1) SST(17);
                    if (vt) MT(1);
                    for (;;) {
                        const u32x4 f = *(volatile LAS u32x4*)(smem + L_S + 16);
                        if (__builtin_amdgcn_readfirstlane(min(min(f[0], f[1]), min(f[2], f[3]))) >= (unsigned)i) break;
                        __builtin_amdgcn_s_sleep(1);
                    }
                    asm volatile("" ::: "memory");
                    u32x4 vf[2][4];
#pragma unroll
                    for (int ks = 0; ks < 4; ++ks)
#pragma unroll
                        for (int t = 0; t < 2; ++t) vf[t][ks] = *(const u32x4*)(tokL + (i & 1) * 32 * QS + (16 * t + fr) * QS + ks * 64 + fq * 16);
                    SB();
                    if (__builtin_expect(hrdy <= jh, 0)) { asm volatile("s_waitcnt vmcnt(0)" : "+v"(hf) :: "memory"); hrdy = min(see_h(hf), jh + 17); }
                    const bool ok = hrdy > jh;
                    if (__builtin_expect((i < ZLAG || !ok) && (int)fp > ZEARLY, 0)) { VMWAIT(); __builtin_amdgcn_raw_buffer_store_b32(fp, rwin, 0u, fz_, 16); }
                    if (__builtin_expect(jo == 0, 0) && w == 7) {
                        if (!(hh & 7)) due(E_ZT + (hh >> 3), lay ? DZF : DZF0);
                        __builtin_amdgcn_raw_buffer_store_b32(((4u * (unsigned)__builtin_amdgcn_s_memrealtime() + 4 * (lay ? DZF : DZF0) + DV0) & 0xFFFFFFu) | (unsigned)lay << 30, rwin, two(), W_T + hh * 128u + 4u, 16);
                    }
                    if (XDL && __builtin_expect(jo == 1, 0)) __builtin_amdgcn_s_sleep(XDL);
                    if (i == 1) SST(18);
                    if (vt) MT(0);
                    const unsigned ztb = O_Z + (unsigned)(jo * ZTSEG) * 2u + (unsigned)(10 * (w - 4) * H_ + hh) * 1024u;
                    f32x4 c[2][2];
                    unsigned hs = hb + (unsigned)(jh * 40960 + (w - 4) * 1024), hd = (unsigned)(L_H + ((b + 1) & 1) * 40960 + (w - 4) * 1024);
                    asm volatile("" : "+s"(hs), "+s"(hd));
#pragma unroll
                    for (int nt = 0; nt < 11; ++nt) {
                        auto& cc = c[nt & 1]; auto& cp = c[(nt + 1) & 1];
#pragma unroll
                        for (int ks = 0; ks < 4; ++ks) {
                            if (nt < 10) { cc[0] = mfma32(vf[0][ks], half8(wt[ks >> 1][nt], ks & 1), ks ? cc[0] : z4); PIN1(cc[0]); }
                            if (nt > 0 && ks >= 1 && ks < 3) { r[2 * ks - 2] = pack2(cp[ks - 1][0], cp[ks - 1][1]); PIN1(r); }
                            if (nt < 10) { cc[1] = mfma32(vf[1][ks], half8(wt[ks >> 1][nt], ks & 1), ks ? cc[1] : z4); PIN1(cc[1]); }
                            if (nt > 0 && ks >= 1 && ks < 3) { r[2 * ks - 1] = pack2(cp[ks - 1][2], cp[ks - 1][3]); PIN1(r); }
                            if (nt > 0 && ks == 2) st128(r, rws, lane * 16u, ztb + (unsigned)((nt - 1) * H_) * 1024u, 16);
                            if (nt < 5 && !(ks & 1) && __builtin_expect(ok, 1)) dma1k(rws, hv, hs + (2 * nt + (ks >> 1)) * 4096u, hd + (2 * nt + (ks >> 1)) * 4096u);
                            if (nt == 5 && ks == 0 && __builtin_expect(ok, 1)) {
                                asm volatile("s_waitcnt vmcnt(14)" ::: "memory");
                                __builtin_amdgcn_raw_buffer_store_b32(fp, rwin, 0u, fz_, 16);
                            }
                            SB();
                        }
                    }
                    if (i == 1) SST(19);
                    if (vt) MT(2);
                    fp = (unsigned)jo;
                    if (__builtin_expect(jo <= ZEARLY || i + 1 == ni || i + 1 == ZLAG || !ok, 0)) {
                        VMWAIT();
                        __builtin_amdgcn_raw_buffer_store_b32(fp, rwin, 0u, fz_, 16);
                    } else asm volatile("s_waitcnt vmcnt(7)" ::: "memory");
                    if (i == 1) SST(20);
                    if (__builtin_expect(!ok, 0)) { wait_h(rws, rwin, fb, jh, hrdy); rows(b); }
                    if (jo < 8) SST(8 + jo);
                    if (vt && (!BSPARSE || jo % 6 == 2)) { const int s = jo; RT(0); RTS(); }
                    if (vt) { MT(3); const int s = i & 63; MTRT(); MTS(); }
                }
            }
            __builtin_amdgcn_s_setprio(0);
            VMWAIT();
            BAR();
        }
    }
    if (bid < 32 || bid >= NQT) {
        LANES();
        const int hh = (bid < 32 ? bid : bid - NQT) & 31;
        u32x4 m0[2];
        get_mem0(m0, tid);
        if (lay == 0 || xt) {
            wait_until(tstart + (bid >= NQT ? DQT : DW1));
            load_w<false>(wt, karg<5>(w >> 2), hh, w, 4 * (w & 3), 2, tokL, !slow && (NTM >> (bid < 32 ? 0 : 3) & 1), gap);
            SST(5);
            if (!xt) settle();
        }
        places();
        const bool qt = bid >= NQT;
        const unsigned long long mq = xt ? MXQ : ~MXQ;
        auto nextq = [&](int j) { return min(seg_after(mq, j), NSEG - 1); };
        const int jq0 = qt ? nextq(1) : 0, jq1 = nextq(jq0);
        if (!qt) put_mem0(m0, rwin, mo, mo1, tid);
        if (tid < 4) *(unsigned*)(smem + L_S + tid * 4) = 0u;
        SST(1);
        soon(jq1);
        SST(2);
        const unsigned so1 = hb + (unsigned)(jq1 * 40960 + w * 1024), lo1 = L_H + 40960 + w * 1024;
        stage_h<5, 8192>(rws, hb + (unsigned)(jq0 * 40960 + w * 1024), lo1 - 40960u, lane);
        BAR0();
        stage_h<5, 8192>(rws, so1, lo1, lane);
        check_h<5, 8192, 5>(rws, hb + (unsigned)(jq0 * 40960 + w * 1024), lo1 - 40960u, lane);
        BAR();
        SST(3);
        int qrdy = 0;
        {
            LANES();
            const unsigned to0 = (unsigned)((w ^ 4) >> 2) * 8192u + (unsigned)((w & 3) * 64 + (lane ^ ((fq & 1) * (w < 4 ? 8 : 5)))) * 16u, to1 = to0 + 4096u, hv = lane * 16u;
            const LAS char* c0 = (const LAS char*)(hL + hoff + ((fq ^ (fr & 7)) << 4));
            const LAS char* c1 = (const LAS char*)(hL + hoff + (((4 + fq) ^ (fr & 7)) << 4));
            u32x4 bb[8], g0, g1;
            asm volatile("" : "=v"(g0), "=v"(g1));
            f32x4 tc[2][2];
            auto pack = [&]() {
                g0 = (u32x4){pack2(tc[0][0][0], tc[0][0][1]), pack2(tc[0][0][2], tc[0][0][3]), pack2(tc[1][0][0], tc[1][0][1]), pack2(tc[1][0][2], tc[1][0][3])};
                g1 = (u32x4){pack2(tc[0][1][0], tc[0][1][1]), pack2(tc[0][1][2], tc[0][1][3]), pack2(tc[1][1][0], tc[1][1][1]), pack2(tc[1][1][2], tc[1][1][3])};
            };
            const LAS char* n0 = c0 + 40960; const LAS char* n1 = c1 + 40960;
            if (!qt) {
                project0(bb, c0, c1);
                for (int j = 0; j < 2; ++j) {
                    project<0>(wt, tc, bb, c0, c1, n0, n1, [&]() { if (j == 0) { check_h<5, 8192, 0>(rws, so1, lo1, lane); BAR(); } }, [](auto) {});
                    pack();
                    *(u32x4*)(tokL + j * 16384 + to0) = g0; *(u32x4*)(tokL + j * 16384 + to1) = g1;
                    c0 = n0; c1 = n1;
                    if (j == 0 && w == 7) __builtin_amdgcn_raw_buffer_store_b32(((4u * (unsigned)__builtin_amdgcn_s_memrealtime() + (lay ? DQ0 : DQ00)) & 0xFFFFFFu) | (unsigned)lay << 30, rwin, two(), W_T + hh * 128u, 16);
                }
                project_end(bb);
                VMWAIT();
            } else {
                project0(bb, c0, c1);
                unsigned hs = hb + (unsigned)(jq1 * 40960 + w * 1024), hd = (unsigned)(L_H + 40960 + w * 1024), fv = tid == 0 ? 0u : 0x40000000u;
                const unsigned fq_ = W_FQ + fb + (unsigned)(hh * 128 + xt * 64);
                const int jl = 63 - __builtin_clzll(mq);
                if (w < 4) __builtin_amdgcn_s_setprio(QTP);
                for (int jp = 1, j = jq0, jn = jq1;; jp = j, j = jn, jn = nextq(jn)) {
                    MT(0);
                    project<0>(wt, tc, bb, c0, c1, n0, n1,
                        [&]() {
                            if (__builtin_expect(hrdy <= nextq(jn), 0)) hrdy = min(see_h(g0), nextq(jn) + 17);
                            hs = hrdy > nextq(jn) ? hb + (unsigned)(nextq(jn) * 40960 + w * 1024) : 0x40000000u; hd = (unsigned)(2 * L_H + 40960 + 2 * w * 1024) - hd;
                            unsigned fj = (unsigned)jp;
                            asm volatile("" : "+s"(hs), "+s"(hd));
                            PIN1(fj);
                            MT(1);
                            if (__builtin_expect(j == jq0, 0)) check_h<5, 8192, 0>(rws, so1, lo1, lane);
                            VMWAIT();
                            BAR();
                            MT(2);
                            if (w == 0) __builtin_amdgcn_raw_buffer_store_b32(fj, rwin, fv, fq_, 16);
                        },
                        [&](auto uu) {
                            constexpr int u = decltype(uu)::v;
                            if (u == QEU && __builtin_expect(j <= QE2, 0)) {
                                asm volatile("s_waitcnt vmcnt(3)\n\ts_barrier" ::: "memory");
                                if (w == 0) __builtin_amdgcn_raw_buffer_store_b32((unsigned)jp, rwin, fv, fq_, 16);
                            }
                            if (u == 20 && __builtin_expect(hrdy <= nextq(jn), 0)) g0 = ask_h(rwin, fb, nextq(jn) + 16);
                            if (u == 34 || u == 38 || u == 2 || u == 6 || u == 10) { constexpr int t = ((u + 6) % 40) / 4; dma1k(rws, hv, hs + t * 8192u, hd + t * 8192u); }
                        });
                    pack();
                    const unsigned qs = O_QK + (unsigned)(j * H_ + hh) * 16384u;
                    st128(g0, rws, to0, qs, 16);
                    st128(g1, rws, to1, qs, 16);
                    { const LAS char* x = c0; c0 = n0; n0 = x; x = c1; c1 = n1; n1 = x; }
                    if (__builtin_expect(j <= QEARLY || j == jl, 0)) {
                        VMWAIT();
                        BAR0();
                        if (tid == 0) __builtin_amdgcn_raw_buffer_store_b32((unsigned)j, rwin, 0u, fq_, 16);
                        if (j == jl) break;
                    }
                    if (__builtin_expect(hrdy <= nextq(jn), 0)) {
                        wait_h(rws, rwin, fb, nextq(jn), hrdy);
                        hs = hb + (unsigned)(nextq(jn) * 40960 + w * 1024);
                        dma1k(rws, hv, hs, hd); dma1k(rws, hv, hs + 8192u, hd + 8192u);
                    }
                    MT(3);
                    if (j < 8) SST(8 + j);
                    if (!BSPARSE || j % 6 == 2) { const int s = j; RT(0); RTS(); MTRT(); MTS(); }
                }
                project_end(bb);
                VMWAIT();
                BAR();
            }
        }
        SST(4);
        auto steps = [&](auto cc) {
        constexpr int C = decltype(cc)::v >> 1, R = decltype(cc)::v & 1;
        LANES();
        const LAS char* ka = (const LAS char*)(smem + L_K + (fr & 1) * TQS + (4 * fq + (fr >> 2)) * 8);
        const LAS char* qa = (const LAS char*)(smem + L_Q + (fr & 1) * 544 + ((fr >> 1) & 1) * 1152 + (16 * fq + 2 * (fr >> 2)) * 4);
        LAS char* qw = (LAS char*)(C < 2 ? smem + L_Q + (fq & 1) * 544 + (fq >> 1) * 1152 + (32 * w + 8 * (fr >> 2) + bs2(fr & 3)) * 4
                                         : smem + L_K + (fq >> 1) * TQS + (32 * (w & 3) + fr) * 8 + (fq & 1) * 4);
        const LAS char* mp0 = (const LAS char*)(memL + ma0);
        const int z = 16 * (fr >> 3) + 4 * (fr & 3) + 2 * ((fr >> 2) & 1);
        const int ko = ((z >> 4) * 256 + fq * 16 + ((z & 15) ^ ((fq & 1) * 5))) * 16;
        unsigned pa0 = (4u * C + 2u * (hh >> 4)) * PAIR + (unsigned)((hh & 15) * 512 + R * 256 + lane * 4);
        unsigned pa2 = ((8u + (fr >> 3)) * PAIR + C * 4096u + (unsigned)((hh >> 1) * 256 + R * 128 + (hh & 1) * 64 + fq * 16 + (fr & 6) * 2)) | (fr & 1) << 30;
        PIN2(ka, qw); PIN1(mp0);
        constexpr bool TK = C == 2 && R == 1;
        Tk tk = {0u, 0u, 0x40000000u, nullptr};
        if (TK) { tk = tk_init(); __builtin_amdgcn_s_setprio(TKP); }
        if (C < 2) { PIN2(qa, pa0); PIN1(pa2); }
        unsigned fqv = w == 4 ? (lane & 1) * 64u : 0x40000000u;
        if (C == 2 && !TK) PIN1(fqv);
        int qf = 1, tbo = 0;
        for (int s = 0; s < NSEG; ++s) {
            RT(0);
            const char* tb = tokL + tbo;
            unsigned pb = __builtin_amdgcn_readfirstlane(s + 1 < NSEG ? W_P + (unsigned)(s & 3) * 10u * PAIR + nh : 0x40000000u);
            unsigned pb1 = pb + PAIR;
            asm volatile("" : "+s"(pb), "+s"(pb1));
            if (s == 0) BAR();
            u32x8 kx[2][2];
            auto keys = [&]() {
#pragma unroll
                for (int u = 0; u < 4; ++u) kx[u >> 1][u & 1] = cat8(*(const u32x4*)(tb + (ko ^ ((u >> 1) * 16)) + (u & 1) * 2048), *(const u32x4*)(tb + (ko ^ ((u >> 1) * 16)) + (u & 1) * 2048 + 1024));
                PIN2(kx[0][0], kx[0][1]); PIN2(kx[1][0], kx[1][1]);
            };
            if (C < 2) keys();
            bool redo = false;
            unsigned sf = 0;
            do {
                unsigned lim = s > 0 ? poll_lim(budget) : ~0u;
                unsigned long long sus;
                f32x4 sc;
                if (C < 2) {
                    f32x4 s0, s1, s2;
                    unsigned late;
                    asm volatile(
                        ".irp r,232,234,236,238,240,242,244,246,248,250\n\tv_mov_b64 v[\\r:\\r+1], 0\n\t.endr\n\t"
                        "s_cmp_lg_u32 %[p0], 2.0\n\t"
                        "s_cbranch_scc1 1f\n\t"
                        "s_mov_b32 %[lt], -1\n\ts_barrier\n\ts_barrier\n\ts_barrier\n\ts_branch 2f\n"
                        AL "1:\n\t"
                        "s_setprio 2\n\t"
                        "s_waitcnt lgkmcnt(0)\n\t"
                        "s_barrier\n\t"
                        MTA(0)
                        "ds_read_b128 v[208:211], %[mp] offset:0\n\t"
                        "ds_read_b128 v[212:215], %[mp] offset:1024\n\t"
                        "ds_read_b128 v[216:219], %[mp] offset:2048\n\t"
                        "s_barrier\n\t"
                        "ds_read_b128 v[220:223], %[mp] offset:3072\n\t"
                        "s_waitcnt lgkmcnt(3)\n\t"
                        "v_smfmac_f32_16x16x64_bf16 v[232:235], v[208:211], %[w00], %[ix]\n\t"
                        "v_smfmac_f32_16x16x64_bf16 v[236:239], v[208:211], %[w10], %[ix]\n\t"
                        "ds_read_b128 v[224:227], %[mp] offset:4096\n\t"
                        "s_waitcnt lgkmcnt(3)\n\t"
                        "v_smfmac_f32_16x16x64_bf16 v[232:235], v[212:215], %[w01], %[ix]\n\t"
                        "v_smfmac_f32_16x16x64_bf16 v[236:239], v[212:215], %[w11], %[ix]\n\t"
                        "s_barrier\n\t"
                        MTA(1)
                        "ds_read_b128 v[228:231], %[mp] offset:5120\n\t"
                        "s_waitcnt lgkmcnt(3)\n\t"
                        "v_smfmac_f32_16x16x64_bf16 v[232:235], v[216:219], %[w02], %[ix]\n\t"
                        "v_smfmac_f32_16x16x64_bf16 v[236:239], v[216:219], %[w12], %[ix]\n\t"
                        "ds_read_b128 v[208:211], %[mp] offset:6144\n\t"
                        "s_waitcnt lgkmcnt(3)\n\t"
                        "v_smfmac_f32_16x16x64_bf16 v[232:235], v[220:223], %[w03], %[ix]\n\t"
                        "v_smfmac_f32_16x16x64_bf16 v[236:239], v[220:223], %[w13], %[ix]\n\t"
                        "ds_read_b128 v[212:215], %[mp] offset:7168\n\t"
                        "s_waitcnt lgkmcnt(3)\n\t"
                        "v_smfmac_f32_16x16x64_bf16 v[232:235], v[224:227], %[w04], %[ix]\n\t"
                        "v_smfmac_f32_16x16x64_bf16 v[236:239], v[224:227], %[w14], %[ix]\n\t"
                        "ds_read_b128 v[216:219], %[mp] offset:8192\n\t"
                        "s_waitcnt lgkmcnt(3)\n\t"
                        "v_smfmac_f32_16x16x64_bf16 v[232:235], v[228:231], %[w05], %[ix]\n\t"
                        "v_smfmac_f32_16x16x64_bf16 v[236:239], v[228:231], %[w15], %[ix]\n\t"
                        "ds_read_b128 v[220:223], %[mp] offset:9216\n\t"
                        "s_waitcnt lgkmcnt(3)\n\t"
                        "v_smfmac_f32_16x16x64_bf16 v[232:235], v[208:211], %[w06], %[ix]\n\t"
                        "v_smfmac_f32_16x16x64_bf16 v[236:239], v[208:211], %[w16], %[ix]\n\t"
                        "ds_read_b32 v207, v250 offset:%[ls]\n\t"
                        "s_waitcnt lgkmcnt(3)\n\t"
                        "v_smfmac_f32_16x16x64_bf16 v[232:235], v[212:215], %[w07], %[ix]\n\t"
                        "v_smfmac_f32_16x16x64_bf16 v[236:239], v[212:215], %[w17], %[ix]\n\t"
                        "s_waitcnt lgkmcnt(0)\n\t"
                        "v_smfmac_f32_16x16x64_bf16 v[232:235], v[216:219], %[w08], %[ix]\n\t"
                        "v_readfirstlane_b32 %[lt], v207\n\t"
                        "v_smfmac_f32_16x16x64_bf16 v[232:235], v[220:223], %[w09], %[ix]\n\t"
                        "v_smfmac_f32_16x16x64_bf16 v[236:239], v[216:219], %[w18], %[ix]\n\t"
                        "v_smfmac_f32_16x16x64_bf16 v[236:239], v[220:223], %[w19], %[ix]\n\t"
                        "s_setprio " STR(QP) "\n\t"
                        "s_nop 5\n\t"
                        "v_add_f32 v232, v232, v234\n\t"
                        "v_add_f32 v233, v233, v235\n\t"
                        "v_add_f32 v236, v236, v238\n\t"
                        "v_add_f32 v237, v237, v239\n\t"
                        "v_cvt_pk_bf16_f32 v232, v232, v233\n\t"
                        "v_cvt_pk_bf16_f32 v236, v236, v237\n\t"
                        "ds_write2_b32 %[qw], v232, v236 offset1:4\n\t"
                        MTA(2)
                        "2:"
                        : [lt] "=&s"(late), "=&{v[240:243]}"(s0), "=&{v[244:247]}"(s1), "=&{v[248:251]}"(s2)
                        : [mp] "v"(mp0), [qw] "v"(qw), [ix] "v"(sidx), [ls] "n"(L_S), [p0] "s"(pb), [w00] "v"(wt[0][0]), [w10] "v"(wt[1][0]), [w01] "v"(wt[0][1]), [w11] "v"(wt[1][1]), [w02] "v"(wt[0][2]), [w12] "v"(wt[1][2]), [w03] "v"(wt[0][3]), [w13] "v"(wt[1][3]), [w04] "v"(wt[0][4]), [w14] "v"(wt[1][4]), [w05] "v"(wt[0][5]), [w15] "v"(wt[1][5]), [w06] "v"(wt[0][6]), [w16] "v"(wt[1][6]), [w07] "v"(wt[0][7]), [w17] "v"(wt[1][7]), [w08] "v"(wt[0][8]), [w18] "v"(wt[1][8]), [w09] "v"(wt[0][9]), [w19] "v"(wt[1][9])
                        : "v207", "v208", "v209", "v210", "v211", "v212", "v213", "v214", "v215", "v216", "v217", "v218", "v219", "v220", "v221", "v222", "v223", "v224", "v225", "v226", "v227", "v228", "v229", "v230", "v231", "v232", "v233", "v234", "v235", "v236", "v237", "v238", "v239", "scc", "memory");
                    if (__builtin_expect(late == (unsigned)s, 0)) keys();
#define TAIL(A0, A2, B0, B2, C0, C2) \
                        "s_cmp_lg_u32 %[p0], 2.0\n\ts_cbranch_scc1 1f\n\ts_barrier\n\ts_barrier\n\t" \
                        "ds_read_b64_tr_b16 v[232:233], %[ka]\n\ts_waitcnt lgkmcnt(0)\n\tv_lshlrev_b32 v232, 16, v232\n\tv_cmp_u_f32 vcc, v232, v232\n\ts_branch 3f\n\t" AL "1:\n\t" \
                        ".if " STR(BQN) "==0\n\t" \
                        "s_setprio 3\n\t" \
                        "s_waitcnt vmcnt(0) lgkmcnt(0)\n\t" \
                        "s_barrier\n\t" \
                        MTA(3) \
                        "ds_read_b64_tr_b16 v[208:209], %[qa]\n\t" \
                        "ds_read_b64_tr_b16 v[210:211], %[qa] offset:32\n\t" \
                        "ds_read_b64_tr_b16 v[212:213], %[qa] offset:256\n\t" \
                        "ds_read_b64_tr_b16 v[214:215], %[qa] offset:288\n\t" \
                        "ds_read_b64_tr_b16 v[216:217], %[ka] offset:0\n\t" \
                        "ds_read_b64_tr_b16 v[218:219], %[ka] offset:128\n\t" \
                        "ds_read_b64_tr_b16 v[220:221], %[ka] offset:256\n\t" \
                        "ds_read_b64_tr_b16 v[222:223], %[ka] offset:384\n\t" \
                        "ds_read_b64_tr_b16 v[224:225], %[ka] offset:512\n\t" \
                        "ds_read_b64_tr_b16 v[226:227], %[ka] offset:640\n\t" \
                        "s_waitcnt lgkmcnt(8)\n\t" \
                        "v_smfmac_f32_16x16x64_bf16 v[240:243], v[208:211], %[k00], %[ix]\n\t" \
                        "ds_read_b64_tr_b16 v[228:229], %[ka] offset:768\n\t" \
                        "s_waitcnt lgkmcnt(7)\n\t" \
                        "v_smfmac_f32_16x16x64_bf16 v[240:243], v[212:215], %[k01], %[ix]\n\t" \
                        "ds_read_b64_tr_b16 v[230:231], %[ka] offset:896\n\t" \
                        "v_smfmac_f32_16x16x64_bf16 v[244:247], v[208:211], %[k10], %[ix]\n\t" \
                        "v_smfmac_f32_16x16x64_bf16 v[244:247], v[212:215], %[k11], %[ix]\n\t" \
                        "s_nop 4\n\t" \
                        ".elseif " STR(TPRE) "==0\n\t" \
                        "s_waitcnt lgkmcnt(0)\n\t" \
                        "s_barrier\n\t" \
                        "ds_read_b64_tr_b16 v[208:209], %[qa]\n\t" \
                        "ds_read_b64_tr_b16 v[210:211], %[qa] offset:32\n\t" \
                        "ds_read_b64_tr_b16 v[212:213], %[qa] offset:256\n\t" \
                        "ds_read_b64_tr_b16 v[214:215], %[qa] offset:288\n\t" \
                        "s_setprio 3\n\t" \
                        "s_waitcnt vmcnt(0) lgkmcnt(0)\n\t" \
                        "s_barrier\n\t" \
                        MTA(3) \
                        "ds_read_b64_tr_b16 v[216:217], %[ka] offset:0\n\t" \
                        "ds_read_b64_tr_b16 v[218:219], %[ka] offset:128\n\t" \
                        "ds_read_b64_tr_b16 v[220:221], %[ka] offset:256\n\t" \
                        "ds_read_b64_tr_b16 v[222:223], %[ka] offset:384\n\t" \
                        "v_smfmac_f32_16x16x64_bf16 v[240:243], v[208:211], %[k00], %[ix]\n\t" \
                        "ds_read_b64_tr_b16 v[224:225], %[ka] offset:512\n\t" \
                        "ds_read_b64_tr_b16 v[226:227], %[ka] offset:640\n\t" \
                        "v_smfmac_f32_16x16x64_bf16 v[240:243], v[212:215], %[k01], %[ix]\n\t" \
                        "ds_read_b64_tr_b16 v[228:229], %[ka] offset:768\n\t" \
                        "ds_read_b64_tr_b16 v[230:231], %[ka] offset:896\n\t" \
                        "v_smfmac_f32_16x16x64_bf16 v[244:247], v[208:211], %[k10], %[ix]\n\t" \
                        "v_smfmac_f32_16x16x64_bf16 v[244:247], v[212:215], %[k11], %[ix]\n\t" \
                        "s_nop 3\n\t" \
                        ".else\n\t" \
                        "s_waitcnt lgkmcnt(0)\n\t" \
                        "s_barrier\n\t" \
                        "ds_read_b64_tr_b16 v[208:209], %[qa]\n\t" \
                        "ds_read_b64_tr_b16 v[210:211], %[qa] offset:32\n\t" \
                        "ds_read_b64_tr_b16 v[212:213], %[qa] offset:256\n\t" \
                        "ds_read_b64_tr_b16 v[214:215], %[qa] offset:288\n\t" \
                        "s_setprio 0\n\t" \
                        "s_waitcnt vmcnt(0) lgkmcnt(0)\n\t" \
                        "v_smfmac_f32_16x16x64_bf16 v[240:243], v[208:211], %[k00], %[ix]\n\t" \
                        "v_smfmac_f32_16x16x64_bf16 v[240:243], v[212:215], %[k01], %[ix]\n\t" \
                        "s_setprio 3\n\t" \
                        "s_barrier\n\t" \
                        MTA(3) \
                        "ds_read_b64_tr_b16 v[216:217], %[ka] offset:0\n\t" \
                        "ds_read_b64_tr_b16 v[218:219], %[ka] offset:128\n\t" \
                        "ds_read_b64_tr_b16 v[220:221], %[ka] offset:256\n\t" \
                        "ds_read_b64_tr_b16 v[222:223], %[ka] offset:384\n\t" \
                        "v_smfmac_f32_16x16x64_bf16 v[244:247], v[208:211], %[k10], %[ix]\n\t" \
                        "ds_read_b64_tr_b16 v[224:225], %[ka] offset:512\n\t" \
                        "ds_read_b64_tr_b16 v[226:227], %[ka] offset:640\n\t" \
                        "v_smfmac_f32_16x16x64_bf16 v[244:247], v[212:215], %[k11], %[ix]\n\t" \
                        "ds_read_b64_tr_b16 v[228:229], %[ka] offset:768\n\t" \
                        "ds_read_b64_tr_b16 v[230:231], %[ka] offset:896\n\t" \
                        ".endif\n\t" \
                        "v_add_f32 v232, " A0 ", " A2 "\n\t" \
                        "v_mul_f32 v236, 0x3e0293ee, v232\n\t" \
                        "v_exp_f32 v236, v236\n\t" \
                        "s_waitcnt lgkmcnt(4)\n\t" \
                        "v_smfmac_f32_16x16x64_bf16 v[248:251], v[208:211], v[216:223], %[ix]\n\t" \
                        "s_waitcnt lgkmcnt(0)\n\t" \
                        "v_smfmac_f32_16x16x64_bf16 v[248:251], v[212:215], v[224:231], %[ix]\n\t" \
                        "v_add_f32 v233, " B0 ", " B2 "\n\t" \
                        "v_mul_f32 v237, 0x3e0293ee, v233\n\t" \
                        "v_exp_f32 v237, v237\n\t" \
                        "s_nop 0\n\t" \
                        "v_add_f32 v239, v236, v237\n\t" \
                        "s_nop 1\n\t" \
                        "v_add_f32_dpp v239, v239, v239 row_ror:8 row_mask:0xf bank_mask:0xf\n\t" \
                        MTA(4) \
                        "v_add_f32 v234, " C0 ", " C2 "\n\t" \
                        "v_mul_f32 v238, 0x3e0293ee, v234\n\t" \
                        "v_exp_f32 v238, v238\n\t" \
                        "s_nop 0\n\t" \
                        "v_add_f32 v239, v239, v238\n\t" \
                        "v_mov_b32_dpp v235, v238 quad_perm:[1,1,3,3] row_mask:0xf bank_mask:0xf\n\t" \
                        "s_nop 0\n\t" \
                        "v_add_f32_dpp v239, v239, v239 row_ror:4 row_mask:0xf bank_mask:0xf\n\t" \
                        "s_nop 1\n\t" \
                        "v_add_f32_dpp v239, v239, v239 row_ror:2 row_mask:0xf bank_mask:0xf\n\t" \
                        "s_nop 1\n\t" \
                        "v_add_f32_dpp v239, v239, v239 row_ror:1 row_mask:0xf bank_mask:0xf\n\t" \
                        "v_rcp_f32 v207, v239\n\t" \
                        "v_add_u32 v239, 0xe8800000, v239\n\t" \
                        "v_mul_f32 v236, v236, v207\n\t" \
                        "v_cmp_lt_u32 vcc, %[rg], v239\n\t" \
                        "v_mul_f32 v237, v237, v207\n\t" \
                        "v_cvt_pk_bf16_f32 v236, v236, v237\n\t" \
                        "s_cbranch_vccnz 3f\n\t" \
                        MTA(5) \
                        "buffer_store_dword v236, %[pa], %[rw], %[p0] offen sc1\n\t" \
                        "v_mul_f32 v238, v238, v207\n\t" \
                        "v_mul_f32 v235, v235, v207\n\t" \
                        "buffer_store_dword v236, %[pa], %[rw], %[p1] offen sc1\n\t" \
                        "v_cvt_pk_bf16_f32 v238, v238, v235\n\t" \
                        "buffer_store_dword v238, %[pm], %[rw], %[p0] offen sc1\n\t" \
                        "3:\n\t" \
                        "s_mov_b64 %[sus], vcc\n\t" \
                        "s_setprio 0\n\t"
#define TAILIO : [sus] "=&s"(sus), "=&{v[232:235]}"(sc), "+{v[240:243]}"(s0), "+{v[244:247]}"(s1), "+{v[248:251]}"(s2) \
                        : [qa] "v"(qa), [ka] "v"(ka), [ix] "v"(sidx), [pa] "v"(pa0), [pm] "v"(pa2), [rw] "s"(rwin), [p0] "s"(pb), [p1] "s"(pb1), [rg] "s"(SRG), \
                          [k00] "v"(kx[0][0]), [k01] "v"(kx[0][1]), [k10] "v"(kx[1][0]), [k11] "v"(kx[1][1]) \
                        : "v207", "v208", "v209", "v210", "v211", "v212", "v213", "v214", "v215", "v216", "v217", "v218", "v219", "v220", "v221", "v222", "v223", "v224", "v225", "v226", "v227", "v228", "v229", "v230", "v231", "v236", "v237", "v238", "v239", "vcc", "scc", "memory"
                    if (R == 0) asm volatile(TAIL("v240", "v242", "v244", "v246", "v248", "v250") TAILIO);
                    else asm volatile(TAIL("v241", "v243", "v245", "v247", "v249", "v251") TAILIO);
                    RT(4);
                    MT(6);
                } else {
                    f32x8 a01;
                    asm volatile("v_mov_b64 v[200:201], 0\n\tv_mov_b64 v[202:203], 0\n\tv_mov_b64 v[204:205], 0\n\tv_mov_b64 v[206:207], 0" : "={v[200:207]}"(a01));
                    unsigned sr = TK ? tro + (unsigned)(s & 3) * 2u * PAIR : qrdy < min(s + 3, NSEG - 1) ? W_FQ + fb + (unsigned)hh * 128u : 0x40000000u;
                    unsigned tb = 0, tg = (unsigned)(s + 64 * lay) << 24, so = s > 0 ? W_T + (unsigned)(s & 3) * 2u * PAIR + (unsigned)hh * 128u : 0x40000000u;
                    asm volatile("" : "+s"(lim), "+s"(sr));
                    if (TK) { asm volatile("" : "+s"(tg), "+s"(so)); tb = fetch(tk, s, !redo); }
                    else asm volatile(AL "s_waitcnt lgkmcnt(0)\n\ts_barrier\n\ts_barrier\n\ts_waitcnt vmcnt(0)\n\ts_barrier" ::: "memory");
                    asm volatile(
                        ".if %[tk]\n\t"
                        "s_waitcnt vmcnt(0)\n\t"
                        "s_memtime s[100:101]\n\t"
                        "s_barrier\n\t"
                        "s_setprio 1\n\t"
                        "s_waitcnt lgkmcnt(0)\n\t"
                        "ds_read2st64_b32 v[208:209], %[sp] offset1:16\n\t"
                        "ds_read_b32 v210, %[sp] offset:6144\n\t"
                        "ds_read_b128 v[212:215], %[mp] offset:0\n\t"
                        "ds_read_b128 v[216:219], %[mp] offset:1024\n\t"
                        "ds_read_b128 v[220:223], %[mp] offset:2048\n\t"
                        "ds_read_b128 v[224:227], %[mp] offset:3072\n\t"
                        "s_sub_u32 %[tb], s100, %[tb]\n\t"
                        "s_mul_i32 %[tb], %[tb], %[qc]\n\t"
                        "s_lshr_b32 %[tb], %[tb], 16\n\t"
                        "s_add_u32 %[tb], %[tb], %[ta]\n\t"
                        "s_and_b32 %[tb], %[tb], 0xffffff\n\t"
                        "s_or_b32 %[tb], %[tb], %[tg]\n\t"
                        "v_mov_b32 v211, %[tb]\n\t"
                        "s_waitcnt lgkmcnt(4)\n\t"
                        "v_max3_u32 v208, v208, v209, v210\n\t"
                        "v_cmp_lt_u32 vcc, %[lim], v208\n\t"
                        "ds_read_b128 v[228:231], %[mp] offset:4096\n\t"
                        "s_cmp_eq_u64 vcc, 0\n\t"
                        "s_cselect_b32 %[so], %[so], 2.0\n\t"
                        "buffer_store_dword v211, %[tw], %[rw], %[so] offen sc1\n\t"
                        "ds_read_b128 v[232:235], %[mp] offset:5120\n\t"
                        ".else\n\t"
                        "s_setprio 1\n\t"
                        "ds_read_b128 v[212:215], %[mp] offset:0\n\t"
                        "ds_read_b128 v[216:219], %[mp] offset:1024\n\t"
                        "ds_read_b128 v[220:223], %[mp] offset:2048\n\t"
                        "ds_read_b128 v[224:227], %[mp] offset:3072\n\t"
                        "ds_read_b128 v[228:231], %[mp] offset:4096\n\t"
                        "ds_read_b128 v[232:235], %[mp] offset:5120\n\t"
                        ".endif\n\t"
                        ".if !%[tk]\n\tbuffer_load_dword %[sf], %[sl], %[rw], %[sr] offen sc1\n\t.endif\n\t"
                        MTA(4)
                        "s_waitcnt lgkmcnt(5)\n\t"
                        "v_smfmac_f32_16x16x64_bf16 v[200:203], v[212:215], %[w00], %[ix]\n\t"
                        "ds_read_b128 v[236:239], %[mp] offset:6144\n\t"
                        "v_smfmac_f32_16x16x64_bf16 v[204:207], v[212:215], %[w10], %[ix]\n\t"
                        "ds_read_b128 v[240:243], %[mp] offset:7168\n\t"
                        "s_waitcnt lgkmcnt(6)\n\t"
                        "v_smfmac_f32_16x16x64_bf16 v[200:203], v[216:219], %[w01], %[ix]\n\t"
                        "ds_read_b128 v[244:247], %[mp] offset:8192\n\t"
                        "v_smfmac_f32_16x16x64_bf16 v[204:207], v[216:219], %[w11], %[ix]\n\t"
                        "ds_read_b128 v[248:251], %[mp] offset:9216\n\t"
                        "s_waitcnt lgkmcnt(4)\n\t"
                        "v_smfmac_f32_16x16x64_bf16 v[200:203], v[220:223], %[w02], %[ix]\n\t"
                        "v_smfmac_f32_16x16x64_bf16 v[204:207], v[220:223], %[w12], %[ix]\n\t"
                        "v_smfmac_f32_16x16x64_bf16 v[200:203], v[224:227], %[w03], %[ix]\n\t"
                        "v_smfmac_f32_16x16x64_bf16 v[204:207], v[224:227], %[w13], %[ix]\n\t"
                        "v_smfmac_f32_16x16x64_bf16 v[200:203], v[228:231], %[w04], %[ix]\n\t"
                        "v_smfmac_f32_16x16x64_bf16 v[204:207], v[228:231], %[w14], %[ix]\n\t"
                        "v_smfmac_f32_16x16x64_bf16 v[200:203], v[232:235], %[w05], %[ix]\n\t"
                        "v_smfmac_f32_16x16x64_bf16 v[204:207], v[232:235], %[w15], %[ix]\n\t"
                        "s_waitcnt lgkmcnt(0)\n\t"
                        "v_smfmac_f32_16x16x64_bf16 v[200:203], v[236:239], %[w06], %[ix]\n\t"
                        "v_smfmac_f32_16x16x64_bf16 v[204:207], v[236:239], %[w16], %[ix]\n\t"
                        BQ(3)
                        "v_smfmac_f32_16x16x64_bf16 v[200:203], v[240:243], %[w07], %[ix]\n\t"
                        "v_smfmac_f32_16x16x64_bf16 v[204:207], v[240:243], %[w17], %[ix]\n\t"
                        BQ(5)
                        "v_smfmac_f32_16x16x64_bf16 v[200:203], v[244:247], %[w08], %[ix]\n\t"
                        "v_smfmac_f32_16x16x64_bf16 v[200:203], v[248:251], %[w09], %[ix]\n\t"
                        BQ(7)
                        "v_smfmac_f32_16x16x64_bf16 v[204:207], v[244:247], %[w18], %[ix]\n\t"
                        "v_smfmac_f32_16x16x64_bf16 v[204:207], v[248:251], %[w19], %[ix]\n\t"
                        BQ(9)
                        "s_nop 5\n\t"
                        "v_pk_add_f32 v[200:201], v[200:201], v[202:203]\n\t"
                        "v_cvt_pk_bf16_f32 v200, v200, v201\n\t"
                        "v_pk_add_f32 v[204:205], v[204:205], v[206:207]\n\t"
                        "v_cvt_pk_bf16_f32 v201, v204, v205\n\t"
                        "ds_write2_b32 %[qw], v200, v201 offset1:32\n\t"
                        ".if %[tk]\n\tbuffer_load_dword %[sf], %[sl], %[rw], %[sr] offen sc1\n\t.endif\n\t"
                        "v_cmp_u_f32 %[sus], v204, v205\n\t"
                        MTA(2)
                        "s_waitcnt lgkmcnt(0)\n\t"
                        "s_barrier\n\t"
                        "s_setprio 0\n\t"
                        ".if %[tk]\n\ts_cmp_lg_u64 %[sus], 0\n\ts_cbranch_scc1 1f\n\t.endif\n\ts_waitcnt vmcnt(0)\n1:"
                        : [sus] "=&s"(sus), [sf] "+&v"(sf), [tb] "+&s"(tb), [so] "+&s"(so), "+{v[200:207]}"(a01)
                        : [mp] "v"(mp0), [qw] "v"(qw), [ix] "v"(sidx), [sl] "v"(TK ? tk.sl : fqv), [tk] "n"(TK ? 1 : 0), [sr] "s"(sr), [rw] "s"(rwin),
                          [sp] "v"(TK ? tk.sp : mp0), [lim] "s"(lim), [qc] "s"(ck.qpc), [ta] "s"(ck.ta), [tg] "s"(tg), [tw] "v"(TK ? tk.tw : fqv), [w00] "v"(wt[0][0]), [w10] "v"(wt[1][0]), [w01] "v"(wt[0][1]), [w11] "v"(wt[1][1]), [w02] "v"(wt[0][2]), [w12] "v"(wt[1][2]), [w03] "v"(wt[0][3]), [w13] "v"(wt[1][3]), [w04] "v"(wt[0][4]), [w14] "v"(wt[1][4]), [w05] "v"(wt[0][5]), [w15] "v"(wt[1][5]), [w06] "v"(wt[0][6]), [w16] "v"(wt[1][6]), [w07] "v"(wt[0][7]), [w17] "v"(wt[1][7]), [w08] "v"(wt[0][8]), [w18] "v"(wt[1][8]), [w09] "v"(wt[0][9]), [w19] "v"(wt[1][9])
                        : "v208", "v209", "v210", "v211", "v212", "v213", "v214", "v215", "v216", "v217", "v218", "v219", "v220", "v221", "v222", "v223", "v224", "v225", "v226", "v227", "v228", "v229", "v230", "v231", "v232", "v233", "v234", "v235", "v236", "v237", "v238", "v239", "v240", "v241", "v242", "v243", "v244", "v245", "v246", "v247", "v248", "v249", "v250", "v251", "vcc", "scc", "s100", "s101", "memory");
                    MT(3);
                }
                redo = false;
                if (__builtin_expect(sus != 0, 0)) {
                    redo = again(TK, tk.vo, sf, s, lim, mp0, tk.sp);
                    if (!redo && C < 2) {
                        const float c = 0.08838834764831845f * 1.44269504088896340736f;
                        float mx = fmx(fmx(sc[0], sc[1]), sc[2]);
                        mx = fmx(mx, dppf<0x128>(mx)); mx = fmx(mx, dppf<0x124>(mx)); mx = fmx(mx, dppf<0x122>(mx)); mx = fmx(mx, dppf<0x121>(mx));
                        const float e0 = __builtin_amdgcn_exp2f((sc[0] - mx) * c), e1 = __builtin_amdgcn_exp2f((sc[1] - mx) * c), e2 = __builtin_amdgcn_exp2f((sc[2] - mx) * c);
                        float d = e0 + e1;
                        d += dppf<0x128>(d); d += e2; d += dppf<0x124>(d); d += dppf<0x122>(d); d += dppf<0x121>(d);
                        d = __builtin_amdgcn_rcpf(d);
                        const unsigned p0 = nosent1(pack2(e0 * d, e1 * d)), p1 = nosent1(pack2(e2 * d, dppf<0xF5>(e2) * d));
                        __builtin_amdgcn_raw_buffer_store_b32(p0, rwin, pa0, pb, 16); __builtin_amdgcn_raw_buffer_store_b32(p0, rwin, pa0, pb1, 16);
                        __builtin_amdgcn_raw_buffer_store_b32(p1, rwin, pa2, pb, 16);
                    }
                }
            } while (__builtin_expect(redo, 0));
            if (__builtin_expect(mb != 0, 0)) { mp0 -= mb; tk.sp -= mb; mb = 0; }
            if (s < 8) SST(8 + s);
            if (s >= 56) SST(s - 40);
            if (C + R == 1) {
                {
                    __builtin_amdgcn_s_sleep(DDMA);
                    unsigned l2 = fresh_lane(), s1;
                    asm volatile("v_mov_b32 %0, -1" : "=v"(s1));
                    const unsigned base = W_P + (unsigned)((s - 1) & 3) * 10u * PAIR + nh;
                    asm volatile("s_waitcnt vmcnt(3)" ::: "memory");
                    st128(((u32x4){s1, s1, s1, s1}), rwin, (l2 >> 5) * PAIR + (l2 & 31u) * 16u, base + (unsigned)(R * 4 + 2 * (hh >> 4)) * PAIR + (unsigned)(hh & 15) * 512u, 16);
                    if (R) __builtin_amdgcn_raw_buffer_store_b64((u32x2){s1, s1}, rwin, (l2 >> 5) * PAIR + (l2 & 16u) * 256u + (l2 & 8u) * 16u + (l2 & 7u) * 8u, base + 8u * PAIR + (unsigned)((hh >> 1) * 256 + (hh & 1) * 64), 16);
                    else __builtin_amdgcn_raw_buffer_store_b32(s1, rwin, two(), W_T + (unsigned)((s + 2) & 3) * 2u * PAIR + (unsigned)hh * 128u, 16);
                }
            } else if (TK) {
                if (s + 1 < NSEG) sched2(tk, s, sf);
            } else if (C == 2 && w == 4) {
                qrdy = max(qrdy, seg_done(MXQ, __builtin_amdgcn_readlane(sf, 0), __builtin_amdgcn_readlane(sf, 1)));
                if (__builtin_expect(qf <= s, 0) && s + 1 < NSEG) {
                    for (unsigned spins = 0; qrdy <= s;) {
                        asm volatile("" ::: "memory");
                        const int f = (int)__builtin_amdgcn_raw_buffer_load_b32(rwin, fqv, W_FQ + fb + (unsigned)hh * 128u, 16);
                        qrdy = seg_done(MXQ, __builtin_amdgcn_readlane(f, 0), __builtin_amdgcn_readlane(f, 1));
                        if (qrdy > s) break;
                        FC(fc2);
                        if (spin_fail(spins, rws)) { qrdy = NSEG; break; }
                        __builtin_amdgcn_s_sleep(2);
                    }
                    *(volatile LAS int*)(smem + L_S) = s + 1;
                }
                while (qf < min(min(s + 2, qrdy), NSEG - 1)) {
                    ++qf;
                    const unsigned so = O_QK + (unsigned)((qf * H_ + hh) * 16384), lo = (unsigned)(L_TOK + qf % 3 * 16384), l2 = fresh_lane();
#pragma unroll
                    for (int i = 0; i < 4; ++i) dma4k(rws, l2 * 16u, so + i * 4096u, lo + i * 4096u);
                }
            } else {
                if (C == 2 && s + 1 < NSEG) __builtin_amdgcn_s_sleep(DPT);
                const float scale = 0.08838834764831845f, l2e = 1.44269504088896340736f * (NORND ? scale : 1.f);
                const int tq = (w >> 1) & 1, qh = w & 1;
                f32x4 sc[3] = {z4, z4, z4};
                u32x4 kt[2][4], qf[4], kf[4];
                const unsigned l2 = fresh_lane(), kto = ((l2 & 8u) * 512u + (l2 & 51u) * 16u + (l2 & 4u) * 32u) ^ ((l2 & 16u) * 5u), qto = ((l2 & 55u) * 16u + qh * 128u) ^ ((l2 & 16u) * 8u);
#pragma unroll
                for (int ks = 0; ks < 4; ++ks) {
                    qf[ks] = *(const u32x4*)(tb + 8192 + (tq * 4 + ks) * 1024 + qto);
                    kt[0][ks] = *(const u32x4*)(tb + ks * 1024 + kto);
                    kt[1][ks] = *(const u32x4*)(tb + ks * 1024 + (kto ^ 64u));
                    SB();
                }
#pragma unroll
                for (int ks = 0; ks < 4; ++ks) {
                    sc[0] = mfma32(kt[0][ks], qf[ks], sc[0]); PIN1(sc[0]);
                    kf[ks] = ldtr2(ka + ks * 256, ka + ks * 256 + 128);
                    SB();
                    sc[1] = mfma32(kt[1][ks], qf[ks], sc[1]); PIN1(sc[1]);
                }
#pragma unroll
                for (int ks = 0; ks < 4; ++ks) sc[2] = mfma32(kf[ks], qf[ks], sc[2]);
                const bool hi = (l2 & 8u) != 0u;
                float v[6];
#pragma unroll
                for (int r = 0; r < 4; ++r) v[r] = hi ? sc[1][r] : sc[0][r];
                v[4] = hi ? sc[2][2] : sc[2][0]; v[5] = hi ? sc[2][3] : sc[2][1];
#pragma unroll
                for (int r = 0; r < 6; r += 2) {
                    if (NORND) break;
                    const unsigned b0 = pack2(v[r], v[r + 1]);
                    const unsigned b1 = pack2(lo2f(b0) * scale, hi2f(b0) * scale);
                    v[r] = lo2f(b1); v[r + 1] = hi2f(b1);
                }
                const int lim = 8 * (w & 3) + (int)(l2 & 7u) - 8 * (int)(l2 >> 4) - (int)((l2 & 8u) >> 1);
#pragma unroll
                for (int r = 0; r < 4; ++r) if (r > lim) v[r] = NORND ? -1000000000.0f / scale : bfr(-1000000000.0f);
                float mx = fmx(fmx(fmx(fmx(fmx(v[0], v[1]), v[2]), v[3]), v[4]), v[5]);
                mx = xmax3216(fmx(mx, ror8f(mx)));
                const float mb = -mx * l2e;
#pragma unroll
                for (int r = 0; r < 6; ++r) v[r] = __builtin_amdgcn_exp2f(fmaf(v[r], l2e, mb));
                float sum = fmaf(v[4] + v[5], 0.5f, (v[0] + v[1]) + (v[2] + v[3]));
                sum = xsum3216(sum + ror8f(sum));
                const float inv = __builtin_amdgcn_rcpf(sum);
                const unsigned p0 = pack2(v[0] * inv, v[1] * inv), p1 = nosent1(pack2(v[2] * inv, v[3] * inv)), p2 = nosent1(pack2(v[4] * inv, v[5] * inv));
                const unsigned base = s + 2 < NSEG ? O_PT + (unsigned)(s * PTSEG) * 2u : W_PL + fb + (s & 1) * 5u * PAIR;
                const __amdgpu_buffer_rsrc_t rp = s + 2 < NSEG ? rws : rwin;
                const unsigned oob = (l2 & 8u) << 27;
                st128(((u32x4){p0, p1, dppu<0x128>(p0), dppu<0x128>(p1)}), rp, (l2 * 16u) | oob, base + (unsigned)((hh * 2 + tq) * 1024 + qh * 128), 16);
                __builtin_amdgcn_raw_buffer_store_b64((u32x2){p2, dppu<0x128>(p2)}, rp, ((l2 & 7u) * 16u + (l2 >> 4) * 8u) | oob | (l2 & 32u) << 25, base + 65536u + (unsigned)((tq * 32 + hh) * 256 + qh * 128), 16);
            }
            tbo = tbo == 32768 ? 0 : tbo + 16384;
            if (__builtin_expect(budget <= 0, 0)) __builtin_amdgcn_raw_buffer_store_b32(1u, rws, 0u, 0u, 16);
            RT(5);
            RTS();
            MT(7);
            MTS();
        }
        };
        if (qt) {} else if (w == 0) steps(IC<0>()); else if (w == 1) steps(IC<1>()); else if (w == 2) steps(IC<2>()); else if (w == 3) steps(IC<3>()); else if (w < 7) steps(IC<4>()); else steps(IC<5>());
        __builtin_amdgcn_s_setprio(0);
    }
    SST(31);
    }
    } else
#pragma nounroll
    for (; lay < 2; ++lay) {
    fresh();
    SST(0);
    {
        LANES();
        const int G = bid < NWRK ? bid - 96 : bid == NWRK ? 80 : -1;
        const int fi = bid >= NWRK ? 255 - bid : bid < NREC ? 16 + G / 10 + 4 * (G % 10) : 99;
        const bool first = fi < 32;
        u32x2 tag = {0u, 0u};
        if (lay == 0) tag = get_tag();
        const int fl = min(lane, lay ? 13 : 15);
        const unsigned fv = w < 4 ? lane * 128u + 64u : (fl & 14) * 512u + (fl & 1) * 128u + (w & 3) * 256u, fs = lay ? W_FA : W_ST;
        auto ask = [&]() { asm volatile("" ::: "memory"); return __builtin_amdgcn_raw_buffer_load_b128(rwin, fv, fs, 16); };
        auto miss = [&](u32x4 f) { return __ballot(((f[0] ^ tag[0]) | (f[1] ^ tag[1]) | (f[2] ^ tag[0]) | (f[3] ^ tag[1])) != 0u); };
        if (G < 0 && w >= 4) for (unsigned spins = 0; spins < SPIN_CAP && miss(ask()); ++spins) __builtin_amdgcn_s_sleep(ASLP);
        BAR();
        if (G >= 0) {
            unsigned long long m = ~0ull;
            unsigned e = ~0u;
            a2_unit(rws, G, w, lane, lay == 0, [&](int r) {
                if (r == 6) { if (lay) e = __builtin_amdgcn_raw_buffer_load_b32(rwin, (lane & 1) * 128u, W_E + (E_A1 + 2 * (w & 3)) * 128u, 16); else if (DFA) __builtin_amdgcn_s_sleep(DFA); }
                if (r >= 7 && lay) { if (r == 7) await(E_A1 + 2 * (w & 3), 2, 0, e); }
                else for (unsigned spins = 0; spins < SPIN_CAP && (r == 6 ? m & 0x3FFFu : m); ++spins) {
                    if (spins) __builtin_amdgcn_s_sleep(ASLP);
                    m = miss(ask());
                }
                if (r == 7) SST(5);
            }, [&](int k) {
                if (k == 2 && w == 3 && blockIdx.x < 136) due(E_A2 + (blockIdx.x & 7), lay ? DA2 : DA20);
                if (k == 7 && YSLP) __builtin_amdgcn_s_sleep(YSLP);
                if (bid < NREC) SST(8 + k);
            });
            SST(6);
            VMWAIT();
            BAR0();
            if (tid == 0) __builtin_amdgcn_raw_buffer_store_b32(0u, rwin, 0u, W_FB + fb + (unsigned)((G >> 2) * 128 + (G & 3) * 4), 16);
            SST(7);
        }
        const RowTw tw = row_tw(lane);
        auto columns = [&](int all) {
            if (w == 7) await(E_A2 + fb / 128u, 8, all);
            if (w == 7 && all) for (unsigned spins = 0;;) {
                asm volatile("" ::: "memory");
                const u32x4 f = __builtin_amdgcn_raw_buffer_load_b128(rwin, (unsigned)min(lane, 20) * 128u, W_FB + fb, 16);
                if (!__any((f[0] | (lane < 20 ? f[1] | f[2] | f[3] : 0u)) != 0u)) break;
                if (spin_fail(spins, rws)) break;
                __builtin_amdgcn_s_sleep(ASLP);
            }
            BAR0();
            if (!all || !first) SST(3);
        };
        const __amdgpu_buffer_rsrc_t rx = mkrsrc(lay ? karg<0>() + OFF_X2 : karg<3>(), S_ * D_ * 2);
        if (first) {
            const unsigned fo = fb + (unsigned)((fi >> 2) * 128 + (fi & 3) * 4);
            a3_units<4>(rws, rx, fi * 8, 0, lay, smem, tw, w, lane, [&](int k) {
                if (k < 0) { if (G < 0) columns(0); } else if (fi < 16) SST(16 + k);
                if (k == 0 && w == 7 && fi < 4) due(E_A3 + fi, lay ? DA3 : DA30);
            }, [&](int) { if (tid == 0) __builtin_amdgcn_raw_buffer_store_b32(0u, rwin, 0u, W_H + fo, 16); });
            SST(4);
        }
                if (!first || bid >= NWRK) {
            const bool two = bid >= NREC && bid < NREC + 32;
            const int u0 = two ? 48 + (bid - NREC) / 8 + 4 * ((bid - NREC) % 8) : bid < NREC ? fi - 24 : bid < NWRK ? bid - NREC - 32 : 32 + fi;
            columns(two ? DFB - 48 * DST + u0 * DST : DFB0);
            a3_units<8>(rws, rx, (16 + u0) * 16, two ? 512 : S_, lay, smem, tw, w, lane, [](int) {}, [&](int r) {
                if (tid == 0) __builtin_amdgcn_raw_buffer_store_b64((u32x2){0u, 0u}, rwin, 0u, W_H + fb + (unsigned)((r >> 5) * 128 + (r & 16) / 2), 16);
                if (r < 1536 && two) SST(2);
            });
            VMWAIT();
        }
        if (lay == 0 && bid < NREC) { config0(); config(); }
        SST(1);
        places();
    }
    const __amdgpu_buffer_rsrc_t rout = mkrsrc(lay ? karg<4>() : karg<0>() + OFF_X2, S_ * D_ * 2);
    if (bid < NREC) {
        LANES();
        const int o = bid - 96, hg = w & 3;
        const unsigned ztoff = (unsigned)((o * H_ + 8 * hg) * 64 + lane) * 16u;
        const unsigned zoff = zpair(o / 10, o % 10) + (unsigned)(lane * 16);
        const unsigned poff = (4u * (mc >> 1) + 2u * (hg >> 1) + (mc & 1u)) * PAIR + nh + (unsigned)((hg & 1) * 4096 + (fq >> 1) * 512 + (fr >> 2) * 128 + (fr & 1) * 64 + (fq & 1) * 32 + (fr & 2) * 8);
        const unsigned pmoff = (8u + (mc & 1u)) * PAIR + nh + (mc >> 1) * 4096u + (unsigned)(fq * 256 + (fr >> 3) * 128 + (fr & 2) * 32 + (fr & 4) * 8 + (fr & 1) * 16);
        const unsigned fzo = (unsigned)(8 * hg + (lane & 7)) * 128u + (unsigned)(o / 10) * 8192u + (lane & 8) * 512u;
        const unsigned moff = (o < 32 ? (unsigned)o * 256u : 4u * PAIR + (unsigned)(o - 32) * 256u) + nh + (unsigned)(lane * 4);
        const unsigned mc2 = o < 32 ? 2u * PAIR : 2048u;
        char* op = smem + 8192;
        SST(2);
        u32x8 zf[4];
        asm volatile("" : "={v[160:167]}"(zf[0]), "={v[168:175]}"(zf[1]), "={v[176:183]}"(zf[2]), "={v[184:191]}"(zf[3]));
        int zr = -1;
        const unsigned ra = (o < 32 ? (unsigned)fq * PAIR + (unsigned)o * 256u : (4u + (fq & 1)) * PAIR + (unsigned)(fq >> 1) * 2048u + (unsigned)(o - 32) * 256u) + nh + (unsigned)(fr * 16);
        auto zt_wait = [&](int s) {
            if (__builtin_expect(zr < s, 0)) for (unsigned spins = 0;;) {
                asm volatile("" ::: "memory");
                if (!__any((int)__builtin_amdgcn_raw_buffer_load_b32(rwin, fzo, W_FZ + fb, 16) < s && ((lane >> 3) & 1) == (int)((MXV >> s) & 1))) break;
                FC(fc2);
                if (spin_fail(spins, rws)) break;
                __builtin_amdgcn_s_sleep(ZPS);
            }
        };
        auto sent = [&](int s) {
            for (;;) {
                const u32x2 x = *(volatile LAS u32x2*)(smem + 4096);
                zr = __builtin_amdgcn_readfirstlane(x[0]);
                if (__builtin_amdgcn_readfirstlane(x[1]) == s) break;
                __builtin_amdgcn_s_sleep(2);
            }
        };
        if (w < 4) {
            await(E_ZT + fb / 128u, 4, 0);
            for (int s = 0; s + 1 < NSEG; ++s) {
                const unsigned pb = W_P + (unsigned)(s & 3) * 10u * PAIR;
                zt_wait(s);
                if (s == 0) SST(1);
#define ZTL \
                    "buffer_load_dwordx4 v[160:163], %[zt], %[rs], %[so] offen sc1\n\t" \
                    "buffer_load_dwordx4 v[164:167], %[zt], %[rs], %[so] offen offset:1024 sc1\n\t" \
                    "buffer_load_dwordx4 v[168:171], %[zt], %[rs], %[so] offen offset:2048 sc1\n\t" \
                    "buffer_load_dwordx4 v[172:175], %[zt], %[rs], %[so] offen offset:3072 sc1\n\t" \
                    "buffer_load_dwordx4 v[176:179], %[zt], %[rs], %[s4] offen sc1\n\t" \
                    "buffer_load_dwordx4 v[180:183], %[zt], %[rs], %[s4] offen offset:1024 sc1\n\t" \
                    "buffer_load_dwordx4 v[184:187], %[zt], %[rs], %[s4] offen offset:2048 sc1\n\t" \
                    "buffer_load_dwordx4 v[188:191], %[zt], %[rs], %[s4] offen offset:3072 sc1\n\t"
                const unsigned zso = O_Z + (unsigned)(s * ZTSEG) * 2u;
                if (__builtin_expect(budget <= 0, 0)) __builtin_amdgcn_raw_buffer_store_b32(1u, rws, 0u, 0u, 16);
                RT(6);
                const unsigned spa = (unsigned)((s & 1) * 2048 + (w < 3 ? w * 512 : 0) + lane * 8);
#define OST(r)
                unsigned long long t0_, t1_, t2_, t3_, t4_ = 0, bad;
                int tries = budget;
                unsigned po = poff, pm = pmoff, x0 = 4u * PAIR, x1 = 4096u;
                const unsigned mb = W_MEM + (unsigned)((s + 1) & 3) * 6u * PAIR;
                if (w < 3) asm volatile("s_nop 4\n\t" ZTL AL
                    "s_barrier\n\t"
                    OST("%[t0]")
                    "v_mov_b64 v[96:97], 0\n\t"
                    "v_mov_b64 v[98:99], 0\n\t"
                    "s_barrier\n\t"
                    "1:\n\t"
                    "buffer_load_dwordx4 v[208:211], %[po], %[rw], %[pb] offen sc1\n\t"
                    "buffer_load_dwordx4 v[212:215], %[po], %[rw], %[pb] offen offset:1024 sc1\n\t"
                    "buffer_load_dwordx4 v[216:219], %[po], %[rw], %[pb] offen offset:2048 sc1\n\t"
                    "buffer_load_dwordx4 v[220:223], %[po], %[rw], %[pb] offen offset:3072 sc1\n\t"
                    OST("%[t1]")
                    "s_waitcnt vmcnt(3)\n\t"
                    OST("%[t3]")
                    "v_smfmac_f32_16x16x64_bf16 v[96:99], v[208:211], v[160:167], %[ix]\n\t"
                    "s_waitcnt vmcnt(2)\n\t"
                    "v_smfmac_f32_16x16x64_bf16 v[96:99], v[212:215], v[168:175], %[ix]\n\t"
                    "s_waitcnt vmcnt(1)\n\t"
                    "v_smfmac_f32_16x16x64_bf16 v[96:99], v[216:219], v[176:183], %[ix]\n\t"
                    "s_waitcnt vmcnt(0)\n\t"
                    "v_smfmac_f32_16x16x64_bf16 v[96:99], v[220:223], v[184:191], %[ix]\n\t"
                    OST("%[t2]")
                    "s_nop 7\n\t"
                    "v_pk_add_f32 v[96:97], v[96:97], v[98:99]\n\t"
                    "ds_write_b64 %[sp], v[96:97]\n\t"
                    "v_cmp_u_f32 vcc, v96, v97\n\t"
                    "s_cbranch_vccz 2f\n\t"
                    "v_max3_u32 v100, v208, v208, v209\n\t"
                    "v_max3_u32 v100, v100, v210, v211\n\t"
                    "v_max3_u32 v100, v100, v212, v213\n\t"
                    "v_max3_u32 v100, v100, v214, v215\n\t"
                    "v_max3_u32 v100, v100, v216, v217\n\t"
                    "v_max3_u32 v100, v100, v218, v219\n\t"
                    "v_max3_u32 v100, v100, v220, v221\n\t"
                    "v_max3_u32 v100, v100, v222, v223\n\t"
                    "v_cmp_eq_u32 vcc, -1, v100\n\t"
                    "s_cbranch_vccz 2f\n\t"
                    "s_sub_i32 %[tr], %[tr], 1\n\t"
                    "s_cmp_lt_i32 %[tr], 1\n\t"
                    "s_cbranch_scc1 2f\n\t"
                    "v_xor_b32 %[po], %[x0], %[po]\n\t"
                    "s_xor_b32 %[x0], %[x0], 0x14000\n\t"
                    "s_waitcnt lgkmcnt(0)\n\t"
                    "v_mov_b64 v[96:97], 0\n\t"
                    "v_mov_b64 v[98:99], 0\n\t"
                    "s_branch 1b\n\t"
                    "2:\n\t"
                    "s_waitcnt lgkmcnt(0)\n\t"
                    "s_barrier\n\t"
                    "s_sleep 3\n\t"
                    : [tr] "+&s"(tries), [po] "+&v"(po), [x0] "+&s"(x0), [t0] "=&s"(t0_), [t1] "=&s"(t1_), [t2] "=&s"(t2_), [t3] "=&s"(t3_), "=&{v[160:167]}"(zf[0]), "=&{v[168:175]}"(zf[1]), "=&{v[176:183]}"(zf[2]), "=&{v[184:191]}"(zf[3])
                    : [zt] "v"(ztoff), [rs] "s"(rws), [so] "s"(zso), [s4] "s"(zso + 4096u), [sp] "v"(spa), [ix] "v"(sidx), [rw] "s"(rwin), [pb] "s"(pb)
                    : "v96", "v97", "v98", "v99", "v100", "v208", "v209", "v210", "v211", "v212", "v213", "v214", "v215", "v216", "v217", "v218", "v219", "v220", "v221", "v222", "v223", "vcc", "scc", "memory");
                else asm volatile("s_nop 4\n\t" ZTL AL
                    "s_barrier\n\t"
                    "buffer_load_dwordx4 v[128:131], %[zo], %[rw], %[zb] offen sc1\n\t"
                    "buffer_load_dwordx4 v[132:135], %[zo], %[rw], %[zb] offen offset:1024 sc1\n\t"
                    "buffer_load_dwordx4 v[136:139], %[zo], %[rw], %[zb] offen offset:2048 sc1\n\t"
                    "buffer_load_dwordx4 v[140:143], %[zo], %[rw], %[zb] offen offset:3072 sc1\n\t"
                    "buffer_load_dwordx4 v[144:147], %[zo], %[rw], %[z4] offen sc1\n\t"
                    "buffer_load_dwordx4 v[148:151], %[zo], %[rw], %[z4] offen offset:1024 sc1\n\t"
                    "buffer_load_dwordx4 v[152:155], %[zo], %[rw], %[z4] offen offset:2048 sc1\n\t"
                    "buffer_load_dwordx4 v[156:159], %[zo], %[rw], %[z4] offen offset:3072 sc1\n\t"
                    OST("%[t0]")
                    "v_mov_b64 v[96:97], 0\n\t"
                    "v_mov_b64 v[98:99], 0\n\t"
                    "s_barrier\n\t"
                    "s_sleep %[fd]\n\t"
                    "buffer_load_dwordx4 v[208:211], %[po], %[rw], %[pb] offen sc1\n\t"
                    "buffer_load_dwordx4 v[212:215], %[po], %[rw], %[pb] offen offset:1024 sc1\n\t"
                    "buffer_load_dwordx4 v[216:219], %[po], %[rw], %[pb] offen offset:2048 sc1\n\t"
                    "buffer_load_dwordx4 v[220:223], %[po], %[rw], %[pb] offen offset:3072 sc1\n\t"
                    "buffer_load_dwordx4 v[224:227], %[pm], %[rw], %[pb] offen sc1\n\t"
                    "buffer_load_dwordx4 v[228:231], %[pm], %[rw], %[pb] offen offset:1024 sc1\n\t"
                    "buffer_load_dwordx4 v[232:235], %[pm], %[rw], %[pb] offen offset:2048 sc1\n\t"
                    "buffer_load_dwordx4 v[236:239], %[pm], %[rw], %[pb] offen offset:3072 sc1\n\t"
                    OST("%[t1]")
                    "s_waitcnt vmcnt(8)\n\t"
                    "v_max3_u32 v101, v129, v131, v133\n\t"
                    "v_max3_u32 v101, v101, v135, v137\n\t"
                    "v_max3_u32 v101, v101, v139, v141\n\t"
                    "v_max3_u32 v101, v101, v143, v145\n\t"
                    "v_max3_u32 v101, v101, v147, v149\n\t"
                    "v_max3_u32 v101, v101, v151, v153\n\t"
                    "v_max3_u32 v101, v101, v155, v157\n\t"
                    "v_max_u32 v101, v101, v159\n\t"
                    "v_cmp_eq_u32 vcc, -1, v101\n\t"
                    "s_cbranch_vccz 3f\n\t"
                    "buffer_load_dwordx4 v[128:131], %[zo], %[rw], %[zb] offen sc1\n\t"
                    "buffer_load_dwordx4 v[132:135], %[zo], %[rw], %[zb] offen offset:1024 sc1\n\t"
                    "buffer_load_dwordx4 v[136:139], %[zo], %[rw], %[zb] offen offset:2048 sc1\n\t"
                    "buffer_load_dwordx4 v[140:143], %[zo], %[rw], %[zb] offen offset:3072 sc1\n\t"
                    "buffer_load_dwordx4 v[144:147], %[zo], %[rw], %[z4] offen sc1\n\t"
                    "buffer_load_dwordx4 v[148:151], %[zo], %[rw], %[z4] offen offset:1024 sc1\n\t"
                    "buffer_load_dwordx4 v[152:155], %[zo], %[rw], %[z4] offen offset:2048 sc1\n\t"
                    "buffer_load_dwordx4 v[156:159], %[zo], %[rw], %[z4] offen offset:3072 sc1\n\t"
                    "s_waitcnt vmcnt(0)\n\t"
                    "v_max3_u32 v101, v129, v131, v133\n\t"
                    "v_max3_u32 v101, v101, v135, v137\n\t"
                    "v_max3_u32 v101, v101, v139, v141\n\t"
                    "v_max3_u32 v101, v101, v143, v145\n\t"
                    "v_max3_u32 v101, v101, v147, v149\n\t"
                    "v_max3_u32 v101, v101, v151, v153\n\t"
                    "v_max3_u32 v101, v101, v155, v157\n\t"
                    "v_max_u32 v101, v101, v159\n\t"
                    "3:\n\t"
                    "v_permlane16_swap_b32 v128, v129\n\t"
                    "v_permlane16_swap_b32 v130, v131\n\t"
                    "v_permlane16_swap_b32 v132, v133\n\t"
                    "v_permlane16_swap_b32 v134, v135\n\t"
                    "v_permlane16_swap_b32 v136, v137\n\t"
                    "v_permlane16_swap_b32 v138, v139\n\t"
                    "v_permlane16_swap_b32 v140, v141\n\t"
                    "v_permlane16_swap_b32 v142, v143\n\t"
                    "v_permlane16_swap_b32 v144, v145\n\t"
                    "v_permlane16_swap_b32 v146, v147\n\t"
                    "v_permlane16_swap_b32 v148, v149\n\t"
                    "v_permlane16_swap_b32 v150, v151\n\t"
                    "v_permlane16_swap_b32 v152, v153\n\t"
                    "v_permlane16_swap_b32 v154, v155\n\t"
                    "v_permlane16_swap_b32 v156, v157\n\t"
                    "v_permlane16_swap_b32 v158, v159\n\t"
                    "s_mov_b32 vcc_lo, 0x00ff00ff\n\t"
                    "s_mov_b32 vcc_hi, 0x00ff00ff\n\t"
                    "s_waitcnt vmcnt(7)\n\t"
                    OST("%[t3]")
                    "v_smfmac_f32_16x16x64_bf16 v[96:99], v[208:211], v[160:167], %[ix]\n\t"
                    "s_waitcnt vmcnt(6)\n\t"
                    "v_smfmac_f32_16x16x64_bf16 v[96:99], v[212:215], v[168:175], %[ix]\n\t"
                    "s_waitcnt vmcnt(5)\n\t"
                    "v_smfmac_f32_16x16x64_bf16 v[96:99], v[216:219], v[176:183], %[ix]\n\t"
                    "s_waitcnt vmcnt(4)\n\t"
                    "v_smfmac_f32_16x16x64_bf16 v[96:99], v[220:223], v[184:191], %[ix]\n\t"
                    OB(4)
                    "s_waitcnt vmcnt(3)\n\t"
                    "v_smfmac_f32_16x16x64_bf16 v[96:99], v[224:227], v[128:135], %[ix]\n\t"
                    OB(5)
                    "s_waitcnt vmcnt(2)\n\t"
                    "v_smfmac_f32_16x16x64_bf16 v[96:99], v[228:231], v[136:143], %[ix]\n\t"
                    OB(6)
                    "s_waitcnt vmcnt(1)\n\t"
                    "v_smfmac_f32_16x16x64_bf16 v[96:99], v[232:235], v[144:151], %[ix]\n\t"
                    "s_waitcnt vmcnt(0)\n\t"
                    "v_smfmac_f32_16x16x64_bf16 v[96:99], v[236:239], v[152:159], %[ix]\n\t"
                    OST("%[t2]")
                    "s_waitcnt lgkmcnt(0)\n\t"
                    "v_pk_add_f32 v[102:103], v[102:103], v[104:105]\n\t"
                    "v_pk_add_f32 v[102:103], v[102:103], v[106:107]\n\t"
                    "s_nop 4\n\t"
                    "6:\n\t"
                    "v_pk_add_f32 v[96:97], v[96:97], v[102:103]\n\t"
                    "v_pk_add_f32 v[96:97], v[96:97], v[98:99]\n\t"
                    "v_cmp_u_f32 %[bad], v96, v97\n\t"
                    "s_nop 0\n\t"
                    "v_cndmask_b32_dpp v98, v97, v96, vcc row_ror:8 row_mask:0xf bank_mask:0xf\n\t"
                    "s_not_b64 vcc, vcc\n\t"
                    "v_cndmask_b32_dpp v99, v96, v97, vcc row_ror:8 row_mask:0xf bank_mask:0xf\n\t"
                    "s_cmp_lg_u64 %[bad], 0\n\t"
                    "v_cvt_pk_bf16_f32 v98, v98, v99\n\t"
                    "s_cbranch_scc1 4f\n\t"
                    "2:\n\t"
                    "buffer_store_dword v98, %[mo], %[rw], %[m0] offen sc1\n\t"
                    "buffer_store_dword v98, %[mo], %[rw], %[m1] offen sc1\n\t"
                    "buffer_store_dword v98, %[mo], %[rw], %[m2] offen sc1\n\t"
                    "buffer_store_dword v98, %[mo], %[rw], %[m3] offen sc1\n\t"
                    OST("%[t4]")
                    "s_branch 5f\n\t"
                    "4:\n\t"
                    "v_min_u32 v98, -2, v98\n\t"
                    "v_max3_u32 v100, v101, v208, v209\n\t"
                    "v_max3_u32 v100, v100, v210, v211\n\t"
                    "v_max3_u32 v100, v100, v212, v213\n\t"
                    "v_max3_u32 v100, v100, v214, v215\n\t"
                    "v_max3_u32 v100, v100, v216, v217\n\t"
                    "v_max3_u32 v100, v100, v218, v219\n\t"
                    "v_max3_u32 v100, v100, v220, v221\n\t"
                    "v_max3_u32 v100, v100, v222, v223\n\t"
                    "v_max3_u32 v100, v100, v224, v225\n\t"
                    "v_max3_u32 v100, v100, v226, v227\n\t"
                    "v_max3_u32 v100, v100, v228, v229\n\t"
                    "v_max3_u32 v100, v100, v230, v231\n\t"
                    "v_max3_u32 v100, v100, v232, v233\n\t"
                    "v_max3_u32 v100, v100, v234, v235\n\t"
                    "v_max3_u32 v100, v100, v236, v237\n\t"
                    "v_max3_u32 v100, v100, v238, v239\n\t"
                    "v_cmp_eq_u32 %[bad], -1, v100\n\t"
                    "s_not_b64 vcc, vcc\n\t"
                    "s_cmp_eq_u64 %[bad], 0\n\t"
                    "s_cbranch_scc1 2b\n\t"
                    "s_sub_i32 %[tr], %[tr], 1\n\t"
                    "s_cmp_lt_i32 %[tr], 1\n\t"
                    "s_cbranch_scc1 2b\n\t"
                    "v_xor_b32 %[po], %[x0], %[po]\n\t"
                    "s_xor_b32 %[x0], %[x0], 0x14000\n\t"
                    "v_xor_b32 %[pm], %[x1], %[pm]\n\t"
                    "s_xor_b32 %[x1], %[x1], 0x5000\n\t"
                    "buffer_load_dwordx4 v[128:131], %[zo], %[rw], %[zb] offen sc1\n\t"
                    "buffer_load_dwordx4 v[132:135], %[zo], %[rw], %[zb] offen offset:1024 sc1\n\t"
                    "buffer_load_dwordx4 v[136:139], %[zo], %[rw], %[zb] offen offset:2048 sc1\n\t"
                    "buffer_load_dwordx4 v[140:143], %[zo], %[rw], %[zb] offen offset:3072 sc1\n\t"
                    "buffer_load_dwordx4 v[144:147], %[zo], %[rw], %[z4] offen sc1\n\t"
                    "buffer_load_dwordx4 v[148:151], %[zo], %[rw], %[z4] offen offset:1024 sc1\n\t"
                    "buffer_load_dwordx4 v[152:155], %[zo], %[rw], %[z4] offen offset:2048 sc1\n\t"
                    "buffer_load_dwordx4 v[156:159], %[zo], %[rw], %[z4] offen offset:3072 sc1\n\t"
                    "buffer_load_dwordx4 v[208:211], %[po], %[rw], %[pb] offen sc1\n\t"
                    "buffer_load_dwordx4 v[212:215], %[po], %[rw], %[pb] offen offset:1024 sc1\n\t"
                    "buffer_load_dwordx4 v[216:219], %[po], %[rw], %[pb] offen offset:2048 sc1\n\t"
                    "buffer_load_dwordx4 v[220:223], %[po], %[rw], %[pb] offen offset:3072 sc1\n\t"
                    "buffer_load_dwordx4 v[224:227], %[pm], %[rw], %[pb] offen sc1\n\t"
                    "buffer_load_dwordx4 v[228:231], %[pm], %[rw], %[pb] offen offset:1024 sc1\n\t"
                    "buffer_load_dwordx4 v[232:235], %[pm], %[rw], %[pb] offen offset:2048 sc1\n\t"
                    "buffer_load_dwordx4 v[236:239], %[pm], %[rw], %[pb] offen offset:3072 sc1\n\t"
                    "v_mov_b64 v[96:97], 0\n\t"
                    "v_mov_b64 v[98:99], 0\n\t"
                    "s_waitcnt vmcnt(0)\n\t"
                    "v_max3_u32 v101, v129, v131, v133\n\t"
                    "v_max3_u32 v101, v101, v135, v137\n\t"
                    "v_max3_u32 v101, v101, v139, v141\n\t"
                    "v_max3_u32 v101, v101, v143, v145\n\t"
                    "v_max3_u32 v101, v101, v147, v149\n\t"
                    "v_max3_u32 v101, v101, v151, v153\n\t"
                    "v_max3_u32 v101, v101, v155, v157\n\t"
                    "v_max_u32 v101, v101, v159\n\t"
                    "v_permlane16_swap_b32 v128, v129\n\t"
                    "v_permlane16_swap_b32 v130, v131\n\t"
                    "v_permlane16_swap_b32 v132, v133\n\t"
                    "v_permlane16_swap_b32 v134, v135\n\t"
                    "v_permlane16_swap_b32 v136, v137\n\t"
                    "v_permlane16_swap_b32 v138, v139\n\t"
                    "v_permlane16_swap_b32 v140, v141\n\t"
                    "v_permlane16_swap_b32 v142, v143\n\t"
                    "v_permlane16_swap_b32 v144, v145\n\t"
                    "v_permlane16_swap_b32 v146, v147\n\t"
                    "v_permlane16_swap_b32 v148, v149\n\t"
                    "v_permlane16_swap_b32 v150, v151\n\t"
                    "v_permlane16_swap_b32 v152, v153\n\t"
                    "v_permlane16_swap_b32 v154, v155\n\t"
                    "v_permlane16_swap_b32 v156, v157\n\t"
                    "v_permlane16_swap_b32 v158, v159\n\t"
                    "s_nop 1\n\t"
                    "v_smfmac_f32_16x16x64_bf16 v[96:99], v[208:211], v[160:167], %[ix]\n\t"
                    "v_smfmac_f32_16x16x64_bf16 v[96:99], v[212:215], v[168:175], %[ix]\n\t"
                    "v_smfmac_f32_16x16x64_bf16 v[96:99], v[216:219], v[176:183], %[ix]\n\t"
                    "v_smfmac_f32_16x16x64_bf16 v[96:99], v[220:223], v[184:191], %[ix]\n\t"
                    "v_smfmac_f32_16x16x64_bf16 v[96:99], v[224:227], v[128:135], %[ix]\n\t"
                    "v_smfmac_f32_16x16x64_bf16 v[96:99], v[228:231], v[136:143], %[ix]\n\t"
                    "v_smfmac_f32_16x16x64_bf16 v[96:99], v[232:235], v[144:151], %[ix]\n\t"
                    "v_smfmac_f32_16x16x64_bf16 v[96:99], v[236:239], v[152:159], %[ix]\n\t"
                    "s_nop 7\n\t"
                    "s_branch 6b\n\t"
                    "5:\n\t"
                    : [tr] "+&s"(tries), [po] "+&v"(po), [pm] "+&v"(pm), [x0] "+&s"(x0), [x1] "+&s"(x1), [bad] "=&s"(bad), [t0] "=&s"(t0_), [t1] "=&s"(t1_), [t2] "=&s"(t2_), [t3] "=&s"(t3_), [t4] "=&s"(t4_), "=&{v[160:167]}"(zf[0]), "=&{v[168:175]}"(zf[1]), "=&{v[176:183]}"(zf[2]), "=&{v[184:191]}"(zf[3])
                    : [zt] "v"(ztoff), [rs] "s"(rws), [so] "s"(zso), [s4] "s"(zso + 4096u), [zo] "v"(zoff), [sp] "v"(spa), [ix] "v"(sidx), [rw] "s"(rwin), [zb] "s"(zb), [z4] "s"(zb + 4096u), [pb] "s"(pb), [mo] "v"(moff), [fd] "n"(FDL),
                      [m0] "s"(mb), [m1] "s"(mb + PAIR), [m2] "s"(mb + mc2), [m3] "s"(mb + mc2 + PAIR)
                    : "v96", "v97", "v98", "v99", "v100", "v101", "v102", "v103", "v104", "v105", "v106", "v107", "v128", "v129", "v130", "v131", "v132", "v133", "v134", "v135", "v136", "v137", "v138", "v139", "v140", "v141", "v142", "v143", "v144", "v145", "v146", "v147", "v148", "v149", "v150", "v151", "v152", "v153", "v154", "v155", "v156", "v157", "v158", "v159", "v208", "v209", "v210", "v211", "v212", "v213", "v214", "v215", "v216", "v217", "v218", "v219", "v220", "v221", "v222", "v223", "v224", "v225", "v226", "v227", "v228", "v229", "v230", "v231", "v232", "v233", "v234", "v235", "v236", "v237", "v238", "v239", "vcc", "scc", "memory");
                budget = tries;
                if (w == 3) *(volatile LAS int*)(smem + 4100) = s;
                __builtin_amdgcn_s_sleep(DPS - 4);
                if (w == 3) {
                    st128(sent4(), rwin, ra, W_MEM + (unsigned)(s & 3) * 6u * PAIR, 16);
                    if (lane == 0) __builtin_amdgcn_raw_buffer_store_b64((u32x2){(unsigned)s - 1u, (unsigned)max(zr, s)}, rwin, 0u, W_GO + fb + (unsigned)o * 128u, 16);
                }
                sent(s);
                zb = zb == W_ZM + 2u * NOWN * PAIR ? W_ZM : zb + NOWN * PAIR;
            }
        } else if (w == 4) {
            const unsigned fo = (unsigned)lane * 128u + (unsigned)(o / 10) * 8192u;
            int zk = -1;
            if (lane < 2) *(volatile LAS int*)(smem + 4096 + lane * 4) = -1;
            for (int s = 0; s + 1 < NSEG; ++s) {
                asm volatile("s_waitcnt lgkmcnt(0)\n\ts_barrier\n\ts_barrier" ::: "memory");
                int f = 0;
                const bool look = zk < min(s + ZLA, NSEG - 1);
                if (look) { __builtin_amdgcn_s_sleep(6); f = (int)__builtin_amdgcn_raw_buffer_load_b32(rwin, fo, W_FZ + fb, 16); }
                SB();
                BAR0();
                if (look) {
                    f = min(f, __builtin_amdgcn_update_dpp(f, f, 0xB1, 0xF, 0xF, false)); f = min(f, __builtin_amdgcn_update_dpp(f, f, 0x4E, 0xF, 0xF, false));
                    f = min(f, __builtin_amdgcn_update_dpp(f, f, 0x141, 0xF, 0xF, false)); f = min(f, __builtin_amdgcn_update_dpp(f, f, 0x140, 0xF, 0xF, false));
                    zk = seg_done(MXV, min(__builtin_amdgcn_readlane(f, 0), __builtin_amdgcn_readlane(f, 16)), min(__builtin_amdgcn_readlane(f, 32), __builtin_amdgcn_readlane(f, 48)));
                    *(volatile LAS int*)(smem + 4096) = zk;
                }
            }
        } else if (w < 7) {
            for (int s = 0; s < 3 * NSEG - 3; ++s) BAR0();
        } else {
            Tk tk = tk_init();
            __builtin_amdgcn_s_setprio(3);
            for (int s = -1; s + 1 < NSEG; ++s) {
                if (s >= 0) {
                    const unsigned ta0 = ck.ta, mm0 = ck.ma, tp = s ? tpp + lt + xs + M1 : tpp - M2 - XS0 - (lay ? PEN0 : PEN00);
                    wait_fine_bar(tp - DZP, ck);
                    wait_cyc_bar(cyc_at(ck, sx24(tp - ck.ta)));
                    clk_rate(ck, ta0, mm0);
                    asm volatile("s_barrier ; 7" ::: "memory");
                }
                if (s) sched(tk, max(s, 0));
                if (s < 0) continue;
                RTS();
            }
            __builtin_amdgcn_s_setprio(0);
            if (o < 2) due(E_OUT + o, (sx24(tpp + ML - 4u * (unsigned)__builtin_amdgcn_s_memrealtime()) >> 2) + DOUT);
        }
        {
            const int e = w >> 2;
            const unsigned ooff = (unsigned)((S_ - 2 * SEG + 8 * w + (lane >> 3)) * D_ + o * 16 + (lane & 7) * 2) * 2u;
            unsigned pl = W_PL + fb + e * 5u * PAIR, zs = W_ZM + (NSEG - 2 + e + lay * NSEG) % 3 * (NOWN * PAIR);
            asm volatile("" : "+s"(pl), "+s"(zs));
            if (e) {
                if (w < 7) __builtin_amdgcn_s_sleep(DPS);
                sent(NSEG - 2);
                zt_wait(NSEG - 1);
                const unsigned zl = O_Z + (unsigned)((NSEG - 1) * ZTSEG) * 2u;
#pragma unroll
                for (int q = 0; q < 4; ++q) zf[q] = cat8(__builtin_amdgcn_raw_buffer_load_b128(rws, ztoff + q * 2048u, zl, 16), __builtin_amdgcn_raw_buffer_load_b128(rws, ztoff + q * 2048u + 1024u, zl, 16));
            }
            if (DR0 && !e) __builtin_amdgcn_s_sleep(DR0);
            const unsigned xr = __builtin_amdgcn_raw_buffer_load_b32(rws, ooff, O_X1, 16);
            SB();
            if (w == 7) wait_fine_bar(tpp + ML, ck); else if (e) BAR0();
            f32x4 c[2];
            for (unsigned spins = 0;;) {
                asm volatile("" ::: "memory");
                u32x4 pt[16], ptm[2][2], zm[2];
#pragma unroll
                for (int q = 0; q < 16; ++q) pt[q] = __builtin_amdgcn_raw_buffer_load_b128(rwin, (unsigned)(16 * hg) * 1024u + lane * 16u + (q & 3) * 1024u, pl + (q >> 2) * 4096u, 16);
#pragma unroll
                for (int q = 0; q < 2; ++q) zm[q] = __builtin_amdgcn_raw_buffer_load_b128(rwin, zoff + hg * 2048u + q * 1024u, zs, 16);
#pragma unroll
                for (int q = 0; q < 4; ++q) ptm[q >> 1][q & 1] = __builtin_amdgcn_raw_buffer_load_b128(rwin, (unsigned)(hg * 2048 + lane * 16) + (q & 1) * 1024u, pl + 65536u + (q >> 1) * 8192u, 16);
                SB();
                if (!e && !spins) BAR0();
                unsigned m = max(smax(zm[0]), smax(zm[1]));
                #pragma unroll
                for (int q = 0; q < 16; ++q) { c[q & 1] = mfma32(half8(zf[q >> 2], q >> 1 & 1), pt[q], q < 2 ? z4 : c[q & 1]); m = max(m, smax(pt[q])); }
#pragma unroll
                for (int q = 0; q < 4; ++q) { c[q >> 1] = mfma32((u32x4){zm[q & 1][0], zm[q & 1][2], zm[q & 1][1], zm[q & 1][3]}, ptm[q >> 1][q & 1], c[q >> 1]); m = max(m, smax(ptm[q >> 1][q & 1])); }
                if (!__any(m == 0xFFFFFFFFu)) break;
                if (spin_fail(spins, rws)) break;
            }
#pragma unroll
            for (int t = 0; t < 2; ++t)
#pragma unroll
                for (int r = 0; r < 4; ++r) *(float*)(op + w * 2048 + (16 * t + fr) * 64 + pi16(4 * fq + r) * 4) = c[t][r];
            BAR();
            const char* src = op + e * 8192 + hg * 512 + lane * 8;
            const f32x2 y = (*(const f32x2*)src + *(const f32x2*)(src + 2048)) + (*(const f32x2*)(src + 4096) + *(const f32x2*)(src + 6144));
            __builtin_amdgcn_raw_buffer_store_b32(nosent(pack2(lo2f(xr) + bfr(y[0]), hi2f(xr) + bfr(y[1]))), rout, ooff, 0u, 16);
            __builtin_amdgcn_s_sleep(4);
            const u32x4 sv = sent4();
            st128(sv, rwin, zoff + hg * 2048u, zs, 16); st128(sv, rwin, zoff + hg * 2048u + 1024u, zs, 16);
            if (w == 3) st128(sv, rwin, ra, W_MEM + 18u * PAIR, 16);
            if (lay && o == 0 && w == 0) __builtin_amdgcn_raw_buffer_store_b64(get_tag(), rws, 0u, 8u, 16);
        }
        SST(23);
    } else if (bid < NREC + 30) {
        LANES();
        const int cg = (bid - NREC) % 10;
        const unsigned ztoff = (unsigned)((4 * cg * H_ + 4 * w) * 64 + lane) * 16u;
        const unsigned ptoff = (unsigned)(8 * w * 64 + lane) * 16u, ptmoff = 65536u + (unsigned)(4 * w * 16 + lane) * 16u;
        const int tk = (w & 3) * 8 + (lane >> 3);
        const unsigned ooff = (unsigned)(tk * D_ + cg * 64 + (lane & 7) * 8) * 2u;
        const unsigned gooff = W_GO + (unsigned)((4 * cg + (lane & 2 ? 3 : 0)) * 128 + (lane & 1) * 4);
        int rdy = -1, go = -1;
        auto ask = [&]() {
            asm volatile("" ::: "memory");
            return (int)__builtin_amdgcn_raw_buffer_load_b32(rwin, gooff, fb, 16);
        };
        auto look = [&](int f) {
            go = min(__builtin_amdgcn_readlane(f, 33), __builtin_amdgcn_readlane(f, 35));
            rdy = max(rdy, max(__builtin_amdgcn_readlane(f, 32), __builtin_amdgcn_readlane(f, 34)));
        };
        auto wait_rdy = [&](int& x, int v, int s) {
            for (unsigned spins = 0; x < v;) {
                look(ask());
                if (x >= v) break;
                if (spin_fail(spins, rws)) break;
                if (s >= NSEG - 5) __builtin_amdgcn_s_sleep(OSLP2); else __builtin_amdgcn_s_sleep(OSLP);
            }
        };
        if (w >= 4) wait_h(rws, rwin, fb, 3, hrdy);
        u32x4 zf[4][4];
        auto load_z = [&](int s) {
#pragma unroll
            for (int q = 0; q < 16; ++q) zf[q >> 2][q & 3] = __builtin_amdgcn_raw_buffer_load_b128(rws, ztoff + (q & 3) * 1024u, O_Z + (unsigned)(s * ZTSEG) * 2u + (q >> 2) * (H_ * 1024u), 16);
            SB();
        };
        wait_rdy(go, (bid - NREC) / 10, 0);
        load_z((bid - NREC) / 10);
        for (int s = (bid - NREC) / 10;; s += 3) {
            u32x4 xr, zx[4], pt[8], ptm[2];
            asm volatile("" : "=v"(xr));
            RT(0);
            wait_rdy(rdy, s, s);
            RT(1);
            if (s >= NSEG - 8 && cg == 0 && w == 0) due(E_OUT + NSEG - 1 - s, s + 3 < NSEG ? DOU3 : DOU2);
            const unsigned ptb = O_PT + (unsigned)(s * PTSEG) * 2u;
#pragma unroll
            for (int q = 0; q < 8; ++q) pt[q] = __builtin_amdgcn_raw_buffer_load_b128(rws, ptoff + q * 1024u, ptb, 16);
#pragma unroll
            for (int q = 0; q < 2; ++q) ptm[q] = __builtin_amdgcn_raw_buffer_load_b128(rws, ptmoff + q * 8192u, ptb, 16);
#pragma unroll
            for (int q = 0; q < 4; ++q) zx[q] = __builtin_amdgcn_raw_buffer_load_b128(rws, tid * 16u, O_Z2 + (unsigned)(s * NOWN + 4 * cg + q) * 8192u, 16);
            if (w >= 4) xr = __builtin_amdgcn_raw_buffer_load_b128(rws, ooff, O_X1 + (unsigned)(s * SEG * D_ * 2), 16);
            const int f = ask();
            SB();
            char* op = smem + (s & 1) * 65536;
            f32x4 c[8];
#pragma unroll
            for (int q = 0; q < 4; ++q)
#pragma unroll
                for (int u = 0; u < 8; ++u) c[u] = mfma32(zf[u >> 1][q], pt[2 * q + (u & 1)], q ? c[u] : z4);
#pragma unroll
            for (int u = 0; u < 8; ++u) {
                const int tq = u & 1, t = u >> 1;
                *(f32x4*)(op + w * 8192 + (16 * tq + fr) * 256 + (((t * 4 + fq) ^ (fr & 7)) << 4)) = mfma32((u32x4){zx[t][0], zx[t][2], zx[t][1], zx[t][3]}, ptm[tq], c[u]);
            }
            look(f);
            const bool pre = go >= s + 3 && s < NSEG - 14;
            if (pre) load_z(s + 3);
            BAR();
            if (lay == 0 && s >= 3 && s < NSEG - 11 && tid == 0) __builtin_amdgcn_raw_buffer_store_b32(0u, rwin, 0u, W_OUT + (unsigned)((s - 3) * 128 + cg * 4), 16);
            if (w >= 4) {
                f32x2 x[4];
#pragma unroll
                for (int q = 0; q < 4; ++q) {
                    const char* sp = op + tk * 256 + ((((lane & 6) * 2 + q) ^ (tk & 7)) << 4) + (lane & 1) * 8;
                    x[q] = *(const f32x2*)sp;
#pragma unroll
                    for (int ww = 1; ww < 8; ++ww) x[q] += *(const f32x2*)(sp + ww * 8192);
                }
                const u32x4 r = {pack2(lo2f(xr[0]) + bfr(x[0][0]), hi2f(xr[0]) + bfr(x[2][0])), pack2(lo2f(xr[1]) + bfr(x[1][0]), hi2f(xr[1]) + bfr(x[3][0])),
                                 pack2(lo2f(xr[2]) + bfr(x[0][1]), hi2f(xr[2]) + bfr(x[2][1])), pack2(lo2f(xr[3]) + bfr(x[1][1]), hi2f(xr[3]) + bfr(x[3][1]))};
                st128(((u32x4){nosent(r[0]), nosent(r[1]), nosent(r[2]), nosent(r[3])}), rout, ooff, (unsigned)(s * SEG * D_ * 2), 16);
            } else if (!pre) __builtin_amdgcn_s_sleep(ODL);
            if (lay == 0 && s >= NSEG - 14 && s < NSEG - 8) {
                VMWAIT();
                BAR0();
                if (tid == 0) __builtin_amdgcn_raw_buffer_store_b32(0u, rwin, 0u, W_OUT + (unsigned)(s * 128 + cg * 4), 16);
            }
            RT(2);
            RTS();
            if (s < 8) SST(8 + s);
            if (s >= 56) SST(s - 40);
            if (s + 3 >= NSEG - 2) break;
            if (!pre) { wait_rdy(go, s + 3, 0); load_z(s + 3); }
        }
    } else if (lay == 0 && bid < NWRK) {
        if (w == 0) for (unsigned spins = 0; (int)__builtin_amdgcn_raw_buffer_load_b32(rwin, 0u, W_E + (E_OUT + 3) * 128u, 16) == -1 && !spin_fail(spins, rws);) {
            __builtin_amdgcn_s_sleep(64);
            asm volatile("" ::: "memory");
        }
    } else if (lay == 0) {
        LANES();
        const RowTw tw = row_tw(lane);
        for (int u = (256 - bid) & 15; u < 256; u += 16) {
            if (w == 0) {
                if (u >= 224) await(E_OUT + (255 - u >> 2), 1, 0);
                else for (unsigned spins = 0;;) {
                    asm volatile("" ::: "memory");
                    const unsigned f = __builtin_amdgcn_raw_buffer_load_b32(rwin, (unsigned)min(lane, 9) * 4u, W_OUT + (unsigned)(u >> 2) * 128u, 16);
                    if (!__any(f != 0u)) break;
                    if (spin_fail(spins, rws)) break;
                    __builtin_amdgcn_s_sleep(WSLP);
                }
            }
            BAR0();
            SST(24);
            const unsigned fo = W_FA + (unsigned)((u >> 2) * 128 + (u & 3) * 4);
            a1_unit<4, 512>(rout, u * 8, u >= 224, rws, smem, tw, w, lane, [&](int k) {
                if (k == 0 && w == 0 && u >= 224 && !(u & 3)) due(E_A1 + (u - 224 >> 2), DA1);
                if (k >= 0) SST(8 + k);
            });
            SST(25);
            if (u >= 224) continue;
            VMWAIT();
            BAR0();
            if (tid == 0) __builtin_amdgcn_raw_buffer_store_b32(0u, rwin, 0u, fo, 16);
        }
    }
    if (lay == 0 && w == 0 && bid >= NREC && bid <= NWRK) await(E_OUT + 1, 1, bid < NREC + 32 ? -DOW : -60);
    SST(31);
    }
}

constexpr int L_DBG = 0;

extern "C" void kernel_launch(void* const* d_in, const int* in_sizes, int n_in, void* d_out, int out_size, void* d_ws, size_t ws_size,
                              hipStream_t stream) {
    char* ws = (char*)d_ws;
    hipFuncSetAttribute((const void*)b3_kernel, hipFuncAttributeMaxDynamicSharedMemorySize, L_END + L_DBG);
    char* win = (char*)(((uintptr_t)(ws + OFF_WIN) + 0x1FFFFFu) & ~(uintptr_t)0x1FFFFFu);
    RecArgs ra{(unsigned*)ws, win, (const u16*)d_in[4], (const u16*)d_in[0], (u16*)d_out, {(const u16*)d_in[5], (const u16*)d_in[6], (const u16*)d_in[7], (const u16*)d_in[8], (const u16*)d_in[1], (const u16*)d_in[2]}};
    b3_kernel<<<256, 512, L_END + L_DBG, stream>>>(ra);
}
```

```cpp
#include <hip/hip_runtime.h>
#include <hip/hip_bf16.h>
#include <stdint.h>

typedef unsigned short u16;
typedef __attribute__((ext_vector_type(8))) __bf16 bf16x8;
typedef __attribute__((ext_vector_type(4))) short s16x4;
typedef __attribute__((ext_vector_type(4))) float f32x4;
typedef __attribute__((ext_vector_type(8))) float f32x8;
typedef __attribute__((ext_vector_type(16))) float f32x16;
typedef __attribute__((ext_vector_type(4))) unsigned u32x4;
typedef __attribute__((ext_vector_type(2))) unsigned u32x2;
typedef __attribute__((ext_vector_type(8))) unsigned u32x8;
typedef __attribute__((ext_vector_type(16))) __bf16 bf16x16;
typedef __attribute__((ext_vector_type(2))) unsigned short u16x2;
#define LAS __attribute__((address_space(3)))
template <int N> struct IC { static constexpr int v = N; };

constexpr int S_ = 2048, D_ = 640, DF = 321, H_ = 32, DH = 128, HD = 4096, NM = 8, SEG = 32, NSEG = 64;
constexpr int NQKV = 3 * HD;
constexpr int NOWN = 40;
constexpr int ZTR = NSEG;
constexpr size_t ZTSEG = (size_t)NOWN * H_ * 512;
constexpr unsigned PAIR = 16384, HALF = 8192;
constexpr unsigned W_MEM = 0, W_P = W_MEM + 24 * PAIR, W_T = W_P + 40 * PAIR, W_GO = W_T + 8 * PAIR, W_OUT = W_GO + PAIR, W_FQ = W_OUT + 8192, W_FZ = W_OUT + PAIR,
                   W_H = W_FZ + 2 * PAIR, W_FA = W_H + 8192, W_FB = W_GO + 8192, W_E = W_FB + 4096, W_PL = W_H + PAIR, W_FL = W_PL + 10 * PAIR - W_GO, W_PROBE = W_GO + 2 * W_FL,
                   W_ZM = 2u * 1024 * 1024, W_END = W_ZM + 120 * PAIR;
constexpr unsigned W_ST = W_PROBE + 2 * PAIR, W_ROT = W_ST + 8192, W_ARM = W_ROT + 128, W_CFG = W_ARM + 128;
static_assert(W_ROT + PAIR <= 2u * 1024 * 1024 && W_PROBE % (2 * PAIR) == 0, "window");
constexpr int PTSEG = 32768 + 8192;

#define WAUX 0
#define FPAD 512
constexpr int GF = 32768 + FPAD, YS = 12288 + FPAD;
static_assert(FPAD >= 16, "factors");
constexpr int NG = 81;
constexpr size_t al(size_t x) { return (x + 255) & ~(size_t)255; }
constexpr size_t OFF_CTL  = 0;
constexpr size_t OFF_WFT  = 4096 + 4 * 1024 * 1024;
constexpr size_t OFF_F    = OFF_WFT + al((size_t)4 * NG * S_ * 4);
constexpr size_t OFF_X1   = OFF_F + (size_t)NG * GF;
constexpr size_t OFF_X2   = OFF_X1 + al((size_t)S_ * D_ * 2);
constexpr size_t OFF_Y0   = OFF_X2 + al((size_t)S_ * D_ * 2);
constexpr size_t OFF_H    = OFF_Y0 + (size_t)NG * YS;
constexpr size_t OFF_ARM  = OFF_Y0 - 256 * D_ * 2, ARM_END = OFF_H + 8 * 40960;
constexpr size_t OFF_Z    = OFF_H + (size_t)(NSEG + 4) * 40960;
constexpr size_t OFF_Z2   = OFF_Z + al(ZTSEG * ZTR * 2);
constexpr size_t OFF_PT   = OFF_Z2 + al((size_t)NSEG * NOWN * 8192);
constexpr size_t OFF_QK   = OFF_PT + al((size_t)NSEG * PTSEG * 2);
constexpr size_t OFF_WIN  = OFF_QK + (size_t)NSEG * H_ * 16384;
constexpr size_t OFF_END  = OFF_WIN + (size_t)6 * 1024 * 1024;
static_assert(OFF_END <= (size_t)256 * 1024 * 1024, "workspace");

#define st128(v, r, vo, so, aux) do { const u32x4 d_ = (v); __builtin_amdgcn_raw_buffer_store_b128(d_, r, vo, so, aux); asm volatile("s_nop 1" :: "v"(d_)); } while (0)
__device__ __forceinline__ float bf2f(u16 v) { return __uint_as_float((unsigned)v << 16); }
__device__ __forceinline__ u16 f2bf(float f) { __bf16 b = (__bf16)f; return __builtin_bit_cast(u16, b); }
__device__ __forceinline__ float bfr(float f) { return (float)(__bf16)f; }
typedef __attribute__((ext_vector_type(2))) float f32x2;
typedef __attribute__((ext_vector_type(2))) __bf16 bf16x2;
__device__ __forceinline__ unsigned pack2(float a, float b) { return __builtin_bit_cast(unsigned, __builtin_convertvector((f32x2){a, b}, bf16x2)); }
typedef __attribute__((ext_vector_type(2))) _Float16 f16x2;
typedef __attribute__((ext_vector_type(8))) _Float16 f16x8;
__device__ __forceinline__ unsigned packh(float a, float b) { return __builtin_bit_cast(unsigned, __builtin_convertvector((f32x2){a, b}, f16x2)); }
__device__ __forceinline__ f32x4 mfmah(u32x4 a, u32x4 b) { return __builtin_amdgcn_mfma_f32_16x16x32_f16(__builtin_bit_cast(f16x8, a), __builtin_bit_cast(f16x8, b), (f32x4){0.f, 0.f, 0.f, 0.f}, 0, 0, 0); }
__device__ __forceinline__ float lo2f(unsigned v) { return __uint_as_float(v << 16); }
__device__ __forceinline__ float hi2f(unsigned v) { return __uint_as_float(v & 0xFFFF0000u); }
__device__ __forceinline__ float fmx(float a, float b) { return __builtin_elementwise_maximum(a, b); }
__device__ __forceinline__ f32x4 mfma32(u32x4 a, u32x4 b, f32x4 c) {
    return __builtin_amdgcn_mfma_f32_16x16x32_bf16(__builtin_bit_cast(bf16x8, a), __builtin_bit_cast(bf16x8, b), c, 0, 0, 0);
}
__device__ __forceinline__ f32x4 mfma16(u32x2 a, u32x2 b, f32x4 c) {
    return __builtin_amdgcn_mfma_f32_16x16x16bf16_1k(__builtin_bit_cast(s16x4, a), __builtin_bit_cast(s16x4, b), c, 0, 0, 0);
}
__device__ __forceinline__ f32x4 smfmac(u32x4 a, u32x8 b, f32x4 c, int idx) {
    return __builtin_amdgcn_smfmac_f32_16x16x64_bf16(__builtin_bit_cast(bf16x8, a), __builtin_bit_cast(bf16x16, b), c, idx, 0, 0);
}
__device__ __forceinline__ int pi16(int k) { return ((k & 3) << 2) | ((k >> 1) & 2) | (k >> 3); }
__device__ __forceinline__ float addswap32(float a, float b) {
    auto r = __builtin_amdgcn_permlane32_swap(__float_as_uint(a), __float_as_uint(b), false, false);
    return __uint_as_float(r[0]) + __uint_as_float(r[1]);
}
__device__ __forceinline__ float xmax3216(float v) {
    auto r = __builtin_amdgcn_permlane32_swap(__float_as_uint(v), __float_as_uint(v), false, false);
    v = fmx(__uint_as_float(r[0]), __uint_as_float(r[1]));
    auto q = __builtin_amdgcn_permlane16_swap(__float_as_uint(v), __float_as_uint(v), false, false);
    return fmx(__uint_as_float(q[0]), __uint_as_float(q[1]));
}
__device__ __forceinline__ float xsum3216(float v) {
    auto r = __builtin_amdgcn_permlane32_swap(__float_as_uint(v), __float_as_uint(v), false, false);
    v = __uint_as_float(r[0]) + __uint_as_float(r[1]);
    auto q = __builtin_amdgcn_permlane16_swap(__float_as_uint(v), __float_as_uint(v), false, false);
    return __uint_as_float(q[0]) + __uint_as_float(q[1]);
}
template <int CTRL> __device__ __forceinline__ float dppf(float v) { return __uint_as_float((unsigned)__builtin_amdgcn_update_dpp(0, (int)__float_as_uint(v), CTRL, 0xF, 0xF, false)); }
__device__ __forceinline__ float wsum(float v) {
    v += dppf<0xB1>(v); v += dppf<0x4E>(v); v += dppf<0x141>(v); v += dppf<0x140>(v);
    return xsum3216(v);
}
__device__ __forceinline__ unsigned ror8(unsigned v) { return (unsigned)__builtin_amdgcn_update_dpp(0, (int)v, 0x128, 0xF, 0xF, false); }
__device__ __forceinline__ float ror8f(float v) { return __uint_as_float(ror8(__float_as_uint(v))); }
__device__ __forceinline__ unsigned nosent(unsigned v) {
    u16x2 lim = {0xFFFE, 0xFFFE};
    return __builtin_bit_cast(unsigned, __builtin_elementwise_min(__builtin_bit_cast(u16x2, v), lim));
}
__device__ __forceinline__ unsigned pkmax(unsigned a, unsigned b) {
    return __builtin_bit_cast(unsigned, __builtin_elementwise_max(__builtin_bit_cast(u16x2, a), __builtin_bit_cast(u16x2, b)));
}
__device__ __forceinline__ bool has_sent(u32x4 v) {
    unsigned m = pkmax(pkmax(v.x, v.y), pkmax(v.z, v.w));
    return (m & 0xFFFFu) == 0xFFFFu || (m >> 16) == 0xFFFFu;
}

typedef f32x2 c32;
__device__ __forceinline__ c32 fma2(c32 a, c32 b, c32 c) { return __builtin_elementwise_fma(a, b, c); }
__device__ __forceinline__ c32 dup(float v) { return (c32){v, v}; }
template <int SG> __device__ __forceinline__ c32 addi(c32 a, c32 b) {
    c32 d;
    if (SG > 0) asm("v_pk_add_f32 %0, %1, %2 op_sel:[0,1] op_sel_hi:[1,0] neg_lo:[0,1]" : "=v"(d) : "v"(a), "v"(b));
    else asm("v_pk_add_f32 %0, %1, %2 op_sel:[0,1] op_sel_hi:[1,0] neg_hi:[0,1]" : "=v"(d) : "v"(a), "v"(b));
    return d;
}
template <int SG> __device__ __forceinline__ c32 ctw(c32 a, c32 w) {
    c32 d = a * w.xx;
    if (SG < 0) asm("v_pk_fma_f32 %0, %1, %2, %0 op_sel:[1,1,0] op_sel_hi:[0,1,1] neg_lo:[0,1,0]" : "+v"(d) : "v"(a), "v"(w));
    else asm("v_pk_fma_f32 %0, %1, %2, %0 op_sel:[1,1,0] op_sel_hi:[0,1,1] neg_hi:[0,1,0]" : "+v"(d) : "v"(a), "v"(w));
    return d;
}
__device__ __forceinline__ c32 cis(float rev) { return (c32){__builtin_amdgcn_cosf(rev), __builtin_amdgcn_sinf(rev)}; }
template <int N> __device__ __forceinline__ void cpow(c32 (&t)[N], float rev) {
    t[0] = cis(rev);
    asm volatile("s_nop 0" : "+v"(t[0]));
#pragma unroll
    for (int i = 1; i < N; ++i) { t[i] = ctw<-1>(t[(i - 1) / 2], t[i / 2]); asm volatile("" : "+v"(t[i])); }
}
#define SB() __builtin_amdgcn_sched_barrier(0)
#define SA() do { SB(); asm volatile(".p2align 3"); SB(); } while (0)
template <int SG, int I0, int N, class F> __device__ __forceinline__ void ctwn(c32 (&u)[N], F w) {
    c32 d[N];
    SA();
#pragma unroll
    for (int i = I0; i < N; ++i) asm("v_pk_mul_f32 %0, %1, %2 op_sel_hi:[1,0]" : "=v"(d[i]) : "v"(u[i]), "v"(w(i)));
    SB();
#pragma unroll
    for (int i = I0; i < N; ++i) {
        if (SG < 0) asm("v_pk_fma_f32 %0, %1, %2, %0 op_sel:[1,1,0] op_sel_hi:[0,1,1] neg_lo:[0,1,0]" : "+v"(d[i]) : "v"(u[i]), "v"(w(i)));
        else asm("v_pk_fma_f32 %0, %1, %2, %0 op_sel:[1,1,0] op_sel_hi:[0,1,1] neg_hi:[0,1,0]" : "+v"(d[i]) : "v"(u[i]), "v"(w(i)));
        u[i] = d[i];
    }
    SB();
}
template <int SG, int M, int A, int B, int CI, int N> __device__ __forceinline__ void dft4n(c32 (&u)[N]) {
    c32 t[M][4];
    SA();
#pragma unroll
    for (int i = 0; i < M; ++i) {
        const c32 a = u[A * i], b = u[A * i + B], c = u[A * i + 2 * B], d = u[A * i + 3 * B];
        t[i][0] = i == CI ? addi<SG>(a, c) : a + c; t[i][1] = i == CI ? addi<-SG>(a, c) : a - c; t[i][2] = b + d; t[i][3] = b - d;
        if (B == 4) SB();
    }
    SB();
#pragma unroll
    for (int i = 0; i < M; ++i) { u[A * i] = t[i][0] + t[i][2]; u[A * i + 2 * B] = t[i][0] - t[i][2]; u[A * i + B] = addi<SG>(t[i][1], t[i][3]); u[A * i + 3 * B] = addi<-SG>(t[i][1], t[i][3]); }
    SB();
}
template <int SG> __device__ __forceinline__ void dft8x2(c32 (&u)[16]) {
    dft4n<SG, 4, 1, 4, -1>(u);
    const float r = 0.70710678118654752440f;
    c32 o[16];
#pragma unroll
    for (int e = 0; e < 2; ++e) { u[6 + e] = addi<SG>(u[6 + e], u[6 + e]); u[14 + e] = addi<-SG>(u[14 + e], u[14 + e]); }
#pragma unroll
    for (int e = 0; e < 2; ++e) { o[e] = u[e] + u[2 + e]; o[8 + e] = u[e] - u[2 + e]; o[4 + e] = addi<SG>(u[8 + e], u[10 + e]); o[12 + e] = addi<-SG>(u[8 + e], u[10 + e]); }
    SB();
#pragma unroll
    for (int e = 0; e < 2; ++e) { o[2 + e] = fma2(u[6 + e], dup(r), u[4 + e]); o[10 + e] = fma2(u[6 + e], dup(-r), u[4 + e]); o[6 + e] = fma2(u[14 + e], dup(-r), u[12 + e]); o[14 + e] = fma2(u[14 + e], dup(r), u[12 + e]); }
    SB();
#pragma unroll
    for (int i = 0; i < 16; ++i) u[i] = o[i];
}
template <int SG> __device__ __forceinline__ void dft16(c32 (&u)[16]) {
    const float c1 = 0.92387953251128675613f, s1 = 0.38268343236508977173f, r = 0.70710678118654752440f;
    dft4n<SG, 4, 1, 4, -1>(u);
    const int ix[8] = {5, 9, 13, 6, 14, 7, 11, 15};
    const float wx[8] = {c1, r, s1, r, -r, s1, -r, -c1}, wy[8] = {s1, r, c1, r, r, c1, r, -s1};
    c32 d[8];
#pragma unroll
    for (int i = 0; i < 8; ++i) d[i] = u[ix[i]] * dup(wx[i]);
    SB();
#pragma unroll
    for (int i = 0; i < 8; ++i) u[ix[i]] = fma2(u[ix[i]].yx, (c32){-SG * wy[i], SG * wy[i]}, d[i]);
    SB();
    dft4n<SG, 4, 4, 1, 2>(u);
    c32 o[16];
#pragma unroll
    for (int i = 0; i < 16; ++i) o[(i >> 2) + 4 * (i & 3)] = u[i];
#pragma unroll
    for (int i = 0; i < 16; ++i) u[i] = o[i];
}
template <int SG> __device__ __forceinline__ void dft10(c32 (&u)[10]) {
    const float c1 = 0.30901699437494742410f, c2 = -0.80901699437494742410f, s1 = 0.95105651629515357212f, s2 = 0.58778525229247312917f;
    c32 x[2][5], t[2][4], a[2][5];
    SA();
#pragma unroll
    for (int i = 0; i < 5; ++i) { x[0][i] = u[2 * i] + u[(2 * i + 5) % 10]; x[1][i] = u[2 * i] - u[(2 * i + 5) % 10]; }
    SB();
#pragma unroll
    for (int e = 0; e < 2; ++e) { t[e][0] = x[e][1] + x[e][4]; t[e][1] = x[e][2] + x[e][3]; t[e][2] = x[e][1] - x[e][4]; t[e][3] = x[e][2] - x[e][3]; }
    SB();
#pragma unroll
    for (int e = 0; e < 2; ++e) { a[e][0] = t[e][0] + t[e][1]; a[e][1] = fma2(t[e][0], dup(c1), x[e][0]); a[e][2] = fma2(t[e][0], dup(c2), x[e][0]); a[e][3] = t[e][2] * dup(s1); a[e][4] = t[e][2] * dup(s2); }
    SB();
#pragma unroll
    for (int e = 0; e < 2; ++e) { a[e][0] += x[e][0]; a[e][1] = fma2(t[e][1], dup(c2), a[e][1]); a[e][2] = fma2(t[e][1], dup(c1), a[e][2]); a[e][3] = fma2(t[e][3], dup(s2), a[e][3]); a[e][4] = fma2(t[e][3], dup(-s1), a[e][4]); }
    SB();
#pragma unroll
    for (int e = 0; e < 2; ++e) {
        u[5 * e] = a[e][0];
        u[(6 + 5 * e) % 10] = addi<SG>(a[e][1], a[e][3]); u[(24 + 5 * e) % 10] = addi<-SG>(a[e][1], a[e][3]);
        u[(12 + 5 * e) % 10] = addi<SG>(a[e][2], a[e][4]); u[(18 + 5 * e) % 10] = addi<-SG>(a[e][2], a[e][4]);
    }
    SB();
}
template <int ST> __device__ __forceinline__ void lds16(c32 (&u)[16], const char* p) {
    const LAS char* q = (const LAS char*)p;
#pragma unroll
    for (int i = 0; i < 16; ++i) asm volatile("ds_read_b64 %0, %1 offset:%2" : "=v"(u[(i >> 2) + 4 * (i & 3)]) : "v"(q), "n"(((i >> 2) + 4 * (i & 3)) * ST) : "memory");
#pragma unroll
    for (int i = 0; i < 4; ++i) asm volatile("s_waitcnt lgkmcnt(%4)" : "+v"(u[i]), "+v"(u[i + 4]), "+v"(u[i + 8]), "+v"(u[i + 12]) : "n"(12 - 4 * i) : "memory");
}
__device__ __forceinline__ c32 unpack2(unsigned v) { return (c32){lo2f(v), hi2f(v)}; }
__device__ __forceinline__ float wsum2(c32 v) {
    float t = addswap32(v.x, v.y);
    auto q = __builtin_amdgcn_permlane16_swap(__float_as_uint(t), __float_as_uint(t), false, false);
    t = __uint_as_float(q[0]) + __uint_as_float(q[1]);
    t += dppf<0xB1>(t); t += dppf<0x4E>(t); t += dppf<0x141>(t); t += dppf<0x140>(t);
    return t;
}
__device__ __forceinline__ c32 halves(float t) { return (c32){__uint_as_float(__builtin_amdgcn_readlane(__float_as_uint(t), 0)), __uint_as_float(__builtin_amdgcn_readlane(__float_as_uint(t), 32))}; }
__device__ __forceinline__ void ln_rows(const c32 (&v)[10], unsigned (&o)[10]) {
    const float mean = wsum2(((v[0] + v[1]) + (v[2] + v[3])) + ((v[4] + v[5]) + (v[6] + v[7])) + (v[8] + v[9])) * (1.f / 640.f);
    const c32 m = halves(mean), mb = halves(bfr(mean));
    c32 d[10], e[10];
#pragma unroll
    for (int r = 0; r < 10; ++r) d[r] = v[r] - m;
    SB();
#pragma unroll
    for (int r = 0; r < 5; ++r) e[r] = d[r] * d[r];
#pragma unroll
    for (int r = 0; r < 5; ++r) d[r] = fma2(d[r + 5], d[r + 5], e[r]);
    SB();
#pragma unroll
    for (int r = 0; r < 10; ++r) { const c32 y = v[r] - mb; e[r] = unpack2(pack2(y.x, y.y)); }
    const float var = wsum2((d[0] + d[1]) + (d[2] + d[3]) + d[4]) * (1.f / 640.f);
    const c32 rs = halves(__builtin_amdgcn_rcpf(bfr(__builtin_amdgcn_sqrtf(bfr(bfr(var) + 1e-5f)))));
    SB();
#pragma unroll
    for (int r = 0; r < 10; ++r) e[r] *= rs;
    SB();
#pragma unroll
    for (int r = 0; r < 10; ++r) o[r] = pack2(e[r].x, e[r].y);
    SB();
}

__device__ __forceinline__ __amdgpu_buffer_rsrc_t mkrsrc(const void* p, unsigned bytes) {
    return __builtin_amdgcn_make_buffer_rsrc((void*)p, 0, bytes, 0x00020000);
}

constexpr unsigned O_F = (unsigned)OFF_F, O_Y0 = (unsigned)OFF_Y0;
__device__ __forceinline__ unsigned nosent1(unsigned v) { return min(v, 0xFFFFFFFEu); }
__device__ __forceinline__ u32x4 nosent4(u32x4 v) { v[1] = nosent1(v[1]); v[3] = nosent1(v[3]); return v; }
__device__ __forceinline__ unsigned smax(u32x4 v) { return max(v[1], v[3]); }
constexpr unsigned SPIN_CAP = 400000u;
constexpr int RW = 5920;
struct RowTw { c32 a[9], b, c; };
__device__ __forceinline__ RowTw row_tw(int lane) {
    RowTw t;
    t.b = cis((float)(lane & 31) * (-1.f / 64.f)); t.c = cis((float)(lane & 15) * (-1.f / 32.f));
    asm volatile("" : "+v"(t.b), "+v"(t.c));
    cpow(t.a, (float)lane * (-1.f / 640.f));
    return t;
}
template <int B> __device__ __forceinline__ void cswap(c32& x, c32& y) {
#pragma unroll
    for (int e = 0; e < 2; ++e) {
        auto r = B == 32 ? __builtin_amdgcn_permlane32_swap(__float_as_uint(x[e]), __float_as_uint(y[e]), false, false) : __builtin_amdgcn_permlane16_swap(__float_as_uint(x[e]), __float_as_uint(y[e]), false, false);
        x[e] = __uint_as_float(r[0]); y[e] = __uint_as_float(r[1]);
    }
}
template <int B> __device__ __forceinline__ void cswap5(c32 (&p)[5], c32 (&q)[5]) {
#pragma unroll
    for (int i = 0; i < 5; ++i) cswap<B>(p[i], q[i]);
    SB();
}
__device__ __forceinline__ void bfly5(c32 (&p)[5], c32 (&q)[5]) {
#pragma unroll
    for (int i = 0; i < 5; ++i) { const c32 t = p[i] - q[i]; p[i] += q[i]; q[i] = t; }
    SB();
}
constexpr unsigned L_X = 16 * RW;
__device__ __forceinline__ void row_x(__amdgpu_buffer_rsrc_t rx, int row, int w, int lane) {
    asm volatile("s_mov_b32 m0, %3\n\ts_nop 0\n\tbuffer_load_dwordx4 %0, %1, %2 offen sc1 lds\n\tbuffer_load_dwordx4 %0, %1, %2 offen offset:1024 sc1 lds\n\tbuffer_load_dwordx4 %0, %1, %2 offen offset:1536 sc1 lds"
                 :: "v"(lane * 16), "s"(rx), "s"(row * (D_ * 2)), "s"(L_X + w * 2560u) : "m0", "memory");
}
template <int M> __device__ __forceinline__ void row_get(u16 (&v)[20], int w, int lane) {
    asm volatile("s_waitcnt vmcnt(%0)" :: "n"(M) : "memory");
#pragma unroll
    for (int r = 0; r < 20; ++r) v[r] = *(const LAS u16*)(size_t)(L_X + w * 2560u + lane * 2u + 128u * (r >> 1) + 1280u * (r & 1));
    SB();
}
template <int NW, int NT, class FS> __device__ __forceinline__ void a1_unit(__amdgpu_buffer_rsrc_t rx, int row0, bool tail, __amdgpu_buffer_rsrc_t rws, char* sm, const RowTw& tw, int w, int lane, FS st_) {
    const unsigned fo = tail ? O_Y0 + 8192u + (unsigned)(row0 & 255) * 16u : O_F + (unsigned)row0 * 16u, gs = tail ? YS : GF;
    if (w < NW) {
        c32 u[10];
        unsigned h[10];
        u16 x[20];
        row_x(rx, row0 + 2 * w, w, lane);
        st_(-1);
        row_get<NW == 8 ? 8 : 0>(x, w, lane);
        if (tail) for (unsigned spins = 0; spins < SPIN_CAP; ++spins) {
            unsigned m = 0;
#pragma unroll
            for (int r = 0; r < 20; ++r) m = max(m, (unsigned)x[r]);
            if (!__any(m == 0xFFFFu)) break;
            row_x(rx, row0 + 2 * w, w, lane);
            row_get<0>(x, w, lane);
        }
#pragma unroll
        for (int r = 0; r < 10; ++r) u[r] = (c32){bf2f(x[2 * r]), bf2f(x[2 * r + 1])};
        st_(0);
        ln_rows(u, h);
        st_(1);
#pragma unroll
        for (int r = 0; r < 10; ++r) u[r] = unpack2(h[r]);
        dft10<-1>(u);
        char* mine = sm + w * RW;
        char* ex = mine + ((lane >> 5) + 10 * ((lane >> 4) & 1)) * 144 + (lane & 15) * 8;
        ctwn<-1, 1>(u, [&](int i) { return tw.a[i - 1]; });
        c32 p[5], q[5];
#pragma unroll
        for (int i = 0; i < 5; ++i) { p[i] = u[2 * i]; q[i] = u[2 * i + 1]; }
        cswap5<32>(p, q); bfly5(p, q); ctwn<-1, 0>(q, [&](int) { return tw.b; });
        cswap5<16>(p, q); bfly5(p, q);
#pragma unroll
        for (int i = 0; i < 5; ++i) *(c32*)(ex + i * 288) = p[i];
        ctwn<-1, 0>(q, [&](int) { return tw.c; });
#pragma unroll
        for (int i = 0; i < 5; ++i) *(c32*)(ex + i * 288 + 2880) = q[i];
        asm volatile("" ::: "memory");
        st_(2);
        if (lane < 40) {
            c32 v[16];
#pragma unroll
            for (int m = 0; m < 8; ++m) { const f32x4 t = *(const f32x4*)(mine + lane * 144 + m * 16); v[2 * m] = t.xy; v[2 * m + 1] = t.zw; }
            dft16<-1>(v);
            asm volatile("" ::: "memory");
#pragma unroll
            for (int m = 0; m < 16; ++m) *(c32*)(mine + lane * 8 + m * 320) = v[m];
        }
        st_(3);
    }
    asm volatile("s_waitcnt lgkmcnt(0)\n\ts_barrier" ::: "memory");
    st_(4);
    const int tid = w * 64 + lane, k0 = tid / (2 * NW) * 4 + (tid & 3), id = tid % (2 * NW);
    const char* X = sm + (id >> 2) * (2 * RW);
#pragma unroll
    for (int c = 0; c * NT < NG * 2 * NW; ++c) {
        const int k4 = k0 + c * (2 * NT / NW), k = min(k4, 320);
        if ((c + 1) * NT <= NG * 2 * NW || k4 < 4 * NG) {
            u32x4 o;
#pragma unroll
            for (int e = 0; e < 2; ++e) {
                const c32 zk = *(const c32*)(X + e * RW + k * 8) * dup(1.f / 32.f), zn = *(const c32*)(X + e * RW + (k == 0 ? 0 : 640 - k) * 8) * dup(1.f / 32.f);
                o[2 * e] = packh(zk.x + zn.x, zk.y - zn.y); o[2 * e + 1] = packh(zk.y + zn.y, zn.x - zk.x);
            }
            st128(nosent4(o), rws, (unsigned)(k4 >> 2) * gs + id * 16u, fo, 16);
        }
    }
}
template <int NW, class FS, class FD> __device__ __forceinline__ void a3_units(__amdgpu_buffer_rsrc_t rws, __amdgpu_buffer_rsrc_t rx, int row0, int step, int lay, char* sm, const RowTw& tw, int w, int lane, FS st_, FD done) {
    constexpr int NT = 512, NC = (NG * 2 * NW + NT - 1) / NT, GS = NW == 4 ? YS : GF;
    const int tid = w * 64 + lane, k0 = tid / (2 * NW) * 4 + (tid & 3), id = tid % (2 * NW);
    u32x4 fv[NC];
    unsigned sc[NC];
    u16 x[20];
    auto get = [&](int row) {
        const unsigned fo = (NW == 4 ? O_Y0 + lay * 4096u : O_F) + (unsigned)row * 16u;
#pragma unroll
        for (int c = 0; c < NC; ++c) fv[c] = __builtin_amdgcn_raw_buffer_load_b128(rws, (unsigned)((min(k0 + c * (2 * NT / NW), 320) >> 2) * GS + id * 16), fo, 16);
        __builtin_amdgcn_sched_barrier(0);
    };
    auto factors = [&]() {
#pragma unroll
        for (int c = 0; c < NC; ++c) { const int k = min(k0 + c * (2 * NT / NW), 320); sc[c] = __builtin_amdgcn_raw_buffer_load_b32(rws, (unsigned)((k >> 2) * YS + (k & 3) * 4), O_Y0 + 12288u, 16); }
    };
    factors();
    if (w < NW) row_x(rx, row0 + 2 * w, w, lane);
    st_(-1);
    for (unsigned spins = 0; spins < SPIN_CAP; ++spins) {
        asm volatile("" ::: "memory");
        if (!lay) factors();
        get(row0);
        if (NW == 8 || !__any(max(max(smax(fv[0]), smax(fv[1])), max(sc[0], sc[1])) == 0xFFFFFFFFu)) break;
    }
    for (int n = 0;; ++n, row0 += step, sm += n & 1 ? 8 * RW : -8 * RW) {
    char* X = sm + (id >> 2) * (2 * RW);
#pragma unroll
    for (int c = 0; c < NC; ++c) {
        const int k = k0 + c * (2 * NT / NW);
        const unsigned re = k == 0 || k == 320 ? 0xFFFFu : ~0u;
        if (c + 1 < NC || k <= 320)
#pragma unroll
            for (int e = 0; e < 2; ++e) {
                const c32 p = __builtin_convertvector(__builtin_bit_cast(f16x2, fv[c][2 * e] & re), c32) * dup(__uint_as_float(sc[c])), q = __builtin_convertvector(__builtin_bit_cast(f16x2, fv[c][2 * e + 1] & re), c32) * dup(__uint_as_float(sc[c]));
                *(c32*)(X + e * RW + k * 8) = addi<1>(p, q); *(c32*)(X + e * RW + (k == 0 ? 0 : 640 - k) * 8) = (c32){p.x + q.y, q.x - p.y};
            }
    }
    asm volatile("s_waitcnt vmcnt(0) lgkmcnt(0)\n\ts_barrier" ::: "memory");
    st_(0);
    if (NW == 8) {
        if (n) done(row0 - step);
        row_get<63>(x, w, lane);
        asm volatile("s_waitcnt lgkmcnt(0)" ::: "memory");
        if (row0 + step < S_) { row_x(rx, row0 + step + 2 * w, w, lane); get(row0 + step); }
    }
    if (w < NW) {
        char* mine = sm + w * RW;
        if (lane < 40) {
            c32 v[16];
            lds16<320>(v, mine + lane * 8);
            dft16<1>(v);
            asm volatile("" ::: "memory");
#pragma unroll
            for (int m = 0; m < 8; ++m) *(f32x4*)(mine + lane * 144 + m * 16) = (f32x4){v[2 * m].x, v[2 * m].y, v[2 * m + 1].x, v[2 * m + 1].y};
        }
        asm volatile("" ::: "memory");
        st_(1);
        const char* ex = mine + ((lane >> 5) + 10 * ((lane >> 4) & 1)) * 144 + (lane & 15) * 8;
        c32 u[10];
        c32 p[5], q[5];
#pragma unroll
        for (int i = 0; i < 5; ++i) q[i] = *(const c32*)(ex + i * 288 + 2880);
#pragma unroll
        for (int i = 0; i < 5; ++i) p[i] = *(const c32*)(ex + i * 288);
        SB();
        ctwn<1, 0>(q, [&](int) { return tw.c; }); bfly5(p, q); cswap5<16>(p, q);
        ctwn<1, 0>(q, [&](int) { return tw.b; }); bfly5(p, q); cswap5<32>(p, q);
#pragma unroll
        for (int i = 0; i < 5; ++i) { u[2 * i] = p[i]; u[2 * i + 1] = q[i]; }
        ctwn<1, 1>(u, [&](int i) { return tw.a[i - 1]; });
        if (NW == 4) row_get<63>(x, w, lane);
        dft10<1>(u);
        st_(2);
        asm volatile("" ::: "memory");
        char* st = mine + lane * 2;
        unsigned h[10];
#pragma unroll
        for (int r = 0; r < 10; ++r) {
            const c32 y = (c32){bf2f(x[2 * r]), bf2f(x[2 * r + 1])} + unpack2(pack2(u[r].x, u[r].y));
            const unsigned p = pack2(y.x, y.y);
            u[r] = unpack2(p);
            *(u16*)(st + r * 128) = (u16)p; *(u16*)(st + 1280 + r * 128) = (u16)(p >> 16);
        }
        st_(3);
        ln_rows(u, h);
        st_(4);
#pragma unroll
        for (int r = 0; r < 10; ++r) { *(u16*)(st + 2560 + r * 128) = (u16)h[r]; *(u16*)(st + 3840 + r * 128) = (u16)(h[r] >> 16); }
        asm volatile("" ::: "memory");
        u32x4 o[6];
#pragma unroll
        for (int c = 0; c < 6; ++c) o[c] = *(const u32x4*)(mine + (c < 3 ? 2560 : 0) + min(c % 3 * 64 + lane, 159) * 16);
#pragma unroll
        for (int c = 0; c < 3; ++c) {
            const int i = min(c * 64 + lane, 159), rr = i >= 80, ch = i - 80 * rr, row = (row0 + 2 * w + rr) & 31;
            st128(nosent4(o[c]), rws, (unsigned)((ch >> 3) * 4096 + row * 128 + (((ch & 7) ^ (row & 7)) << 4)) | (c == 2 ? (lane & 32u) << 25 : 0u), (unsigned)OFF_H + (unsigned)((row0 >> 5) + 4 - 4 * lay) * 40960u, 16);
        }
#pragma unroll
        for (int c = 0; c < 3; ++c) st128(o[3 + c], rws, (unsigned)(c * 64 + lane) * 16u | (c == 2 ? (lane & 32u) << 25 : 0u), (unsigned)OFF_X1 + (unsigned)(row0 + 2 * w) * (D_ * 2), 16);
    }
    if (NW == 4 || row0 + step >= S_) break;
    }
    st_(5);
    asm volatile("s_waitcnt vmcnt(0)\n\ts_barrier" ::: "memory");
    done(row0);
}

__device__ __forceinline__ unsigned xcc_id() { unsigned x; asm volatile("s_getreg_b32 %0, hwreg(HW_REG_XCC_ID)" : "=s"(x)); return x & 7u; }
__device__ __forceinline__ unsigned probe(__amdgpu_buffer_rsrc_t rw, unsigned base) {
    unsigned mn[4] = {~0u, ~0u, ~0u, ~0u};
#pragma unroll 1
    for (int r = 0; r < 2; ++r) {
#pragma unroll
        for (int i = 0; i < 4; ++i) {
            const unsigned long long t1 = __builtin_amdgcn_s_memtime();
            asm volatile("s_waitcnt lgkmcnt(0)" ::: "memory");
            unsigned v = __builtin_amdgcn_raw_buffer_load_b32(rw, i * HALF, base + r * 4096u, 16);
            asm volatile("s_waitcnt vmcnt(0)" : "+v"(v) :: "memory");
            const unsigned long long t2 = __builtin_amdgcn_s_memtime();
            mn[i] = min(mn[i], (unsigned)(t2 - t1) + v);
        }
    }
    return mn[1] + mn[3] < mn[0] + mn[2] ? (mn[3] < mn[1] ? 3u : 1u) : (mn[2] < mn[0] ? 2u : 0u);
}
__device__ __forceinline__ void filter_unit(const u16* wr, const u16* wi, int s0, __amdgpu_buffer_rsrc_t rws, u16* tf, int tid) {
    u32x4 v[6];
#pragma unroll
    for (int i = 0; i < 6; ++i) v[i] = ((const u32x4*)((i < 3 ? wr : wi) + (size_t)s0 * DF))[min(tid + 512 * (i % 3), 1283)];
    __builtin_amdgcn_sched_barrier(0);
#pragma unroll
    for (int i = 0; i < 6; ++i) ((u32x4*)tf)[min(tid + 512 * (i % 3), 1283) + i / 3 * 1284] = v[i];
    __syncthreads();
    const int k = tid >> 3, sq = (tid & 7) * 4;
    u16 t[6][8];
#pragma unroll
    for (int i = 0; i < 6; ++i)
#pragma unroll
        for (int j = 0; j < 8; ++j) t[i][j] = tf[((j & 1) * 32 + sq + (j >> 1)) * DF + k + 64 * i];
    __builtin_amdgcn_sched_barrier(0);
#pragma unroll
    for (int i = 0; i < 6; ++i) {
        u32x4 o;
#pragma unroll
        for (int j = 0; j < 4; ++j) o[j] = (unsigned)t[i][2 * j] | (unsigned)t[i][2 * j + 1] << 16;
        if (i < 5 || tid < 8) st128(o, rws, (unsigned)(k * S_ + sq) * 4u, (unsigned)OFF_WFT + (unsigned)(s0 * 4 + i * 64 * S_ * 4), 16);
    }
}

__device__ __forceinline__ void dma1k(__amdgpu_buffer_rsrc_t r, unsigned vo, unsigned so, unsigned lds) {
    asm volatile("s_mov_b32 m0, %3\n\ts_nop 0\n\tbuffer_load_dwordx4 %0, %1, %2 offen sc1 lds" :: "v"(vo), "s"(r), "s"(so), "s"(lds) : "m0", "memory");
}
template <class FW, class FS> __device__ __forceinline__ void a2_unit(__amdgpu_buffer_rsrc_t rws, int G, int w, int lane, bool late, FW rdy, FS st_) {
    const int fr = lane & 15, g = lane >> 4;
    const unsigned fo = O_F + (unsigned)(G * GF), yo = O_Y0 + (unsigned)(G * YS);
    const unsigned sw = (unsigned)(lane ^ (4 * (w & 1) + g)) * 16u;
    if (w >= 4) {
        const unsigned v = (w - 4) * 1024u, t = late ? fo + 28672u + v : yo + 8192u + v;
        rdy(6);
#pragma unroll
        for (int i = 0; i < 7; ++i) dma1k(rws, sw, fo + v + i * 4096u, v + i * 4096u);
        rdy(7);
        for (unsigned spins = 0; spins < SPIN_CAP; ++spins) {
            dma1k(rws, sw, t, v + 28672u);
            asm volatile("s_waitcnt vmcnt(0)" ::: "memory");
            if (late || !__any(smax(*(const LAS u32x4*)(size_t)(v + 28672u + lane * 16u)) == 0xFFFFFFFFu)) break;
        }
        asm volatile("s_barrier" ::: "memory");
    } else {
        const unsigned wl = 32768u + w * 8192u;
        c32 wf[4][8], t2[4][7];
        f32x4 tr, ti, tn;
        u32x4 mb[2], ma[2];
        float back;
        auto ldw = [&]() {
#pragma unroll
            for (int i = 0; i < 8; ++i) dma1k(rws, lane * 16u, (unsigned)OFF_WFT + (unsigned)(min(4 * G + w, 320) * 8192 + i * 1024), wl + i * 1024u);
        };
        auto cvw = [&]() {
            unsigned wv[4][8], m = 0;
            asm volatile("s_waitcnt vmcnt(0)" ::: "memory");
#pragma unroll
            for (int i = 0; i < 32; ++i) wv[i >> 3][i & 7] = *(const LAS unsigned*)(size_t)(wl + (fr + 32 * g) * 4u + ((i >> 3 & 1) * 16 + (i >> 4) * 128 + (i & 7) * 256) * 4u);
            SB();
#pragma unroll
            for (int i = 0; i < 32; ++i) m = pkmax(m, wv[i >> 3][i & 7] & 0x7F807F80u);
            float x = __uint_as_float(max(m << 16, m & 0xFFFF0000u));
            x = fmaxf(x, dppf<0xB1>(x)); x = fmaxf(x, dppf<0x4E>(x)); x = fmaxf(x, dppf<0x141>(x)); x = fmaxf(x, dppf<0x140>(x));
            const unsigned e = min(max(__float_as_uint(xmax3216(x)), 8u << 23), 246u << 23);
            back = __uint_as_float(e) * (1.f / 40.f);
#pragma unroll
            for (int i = 0; i < 32; ++i) { wf[i >> 3][i & 7] = unpack2(wv[i >> 3][i & 7]) * dup(__uint_as_float((251u << 23) - e)); asm volatile("" : "+v"(wf[i >> 3][i & 7])); }
        };
        rdy(8);
        ldw();
#pragma unroll
        for (int i = 0; i < 4; ++i) {
            const int s = 2 * g + (i & 1) + (i >> 1) * 8;
            cpow(t2[i], (float)(fr + 16 * s) * (-1.f / 2048.f));
            const c32 t = cis((float)((4 * g + i) * fr) * (-1.f / 256.f)), p = cis((float)(s * fr) * (1.f / 16.f)) * dup(0.25f);
            tr[i] = t.x; ti[i] = t.y;
            mb[0][i] = packh(p.x, p.y); mb[1][i] = packh(-p.y, p.x);
#pragma unroll
            for (int h = 0; h < 2; ++h) { const c32 q = cis((float)((4 * g + i) * (8 * h + (fr >> 1)) + 4 * (fr & 1)) * (1.f / 16.f)) * dup(0.25f); ma[h][i] = packh(q.x, q.y); }
        }
        tn = -ti;
        asm volatile("" : "+v"(tn), "+v"(tr), "+v"(ti), "+v"(mb[0]), "+v"(mb[1]), "+v"(ma[0]), "+v"(ma[1]));
        cvw();
        LAS u32x4* im = (LAS u32x4*)(size_t)((fr + 32 * g) * 128u + (unsigned)(w ^ (fr >> 1)) * 16u);
        LAS u32x4* im1 = (LAS u32x4*)((size_t)im ^ 64u);
        asm volatile("s_barrier" ::: "memory");
        st_(2);
        u32x4 x[4][2];
#pragma unroll
        for (int h = 0; h < 2; ++h)
#pragma unroll
            for (int i = 0; i < 4; ++i) x[i][h] = (h ? im1 : im)[(i & 1) * 128 + (i >> 1) * 1024];
        SB();
        if (late && lane == 0) __builtin_amdgcn_raw_buffer_store_b32(__float_as_uint(back), rws, 0u, yo + 12288u + w * 4u, 16);
        f32x4 c[8][2];
        auto cvt = [](float a, float b) { unsigned r; asm("v_cvt_pk_f16_f32 %0, %1, %2" : "=v"(r) : "v"(a), "v"(b)); return r; };
        auto two = [&](auto in) {
            u32x4 d = in(0), n;
            SA();
#pragma unroll
            for (int e = 0; e < 8; ++e) {
                asm volatile("s_nop 1" : "+v"(d));
                c[e][0] = mfmah(d, mb[0]);
                if (e < 7) n = in(e + 1);
                c[e][1] = mfmah(d, mb[1]);
                d = n;
                SB();
            }
            SA();
#pragma unroll
            for (int e = 0; e < 8; e += 2) {
                const f32x4 p0 = c[e][0] * tr, p1 = c[e][0] * ti, p2 = c[e + 1][0] * tr, p3 = c[e + 1][0] * ti;
                SB();
                c[e][0] = __builtin_elementwise_fma(c[e][1], tn, p0); c[e][1] = __builtin_elementwise_fma(c[e][1], tr, p1);
                c[e + 1][0] = __builtin_elementwise_fma(c[e + 1][1], tn, p2); c[e + 1][1] = __builtin_elementwise_fma(c[e + 1][1], tr, p3);
                SB();
            }
            auto mid = [&](int e) { return (u32x4){packh(c[e][0][0], c[e][1][0]), packh(c[e][0][1], c[e][1][1]), packh(c[e][0][2], c[e][1][2]), packh(c[e][0][3], c[e][1][3])}; };
            d = mid(0);
#pragma unroll
            for (int e = 0; e < 8; ++e) {
                SB();
                c[e][0] = mfmah(ma[0], d);
                if (e < 7) n = mid(e + 1);
                c[e][1] = mfmah(ma[1], d);
                d = n;
            }
            SB();
        };
        two([&](int e) { return (u32x4){x[0][e >> 2][e & 3], x[1][e >> 2][e & 3], x[2][e >> 2][e & 3], x[3][e >> 2][e & 3]}; });
        asm volatile("s_nop 7" : "+v"(c[0][0]), "+v"(c[0][1]), "+v"(c[1][0]), "+v"(c[1][1]), "+v"(c[2][0]), "+v"(c[2][1]), "+v"(c[3][0]), "+v"(c[3][1]), "+v"(c[4][0]), "+v"(c[4][1]), "+v"(c[5][0]), "+v"(c[5][1]), "+v"(c[6][0]), "+v"(c[6][1]), "+v"(c[7][0]), "+v"(c[7][1]));
        st_(3);
        c32 u[2][16];
#pragma unroll
        for (int h = 0; h < 2; ++h) {
#pragma unroll
            for (int e = 0; e < 8; ++e) { u[h][2 * e] = c[e][h].xy; u[h][2 * e + 1] = c[e][h].zw; }
            ctwn<-1, 2>(u[h], [&](int i) { return t2[2 * h + (i & 1)][(i >> 1) - 1]; });
            dft8x2<-1>(u[h]);
            ctwn<-1, 0>(u[h], [&](int i) { return wf[2 * h + (i & 1)][i >> 1]; });
            dft8x2<1>(u[h]);
            ctwn<1, 2>(u[h], [&](int i) { return t2[2 * h + (i & 1)][(i >> 1) - 1]; });
            if (h == 0) st_(4);
        }
        two([&](int e) { return (u32x4){cvt(u[0][2 * e].y, u[0][2 * e].x), cvt(u[0][2 * e + 1].y, u[0][2 * e + 1].x), cvt(u[1][2 * e].y, u[1][2 * e].x), cvt(u[1][2 * e + 1].y, u[1][2 * e + 1].x)}; });
#pragma unroll
        for (int h = 0; h < 2; ++h) {
            if (h) asm volatile("" : "+v"(c[4][0]), "+v"(c[7][0]), "+v"(c[7][1]));
            SA();
#pragma unroll
            for (int i = 0; i < 4; ++i) {
                u32x4 o;
#pragma unroll
                for (int j = 0; j < 4; ++j) { const f32x4 y = c[4 * h + j][i >> 1]; o[j] = cvt(y[2 * (i & 1) + 1], y[2 * (i & 1)]); }
                (h ? im1 : im)[(i & 1) * 128 + (i >> 1) * 1024] = o;
            }
        }
        st_(5);
    }
    asm volatile("s_waitcnt lgkmcnt(0)\n\ts_barrier" ::: "memory");
    st_(6);
    u32x4 o[4];
#pragma unroll
    for (int j = 0; j < 4; ++j) o[j] = *(const LAS u32x4*)(size_t)(w * 1024u + j * 8192u + sw);
    SB();
#pragma unroll
    for (int j = 0; j < 4; ++j) {
        st128(j ? o[j] : nosent4(o[j]), rws, lane * 16u, (j || w >= 4 ? fo + j * 8192u : late ? yo : yo + 4096u) + w * 1024u, 16);
        if (j == 0) st_(7);
    }
}

struct RecArgs {
    unsigned* ctl; char* win; const u16* mem0; const u16* xin; u16* out;
    const u16* W[6];
};
template <int I> __device__ __forceinline__ char* karg(int i = 0) {
    unsigned long long p;
    asm volatile("s_load_dwordx2 %0, %1, %2\n\ts_waitcnt lgkmcnt(0)" : "=s"(p) : "s"(__builtin_amdgcn_kernarg_segment_ptr()), "s"(I * 8 + i * 8) : "memory");
    return (char*)p;
}
constexpr unsigned O_H = (unsigned)OFF_H, O_X1 = (unsigned)OFF_X1, O_Z = (unsigned)OFF_Z, O_Z2 = (unsigned)OFF_Z2, O_PT = (unsigned)OFF_PT, O_QK = (unsigned)OFF_QK;
#define MXV 0x02A80000000AAAAAull
#define MXQ 0x888888000000ull
static_assert((MXV & 15) == 10 && __builtin_popcountll(MXV >> 32) >= 2 && __builtin_popcountll(MXQ) >= 2 && !(MXQ & 15) && !((MXV | MXQ) >> 63), "segment sets");
constexpr int NREC = 96 + NOWN, NQT = NREC + NOWN, NXT = NQT + 32, NWRK = NXT + 32;
__device__ __forceinline__ bool spin_fail(unsigned& spins, __amdgpu_buffer_rsrc_t rws) {
    if (++spins > SPIN_CAP) { __builtin_amdgcn_raw_buffer_store_b32(1u, rws, 0u, 0u, 16); return true; }
    return (spins & 1023u) == 0u && __builtin_amdgcn_readfirstlane(__builtin_amdgcn_raw_buffer_load_b32(rws, 0u, 0u, 16));
}
#define ALN 6
#define AL ".p2align " STR(ALN) "\n\t"
#define STR2(x) #x
#define STR(x) STR2(x)
#define TPRE 0
#define QP 2
#define BQN 7
#define BQ(n) ".if " #n "==" STR(BQN) "\n\ts_barrier\n\t.endif\n\t"
#define SST(k) do {} while (0)
#define LSEL 1
#define RT(k) do {} while (0)
#define RTS() do {} while (0)
#define B3ATTR
#define MTA(k)
#define MTRT() do {} while (0)
#define MT(k) do {} while (0)
#define MTS() do {} while (0)
#define FC(x) do {} while (0)
#define DQC 1130
#define DVC 1200
#define M1 44
#define MZ 40
#define MZP 20
#define M2 224
#define LTP (LT0 + 15)
#define LTZ 340
#define PEN 280
#define KSL 1
#define SVO 64
#define DZP 46
#define LT0 330
#define SAD 1
#define PEN2 80
#define PEN1 50
#define PEN0 60
#define DDM2 0
#define DRA 700
#define TSLP 1
#define PFS 0
#define PFOFF 1
#define OPR 2
#define BSPARSE 0
#define ZLAG 7
#define TKP 0
#define DPS 12
#define RZ 63
#define RQ 63
#define R2 63
#define DXT 3000
#define SSW 99
#define LTM 18
#define XS0 13
#define XSU 8
#define XSM 32
#define LTU 4
#define LTD 1
#define QUIET 0
#define QUAD 1
#define LTS 3
#define XSS 3
#define TKS 1
#define LT1 358
#define FDL 2
#define ZLA 3
#define DPT2 3
#define NORND 0
#define LTX 28
#define TKE 200
#define DVO 0
#define ZPS 4
#define ASLP 4
#define BADG 0
#define DW1 0
#define WLA 3
#define XSLP 30
#define DOUT 215
#define DA1 145
#define DA2 215
#define DA20 270
#define DFB 150
#define DA3 185
#define DA30 230
#define YSLP 0
#define QEU 22
#define QE2 30
#define PEN00 120
#define DQ0 930
#define DQ00 1080
#define DZ0 420
#define DZ00 430
#define DOU2 200
#define DFA 0
#define SRG 0x5A000000u
#define OBN 5
#define OB(n) ".if " #n "==" STR(OBN) "\n\ts_barrier\n\tds_read_b64 v[102:103], %[sp]\n\tds_read_b64 v[104:105], %[sp] offset:512\n\tds_read_b64 v[106:107], %[sp] offset:1024\n\t.endif\n\t"
#define VSLP 0
#define CPF 1
#define DST 4
#define XST 0
#define ODL 8
#define VTS 1
#define XF 4
#define XFS 48
#define DOU1 50
#define DR0 0
#define DFB0 120
#define TPF 21
#define DZF 160
#define DZF0 175
#define DV0 260
#define QTP 1
#define XDL 0
#define DOW 200
#define DOU3 300
#define NTM 31
#define WK3 0
#define QEV 8
#define HLA 8
#define PSLP 16
#define PFD 4
#define NPF 3
#define KSL2 1
#define KBQ 4
#define VB1 6
#define VB2 5
#define KR1 3
#define QB2 2
#define NB2 6
#define DZR 24
#define ML 470
#define MN 12
#define CF 190
#define DB 1500
#define DP 200
#define ZSL 1
#define NB1 3
#define DPT 3
#define DDMA 8
#define DZ2 800
#define QEARLY 0
#define OSLP 12
#define OSLP2 4
#define WSLP 32
#define DO0 12000
#define DW0 1200
#define DQT 9000
#define DQ4 700
#define ZEARLY 3
#define HSLP 4
#define RMAP 0
#define DIE 0
#define VMWAIT() do { asm volatile("" ::: "memory"); __builtin_amdgcn_s_waitcnt(0x0F70); asm volatile("" ::: "memory"); } while (0)
#define BAR() asm volatile("s_waitcnt lgkmcnt(0)\n\ts_barrier" ::: "memory")
#define PIN1(a) asm volatile("" : "+v"(a) :: "memory")
#define PIN2(a, b) asm volatile("" : "+v"(a), "+v"(b) :: "memory")
#define BAR0() asm volatile("s_barrier" ::: "memory")
__device__ __forceinline__ unsigned fresh_lane() { unsigned l; asm volatile("v_mbcnt_lo_u32_b32 %0, -1, 0\n\tv_mbcnt_hi_u32_b32 %0, -1, %0" : "=v"(l)); __builtin_assume(l < 64u); return l; }
__device__ __forceinline__ u32x4 sent4() { u32x2 a, b; asm volatile("v_mov_b64 %0, -1\n\tv_mov_b64 %1, -1" : "=v"(a), "=v"(b)); return (u32x4){a[0], a[1], b[0], b[1]}; }
__device__ __forceinline__ void wait_until(unsigned long long t) { while ((long long)(__builtin_amdgcn_s_memtime() - t) < 0) __builtin_amdgcn_s_sleep(1); }
struct Clk { unsigned ta, ma, cpq, qpc; };
__device__ __forceinline__ int sx24(unsigned v) { return (int)(v << 8) >> 8; }
#define WAIT_FINE AL \
        "s_memrealtime vcc\n\t" \
        "s_memtime s[100:101]\n\t" \
        "s_brev_b32 %[fi], -2\n\t" \
        "s_waitcnt lgkmcnt(0)\n\t" \
        "s_mov_b32 %[rp], vcc_lo\n\t" \
        "s_lshl_b32 vcc_hi, vcc_lo, 2\n\t" \
        "s_sub_u32 %[x], %[t], vcc_hi\n\t" \
        "s_bfe_i32 %[x], %[x], 0x180000\n\t" \
        "s_min_i32 %[x], %[x], 4000\n\t" \
        "s_max_i32 %[x], %[x], -4000\n\t" \
        "s_add_u32 %[t], vcc_hi, %[x]\n\t" \
        "s_mul_i32 %[x], %[x], %[cq]\n\t" \
        "s_ashr_i32 %[x], %[x], 8\n\t" \
        "s_sub_u32 %[tm], %[t], %[ta]\n\t" \
        "s_bfe_i32 %[tm], %[tm], 0x180000\n\t" \
        "s_min_i32 %[tm], %[tm], 1000000\n\t" \
        "s_max_i32 %[tm], %[tm], -1000000\n\t" \
        "s_mul_i32 %[tm], %[tm], %[cq]\n\t" \
        "s_ashr_i32 %[tm], %[tm], 8\n\t" \
        "s_add_u32 %[tm], %[tm], %[ma]\n\t" \
        "s_sub_u32 %[tm], %[tm], s100\n\t" \
        "s_lshr_b32 vcc_hi, %[cq], 4\n\t" \
        "s_sub_u32 vcc_hi, %[x], vcc_hi\n\t" \
        "s_max_i32 %[tm], %[tm], vcc_hi\n\t" \
        "s_min_i32 %[tm], %[tm], %[x]\n\t" \
        "s_add_u32 %[tm], %[tm], s100\n" \
        "1:\n\t" \
        "s_memrealtime vcc\n\t" \
        "s_memtime s[100:101]\n\t" \
        "s_waitcnt lgkmcnt(0)\n\t" \
        "s_cmp_eq_u32 vcc_lo, %[rp]\n\t" \
        "s_cbranch_scc1 2f\n\t" \
        "s_mov_b32 %[rp], vcc_lo\n\t" \
        "s_lshl_b32 %[x], vcc_lo, 2\n\t" \
        "s_sub_u32 %[x], %[t], %[x]\n\t" \
        "s_bfe_i32 %[x], %[x], 0x180000\n\t" \
        "s_mul_i32 %[x], %[x], %[cq]\n\t" \
        "s_ashr_i32 %[x], %[x], 8\n\t" \
        "s_add_u32 %[x], %[x], s100\n\t" \
        "s_sub_u32 s101, %[x], %[tm]\n\t" \
        "s_cmp_lt_i32 s101, %[fi]\n\t" \
        "s_cselect_b32 %[tm], %[x], %[tm]\n\t" \
        "s_mov_b32 %[fi], 0\n" \
        "2:\n\t" \
        "s_sub_u32 %[x], %[tm], s100\n\t" \
        "s_cmp_gt_i32 %[x], 260\n\t" \
        "s_cbranch_scc1 1b\n\t" \
        "s_cmp_eq_u32 %[fi], 0\n\t" \
        "s_cselect_b32 %[ta], %[t], %[ta]\n\t" \
        "s_cselect_b32 %[ma], %[tm], %[ma]\n" \
        "3:\n\t" \
        "s_memtime s[100:101]\n\t" \
        "s_waitcnt lgkmcnt(0)\n\t" \
        "s_sub_u32 %[x], %[tm], s100\n\t" \
        "s_cmp_gt_i32 %[x], 92\n\t" \
        "s_cbranch_scc1 3b\n\t" \
        "s_max_i32 %[x], %[x], 0\n\t" \
        "s_andn2_b32 %[x], %[x], 3\n\t" \
        "s_sub_u32 %[x], 104, %[x]\n\t" \
        "s_getpc_b64 vcc\n\t" \
        "s_add_u32 vcc_lo, vcc_lo, %[x]\n\t" \
        "s_addc_u32 vcc_hi, vcc_hi, 0\n\t" \
        "s_setpc_b64 vcc\n\t" \
        ".rept 23\n\t" \
        "s_nop 0\n\t" \
        ".endr\n\t"
#define WAIT_FINE_OUT [tm] "=&s"(tm), [rp] "=&s"(rp), [x] "=&s"(x), [fi] "=&s"(fi), [t] "+s"(t), [ta] "+s"(c.ta), [ma] "+s"(c.ma)
__device__ __forceinline__ void wait_fine_bar(unsigned t, Clk& c) {
    unsigned tm, rp, x, fi;
    asm volatile(WAIT_FINE "s_barrier" : WAIT_FINE_OUT : [cq] "s"(c.cpq) : "vcc", "scc", "s100", "s101", "memory");
}
__device__ __forceinline__ void wait_cyc_bar(unsigned tm) {
    unsigned x;
    asm volatile(AL
        "3:\n\t"
        "s_memtime s[100:101]\n\t"
        "s_waitcnt lgkmcnt(0)\n\t"
        "s_sub_u32 %0, %1, s100\n\t"
        "s_cmp_gt_i32 %0, 92\n\t"
        "s_cbranch_scc1 3b\n\t"
        "s_max_i32 %0, %0, 0\n\t"
        "s_andn2_b32 %0, %0, 3\n\t"
        "s_sub_u32 %0, 104, %0\n\t"
        "s_getpc_b64 vcc\n\t"
        "s_add_u32 vcc_lo, vcc_lo, %0\n\t"
        "s_addc_u32 vcc_hi, vcc_hi, 0\n\t"
        "s_setpc_b64 vcc\n\t"
        ".rept 23\n\t"
        "s_nop 0\n\t"
        ".endr\n\t"
        "s_barrier"
        : "=&s"(x) : "s"(tm) : "vcc", "scc", "s100", "s101", "memory");
}
__device__ __forceinline__ unsigned cyc_at(const Clk& c, int d) {
    d = min(d, 20000);
    asm volatile("" : "+s"(d));
    return c.ma + (unsigned)((max(d, -4000) * (int)c.cpq) >> 8);
}
__device__ __forceinline__ void clk_rate(Clk& c, unsigned ta0, unsigned ma0) {
    const int dq = sx24(c.ta - ta0);
    const float r = fminf(fmaxf((float)(c.ma - ma0) * __builtin_amdgcn_rcpf((float)dq), 1.f), 8.f);
    unsigned x, y;
    asm volatile("s_nop 1\n\tv_readfirstlane_b32 %0, %2\n\tv_readfirstlane_b32 %1, %3\n\ts_nop 3" : "=s"(x), "=s"(y)
                 : "v"((unsigned)(256.f * r + 0.5f)), "v"((unsigned)(65536.f * __builtin_amdgcn_rcpf(r))));
    c.cpq = dq > 200 ? x : c.cpq; c.qpc = dq > 200 ? y : c.qpc;
}
__device__ __forceinline__ Clk clk_init() { return Clk{4u * (unsigned)__builtin_amdgcn_s_memrealtime(), (unsigned)__builtin_amdgcn_s_memtime(), 1470u, 11413u}; }
__device__ __forceinline__ int read_sched(__amdgpu_buffer_rsrc_t rws, __amdgpu_buffer_rsrc_t rwin, int s, unsigned tg, unsigned vo, unsigned to, unsigned f, unsigned g0, int& lt, int& xs, unsigned& fc) {
    if (__builtin_expect(__any((f >> 24) != tg), 0)) {
        for (unsigned spins = 0; __any((f >> 24) != tg);) {
            FC(fc);
            if (spin_fail(spins, rws)) break;
            __builtin_amdgcn_s_sleep(TSLP);
            asm volatile("" ::: "memory");
            if ((f >> 24) != tg) f = __builtin_amdgcn_raw_buffer_load_b32(rwin, vo, to, 16);
        }
    }
    int d = (int)((f - g0) & 0xFFFFFFu);
    d = max(d, __builtin_amdgcn_update_dpp(0, d, 0xB1, 0xF, 0xF, true));
    d = max(d, __builtin_amdgcn_update_dpp(0, d, 0x4E, 0xF, 0xF, true));
    d = max(d, __builtin_amdgcn_update_dpp(0, d, 0x141, 0xF, 0xF, true));
    d = max(d, __builtin_amdgcn_update_dpp(0, d, 0x140, 0xF, 0xF, true));
    d = max(d, __builtin_amdgcn_update_dpp(0, d, 0x142, 0xA, 0xF, true));
    d = max(__builtin_amdgcn_readlane(d, 16), __builtin_amdgcn_readlane(d, 48));
    const int l0 = lt, x0 = xs;
    d -= 0x400000;
    auto sel = [](int c, int x, int y) { int r; asm("s_cmp_lt_i32 %1, 0\n\ts_cselect_b32 %0, %2, %3" : "=s"(r) : "s"(c), "s"(x), "s"(y) : "scc"); return r; };
    const int miss = l0 + min(x0 + LTM, LTX) - d, first = s - 1;
    lt = sel((s ^ 1) - 1, LT0, sel(miss | first, l0, min(max(d, l0 - 1 - ((l0 - d) >> LTS)), l0 + LTU)));
    xs = sel(first, x0, sel(miss, min(x0 + XSU, XSM), max(x0 - 1 - (x0 >> XSS), sel(s - XFS, XF, 0))));
    return 0x400000 + sel(miss | first, d + sel(first, sel((int)tg - 1, PEN00, PEN0), PEN), l0 + x0) + M1 + M2 + x0;
}
constexpr int QS = 272;
constexpr int TQS = 1152;
constexpr int L_Q = 10240, L_K = L_Q + 2 * TQS, L_S = L_K + 2 * TQS, L_TOK = L_S + 256, L_H = L_TOK + 49152, L_END = L_H + 2 * 40960;
typedef __attribute__((ext_vector_type(4))) short v4i16_t;
__device__ __forceinline__ u32x2 ldtr(const char* p) { return __builtin_bit_cast(u32x2, __builtin_amdgcn_ds_read_tr16_b64_v4i16((LAS v4i16_t*)p)); }
__device__ __forceinline__ u32x2 ldtr1(const LAS char* p) { return __builtin_bit_cast(u32x2, __builtin_amdgcn_ds_read_tr16_b64_v4i16((LAS v4i16_t*)p)); }
__device__ __forceinline__ u32x4 ldtr2(const LAS char* p0, const LAS char* p1) {
    const u32x2 x = __builtin_bit_cast(u32x2, __builtin_amdgcn_ds_read_tr16_b64_v4i16((LAS v4i16_t*)p0)), y = __builtin_bit_cast(u32x2, __builtin_amdgcn_ds_read_tr16_b64_v4i16((LAS v4i16_t*)p1));
    return (u32x4){x[0], x[1], y[0], y[1]};
}
__device__ __forceinline__ u32x4 ldtr2(const char* p0, const char* p1) { const u32x2 x = ldtr(p0), y = ldtr(p1); return (u32x4){x[0], x[1], y[0], y[1]}; }

__device__ __forceinline__ unsigned poll_lim(int& budget) {
    --budget;
    unsigned lim = budget > 0 ? 0xFFFFFFFEu : 0xFFFFFFFFu;
    asm volatile("" : "+s"(lim));
    return lim;
}
__device__ __forceinline__ bool mem_missing(unsigned lds, unsigned lim) {
    const LAS char* p = (const LAS char*)(size_t)(lds + fresh_lane() * 16u);
    unsigned m = 0;
#pragma unroll 1
    for (int h = 0; h < 2; ++h) {
        u32x4 v[5];
#pragma unroll
        for (int i = 0; i < 5; ++i) v[i] = *(const LAS u32x4*)(p + h * 5120 + i * 1024);
        SB();
#pragma unroll
        for (int i = 0; i < 5; ++i) m = max(max(m, max(v[i][0], v[i][1])), max(v[i][2], v[i][3]));
    }
    return __any(m > lim);
}
#define GATHER \
        "buffer_load_dwordx4 %[vo], %[rw], %[s0] offen sc1 lds\n\t" \
        "buffer_load_dwordx4 %[vo], %[rw], %[s0] offen offset:1024 sc1 lds\n\t" \
        "buffer_load_dwordx4 %[vo], %[rw], %[s0] offen offset:2048 sc1 lds\n\t" \
        "buffer_load_dwordx4 %[vo], %[rw], %[s0] offen offset:3072 sc1 lds\n\t" \
        "s_movk_i32 m0, 0x1000\n\t" \
        "s_nop 0\n\t" \
        "buffer_load_dwordx4 %[vo], %[rw], %[s4] offen sc1 lds\n\t" \
        "buffer_load_dwordx4 %[vo], %[rw], %[s4] offen offset:1024 sc1 lds\n\t" \
        "buffer_load_dwordx4 %[vo], %[rw], %[s4] offen offset:2048 sc1 lds\n\t" \
        "buffer_load_dwordx4 %[vo], %[rw], %[s4] offen offset:3072 sc1 lds\n\t" \
        "s_movk_i32 m0, 0x2000\n\t" \
        "s_nop 0\n\t" \
        "buffer_load_dwordx4 %[vo], %[rw], %[s1] offen sc1 lds\n\t" \
        "buffer_load_dwordx4 %[vo], %[rw], %[s1] offen offset:1024 sc1 lds"
#define GATHER_IN [vo] "v"(vo), [rw] "s"(rwin), [s0] "s"(so0), [s4] "s"(so0 + 4096u), [s1] "s"(so1)
__device__ __forceinline__ void gather_at(unsigned t, Clk& c, __amdgpu_buffer_rsrc_t rwin, unsigned vo, unsigned so0, unsigned so1) {
    unsigned tm, rp, x, fi;
    asm volatile("s_mov_b32 m0, 0\n\t" WAIT_FINE GATHER : WAIT_FINE_OUT : [cq] "s"(c.cpq), GATHER_IN : "vcc", "scc", "s100", "s101", "m0", "memory");
}
__device__ __forceinline__ void dma4k(__amdgpu_buffer_rsrc_t r, unsigned vo, unsigned so, unsigned lds) {
    asm volatile(
        "s_mov_b32 m0, %3\n\t"
        "s_nop 0\n\t"
        "buffer_load_dwordx4 %0, %1, %2 offen sc1 lds\n\t"
        "buffer_load_dwordx4 %0, %1, %2 offen offset:1024 sc1 lds\n\t"
        "buffer_load_dwordx4 %0, %1, %2 offen offset:2048 sc1 lds\n\t"
        "buffer_load_dwordx4 %0, %1, %2 offen offset:3072 sc1 lds"
        :: "v"(vo), "s"(r), "s"(so), "s"(lds) : "m0", "memory");
}
__device__ __forceinline__ int bs2(int c) { return (c & ~3) | ((c & 1) << 1) | ((c >> 1) & 1); }
__device__ __forceinline__ void get_mem0(u32x4 (&m)[2], int t) {
    const u16* mem0 = (const u16*)karg<2>();
#pragma unroll
    for (int i = 0; i < 2; ++i) {
        const int c = min(t + 512 * i, 639);
        const unsigned* src = (const unsigned*)(mem0 + ((c & 6) + ((c >> 3) & 1)) * D_ + (c >> 4) * 16 + (c & 1) * 2);
        m[i] = (u32x4){src[0], src[2], src[4], src[6]};
    }
    SB();
}
__device__ __forceinline__ void put_mem0(const u32x4 (&m)[2], __amdgpu_buffer_rsrc_t rwin, unsigned so0, unsigned so1, int t) { st128(m[0], rwin, t * 16u, so0, 16); st128(m[1], rwin, min(t, 127) * 16u, so1, 16); }
__device__ __forceinline__ f32x2 hadd(f32x4 c) { return (f32x2){c[0], c[1]} + (f32x2){c[2], c[3]}; }
__device__ __forceinline__ unsigned hpack(f32x4 c) { const f32x2 y = hadd(c); return pack2(y[0], y[1]); }
__device__ __forceinline__ u32x8 cat8(u32x4 lo, u32x4 hi) { return (u32x8){lo[0], lo[1], lo[2], lo[3], hi[0], hi[1], hi[2], hi[3]}; }
__device__ __forceinline__ u32x4 half8(const u32x8& v, int u) { return u ? (u32x4){v[4], v[5], v[6], v[7]} : (u32x4){v[0], v[1], v[2], v[3]}; }
template <int N, int ST> __device__ __forceinline__ void stage_h(__amdgpu_buffer_rsrc_t rws, unsigned so, unsigned lds, int lane) {
#pragma unroll
    for (int i = 0; i < N; ++i) dma1k(rws, lane * 16u, so + i * ST, lds + i * ST);
}
template <int N, int ST, int M> __device__ __forceinline__ void check_h(__amdgpu_buffer_rsrc_t rws, unsigned so, unsigned lds, int lane) {
    asm volatile("s_waitcnt vmcnt(%0)" :: "n"(M) : "memory");
    for (unsigned spins = 0; spins < SPIN_CAP; ++spins) {
        unsigned m = 0;
#pragma unroll
        for (int i = 0; i < N; ++i) m = max(m, smax(*(const LAS u32x4*)(size_t)(lds + lane * 16u + i * ST)));
        if (!__any(m == 0xFFFFFFFFu)) break;
        stage_h<N, ST>(rws, so, lds, lane);
        asm volatile("s_waitcnt vmcnt(0)" ::: "memory");
    }
}
__device__ __forceinline__ void wait_h(__amdgpu_buffer_rsrc_t rws, __amdgpu_buffer_rsrc_t rfl, unsigned fb, int seg, int& hrdy) {
    unsigned lim = hrdy ? 63u : (unsigned)min(seg + HLA, 63);
    if (__builtin_expect(hrdy <= seg, 0)) for (unsigned spins = 0;;) {
        asm volatile("" ::: "memory");
        const u32x4 f = __builtin_amdgcn_raw_buffer_load_b128(rfl, min(fresh_lane(), lim) * 128u, W_H + fb, 16);
        const unsigned long long miss = __ballot((f[0] | f[1] | f[2] | f[3]) != 0u);
        hrdy = miss ? __builtin_ctzll(miss) : (int)lim + 1;
        if (hrdy > seg) break;
        if (spin_fail(spins, rws)) break;
        lim = (unsigned)min(seg + HLA, 63);
        __builtin_amdgcn_s_sleep(HSLP);
    }
}
__device__ __forceinline__ u32x4 ask_h(__amdgpu_buffer_rsrc_t rfl, unsigned fb, unsigned lim) { return __builtin_amdgcn_raw_buffer_load_b128(rfl, min(fresh_lane(), lim) * 128u, W_H + fb, 16); }
__device__ __forceinline__ int see_h(u32x4 f) { const unsigned long long miss = __ballot((f[0] | f[1] | f[2] | f[3]) != 0u); return miss ? __builtin_ctzll(miss) : 64; }
template <int CTRL> __device__ __forceinline__ unsigned dppu(unsigned v) { return (unsigned)__builtin_amdgcn_mov_dpp((int)v, CTRL, 0xF, 0xF, true); }
template <int CTRL> __device__ __forceinline__ u32x4 ror4(u32x4 v) { return (u32x4){dppu<CTRL>(v[0]), dppu<CTRL>(v[1]), dppu<CTRL>(v[2]), dppu<CTRL>(v[3])}; }
__device__ __forceinline__ float xmax16(float v) {
    auto q = __builtin_amdgcn_permlane16_swap(__float_as_uint(v), __float_as_uint(v), false, false);
    return fmx(__uint_as_float(q[0]), __uint_as_float(q[1]));
}
__device__ __forceinline__ float xsum16(float v) {
    auto q = __builtin_amdgcn_permlane16_swap(__float_as_uint(v), __float_as_uint(v), false, false);
    return __uint_as_float(q[0]) + __uint_as_float(q[1]);
}
__device__ __forceinline__ float addswap16(float a, float b) {
    auto r = __builtin_amdgcn_permlane16_swap(__float_as_uint(a), __float_as_uint(b), false, false);
    return __uint_as_float(r[0]) + __uint_as_float(r[1]);
}
#define LANES() \
    const int lane = (int)fresh_lane(), tid = w * 64 + lane, fr = lane & 15, fq = lane >> 4; \
    const int sidx = (lane & 2) ? 0xEEEE : 0x4444; \
    const int ma0 = fq * 256 + (fr & 1) * 128 + (fr >> 1) * 16; \
    const int prow = 16 * ((fr >> 2) & 1) + 4 * (fr >> 3) + (fr & 3), hoff = fr * 128; \
    (void)tid; (void)sidx; (void)ma0; (void)prow; (void)hoff

__device__ __forceinline__ int seg_after(unsigned long long m, int j) { m = j >= 63 ? 0ull : m >> (j + 1); return m ? j + 1 + __builtin_ctzll(m) : NSEG; }
__device__ __forceinline__ int seg_done(unsigned long long m, int f0, int f1) { return min(seg_after(~m, f0), seg_after(m, f1)) - 1; }
template <int OFF> __device__ __forceinline__ void ldfrag(u32x4& d, const LAS char* p) { asm volatile("ds_read_b128 %0, %1 offset:%2" : "=v"(d) : "v"(p), "n"(OFF) : "memory"); }
template <int U, class FM, class FV> __device__ __forceinline__ void project(const u32x8 (&wt)[2][10], f32x4 (&tc)[2][2], u32x4 (&bb)[8], const LAS char* c0, const LAS char* c1, const LAS char* n0, const LAS char* n1, FM mid, FV vm) {
    constexpr int t = U >> 2, k = U & 3, v = (U + 7) % 40;
    if (U == 33) mid();
    vm(IC<U>());
    const LAS char* p = U < 33 ? (v >> 1 & 1 ? c1 : c0) : (v >> 1 & 1 ? n1 : n0);
    if (U < 2) asm volatile("s_nop 0\n\ts_waitcnt lgkmcnt(6)\n\tv_mfma_f32_16x16x32_bf16 %0, %3, %7, 0\n\tds_read_b128 %2, %5 offset:%6\n\tv_mfma_f32_16x16x32_bf16 %1, %4, %7, 0"
                            : "=&v"(tc[0][k & 1]), "=&v"(tc[1][k & 1]), "+v"(bb[(U + 7) & 7]) : "v"(half8(wt[0][t], k >> 1)), "v"(half8(wt[1][t], k >> 1)), "v"(p), "n"((v >> 2) * 4096 + (v & 1) * 2048), "v"(bb[U & 7]) : "memory");
    else asm volatile("s_waitcnt lgkmcnt(6)\n\tv_mfma_f32_16x16x32_bf16 %0, %3, %7, %0\n\tds_read_b128 %2, %5 offset:%6\n\tv_mfma_f32_16x16x32_bf16 %1, %4, %7, %1"
                      : "+v"(tc[0][k & 1]), "+v"(tc[1][k & 1]), "+v"(bb[(U + 7) & 7]) : "v"(half8(wt[0][t], k >> 1)), "v"(half8(wt[1][t], k >> 1)), "v"(p), "n"((v >> 2) * 4096 + (v & 1) * 2048), "v"(bb[U & 7]) : "memory");
    if constexpr (U < 39) project<U + 1>(wt, tc, bb, c0, c1, n0, n1, mid, vm);
    else asm volatile("s_nop 7" : "+v"(tc[0][0]), "+v"(tc[0][1]), "+v"(tc[1][0]), "+v"(tc[1][1]));
}
__device__ __forceinline__ void project_end(u32x4 (&bb)[8]) {
    asm volatile("s_waitcnt lgkmcnt(0)" : "+v"(bb[0]), "+v"(bb[1]), "+v"(bb[2]), "+v"(bb[3]), "+v"(bb[4]), "+v"(bb[5]), "+v"(bb[6]), "+v"(bb[7]) :: "memory");
}
__device__ __forceinline__ void project0(u32x4 (&bb)[8], const LAS char* c0, const LAS char* c1) {
    ldfrag<0>(bb[0], c0); ldfrag<2048>(bb[1], c0); ldfrag<0>(bb[2], c1); ldfrag<2048>(bb[3], c1);
    ldfrag<4096>(bb[4], c0); ldfrag<6144>(bb[5], c0); ldfrag<4096>(bb[6], c1); ldfrag<6144>(bb[7], c1);
    project_end(bb);
}
constexpr int WLB = 20992;
template <bool O, class F> __device__ __forceinline__ void load_w(u32x8 (&wt)[2][10], const char* W, int hh, int w, int c0, int cd, char* st, bool once, F gap) {
    const int lane = (int)fresh_lane(), fr = lane & 15, fq = lane >> 4, x = w & 3;
    const __amdgpu_buffer_rsrc_t r = mkrsrc(W, D_ * HD * 2);
    u32x4 raw[40];
    const unsigned vo = O ? (unsigned)(lane * 16) : (unsigned)(fq * 8192 + fr * 16);
    unsigned so = O ? (unsigned)(hh * 128 + 8 * x) * 1280u : (unsigned)(8 * x * 8192 + hh * 256);
    asm volatile("" : "+s"(so));
    auto fetch1 = [&](int p, auto aux) {
#pragma unroll
        for (int i = 5 * p; i < 5 * p + 5; ++i) {
            if (O) raw[i] = __builtin_amdgcn_raw_buffer_load_b128(r, vo + (i % 5 & 3) * 1024u, so + (unsigned)((32 * (i / 10) + 4 * (i / 5 & 1)) * 1280 + (i % 5 >> 2) * 4096), decltype(aux)::v);
            else raw[i] = __builtin_amdgcn_raw_buffer_load_b128(r, vo, so + (unsigned)(32 * (i >> 1) + 4 * (i & 1)) * 8192u, decltype(aux)::v);
        }
    };
    auto fetch = [&](int p) { if (WAUX && once) fetch1(p, IC<WAUX>()); else fetch1(p, IC<0>()); };
    gap(-1);
#pragma unroll
    for (int p = 0; p < WLA; ++p) { fetch(p); BAR0(); gap(p); }
    st += (w >> 2) * WLB;
    const int row = 4 * fq + (fr >> 2);
    int wo[5];
#pragma unroll
    for (int e = 0; e < 5; ++e) {
        const int id = 64 * e + lane, rr = id / 80, ch = id - 80 * rr;
        wo[e] = O ? (4 * x + rr) * 1312 + (ch >> 1) * 32 + (ch & 1) * 4 : (4 * x + fq) * 256 + ((fr ^ (2 * ((4 * x + fq) & 7))) * 16) + e * 4096;
    }
    const int r0 = O ? row * 1312 + x * 320 + (fr & 3) * 8 : row * 256 + (((c0 + ((fr >> 1) & 1)) ^ (2 * (row & 7))) * 16) + (fr & 1) * 8;
    const int r1 = O ? 0 : row * 256 + (((c0 + cd + ((fr >> 1) & 1)) ^ (2 * (row & 7))) * 16) + (fr & 1) * 8;
#pragma unroll
    for (int p = 0; p < 8; ++p) {
        char* b = st + (p & 1) * 2 * WLB;
#pragma unroll
        for (int e = 0; e < 5; ++e) {
            const u32x4 v = raw[5 * p + e];
            if (O) {
                unsigned* d = (unsigned*)(b + wo[e]);
                d[0] = __builtin_amdgcn_perm(v[2], v[0], 0x05040100); d[2] = __builtin_amdgcn_perm(v[3], v[1], 0x05040100);
                d[4] = __builtin_amdgcn_perm(v[2], v[0], 0x07060302); d[6] = __builtin_amdgcn_perm(v[3], v[1], 0x07060302);
            } else *(u32x4*)(b + wo[e]) = v;
        }
        BAR();
        if (p + WLA < 8) fetch(p + WLA);
        SB();
        gap(p + WLA);
#pragma unroll
        for (int e = 0; e < 10; ++e) {
            const int P = O ? p : 5 * p + (e >> 1), d = 2 * (P & 3);
            const u32x2 y = ldtr(b + (O ? r0 + e * 32 : (e & 1 ? r1 : r0) + (e >> 1) * 4096));
            u32x8& t = O ? wt[P >> 2][e] : wt[e & 1][P >> 2];
            t[d] = y[0]; t[d + 1] = y[1];
        }
    }
    BAR();
}

__global__ __launch_bounds__(512, 2) B3ATTR void b3_kernel(RecArgs a) {
    extern __shared__ __attribute__((aligned(16))) char smem[];
    char* memL = smem;
    char* tokL = smem + L_TOK;
    char* hL = smem + L_H;
    int w = __builtin_amdgcn_readfirstlane(threadIdx.x >> 6);
    __amdgpu_buffer_rsrc_t rwin = mkrsrc(a.win, W_END + 2 * PAIR);
    __amdgpu_buffer_rsrc_t rws = mkrsrc(a.ctl, (unsigned)OFF_WIN);
    asm volatile("" : "+s"(rwin), "+s"(rws));
    unsigned long long tref = 0;
    const unsigned long long tstart = __builtin_amdgcn_s_memtime();
    auto get_tag = [&]() __attribute__((always_inline)) {
        unsigned long long e;
        asm volatile("s_buffer_load_dwordx2 %0, %1, 8\n\ts_waitcnt lgkmcnt(0)" : "=s"(e) : "s"(rws) : "memory");
        e += 0x9E3779B97F4A7C15ull;
        return (u32x2){(unsigned)e, (unsigned)(e >> 32)};
    };
    auto role = [&](unsigned rot) {
        const unsigned vx = blockIdx.x + rot;
        const int j = blockIdx.x >> 3, x = vx & 3, hh = 8 * (blockIdx.x & 3) + (j & 7);
        int r;
        if (!(vx & 4)) r = j < 8 ? hh : j < 16 ? 32 + hh : j < 26 ? 80 + 10 * x + j : j < 30 ? 255 - 4 * (j - 26) - x : NREC + 2 + 2 * x + j;
        else r = j < 8 ? NQT + hh : j < 16 ? 64 + hh : j < 24 ? NREC - 16 + 8 * x + j : NXT + hh;
        return r;
    };
    int bid = role(xcc_id() - blockIdx.x + BADG * (blockIdx.x >> 3));
    unsigned nh = 0, nz = 0, mc = 0, qz = 0;
    auto hint = [&](unsigned vo, unsigned so, u32x2 f) __attribute__((always_inline)) {
        const u32x2 tag = get_tag();
        for (unsigned spins = 0; spins < SPIN_CAP && __any(f[1] != tag[1] || (f[0] ^ tag[0]) > 255u); ++spins) {
            __builtin_amdgcn_s_sleep(4);
            asm volatile("" ::: "memory");
            f = __builtin_amdgcn_raw_buffer_load_b64(rwin, vo, so, 16);
        }
        return f[0] & 255u;
    };
    auto tell = [&](unsigned so, unsigned v) __attribute__((always_inline)) {
        const u32x2 tag = get_tag();
        __builtin_amdgcn_raw_buffer_store_b64((u32x2){(tag[0] & ~255u) | v, tag[1]}, rwin, 0u, so, 16);
    };
    auto share = [&](unsigned v) __attribute__((always_inline)) {
        if (w == 0) *(volatile LAS unsigned*)(smem + L_S + 64) = v;
        BAR();
        v = __builtin_amdgcn_readfirstlane(*(volatile LAS unsigned*)(smem + L_S + 64));
        BAR();
        return v;
    };
    u32x2 cf0, cf1;
    auto config0 = [&]() __attribute__((always_inline)) { cf0 = __builtin_amdgcn_raw_buffer_load_b64(rwin, fresh_lane() * 8u, W_CFG, 16); cf1 = __builtin_amdgcn_raw_buffer_load_b64(rwin, 0u, W_ARM, 16); };
    auto config = [&]() __attribute__((always_inline)) {
        unsigned v = 0;
        if (w == 0) {
            const unsigned l = fresh_lane(), f = hint(l * 8u, W_CFG, cf0), rot = __builtin_amdgcn_readfirstlane(hint(0u, W_ARM, cf1));
            const unsigned long long ok = __ballot((f & 4u) && !((l + rot) & 4u)), h = __ballot(f & 1u), q = __ballot(((f >> 1) ^ (l + rot)) & 1u);
            v = rot;
#pragma unroll
            for (int p = 0; p < 2; ++p) {
                unsigned long long m = ok & (p ? 0xFF00FF00FF00FF00ull : 0x00FF00FF00FF00FFull);
                const bool hb = 2 * __builtin_popcountll(m & h) > __builtin_popcountll(m);
                m &= hb ? h : ~h;
                v |= (hb ? 8u : 0u) << p | (2 * __builtin_popcountll(m & q) > __builtin_popcountll(m) ? 32u : 0u) << p;
            }
        }
        v = share(v);
        nh = (v >> 3 & 1u) * HALF; nz = (v >> 4 & 1u) * HALF;
        mc = ((blockIdx.x + v) & 3u) ^ (v >> 5 & 1u); qz = v >> 6 & 1u;
        return v;
    };
    auto zpair = [&](unsigned x, unsigned k) { return (((x >> 1) * 10u + k) * 2u + ((x & 1u) ^ qz)) * PAIR + nz; };
    unsigned mo, mo1, tro;
    auto two = [&]() { const unsigned l = fresh_lane(); return l < 4 ? (l >> 1) * PAIR + (l & 1) * 4096u + nh : 0x40000000u; };
    const u32x4 SENT = {0xFFFFFFFFu, 0xFFFFFFFFu, 0xFFFFFFFFu, 0xFFFFFFFFu};
    const f32x4 z4 = {0.f, 0.f, 0.f, 0.f};
    int hrdy = 0, budget = (int)SPIN_CAP;
    unsigned fc0 = 0, fc1 = 0, fc2 = 0;
    Clk ck = {0u, 0u, 1600u, 10486u};
    int lay = 0;
    unsigned fb = 0, zb = W_ZM, hb = 0;
    unsigned tpp = 0;
    int lt = LT1, xs = XS0;
    struct Tk { unsigned vo, tw, sl; const LAS char* sp; };
    auto tk_init = [&]() {
        const unsigned l = fresh_lane();
        Tk k = {l * 16u, two(), (l & 31u) * 128u + (l >> 5) * 4u, (const LAS char*)(memL + l * 64 + 4 + ((l >> 1) & 7) * 8)};
        PIN2(k.vo, k.tw); PIN2(k.sl, k.sp);
        ck = clk_init();
        tpp = ck.ta;
        return k;
    };
    auto sched2 = [&](Tk& k, int s, unsigned f) {
        const unsigned g0 = tpp - 0x400000u;
        tpp = g0 + (unsigned)read_sched(rws, rwin, s, (unsigned)(s + 64 * lay), k.sl, tro + (unsigned)(s & 3) * 2u * PAIR, f, g0, lt, xs, fc1);
        k.sl &= ~4u;
        RT(6);
    };
    auto sched = [&](Tk& k, int s) { sched2(k, s, __builtin_amdgcn_raw_buffer_load_b32(rwin, k.sl, tro + (unsigned)(s & 3) * 2u * PAIR, 16)); };
    auto own0 = [&](int i) { return W_ZM + (nz ^ HALF) + (i ? (64u + (bid >> 2)) * PAIR + (bid & 3) * 2048u : bid * PAIR); };
    auto fetch = [&](const Tk& k, int s, bool first) {
        const unsigned sb = (unsigned)(s & 3) * 6u * PAIR, ta0 = ck.ta, mm0 = ck.ma;
        if (__builtin_expect(first, 1)) {
            while ((unsigned)sx24(tpp - TKE - 4u * (unsigned)__builtin_amdgcn_s_memrealtime()) < 20000u) __builtin_amdgcn_s_sleep(1);
            gather_at(tpp, ck, rwin, k.vo, s ? mo + sb : own0(0), s ? mo1 + sb : own0(1));
        }
        __builtin_amdgcn_s_setprio(0);
        RT(1);
        clk_rate(ck, ta0, mm0);
        unsigned tb = ck.ma - DQC;
        asm volatile(AL "s_waitcnt vmcnt(%1) lgkmcnt(0)\n\ts_barrier\n\ts_waitcnt vmcnt(%2)\n\ts_barrier" : "+s"(tb) : "n"(10 - NB1), "n"(10 - NB2) : "memory");
        return tb;
    };
    unsigned mb = 0;
    auto again = [&](bool tk, unsigned vo, unsigned& sf, int s, unsigned lim, const LAS char*& p, const LAS char*& q) __attribute__((always_inline)) {
        const unsigned nb = mb == 0x10000u ? 0x14000u : 0x10000u;
        if (tk) {
            const unsigned c = (mc + 1u + (nb >> 14 & 1u)) & 3u, sb = W_MEM + (unsigned)(s & 3) * 6u * PAIR + nh, t = sb + (4u + (c & 1u)) * PAIR + (c >> 1) * 2048u;
            dma4k(rwin, vo, sb + c * PAIR, nb); dma4k(rwin, vo, sb + c * PAIR + 4096u, nb + 4096u);
            dma1k(rwin, vo, t, nb + 8192u); dma1k(rwin, vo, t + 1024u, nb + 9216u);
            asm volatile("s_waitcnt vmcnt(10)" : "+v"(sf) :: "memory");
        }
        const bool r = mem_missing(mb, lim);
        if (tk && !r) VMWAIT();
        if (r) { p += nb - mb; q += nb - mb; mb = nb; FC(fc0); }
        return r;
    };
    auto places = [&]() {
        mo = W_MEM + mc * PAIR + nh; mo1 = W_MEM + (4u + (mc & 1u)) * PAIR + nh + (mc >> 1) * 2048u;
        tro = W_T + (mc & 1u) * PAIR + nh + (mc >> 1) * 4096u;
    };
    auto fresh = [&]() {
        asm volatile("" : "+s"(bid), "+s"(w), "+s"(nh), "+s"(nz), "+s"(mc), "+s"(qz), "+s"(lay));
        fb = lay * W_FL; hb = O_H + (1 - lay) * (4u * 40960u); zb = W_ZM + lay * (NSEG % 3 * NOWN * PAIR);
        tref = 0; hrdy = 0; budget = (int)SPIN_CAP; ck = Clk{0u, 0u, 1600u, 10486u}; tpp = 0; lt = LT1; xs = XS0;
    };
    auto filter = [&]() __attribute__((always_inline)) {
        if ((bid >= 32 && bid < 64) || (bid >= NQT && bid < NXT)) {
            const unsigned u = bid < 64 ? bid : bid - NQT, l = fresh_lane();
            filter_unit((const u16*)karg<9>(), (const u16*)karg<10>(), u * 32, rws, (u16*)tokL, w * 64 + l);
            VMWAIT();
            BAR();
            if (w == 0) __builtin_amdgcn_raw_buffer_store_b64(get_tag(), rwin, (l & 1u) * 8u, W_ST + u * 128u + 64u, 16);
        }
    };
    auto settle = [&]() __attribute__((always_inline)) {
        if (TPF && w == 1 && (bid >> 5 == 1 || bid >= NQT)) for (unsigned i = 0; i < (unsigned)OFF_WIN; i += 64u << TPF) dma1k(rws, fresh_lane() << TPF, i, L_Q);
        const int b = role(config()); if (__builtin_expect(b != bid, 0)) { bid = b; filter(); } };
    auto warm = [&]() __attribute__((always_inline)) {
        unsigned long long pc;
        unsigned len;
        asm volatile("s_getpc_b64 %0\n1:\n\ts_mov_b32 %1, 2f-1b\n\t.subsection 1\n2:\n\t.subsection 0" : "=s"(pc), "=s"(len));
        if (CPF) dma1k(mkrsrc((const void*)pc, len), fresh_lane() * 16u, (blockIdx.x >> 3) * 1024u + w * 16384u, L_END - 8192 + w * 1024);
    };
    auto a1_done = [&]() __attribute__((always_inline)) {
        VMWAIT();
        BAR();
        if (w == 0) __builtin_amdgcn_raw_buffer_store_b64(get_tag(), rwin, 0u, W_ST + (blockIdx.x - 128u >> 1) * 128u + (blockIdx.x & 1u) * 8u, 16);
        SST(29);
    };
    auto due = [&](int i, int d) __attribute__((always_inline)) {
        __builtin_amdgcn_raw_buffer_store_b32(nosent1((unsigned)__builtin_amdgcn_s_memrealtime() + d), rwin, 0u, W_E + fb + i * 128u, 16);
    };
    auto await = [&](int i, int n, int d, unsigned f = ~0u) __attribute__((always_inline)) {
        if (__any(f == ~0u)) for (unsigned spins = 0;;) {
            asm volatile("" ::: "memory");
            f = __builtin_amdgcn_raw_buffer_load_b32(rwin, min(fresh_lane(), n - 1u) * 128u, W_E + i * 128u, 16);
            if (!__any(f == ~0u)) break;
            if (spin_fail(spins, rws)) break;
            __builtin_amdgcn_s_sleep(HSLP);
        }
        while (__any(f + d - (unsigned)__builtin_amdgcn_s_memrealtime() - 1u < 1000u)) {}
    };
    constexpr int E_OUT = 0, E_A1 = 8, E_A2 = 16, E_A3 = 24, E_ZT = 28;
    auto soon = [&](int j) __attribute__((always_inline)) {
        if (w == 0) { if (j >= 4) wait_h(rws, rwin, fb, j, hrdy); else await(E_A3 + fb / 128u, 4, 0); }
        BAR();
    };
    bool slow = false;
    auto gap = [&](int p) __attribute__((always_inline)) {
        if (p == 7) config0();
        if (slow) { if (p < 0) for (int i = (bid * 5 & 31) * XST; i > 0; i -= 16) __builtin_amdgcn_s_sleep(1); else if (p < 7) __builtin_amdgcn_s_sleep(XSLP); }
    };
    SST(27);
    {
        LANES();
        const unsigned u = blockIdx.x - 128u;
        if (blockIdx.x < 128) {
            const unsigned po = (blockIdx.x & 8u ? W_END : W_PROBE) + ((blockIdx.x >> 1 & 24u) | (blockIdx.x & 7u)) * 128u;
            const bool pr = bid >= 32 && bid < 64 && w == 7;
            if (pr) __builtin_amdgcn_raw_buffer_store_b32(0u, rwin, (lane & 3) * HALF + (lane >> 2 & 1) * 4096u, po, 16);
            warm();
            filter();
            if (blockIdx.x >= 64 && w == 7) tell(W_CFG + (blockIdx.x - 64u) * 8u, pr ? 4u | probe(rwin, po) : 0u);
            BAR0();
        } else a1_unit<8, 512>(mkrsrc(karg<3>(), S_ * D_ * 2), u * 16, false, rws, smem, row_tw(lane), w, lane, [&](int k) {
            if (k == 0 && u == 0 && w == 0) { tell(W_ROT, xcc_id()); __builtin_amdgcn_raw_buffer_store_b32(0u, rws, 0u, 0u, 16); }
            if (k == -1) {
                u32x4 sv = SENT;
                PIN1(sv);
#pragma unroll
                for (int i = 0; i < 4; ++i) {
                    static_assert(ARM_END - OFF_ARM <= 512 * 8192 && W_PROBE + W_END - W_ZM <= 512 * 8192, "arming");
                    const unsigned g = (i * 128u + u) * 8192u;
                    st128(sv, rwin, tid * 16u, g < W_PROBE ? g : g < W_PROBE + W_END - W_ZM ? g + W_ZM - W_PROBE : 0x40000000u, 16);
                    st128(sv, rws, tid * 16u, g < ARM_END - OFF_ARM ? (unsigned)OFF_ARM + g : 0x40000000u, 16);
                }
            }
        });
        SST(28);
        if (blockIdx.x >= 128) {
            if (w == 0) *(volatile LAS unsigned*)(smem + L_END - 16) = hint(0u, W_ROT, __builtin_amdgcn_raw_buffer_load_b64(rwin, 0u, W_ROT, 16));
            a1_done();
            const unsigned rot = __builtin_amdgcn_readfirstlane(*(volatile LAS unsigned*)(smem + L_END - 16));
            bid = role(rot);
            if (bid == 255 && w == 0) {
                const u32x2 tag = get_tag();
                for (unsigned spins = 0; spins < SPIN_CAP; ++spins) {
                    asm volatile("" ::: "memory");
                    const u32x4 g = __builtin_amdgcn_raw_buffer_load_b128(rwin, lane * 128u, W_ST, 16);
                    if (!__any(((g[0] ^ tag[0]) | (g[1] ^ tag[1]) | (g[2] ^ tag[0]) | (g[3] ^ tag[1])) != 0u)) break;
                    __builtin_amdgcn_s_sleep(4);
                }
                tell(W_ARM, rot);
            }
            SST(30);
        }
    }
    if (bid < 96 || (bid >= NQT && bid < NWRK)) {
    u32x8 wt[2][10];
#pragma nounroll
    for (int li = 0; li < 4; li += bid >= NXT ? 1 : 2) {
    lay = li >> 1;
    fresh();
    const bool xt = bid >= NXT, ph = li & 1;
    SST(0);
    if ((bid >= 32 && bid < 96) || xt) {
        const int hh = (xt ? bid - NXT : bid) & 31;
        if (li == 0 || ph) {
            u32x4 m0[2];
            get_mem0(m0, w * 64 + fresh_lane());
            wait_until(tstart + (bid < 64 ? DVO : xt ? DXT : DW1));
            const bool once = !slow && (NTM >> (xt ? 4 : bid >> 5) & 1);
            if (w < 4) load_w<false>(wt, karg<7>(), hh, w, 2 * w, 8, tokL, once, gap); else load_w<true>(wt, karg<8>(), hh, w, 0, 0, tokL, once, gap);
            SST(5);
            slow = true;
            if (li == 0) settle();
            if (bid < 64) put_mem0(m0, rwin, own0(0), own0(1), w * 64 + fresh_lane());
        }
        places();
        const bool vt = bid >= 64;
        const unsigned long long mv = xt ? MXV & (ph ? ~0xFFFFFFFFull : 0xFFFFFFFFull) : ~MXV;
        auto nextv = [&](int j) { return min(seg_after(mv, j), NSEG - 1); };
        const int jv1 = nextv(-1), jv2 = nextv(jv1);
        {
            LANES();
            VMWAIT();
            BAR0();
            if (tid < 8) *(unsigned*)(smem + L_S + tid * 4) = 0u;
            SST(1);
            if (vt) {
                soon(ph ? jv2 : jv1);
                SST(2);
                const unsigned so = hb + (unsigned)(jv1 * 40960 + w * 1024), lo = L_H + w * 1024;
                stage_h<5, 8192>(rws, so, lo, lane);
                BAR0();
                if (w < 4) check_h<5, 8192, 0>(rws, so, lo, lane);
                else { stage_h<10, 4096>(rws, hb + (unsigned)(jv2 * 40960 + (w - 4) * 1024), lo + 36864u, lane); check_h<5, 8192, 10>(rws, so, lo, lane); }
            }
        }
        auto steps = [&](auto cc) {
            constexpr int C = decltype(cc)::v & 1;
            constexpr bool TK = decltype(cc)::v == 3;
            LANES();
            Tk tk = {0u, 0u, 0x40000000u, nullptr};
            if (TK) { tk = tk_init(); __builtin_amdgcn_s_setprio(TKP); }
            const unsigned zi = (unsigned)(hh * 128 + fr * 8) * 2u;
            unsigned zo0 = zpair(w & 3, fq) + zi, zo1 = zo0 + 8 * PAIR, zo2 = zpair(w & 3, 8 + (fq >> 1)) + zi + (fq & 1) * 8;
            const LAS char* va = (const LAS char*)(smem + L_Q + (fr & 1) * 544 + ((fr >> 1) & 1) * 1152 + (16 * fq + 2 * (fr >> 2)) * 4);
            LAS char* vw = (LAS char*)(smem + L_Q + (fq & 1) * 544 + (fq >> 1) * 1152 + (16 * w + bs2(fr)) * 4);
            const LAS char* mp0 = (const LAS char*)(memL + ma0);
            PIN2(zo0, zo1); PIN1(zo2);
            if (C == 0) { PIN2(mp0, vw); } else PIN1(va);
            u32x4 za = SENT, zc = SENT;
            u32x2 ze = {0u, 0u};
            auto put_z2 = [&](int s) {
                const unsigned z2b = s < NSEG ? O_Z2 + (unsigned)s * (NOWN * 8192u) + (unsigned)(10 * (w & 3) * 8192 + hh * 256) : 0x40000000u, l2 = fresh_lane();
                st128(za, rws, (l2 >> 4) * 8192u + (l2 & 15u) * 16u, z2b, 16);
                st128(zc, rws, (l2 >> 4) * 8192u + (l2 & 15u) * 16u, z2b + 4u * 8192u, 16);
                __builtin_amdgcn_raw_buffer_store_b64(ze, rws, (l2 >> 5) * 8192u + (l2 & 15u) * 16u + ((l2 >> 4) & 1u) * 8u, z2b + 8u * 8192u, 16);
            };
            for (int s = 0; s < NSEG; ++s) {
                RT(0);
                bool redo = false;
                unsigned sf = 0;
                do {
                    unsigned lim = s > 0 ? poll_lim(budget) : ~0u;
                    unsigned long long sus;
                    asm volatile("" : "+s"(lim));
                    if (C == 0) {
                        asm volatile(
                            "v_mov_b64 v[200:201], 0\n\t"
                            "v_mov_b64 v[202:203], 0\n\t"
                            "v_mov_b64 v[204:205], 0\n\t"
                            "v_mov_b64 v[206:207], 0\n\t"
                            AL "s_setprio 2\n\t"
                            "s_waitcnt lgkmcnt(0)\n\t"
                            "s_barrier\n\t"
                            MTA(0)
                            "ds_read_b128 v[212:215], %[mp] offset:0\n\t"
                            "ds_read_b128 v[216:219], %[mp] offset:1024\n\t"
                            "ds_read_b128 v[220:223], %[mp] offset:2048\n\t"
                            "s_barrier\n\t"
                            "ds_read_b128 v[224:227], %[mp] offset:3072\n\t"
                            "ds_read_b128 v[228:231], %[mp] offset:4096\n\t"
                            "ds_read_b128 v[232:235], %[mp] offset:5120\n\t"
                            "s_waitcnt lgkmcnt(5)\n\t"
                            "v_smfmac_f32_16x16x64_bf16 v[200:203], v[212:215], %[w00], %[ix]\n\t"
                            "v_smfmac_f32_16x16x64_bf16 v[204:207], v[212:215], %[w10], %[ix]\n\t"
                            "s_waitcnt lgkmcnt(4)\n\t"
                            "v_smfmac_f32_16x16x64_bf16 v[200:203], v[216:219], %[w01], %[ix]\n\t"
                            "v_smfmac_f32_16x16x64_bf16 v[204:207], v[216:219], %[w11], %[ix]\n\t"
                            "s_barrier\n\t"
                            MTA(1)
                            "ds_read_b128 v[236:239], %[mp] offset:6144\n\t"
                            "ds_read_b128 v[240:243], %[mp] offset:7168\n\t"
                            "s_waitcnt lgkmcnt(5)\n\t"
                            "v_smfmac_f32_16x16x64_bf16 v[200:203], v[220:223], %[w02], %[ix]\n\t"
                            "ds_read_b128 v[244:247], %[mp] offset:8192\n\t"
                            "ds_read_b128 v[248:251], %[mp] offset:9216\n\t"
                            "s_waitcnt lgkmcnt(4)\n\t"
                            "v_smfmac_f32_16x16x64_bf16 v[200:203], v[224:227], %[w03], %[ix]\n\t"
                            "v_smfmac_f32_16x16x64_bf16 v[200:203], v[228:231], %[w04], %[ix]\n\t"
                            "v_smfmac_f32_16x16x64_bf16 v[200:203], v[232:235], %[w05], %[ix]\n\t"
                            "s_waitcnt lgkmcnt(3)\n\t"
                            "v_smfmac_f32_16x16x64_bf16 v[200:203], v[236:239], %[w06], %[ix]\n\t"
                            "s_waitcnt lgkmcnt(2)\n\t"
                            "v_smfmac_f32_16x16x64_bf16 v[200:203], v[240:243], %[w07], %[ix]\n\t"
                            "s_waitcnt lgkmcnt(1)\n\t"
                            "v_smfmac_f32_16x16x64_bf16 v[200:203], v[244:247], %[w08], %[ix]\n\t"
                            "s_waitcnt lgkmcnt(0)\n\t"
                            "v_smfmac_f32_16x16x64_bf16 v[200:203], v[248:251], %[w09], %[ix]\n\t"
                            "v_smfmac_f32_16x16x64_bf16 v[204:207], v[220:223], %[w12], %[ix]\n\t"
                            "s_nop 2\n\t"
                            "v_smfmac_f32_16x16x64_bf16 v[204:207], v[224:227], %[w13], %[ix]\n\t"
                            "s_nop 2\n\t"
                            "v_add_f32 v200, v200, v202\n\t"
                            "v_add_f32 v201, v201, v203\n\t"
                            "v_smfmac_f32_16x16x64_bf16 v[204:207], v[228:231], %[w14], %[ix]\n\t"
                            "v_cvt_pk_bf16_f32 v200, v200, v201\n\t"
                            "ds_write_b32 %[vw], v200\n\t"
                            MTA(2)
                            "v_smfmac_f32_16x16x64_bf16 v[204:207], v[232:235], %[w15], %[ix]\n\t"
                            "v_smfmac_f32_16x16x64_bf16 v[204:207], v[236:239], %[w16], %[ix]\n\t"
                            "v_smfmac_f32_16x16x64_bf16 v[204:207], v[240:243], %[w17], %[ix]\n\t"
                            "v_smfmac_f32_16x16x64_bf16 v[204:207], v[244:247], %[w18], %[ix]\n\t"
                            "s_waitcnt lgkmcnt(0)\n\t"
                            "s_barrier\n\t"
                            MTA(5)
                            "v_smfmac_f32_16x16x64_bf16 v[204:207], v[248:251], %[w19], %[ix]\n\t"
                            "s_nop 7\n\t"
                            "v_pk_add_f32 v[204:205], v[204:205], v[206:207]\n\t"
                            "v_cvt_pk_bf16_f32 v200, v204, v205\n\t"
                            "ds_write_b32 %[vw], v200 offset:256\n\t"
                            MTA(3)
                            "s_waitcnt vmcnt(0) lgkmcnt(0)\n\t"
                            "s_barrier\n\t"
                            "s_setprio 0\n\t"
                            "v_cmp_u_f32 %[sus], v204, v205\n\t"
                            : [sus] "=s"(sus) : [mp] "v"(mp0), [vw] "v"(vw), [ix] "v"(sidx),
                              [w00] "v"(wt[0][0]), [w01] "v"(wt[0][1]), [w02] "v"(wt[0][2]), [w03] "v"(wt[0][3]), [w04] "v"(wt[0][4]), [w05] "v"(wt[0][5]), [w06] "v"(wt[0][6]), [w07] "v"(wt[0][7]), [w08] "v"(wt[0][8]), [w09] "v"(wt[0][9]), [w10] "v"(wt[1][0]), [w11] "v"(wt[1][1]), [w12] "v"(wt[1][2]), [w13] "v"(wt[1][3]), [w14] "v"(wt[1][4]), [w15] "v"(wt[1][5]), [w16] "v"(wt[1][6]), [w17] "v"(wt[1][7]), [w18] "v"(wt[1][8]), [w19] "v"(wt[1][9])
                            : "v200", "v201", "v202", "v203", "v204", "v205", "v206", "v207", "v208", "v209", "v212", "v213", "v214", "v215", "v216", "v217", "v218", "v219", "v220", "v221", "v222", "v223", "v224", "v225", "v226", "v227", "v228", "v229", "v230", "v231", "v232", "v233", "v234", "v235", "v236", "v237", "v238", "v239", "v240", "v241", "v242", "v243", "v244", "v245", "v246", "v247", "v248", "v249", "v250", "v251", "vcc", "memory");
                        MT(4);
                    } else {
                        f32x16 ca, cb;
                        f32x8 cc;
                        asm volatile(".irp r,212,214,216,218,220,222,224,226,228,230,232,234,236,238,240,242,244,246,248,250\n\tv_mov_b64 v[\\r:\\r+1], 0\n\t.endr"
                                     : "={v[212:227]}"(ca), "={v[228:243]}"(cb), "={v[244:251]}"(cc));
                        unsigned sr = tro + (unsigned)(s & 3) * 2u * PAIR;
                        asm volatile("" : "+s"(sr));
                        if (TK) { fetch(tk, s, !redo); asm volatile("s_waitcnt vmcnt(0)\n\ts_barrier" ::: "memory"); RT(3); }
                        else asm volatile("s_waitcnt lgkmcnt(0)\n\ts_barrier\n\ts_barrier\n\ts_barrier" ::: "memory");
                        unsigned zs = zb;
                        asm volatile(
                            AL "s_setprio 1\n\t"
                            "s_waitcnt vmcnt(0) lgkmcnt(0)\n\t"
                            "s_barrier\n\t"
                            "ds_read_b64_tr_b16 v[192:193], %[va]\n\t"
                            "ds_read_b64_tr_b16 v[194:195], %[va] offset:32\n\t"
                            "s_waitcnt lgkmcnt(0)\n\t"
                            "v_smfmac_f32_16x16x64_bf16 v[212:215], v[192:195], %[w0], %[ix]\n\t"
                            "v_smfmac_f32_16x16x64_bf16 v[220:223], v[192:195], %[w2], %[ix]\n\t"
                            "v_smfmac_f32_16x16x64_bf16 v[216:219], v[192:195], %[w1], %[ix]\n\t"
                            "v_smfmac_f32_16x16x64_bf16 v[224:227], v[192:195], %[w3], %[ix]\n\t"
                            "s_barrier\n\t"
                            MTA(4)
                            "ds_read_b64_tr_b16 v[196:197], %[va] offset:256\n\t"
                            "v_smfmac_f32_16x16x64_bf16 v[228:231], v[192:195], %[w4], %[ix]\n\t"
                            "ds_read_b64_tr_b16 v[198:199], %[va] offset:288\n\t"
                            "v_smfmac_f32_16x16x64_bf16 v[236:239], v[192:195], %[w6], %[ix]\n\t"
                            "v_smfmac_f32_16x16x64_bf16 v[232:235], v[192:195], %[w5], %[ix]\n\t"
                            "v_smfmac_f32_16x16x64_bf16 v[240:243], v[192:195], %[w7], %[ix]\n\t"
                            "v_cmp_u_f32 %[sus], v212, v213\n\t"
                            "v_smfmac_f32_16x16x64_bf16 v[244:247], v[192:195], %[w8], %[ix]\n\t"
                            "v_smfmac_f32_16x16x64_bf16 v[248:251], v[192:195], %[w9], %[ix]\n\t"
                            "s_waitcnt lgkmcnt(0)\n\t"
                            "v_smfmac_f32_16x16x64_bf16 v[212:215], v[196:199], %[x0], %[ix]\n\t"
                            "s_nop 0\n\t"
                            "v_smfmac_f32_16x16x64_bf16 v[220:223], v[196:199], %[x2], %[ix]\n\t"
                            "s_cmp_lg_u64 %[sus], 0\n\t"
                            "s_cselect_b32 %[zs], 2.0, %[zs]\n\t"
                            "v_smfmac_f32_16x16x64_bf16 v[216:219], v[196:199], %[x1], %[ix]\n\t"
                            "s_nop 1\n\t"
                            "v_smfmac_f32_16x16x64_bf16 v[224:227], v[196:199], %[x3], %[ix]\n\t"
                            "v_add_f32 v212, v212, v214\n\t"
                            "v_smfmac_f32_16x16x64_bf16 v[228:231], v[196:199], %[x4], %[ix]\n\t"
                            "v_add_f32 v213, v213, v215\n\t"
                            "v_smfmac_f32_16x16x64_bf16 v[236:239], v[196:199], %[x6], %[ix]\n\t"
                            "v_add_f32 v220, v220, v222\n\t"
                            "v_smfmac_f32_16x16x64_bf16 v[232:235], v[196:199], %[x5], %[ix]\n\t"
                            "v_add_f32 v221, v221, v223\n\t"
                            "v_smfmac_f32_16x16x64_bf16 v[240:243], v[196:199], %[x7], %[ix]\n\t"
                            "v_cvt_pk_bf16_f32 v200, v212, v213\n\t"
                            "v_smfmac_f32_16x16x64_bf16 v[244:247], v[196:199], %[x8], %[ix]\n\t"
                            "v_cvt_pk_bf16_f32 v201, v220, v221\n\t"
                            "v_smfmac_f32_16x16x64_bf16 v[248:251], v[196:199], %[x9], %[ix]\n\t"
                            ".if %[tk]\n\tbuffer_load_dword %[sf], %[sl], %[rw], %[sr] offen sc1\n\t.endif\n\t"
                            "v_add_f32 v216, v216, v218\n\t"
                            "v_add_f32 v217, v217, v219\n\t"
                            "v_add_f32 v224, v224, v226\n\t"
                            "v_add_f32 v225, v225, v227\n\t"
                            "v_cvt_pk_bf16_f32 v202, v216, v217\n\t"
                            "v_cvt_pk_bf16_f32 v203, v224, v225\n\t"
                            "v_permlane32_swap_b32 v200, v201\n\t"
                            "v_pk_add_f32 v[228:229], v[228:229], v[230:231]\n\t"
                            "v_permlane32_swap_b32 v202, v203\n\t"
                            "v_min_u32 v201, -2, v201\n\t"
                            "v_pk_add_f32 v[236:237], v[236:237], v[238:239]\n\t"
                            "v_min_u32 v203, -2, v203\n\t"
                            "v_permlane16_swap_b32 v200, v202\n\t"
                            "v_pk_add_f32 v[232:233], v[232:233], v[234:235]\n\t"
                            "v_permlane16_swap_b32 v201, v203\n\t"
                            "v_pk_add_f32 v[240:241], v[240:241], v[242:243]\n\t"
                            MTA(5)
                            "buffer_store_dwordx4 v[200:203], %[z0], %[rw], %[zs] offen sc1\n\t"
                            "v_cvt_pk_bf16_f32 v204, v228, v229\n\t"
                            "v_cvt_pk_bf16_f32 v205, v236, v237\n\t"
                            "v_cvt_pk_bf16_f32 v206, v232, v233\n\t"
                            "v_cvt_pk_bf16_f32 v207, v240, v241\n\t"
                            "v_permlane32_swap_b32 v204, v205\n\t"
                            "v_pk_add_f32 v[244:245], v[244:245], v[246:247]\n\t"
                            "v_permlane32_swap_b32 v206, v207\n\t"
                            "v_min_u32 v205, -2, v205\n\t"
                            "v_pk_add_f32 v[248:249], v[248:249], v[250:251]\n\t"
                            "v_min_u32 v207, -2, v207\n\t"
                            "v_permlane16_swap_b32 v204, v206\n\t"
                            "v_cvt_pk_bf16_f32 v208, v244, v245\n\t"
                            "v_permlane16_swap_b32 v205, v207\n\t"
                            "v_cvt_pk_bf16_f32 v209, v248, v249\n\t"
                            "buffer_store_dwordx4 v[204:207], %[z1], %[rw], %[zs] offen sc1\n\t"
                            "s_nop 0\n\t"
                            "v_permlane32_swap_b32 v208, v209\n\t"
                            "v_min_u32 v209, -2, v209\n\t"
                            "buffer_store_dwordx2 v[208:209], %[z2], %[rw], %[zs] offen sc1\n\t"
                            ".if %[tk]\n\ts_setprio 0\n\ts_cmp_lg_u64 %[sus], 0\n\ts_cbranch_scc1 1f\n\ts_waitcnt vmcnt(3)\n1:\n\t.endif"
                            : [sf] "+&v"(sf), [zs] "+&s"(zs), [sus] "=&s"(sus), "=&{v[200:203]}"(za), "=&{v[204:207]}"(zc), "=&{v[208:209]}"(ze), "+{v[212:227]}"(ca), "+{v[228:243]}"(cb), "+{v[244:251]}"(cc)
                            : [va] "v"(va), [ix] "v"(sidx), [sl] "v"(tk.sl), [sr] "s"(sr), [rw] "s"(rwin), [z0] "v"(zo0), [z1] "v"(zo1), [z2] "v"(zo2), [tk] "n"(TK ? 1 : 0),
                              [w0] "v"(wt[0][0]), [w1] "v"(wt[0][1]), [w2] "v"(wt[0][2]), [w3] "v"(wt[0][3]), [w4] "v"(wt[0][4]), [w5] "v"(wt[0][5]), [w6] "v"(wt[0][6]), [w7] "v"(wt[0][7]), [w8] "v"(wt[0][8]), [w9] "v"(wt[0][9]),
                              [x0] "v"(wt[1][0]), [x1] "v"(wt[1][1]), [x2] "v"(wt[1][2]), [x3] "v"(wt[1][3]), [x4] "v"(wt[1][4]), [x5] "v"(wt[1][5]), [x6] "v"(wt[1][6]), [x7] "v"(wt[1][7]), [x8] "v"(wt[1][8]), [x9] "v"(wt[1][9])
                            : "v192", "v193", "v194", "v195", "v196", "v197", "v198", "v199", "scc", "memory");
                        RT(4);
                        MT(6);
                        tref = __builtin_amdgcn_s_memtime();
                        __builtin_amdgcn_s_setprio(TKP);
                    }
                    redo = false;
                    if (__builtin_expect(sus != 0, 0)) {
                        redo = again(TK, tk.vo, sf, s, lim, mp0, tk.sp);
                        if (!redo && C == 1) { st128(za, rwin, zo0, zb, 16); st128(zc, rwin, zo1, zb, 16); __builtin_amdgcn_raw_buffer_store_b64(ze, rwin, zo2, zb, 16); }
                    }
                } while (__builtin_expect(redo, 0));
                if (__builtin_expect(mb != 0, 0)) { mp0 -= mb; mb = 0; }
                if (s < 8) SST(8 + s);
                if (C == 0) {
                    if (s + 1 < NSEG) {
                        __builtin_amdgcn_s_sleep(DZR);
                        const unsigned zq = s == 0 ? 0x40000000u : zb == W_ZM ? W_ZM + 2u * NOWN * PAIR : zb - NOWN * PAIR;
                        unsigned s1;
                        asm volatile("v_mov_b32 %0, -1" : "=v"(s1));
                        st128(((u32x4){s1, s1, s1, s1}), rwin, zo0, zq, 16);
                        st128(((u32x4){s1, s1, s1, s1}), rwin, zo1, zq, 16);
                        __builtin_amdgcn_raw_buffer_store_b64((u32x2){s1, s1}, rwin, zo2, zq, 16);
                    }
                } else {
                    if (TK && s + 1 < NSEG) sched2(tk, s, sf);
                    if (!TK) wait_until(tref + DZ2);
                    if (s + 2 < NSEG) put_z2(s);
                }
                if (budget <= 0) __builtin_amdgcn_raw_buffer_store_b32(1u, rws, 0u, 0u, 16);
                zb = zb == W_ZM + 2u * NOWN * PAIR ? W_ZM : zb + NOWN * PAIR;
                RT(5);
                RTS();
                MT(7);
                MTS();
            }
        };
        if (!vt) {
            if (w < 4) steps(IC<0>()); else if (w < 7) steps(IC<1>()); else steps(IC<3>());
        } else {
            BAR();
            SST(3);
            LANES();
            const int ni = __builtin_popcountll(mv) + 1;
            if (w < 4) {
                const LAS char* c0 = (const LAS char*)(hL + hoff + ((fq ^ (fr & 7)) << 4));
                const LAS char* c1 = (const LAS char*)(hL + hoff + (((4 + fq) ^ (fr & 7)) << 4));
                const LAS char* n0 = c0 + 40960; const LAS char* n1 = c1 + 40960;
                u32x4 bb[8];
                project0(bb, c0, c1);
                for (int i = 1; i < ni; ++i) {
                    if (vt) MT(0);
                    f32x4 tc[2][2];
                    project<0>(wt, tc, bb, c0, c1, n0, n1,
                        [&]() {
                            MT(1);
                            if (__builtin_expect(i > 1, 1)) BAR();
                            MT(2);
                        }, [](auto) {});
                    { const LAS char* x = c0; c0 = n0; n0 = x; x = c1; c1 = n1; n1 = x; }
                    char* tb = tokL + (i & 1) * 32 * QS + (16 * w + 4 * fq) * 2;
#pragma unroll
                    for (int g = 0; g < 2; ++g)
#pragma unroll
                        for (int tt = 0; tt < 2; ++tt) *(u32x2*)(tb + (prow + 8 * tt) * QS + g * 128) = (u32x2){pack2(tc[g][tt][0], tc[g][tt][1]), pack2(tc[g][tt][2], tc[g][tt][3])};
                    asm volatile("" ::: "memory");
                    *(volatile LAS int*)(smem + L_S + 16 + w * 4) = i;
                    if (__builtin_expect(i == 1, 0)) { BAR(); if (VSLP) __builtin_amdgcn_s_sleep(VSLP); project0(bb, c0, c1); }
                    if (vt) { MT(3); const int s = i & 63; MTRT(); MTS(); }
                }
                project_end(bb);
            } else {
                __builtin_amdgcn_s_setprio(OPR);
                const unsigned hv = lane * 16u, fz_ = W_FZ + fb + (unsigned)(hh * 128 + xt * 4096 + (w - 4) * 8192);
                unsigned fp = jv1 - 1;
                u32x4 r = SENT;
                int jo = jv1, jh = jv2;
                check_h<10, 4096, 0>(rws, hb + (unsigned)(jv2 * 40960 + (w - 4) * 1024), L_H + 40960 + (w - 4) * 1024, lane);
                SST(16);
                auto rows = [&](int b) {
                    for (int t = 0; t < 10; ++t) dma1k(rws, hv, hb + (unsigned)(jh * 40960 + (w - 4) * 1024 + t * 4096), (unsigned)(L_H + ((b + 1) & 1) * 40960 + (w - 4) * 1024 + t * 4096));
                    VMWAIT();
                };
                for (int i = 1; i < ni; ++i, jo = nextv(jo)) {
                    const int b = i + (i >= ZLAG);
                    if (__builtin_expect(i == ZLAG, 0)) {
                        VMWAIT();
                        jh = nextv(jh);
                        wait_h(rws, rwin, fb, jh, hrdy);
                        BAR0();
                        rows(i);
                    }
                    jh = nextv(jh);
                    u32x4 hf;
                    asm volatile("" : "=v"(hf));
                    if (__builtin_expect(hrdy <= jh, 0)) asm volatile("buffer_load_dwordx4 %0, %1, %2, %3 offen sc1" : "+v"(hf) : "v"(min(lane, jh + 16) * 128), "s"(rwin), "s"(W_H + fb) : "memory");
                    if (b < ni) BAR();
                    if (i == 1) SST(17);
                    if (vt) MT(1);
                    for (;;) {
                        const u32x4 f = *(volatile LAS u32x4*)(smem + L_S + 16);
                        if (__builtin_amdgcn_readfirstlane(min(min(f[0], f[1]), min(f[2], f[3]))) >= (unsigned)i) break;
                        __builtin_amdgcn_s_sleep(1);
                    }
                    asm volatile("" ::: "memory");
                    u32x4 vf[2][4];
#pragma unroll
                    for (int ks = 0; ks < 4; ++ks)
#pragma unroll
                        for (int t = 0; t < 2; ++t) vf[t][ks] = *(const u32x4*)(tokL + (i & 1) * 32 * QS + (16 * t + fr) * QS + ks * 64 + fq * 16);
                    SB();
                    if (__builtin_expect(hrdy <= jh, 0)) { asm volatile("s_waitcnt vmcnt(0)" : "+v"(hf) :: "memory"); hrdy = min(see_h(hf), jh + 17); }
                    const bool ok = hrdy > jh;
                    if (__builtin_expect((i < ZLAG || !ok) && (int)fp > ZEARLY, 0)) { VMWAIT(); __builtin_amdgcn_raw_buffer_store_b32(fp, rwin, 0u, fz_, 16); }
                    if (__builtin_expect(jo == 0, 0) && w == 7) {
                        if (!(hh & 7)) due(E_ZT + (hh >> 3), lay ? DZF : DZF0);
                        __builtin_amdgcn_raw_buffer_store_b32(((4u * (unsigned)__builtin_amdgcn_s_memrealtime() + 4 * (lay ? DZF : DZF0) + DV0) & 0xFFFFFFu) | (unsigned)lay << 30, rwin, two(), W_T + hh * 128u + 4u, 16);
                    }
                    if (XDL && __builtin_expect(jo == 1, 0)) __builtin_amdgcn_s_sleep(XDL);
                    if (i == 1) SST(18);
                    if (vt) MT(0);
                    const unsigned ztb = O_Z + (unsigned)(jo * ZTSEG) * 2u + (unsigned)(10 * (w - 4) * H_ + hh) * 1024u;
                    f32x4 c[2][2];
                    unsigned hs = hb + (unsigned)(jh * 40960 + (w - 4) * 1024), hd = (unsigned)(L_H + ((b + 1) & 1) * 40960 + (w - 4) * 1024);
                    asm volatile("" : "+s"(hs), "+s"(hd));
#pragma unroll
                    for (int nt = 0; nt < 11; ++nt) {
                        auto& cc = c[nt & 1]; auto& cp = c[(nt + 1) & 1];
#pragma unroll
                        for (int ks = 0; ks < 4; ++ks) {
                            if (nt < 10) { cc[0] = mfma32(vf[0][ks], half8(wt[ks >> 1][nt], ks & 1), ks ? cc[0] : z4); PIN1(cc[0]); }
                            if (nt > 0 && ks >= 1 && ks < 3) { r[2 * ks - 2] = pack2(cp[ks - 1][0], cp[ks - 1][1]); PIN1(r); }
                            if (nt < 10) { cc[1] = mfma32(vf[1][ks], half8(wt[ks >> 1][nt], ks & 1), ks ? cc[1] : z4); PIN1(cc[1]); }
                            if (nt > 0 && ks >= 1 && ks < 3) { r[2 * ks - 1] = pack2(cp[ks - 1][2], cp[ks - 1][3]); PIN1(r); }
                            if (nt > 0 && ks == 2) st128(r, rws, lane * 16u, ztb + (unsigned)((nt - 1) * H_) * 1024u, 16);
                            if (nt < 5 && !(ks & 1) && __builtin_expect(ok, 1)) dma1k(rws, hv, hs + (2 * nt + (ks >> 1)) * 4096u, hd + (2 * nt + (ks >> 1)) * 4096u);
                            if (nt == 5 && ks == 0 && __builtin_expect(ok, 1)) {
                                asm volatile("s_waitcnt vmcnt(14)" ::: "memory");
                                __builtin_amdgcn_raw_buffer_store_b32(fp, rwin, 0u, fz_, 16);
                            }
                            SB();
                        }
                    }
                    if (i == 1) SST(19);
                    if (vt) MT(2);
                    fp = (unsigned)jo;
                    if (__builtin_expect(jo <= ZEARLY || i + 1 == ni || i + 1 == ZLAG || !ok, 0)) {
                        VMWAIT();
                        __builtin_amdgcn_raw_buffer_store_b32(fp, rwin, 0u, fz_, 16);
                    } else asm volatile("s_waitcnt vmcnt(7)" ::: "memory");
                    if (i == 1) SST(20);
                    if (__builtin_expect(!ok, 0)) { wait_h(rws, rwin, fb, jh, hrdy); rows(b); }
                    if (jo < 8) SST(8 + jo);
                    if (vt && (!BSPARSE || jo % 6 == 2)) { const int s = jo; RT(0); RTS(); }
                    if (vt) { MT(3); const int s = i & 63; MTRT(); MTS(); }
                }
            }
            __builtin_amdgcn_s_setprio(0);
            VMWAIT();
            BAR();
        }
    }
    if ((bid < 32 || bid >= NQT) && !ph) {
        LANES();
        const int hh = (bid < 32 ? bid : bid - NQT) & 31;
        if (lay == 0 || xt) {
            u32x4 m0[2];
            get_mem0(m0, tid);
            wait_until(tstart + (bid >= NQT ? DQT : DW1));
            load_w<false>(wt, karg<5>(w >> 2), hh, w, 4 * (w & 3), 2, tokL, !slow && (NTM >> (bid < 32 ? 0 : 3) & 1), gap);
            SST(5);
            if (!xt) settle();
            if (bid < 64) put_mem0(m0, rwin, own0(0), own0(1), tid);
        }
        places();
        const bool qt = bid >= NQT;
        const unsigned long long mq = xt ? MXQ : ~MXQ;
        auto nextq = [&](int j) { return min(seg_after(mq, j), NSEG - 1); };
        const int jq0 = qt ? nextq(1) : 0, jq1 = nextq(jq0);
        if (tid < 4) *(unsigned*)(smem + L_S + tid * 4) = 0u;
        SST(1);
        VMWAIT();
        soon(jq1);
        SST(2);
        const unsigned so1 = hb + (unsigned)(jq1 * 40960 + w * 1024), lo1 = L_H + 40960 + w * 1024;
        stage_h<5, 8192>(rws, hb + (unsigned)(jq0 * 40960 + w * 1024), lo1 - 40960u, lane);
        BAR0();
        stage_h<5, 8192>(rws, so1, lo1, lane);
        dma1k(rwin, lane * 16u, qt ? 0x40000000u : own0(0) + w * 1024u, w * 1024u); dma1k(rwin, lane * 16u, qt ? 0x40000000u : own0(1) + (w & 1) * 1024u, 8192u + (w & 1) * 1024u);
        check_h<5, 8192, 7>(rws, hb + (unsigned)(jq0 * 40960 + w * 1024), lo1 - 40960u, lane);
        BAR();
        SST(3);
        int qrdy = 0;
        {
            LANES();
            const unsigned to0 = (unsigned)((w ^ 4) >> 2) * 8192u + (unsigned)((w & 3) * 64 + (lane ^ ((fq & 1) * (w < 4 ? 8 : 5)))) * 16u, to1 = to0 + 4096u, hv = lane * 16u;
            const LAS char* c0 = (const LAS char*)(hL + hoff + ((fq ^ (fr & 7)) << 4));
            const LAS char* c1 = (const LAS char*)(hL + hoff + (((4 + fq) ^ (fr & 7)) << 4));
            u32x4 bb[8], g0, g1;
            f32x4 tc[2][2];
            auto pack = [&]() {
                g0 = (u32x4){pack2(tc[0][0][0], tc[0][0][1]), pack2(tc[0][0][2], tc[0][0][3]), pack2(tc[1][0][0], tc[1][0][1]), pack2(tc[1][0][2], tc[1][0][3])};
                g1 = (u32x4){pack2(tc[0][1][0], tc[0][1][1]), pack2(tc[0][1][2], tc[0][1][3]), pack2(tc[1][1][0], tc[1][1][1]), pack2(tc[1][1][2], tc[1][1][3])};
            };
            const LAS char* n0 = c0 + 40960; const LAS char* n1 = c1 + 40960;
            if (!qt) {
                project0(bb, c0, c1);
                for (int j = 0; j < 2; ++j) {
                    project<0>(wt, tc, bb, c0, c1, n0, n1, [&]() { if (j == 0) { check_h<5, 8192, 0>(rws, so1, lo1, lane); BAR(); } }, [](auto) {});
                    pack();
                    *(u32x4*)(tokL + j * 16384 + to0) = g0; *(u32x4*)(tokL + j * 16384 + to1) = g1;
                    c0 = n0; c1 = n1;
                    if (j == 0 && w == 7) __builtin_amdgcn_raw_buffer_store_b32(((4u * (unsigned)__builtin_amdgcn_s_memrealtime() + (lay ? DQ0 : DQ00)) & 0xFFFFFFu) | (unsigned)lay << 30, rwin, two(), W_T + hh * 128u, 16);
                }
                project_end(bb);
                VMWAIT();
            } else {
                const unsigned fq_ = W_FQ + fb + (unsigned)(hh * 128 + xt * 64), fv = tid == 0 ? 0u : 0x40000000u;
                const int jl = 63 - __builtin_clzll(mq);
                auto rounds = [&](auto ld) {
                constexpr bool LD = decltype(ld)::v;
                unsigned hs = 0x40000000u, hd = (unsigned)(L_H + 40960 + w * 1024);
                if (LD) __builtin_amdgcn_s_setprio(QTP);
                u32x4 bb[8];
                project0(bb, c0, c1);
                asm volatile("" : "=v"(g0), "=v"(g1));
                for (int jp = 1, j = jq0, jn = jq1;; jp = j, j = jn, jn = nextq(jn)) {
                    MT(0);
                    project<0>(wt, tc, bb, c0, c1, n0, n1,
                        [&]() {
                            unsigned fj = (unsigned)jp;
                            if (LD) {
                                if (__builtin_expect(hrdy <= nextq(jn), 0)) hrdy = min(see_h(g0), nextq(jn) + 17);
                                hs = hrdy > nextq(jn) ? hb + (unsigned)(nextq(jn) * 40960 + w * 1024) : 0x40000000u; hd = (unsigned)(2 * L_H + 40960 + 2 * w * 1024) - hd;
                                asm volatile("" : "+s"(hs), "+s"(hd));
                                PIN1(fj);
                            }
                            MT(1);
                            if (__builtin_expect(j == jq0, 0)) check_h<5, 8192, 0>(rws, so1, lo1, lane);
                            VMWAIT();
                            BAR();
                            MT(2);
                            if (LD && w == 0) __builtin_amdgcn_raw_buffer_store_b32(fj, rwin, fv, fq_, 16);
                        },
                        [&](auto uu) {
                            constexpr int u = decltype(uu)::v, t = (u + 6) % 40 / 2;
                            if (u == (LD ? QEU : QEV) && __builtin_expect(j <= QE2, 0)) {
                                asm volatile("s_waitcnt vmcnt(%0)\n\ts_barrier" :: "n"(LD ? 7 : 0) : "memory");
                                if (LD && w == 0) __builtin_amdgcn_raw_buffer_store_b32((unsigned)jp, rwin, fv, fq_, 16);
                            }
                            if (LD && u == QEU + 1 && __builtin_expect(hrdy <= nextq(jn), 0)) g0 = ask_h(rwin, fb, nextq(jn) + 16);
                            if (LD && !(u & 1) && t < 10 && (t < 3 || __builtin_expect(j != jq0, 1))) dma1k(rws, hv, hs + t * 4096u, hd + t * 4096u);
                        });
                    pack();
                    const unsigned qs = O_QK + (unsigned)(j * H_ + hh) * 16384u;
                    st128(g0, rws, to0, qs, 16);
                    st128(g1, rws, to1, qs, 16);
                    { const LAS char* x = c0; c0 = n0; n0 = x; x = c1; c1 = n1; n1 = x; }
                    if (__builtin_expect(j == jl, 0)) {
                        VMWAIT();
                        BAR0();
                        if (tid == 0) __builtin_amdgcn_raw_buffer_store_b32((unsigned)j, rwin, 0u, fq_, 16);
                        break;
                    }
                    if (LD && __builtin_expect(hrdy <= nextq(jn), 0)) {
                        wait_h(rws, rwin, fb, nextq(jn), hrdy);
                        hs = hb + (unsigned)(nextq(jn) * 40960 + w * 1024);
                        for (int t = 0; t < 3; ++t) dma1k(rws, hv, hs + t * 4096u, hd + t * 4096u);
                    }
                    MT(3);
                    if (j < 8) SST(8 + j);
                    if (!BSPARSE || j % 6 == 2) { const int s = j; RT(0); RTS(); MTRT(); MTS(); }
                }
                project_end(bb);
                };
                if (w < 4) rounds(IC<1>()); else rounds(IC<0>());
                VMWAIT();
                BAR();
            }
        }
        SST(4);
        auto steps = [&](auto cc) {
        constexpr int C = decltype(cc)::v >> 1, R = decltype(cc)::v & 1;
        LANES();
        const LAS char* ka = (const LAS char*)(smem + L_K + (fr & 1) * TQS + (4 * fq + (fr >> 2)) * 8);
        const LAS char* qa = (const LAS char*)(smem + L_Q + (fr & 1) * 544 + ((fr >> 1) & 1) * 1152 + (16 * fq + 2 * (fr >> 2)) * 4);
        LAS char* qw = (LAS char*)(C < 2 ? smem + L_Q + (fq & 1) * 544 + (fq >> 1) * 1152 + (32 * w + 8 * (fr >> 2) + bs2(fr & 3)) * 4
                                         : smem + L_K + (fq >> 1) * TQS + (32 * (w & 3) + fr) * 8 + (fq & 1) * 4);
        const LAS char* mp0 = (const LAS char*)(memL + ma0);
        const int z = 16 * (fr >> 3) + 4 * (fr & 3) + 2 * ((fr >> 2) & 1);
        const int ko = ((z >> 4) * 256 + fq * 16 + ((z & 15) ^ ((fq & 1) * 5))) * 16;
        unsigned pa0 = (4u * C + 2u * (hh >> 4)) * PAIR + (unsigned)((hh & 15) * 512 + R * 256 + lane * 4);
        unsigned pa2 = ((8u + (fr >> 3)) * PAIR + C * 4096u + (unsigned)((hh >> 1) * 256 + R * 128 + (hh & 1) * 64 + fq * 16 + (fr & 6) * 2)) | (fr & 1) << 30;
        PIN2(ka, qw); PIN1(mp0);
        constexpr bool TK = C == 2 && R == 1;
        Tk tk = {0u, 0u, 0x40000000u, nullptr};
        if (TK) { tk = tk_init(); __builtin_amdgcn_s_setprio(TKP); }
        if (C < 2) { PIN2(qa, pa0); PIN1(pa2); }
        unsigned fqv = w == 4 ? (lane & 1) * 64u : 0x40000000u;
        if (C == 2 && !TK) PIN1(fqv);
        int qf = 1, tbo = 0;
        for (int s = 0; s < NSEG; ++s) {
            RT(0);
            const char* tb = tokL + tbo;
            unsigned pb = __builtin_amdgcn_readfirstlane(s + 1 < NSEG ? W_P + (unsigned)(s & 3) * 10u * PAIR + nh : 0x40000000u);
            unsigned pb1 = pb + PAIR;
            asm volatile("" : "+s"(pb), "+s"(pb1));
            if (s == 0) BAR();
            u32x8 kx[2][2];
            auto keys = [&]() {
#pragma unroll
                for (int u = 0; u < 4; ++u) kx[u >> 1][u & 1] = cat8(*(const u32x4*)(tb + (ko ^ ((u >> 1) * 16)) + (u & 1) * 2048), *(const u32x4*)(tb + (ko ^ ((u >> 1) * 16)) + (u & 1) * 2048 + 1024));
                PIN2(kx[0][0], kx[0][1]); PIN2(kx[1][0], kx[1][1]);
            };
            if (C < 2) keys();
            bool redo = false;
            unsigned sf = 0;
            do {
                unsigned lim = s > 0 ? poll_lim(budget) : ~0u;
                unsigned long long sus;
                f32x4 sc;
                if (C < 2) {
                    f32x4 s0, s1, s2;
                    unsigned late;
                    asm volatile(
                        ".irp r,232,234,236,238,240,242,244,246,248,250\n\tv_mov_b64 v[\\r:\\r+1], 0\n\t.endr\n\t"
                        "s_cmp_lg_u32 %[p0], 2.0\n\t"
                        "s_cbranch_scc1 1f\n\t"
                        "s_mov_b32 %[lt], -1\n\ts_barrier\n\ts_barrier\n\ts_barrier\n\ts_branch 2f\n"
                        AL "1:\n\t"
                        "s_setprio 2\n\t"
                        "s_waitcnt lgkmcnt(0)\n\t"
                        "s_barrier\n\t"
                        MTA(0)
                        "ds_read_b128 v[208:211], %[mp] offset:0\n\t"
                        "ds_read_b128 v[212:215], %[mp] offset:1024\n\t"
                        "ds_read_b128 v[216:219], %[mp] offset:2048\n\t"
                        "s_barrier\n\t"
                        "ds_read_b128 v[220:223], %[mp] offset:3072\n\t"
                        "s_waitcnt lgkmcnt(3)\n\t"
                        "v_smfmac_f32_16x16x64_bf16 v[232:235], v[208:211], %[w00], %[ix]\n\t"
                        "v_smfmac_f32_16x16x64_bf16 v[236:239], v[208:211], %[w10], %[ix]\n\t"
                        "ds_read_b128 v[224:227], %[mp] offset:4096\n\t"
                        "s_waitcnt lgkmcnt(3)\n\t"
                        "v_smfmac_f32_16x16x64_bf16 v[232:235], v[212:215], %[w01], %[ix]\n\t"
                        "v_smfmac_f32_16x16x64_bf16 v[236:239], v[212:215], %[w11], %[ix]\n\t"
                        "s_barrier\n\t"
                        MTA(1)
                        "ds_read_b128 v[228:231], %[mp] offset:5120\n\t"
                        "s_waitcnt lgkmcnt(3)\n\t"
                        "v_smfmac_f32_16x16x64_bf16 v[232:235], v[216:219], %[w02], %[ix]\n\t"
                        "v_smfmac_f32_16x16x64_bf16 v[236:239], v[216:219], %[w12], %[ix]\n\t"
                        "ds_read_b128 v[208:211], %[mp] offset:6144\n\t"
                        "s_waitcnt lgkmcnt(3)\n\t"
                        "v_smfmac_f32_16x16x64_bf16 v[232:235], v[220:223], %[w03], %[ix]\n\t"
                        "v_smfmac_f32_16x16x64_bf16 v[236:239], v[220:223], %[w13], %[ix]\n\t"
                        "ds_read_b128 v[212:215], %[mp] offset:7168\n\t"
                        "s_waitcnt lgkmcnt(3)\n\t"
                        "v_smfmac_f32_16x16x64_bf16 v[232:235], v[224:227], %[w04], %[ix]\n\t"
                        "v_smfmac_f32_16x16x64_bf16 v[236:239], v[224:227], %[w14], %[ix]\n\t"
                        "ds_read_b128 v[216:219], %[mp] offset:8192\n\t"
                        "s_waitcnt lgkmcnt(3)\n\t"
                        "v_smfmac_f32_16x16x64_bf16 v[232:235], v[228:231], %[w05], %[ix]\n\t"
                        "v_smfmac_f32_16x16x64_bf16 v[236:239], v[228:231], %[w15], %[ix]\n\t"
                        "ds_read_b128 v[220:223], %[mp] offset:9216\n\t"
                        "s_waitcnt lgkmcnt(3)\n\t"
                        "v_smfmac_f32_16x16x64_bf16 v[232:235], v[208:211], %[w06], %[ix]\n\t"
                        "v_smfmac_f32_16x16x64_bf16 v[236:239], v[208:211], %[w16], %[ix]\n\t"
                        "ds_read_b32 v207, v250 offset:%[ls]\n\t"
                        "s_waitcnt lgkmcnt(3)\n\t"
                        "v_smfmac_f32_16x16x64_bf16 v[232:235], v[212:215], %[w07], %[ix]\n\t"
                        "v_smfmac_f32_16x16x64_bf16 v[236:239], v[212:215], %[w17], %[ix]\n\t"
                        "s_waitcnt lgkmcnt(0)\n\t"
                        "v_smfmac_f32_16x16x64_bf16 v[232:235], v[216:219], %[w08], %[ix]\n\t"
                        "v_readfirstlane_b32 %[lt], v207\n\t"
                        "v_smfmac_f32_16x16x64_bf16 v[232:235], v[220:223], %[w09], %[ix]\n\t"
                        "v_smfmac_f32_16x16x64_bf16 v[236:239], v[216:219], %[w18], %[ix]\n\t"
                        "v_smfmac_f32_16x16x64_bf16 v[236:239], v[220:223], %[w19], %[ix]\n\t"
                        "s_setprio " STR(QP) "\n\t"
                        "s_nop 5\n\t"
                        "v_add_f32 v232, v232, v234\n\t"
                        "v_add_f32 v233, v233, v235\n\t"
                        "v_add_f32 v236, v236, v238\n\t"
                        "v_add_f32 v237, v237, v239\n\t"
                        "v_cvt_pk_bf16_f32 v232, v232, v233\n\t"
                        "v_cvt_pk_bf16_f32 v236, v236, v237\n\t"
                        "ds_write2_b32 %[qw], v232, v236 offset1:4\n\t"
                        MTA(2)
                        "2:"
                        : [lt] "=&s"(late), "=&{v[240:243]}"(s0), "=&{v[244:247]}"(s1), "=&{v[248:251]}"(s2)
                        : [mp] "v"(mp0), [qw] "v"(qw), [ix] "v"(sidx), [ls] "n"(L_S), [p0] "s"(pb), [w00] "v"(wt[0][0]), [w10] "v"(wt[1][0]), [w01] "v"(wt[0][1]), [w11] "v"(wt[1][1]), [w02] "v"(wt[0][2]), [w12] "v"(wt[1][2]), [w03] "v"(wt[0][3]), [w13] "v"(wt[1][3]), [w04] "v"(wt[0][4]), [w14] "v"(wt[1][4]), [w05] "v"(wt[0][5]), [w15] "v"(wt[1][5]), [w06] "v"(wt[0][6]), [w16] "v"(wt[1][6]), [w07] "v"(wt[0][7]), [w17] "v"(wt[1][7]), [w08] "v"(wt[0][8]), [w18] "v"(wt[1][8]), [w09] "v"(wt[0][9]), [w19] "v"(wt[1][9])
                        : "v207", "v208", "v209", "v210", "v211", "v212", "v213", "v214", "v215", "v216", "v217", "v218", "v219", "v220", "v221", "v222", "v223", "v224", "v225", "v226", "v227", "v228", "v229", "v230", "v231", "v232", "v233", "v234", "v235", "v236", "v237", "v238", "v239", "scc", "memory");
                    if (__builtin_expect(late == (unsigned)s, 0)) keys();
#define TAIL(A0, A2, B0, B2, C0, C2) \
                        "s_cmp_lg_u32 %[p0], 2.0\n\ts_cbranch_scc1 1f\n\ts_barrier\n\ts_barrier\n\t" \
                        "ds_read_b64_tr_b16 v[232:233], %[ka]\n\ts_waitcnt lgkmcnt(0)\n\tv_lshlrev_b32 v232, 16, v232\n\tv_cmp_u_f32 vcc, v232, v232\n\ts_branch 3f\n\t" AL "1:\n\t" \
                        ".if " STR(BQN) "==0\n\t" \
                        "s_setprio 3\n\t" \
                        "s_waitcnt vmcnt(0) lgkmcnt(0)\n\t" \
                        "s_barrier\n\t" \
                        MTA(3) \
                        "ds_read_b64_tr_b16 v[208:209], %[qa]\n\t" \
                        "ds_read_b64_tr_b16 v[210:211], %[qa] offset:32\n\t" \
                        "ds_read_b64_tr_b16 v[212:213], %[qa] offset:256\n\t" \
                        "ds_read_b64_tr_b16 v[214:215], %[qa] offset:288\n\t" \
                        "ds_read_b64_tr_b16 v[216:217], %[ka] offset:0\n\t" \
                        "ds_read_b64_tr_b16 v[218:219], %[ka] offset:128\n\t" \
                        "ds_read_b64_tr_b16 v[220:221], %[ka] offset:256\n\t" \
                        "ds_read_b64_tr_b16 v[222:223], %[ka] offset:384\n\t" \
                        "ds_read_b64_tr_b16 v[224:225], %[ka] offset:512\n\t" \
                        "ds_read_b64_tr_b16 v[226:227], %[ka] offset:640\n\t" \
                        "s_waitcnt lgkmcnt(8)\n\t" \
                        "v_smfmac_f32_16x16x64_bf16 v[240:243], v[208:211], %[k00], %[ix]\n\t" \
                        "ds_read_b64_tr_b16 v[228:229], %[ka] offset:768\n\t" \
                        "s_waitcnt lgkmcnt(7)\n\t" \
                        "v_smfmac_f32_16x16x64_bf16 v[240:243], v[212:215], %[k01], %[ix]\n\t" \
                        "ds_read_b64_tr_b16 v[230:231], %[ka] offset:896\n\t" \
                        "v_smfmac_f32_16x16x64_bf16 v[244:247], v[208:211], %[k10], %[ix]\n\t" \
                        "v_smfmac_f32_16x16x64_bf16 v[244:247], v[212:215], %[k11], %[ix]\n\t" \
                        "s_nop 4\n\t" \
                        ".elseif " STR(TPRE) "==0\n\t" \
                        "s_waitcnt lgkmcnt(0)\n\t" \
                        "s_barrier\n\t" \
                        "ds_read_b64_tr_b16 v[208:209], %[qa]\n\t" \
                        "ds_read_b64_tr_b16 v[210:211], %[qa] offset:32\n\t" \
                        "ds_read_b64_tr_b16 v[212:213], %[qa] offset:256\n\t" \
                        "ds_read_b64_tr_b16 v[214:215], %[qa] offset:288\n\t" \
                        "s_setprio 3\n\t" \
                        "s_waitcnt vmcnt(0) lgkmcnt(0)\n\t" \
                        "s_barrier\n\t" \
                        MTA(3) \
                        "ds_read_b64_tr_b16 v[216:217], %[ka] offset:0\n\t" \
                        "ds_read_b64_tr_b16 v[218:219], %[ka] offset:128\n\t" \
                        "ds_read_b64_tr_b16 v[220:221], %[ka] offset:256\n\t" \
                        "ds_read_b64_tr_b16 v[222:223], %[ka] offset:384\n\t" \
                        "v_smfmac_f32_16x16x64_bf16 v[240:243], v[208:211], %[k00], %[ix]\n\t" \
                        "ds_read_b64_tr_b16 v[224:225], %[ka] offset:512\n\t" \
                        "ds_read_b64_tr_b16 v[226:227], %[ka] offset:640\n\t" \
                        "v_smfmac_f32_16x16x64_bf16 v[240:243], v[212:215], %[k01], %[ix]\n\t" \
                        "ds_read_b64_tr_b16 v[228:229], %[ka] offset:768\n\t" \
                        "ds_read_b64_tr_b16 v[230:231], %[ka] offset:896\n\t" \
                        "v_smfmac_f32_16x16x64_bf16 v[244:247], v[208:211], %[k10], %[ix]\n\t" \
                        "v_smfmac_f32_16x16x64_bf16 v[244:247], v[212:215], %[k11], %[ix]\n\t" \
                        "s_nop 3\n\t" \
                        ".else\n\t" \
                        "s_waitcnt lgkmcnt(0)\n\t" \
                        "s_barrier\n\t" \
                        "ds_read_b64_tr_b16 v[208:209], %[qa]\n\t" \
                        "ds_read_b64_tr_b16 v[210:211], %[qa] offset:32\n\t" \
                        "ds_read_b64_tr_b16 v[212:213], %[qa] offset:256\n\t" \
                        "ds_read_b64_tr_b16 v[214:215], %[qa] offset:288\n\t" \
                        "s_setprio 0\n\t" \
                        "s_waitcnt vmcnt(0) lgkmcnt(0)\n\t" \
                        "v_smfmac_f32_16x16x64_bf16 v[240:243], v[208:211], %[k00], %[ix]\n\t" \
                        "v_smfmac_f32_16x16x64_bf16 v[240:243], v[212:215], %[k01], %[ix]\n\t" \
                        "s_setprio 3\n\t" \
                        "s_barrier\n\t" \
                        MTA(3) \
                        "ds_read_b64_tr_b16 v[216:217], %[ka] offset:0\n\t" \
                        "ds_read_b64_tr_b16 v[218:219], %[ka] offset:128\n\t" \
                        "ds_read_b64_tr_b16 v[220:221], %[ka] offset:256\n\t" \
                        "ds_read_b64_tr_b16 v[222:223], %[ka] offset:384\n\t" \
                        "v_smfmac_f32_16x16x64_bf16 v[244:247], v[208:211], %[k10], %[ix]\n\t" \
                        "ds_read_b64_tr_b16 v[224:225], %[ka] offset:512\n\t" \
                        "ds_read_b64_tr_b16 v[226:227], %[ka] offset:640\n\t" \
                        "v_smfmac_f32_16x16x64_bf16 v[244:247], v[212:215], %[k11], %[ix]\n\t" \
                        "ds_read_b64_tr_b16 v[228:229], %[ka] offset:768\n\t" \
                        "ds_read_b64_tr_b16 v[230:231], %[ka] offset:896\n\t" \
                        ".endif\n\t" \
                        "v_add_f32 v232, " A0 ", " A2 "\n\t" \
                        "v_mul_f32 v236, 0x3e0293ee, v232\n\t" \
                        "v_exp_f32 v236, v236\n\t" \
                        "s_waitcnt lgkmcnt(4)\n\t" \
                        "v_smfmac_f32_16x16x64_bf16 v[248:251], v[208:211], v[216:223], %[ix]\n\t" \
                        "s_waitcnt lgkmcnt(0)\n\t" \
                        "v_smfmac_f32_16x16x64_bf16 v[248:251], v[212:215], v[224:231], %[ix]\n\t" \
                        "v_add_f32 v233, " B0 ", " B2 "\n\t" \
                        "v_mul_f32 v237, 0x3e0293ee, v233\n\t" \
                        "v_exp_f32 v237, v237\n\t" \
                        "s_nop 0\n\t" \
                        "v_add_f32 v239, v236, v237\n\t" \
                        "s_nop 1\n\t" \
                        "v_add_f32_dpp v239, v239, v239 row_ror:8 row_mask:0xf bank_mask:0xf\n\t" \
                        MTA(4) \
                        "v_add_f32 v234, " C0 ", " C2 "\n\t" \
                        "v_mul_f32 v238, 0x3e0293ee, v234\n\t" \
                        "v_exp_f32 v238, v238\n\t" \
                        "s_nop 0\n\t" \
                        "v_add_f32 v239, v239, v238\n\t" \
                        "v_mov_b32_dpp v235, v238 quad_perm:[1,1,3,3] row_mask:0xf bank_mask:0xf\n\t" \
                        "s_nop 0\n\t" \
                        "v_add_f32_dpp v239, v239, v239 row_ror:4 row_mask:0xf bank_mask:0xf\n\t" \
                        "s_nop 1\n\t" \
                        "v_add_f32_dpp v239, v239, v239 row_ror:2 row_mask:0xf bank_mask:0xf\n\t" \
                        "s_nop 1\n\t" \
                        "v_add_f32_dpp v239, v239, v239 row_ror:1 row_mask:0xf bank_mask:0xf\n\t" \
                        "v_rcp_f32 v207, v239\n\t" \
                        "v_add_u32 v239, 0xe8800000, v239\n\t" \
                        "v_mul_f32 v236, v236, v207\n\t" \
                        "v_cmp_lt_u32 vcc, %[rg], v239\n\t" \
                        "v_mul_f32 v237, v237, v207\n\t" \
                        "v_cvt_pk_bf16_f32 v236, v236, v237\n\t" \
                        "s_cbranch_vccnz 3f\n\t" \
                        MTA(5) \
                        "buffer_store_dword v236, %[pa], %[rw], %[p0] offen sc1\n\t" \
                        "v_mul_f32 v238, v238, v207\n\t" \
                        "v_mul_f32 v235, v235, v207\n\t" \
                        "buffer_store_dword v236, %[pa], %[rw], %[p1] offen sc1\n\t" \
                        "v_cvt_pk_bf16_f32 v238, v238, v235\n\t" \
                        "buffer_store_dword v238, %[pm], %[rw], %[p0] offen sc1\n\t" \
                        "3:\n\t" \
                        "s_mov_b64 %[sus], vcc\n\t" \
                        "s_setprio 0\n\t"
#define TAILIO : [sus] "=&s"(sus), "=&{v[232:235]}"(sc), "+{v[240:243]}"(s0), "+{v[244:247]}"(s1), "+{v[248:251]}"(s2) \
                        : [qa] "v"(qa), [ka] "v"(ka), [ix] "v"(sidx), [pa] "v"(pa0), [pm] "v"(pa2), [rw] "s"(rwin), [p0] "s"(pb), [p1] "s"(pb1), [rg] "s"(SRG), \
                          [k00] "v"(kx[0][0]), [k01] "v"(kx[0][1]), [k10] "v"(kx[1][0]), [k11] "v"(kx[1][1]) \
                        : "v207", "v208", "v209", "v210", "v211", "v212", "v213", "v214", "v215", "v216", "v217", "v218", "v219", "v220", "v221", "v222", "v223", "v224", "v225", "v226", "v227", "v228", "v229", "v230", "v231", "v236", "v237", "v238", "v239", "vcc", "scc", "memory"
                    if (R == 0) asm volatile(TAIL("v240", "v242", "v244", "v246", "v248", "v250") TAILIO);
                    else asm volatile(TAIL("v241", "v243", "v245", "v247", "v249", "v251") TAILIO);
                    RT(4);
                    MT(6);
                } else {
                    f32x8 a01;
                    asm volatile("v_mov_b64 v[200:201], 0\n\tv_mov_b64 v[202:203], 0\n\tv_mov_b64 v[204:205], 0\n\tv_mov_b64 v[206:207], 0" : "={v[200:207]}"(a01));
                    unsigned sr = TK ? tro + (unsigned)(s & 3) * 2u * PAIR : qrdy < min(s + 3, NSEG - 1) ? W_FQ + fb + (unsigned)hh * 128u : 0x40000000u;
                    unsigned tb = 0, tg = (unsigned)(s + 64 * lay) << 24, so = s > 0 ? W_T + (unsigned)(s & 3) * 2u * PAIR + (unsigned)hh * 128u : 0x40000000u;
                    asm volatile("" : "+s"(lim), "+s"(sr));
                    if (TK) { asm volatile("" : "+s"(tg), "+s"(so)); tb = fetch(tk, s, !redo && s >= lay); }
                    else asm volatile(AL "s_waitcnt lgkmcnt(0)\n\ts_barrier\n\ts_barrier\n\ts_waitcnt vmcnt(0)\n\ts_barrier" ::: "memory");
                    asm volatile(
                        ".if %[tk]\n\t"
                        "s_waitcnt vmcnt(0)\n\t"
                        "s_memtime s[100:101]\n\t"
                        "s_barrier\n\t"
                        "s_setprio 1\n\t"
                        "s_waitcnt lgkmcnt(0)\n\t"
                        "ds_read2st64_b32 v[208:209], %[sp] offset1:16\n\t"
                        "ds_read_b32 v210, %[sp] offset:6144\n\t"
                        "ds_read_b128 v[212:215], %[mp] offset:0\n\t"
                        "ds_read_b128 v[216:219], %[mp] offset:1024\n\t"
                        "ds_read_b128 v[220:223], %[mp] offset:2048\n\t"
                        "ds_read_b128 v[224:227], %[mp] offset:3072\n\t"
                        "s_sub_u32 %[tb], s100, %[tb]\n\t"
                        "s_mul_i32 %[tb], %[tb], %[qc]\n\t"
                        "s_lshr_b32 %[tb], %[tb], 16\n\t"
                        "s_add_u32 %[tb], %[tb], %[ta]\n\t"
                        "s_and_b32 %[tb], %[tb], 0xffffff\n\t"
                        "s_or_b32 %[tb], %[tb], %[tg]\n\t"
                        "v_mov_b32 v211, %[tb]\n\t"
                        "s_waitcnt lgkmcnt(4)\n\t"
                        "v_max3_u32 v208, v208, v209, v210\n\t"
                        "v_cmp_lt_u32 vcc, %[lim], v208\n\t"
                        "ds_read_b128 v[228:231], %[mp] offset:4096\n\t"
                        "s_cmp_eq_u64 vcc, 0\n\t"
                        "s_cselect_b32 %[so], %[so], 2.0\n\t"
                        "buffer_store_dword v211, %[tw], %[rw], %[so] offen sc1\n\t"
                        "ds_read_b128 v[232:235], %[mp] offset:5120\n\t"
                        ".else\n\t"
                        "s_setprio 1\n\t"
                        "ds_read_b128 v[212:215], %[mp] offset:0\n\t"
                        "ds_read_b128 v[216:219], %[mp] offset:1024\n\t"
                        "ds_read_b128 v[220:223], %[mp] offset:2048\n\t"
                        "ds_read_b128 v[224:227], %[mp] offset:3072\n\t"
                        "ds_read_b128 v[228:231], %[mp] offset:4096\n\t"
                        "ds_read_b128 v[232:235], %[mp] offset:5120\n\t"
                        ".endif\n\t"
                        ".if !%[tk]\n\tbuffer_load_dword %[sf], %[sl], %[rw], %[sr] offen sc1\n\t.endif\n\t"
                        MTA(4)
                        "s_waitcnt lgkmcnt(5)\n\t"
                        "v_smfmac_f32_16x16x64_bf16 v[200:203], v[212:215], %[w00], %[ix]\n\t"
                        "ds_read_b128 v[236:239], %[mp] offset:6144\n\t"
                        "v_smfmac_f32_16x16x64_bf16 v[204:207], v[212:215], %[w10], %[ix]\n\t"
                        "ds_read_b128 v[240:243], %[mp] offset:7168\n\t"
                        "s_waitcnt lgkmcnt(6)\n\t"
                        "v_smfmac_f32_16x16x64_bf16 v[200:203], v[216:219], %[w01], %[ix]\n\t"
                        "ds_read_b128 v[244:247], %[mp] offset:8192\n\t"
                        "v_smfmac_f32_16x16x64_bf16 v[204:207], v[216:219], %[w11], %[ix]\n\t"
                        "ds_read_b128 v[248:251], %[mp] offset:9216\n\t"
                        "s_waitcnt lgkmcnt(4)\n\t"
                        "v_smfmac_f32_16x16x64_bf16 v[200:203], v[220:223], %[w02], %[ix]\n\t"
                        "v_smfmac_f32_16x16x64_bf16 v[204:207], v[220:223], %[w12], %[ix]\n\t"
                        "v_smfmac_f32_16x16x64_bf16 v[200:203], v[224:227], %[w03], %[ix]\n\t"
                        "v_smfmac_f32_16x16x64_bf16 v[204:207], v[224:227], %[w13], %[ix]\n\t"
                        "v_smfmac_f32_16x16x64_bf16 v[200:203], v[228:231], %[w04], %[ix]\n\t"
                        "v_smfmac_f32_16x16x64_bf16 v[204:207], v[228:231], %[w14], %[ix]\n\t"
                        "v_smfmac_f32_16x16x64_bf16 v[200:203], v[232:235], %[w05], %[ix]\n\t"
                        "v_smfmac_f32_16x16x64_bf16 v[204:207], v[232:235], %[w15], %[ix]\n\t"
                        "s_waitcnt lgkmcnt(0)\n\t"
                        "v_smfmac_f32_16x16x64_bf16 v[200:203], v[236:239], %[w06], %[ix]\n\t"
                        "v_smfmac_f32_16x16x64_bf16 v[204:207], v[236:239], %[w16], %[ix]\n\t"
                        BQ(3)
                        "v_smfmac_f32_16x16x64_bf16 v[200:203], v[240:243], %[w07], %[ix]\n\t"
                        "v_smfmac_f32_16x16x64_bf16 v[204:207], v[240:243], %[w17], %[ix]\n\t"
                        BQ(5)
                        "v_smfmac_f32_16x16x64_bf16 v[200:203], v[244:247], %[w08], %[ix]\n\t"
                        "v_smfmac_f32_16x16x64_bf16 v[200:203], v[248:251], %[w09], %[ix]\n\t"
                        BQ(7)
                        "v_smfmac_f32_16x16x64_bf16 v[204:207], v[244:247], %[w18], %[ix]\n\t"
                        "v_smfmac_f32_16x16x64_bf16 v[204:207], v[248:251], %[w19], %[ix]\n\t"
                        BQ(9)
                        "s_nop 5\n\t"
                        "v_pk_add_f32 v[200:201], v[200:201], v[202:203]\n\t"
                        "v_cvt_pk_bf16_f32 v200, v200, v201\n\t"
                        "v_pk_add_f32 v[204:205], v[204:205], v[206:207]\n\t"
                        "v_cvt_pk_bf16_f32 v201, v204, v205\n\t"
                        "ds_write2_b32 %[qw], v200, v201 offset1:32\n\t"
                        ".if %[tk]\n\tbuffer_load_dword %[sf], %[sl], %[rw], %[sr] offen sc1\n\t.endif\n\t"
                        "v_cmp_u_f32 %[sus], v204, v205\n\t"
                        MTA(2)
                        "s_waitcnt lgkmcnt(0)\n\t"
                        "s_barrier\n\t"
                        "s_setprio 0\n\t"
                        ".if %[tk]\n\ts_cmp_lg_u64 %[sus], 0\n\ts_cbranch_scc1 1f\n\t.endif\n\ts_waitcnt vmcnt(0)\n1:"
                        : [sus] "=&s"(sus), [sf] "+&v"(sf), [tb] "+&s"(tb), [so] "+&s"(so), "+{v[200:207]}"(a01)
                        : [mp] "v"(mp0), [qw] "v"(qw), [ix] "v"(sidx), [sl] "v"(TK ? tk.sl : fqv), [tk] "n"(TK ? 1 : 0), [sr] "s"(sr), [rw] "s"(rwin),
                          [sp] "v"(TK ? tk.sp : mp0), [lim] "s"(lim), [qc] "s"(ck.qpc), [ta] "s"(ck.ta), [tg] "s"(tg), [tw] "v"(TK ? tk.tw : fqv), [w00] "v"(wt[0][0]), [w10] "v"(wt[1][0]), [w01] "v"(wt[0][1]), [w11] "v"(wt[1][1]), [w02] "v"(wt[0][2]), [w12] "v"(wt[1][2]), [w03] "v"(wt[0][3]), [w13] "v"(wt[1][3]), [w04] "v"(wt[0][4]), [w14] "v"(wt[1][4]), [w05] "v"(wt[0][5]), [w15] "v"(wt[1][5]), [w06] "v"(wt[0][6]), [w16] "v"(wt[1][6]), [w07] "v"(wt[0][7]), [w17] "v"(wt[1][7]), [w08] "v"(wt[0][8]), [w18] "v"(wt[1][8]), [w09] "v"(wt[0][9]), [w19] "v"(wt[1][9])
                        : "v208", "v209", "v210", "v211", "v212", "v213", "v214", "v215", "v216", "v217", "v218", "v219", "v220", "v221", "v222", "v223", "v224", "v225", "v226", "v227", "v228", "v229", "v230", "v231", "v232", "v233", "v234", "v235", "v236", "v237", "v238", "v239", "v240", "v241", "v242", "v243", "v244", "v245", "v246", "v247", "v248", "v249", "v250", "v251", "vcc", "scc", "s100", "s101", "memory");
                    MT(3);
                }
                redo = false;
                if (__builtin_expect(sus != 0, 0)) {
                    redo = again(TK, tk.vo, sf, s, lim, mp0, tk.sp);
                    if (!redo && C < 2) {
                        const float c = 0.08838834764831845f * 1.44269504088896340736f;
                        float mx = fmx(fmx(sc[0], sc[1]), sc[2]);
                        mx = fmx(mx, dppf<0x128>(mx)); mx = fmx(mx, dppf<0x124>(mx)); mx = fmx(mx, dppf<0x122>(mx)); mx = fmx(mx, dppf<0x121>(mx));
                        const float e0 = __builtin_amdgcn_exp2f((sc[0] - mx) * c), e1 = __builtin_amdgcn_exp2f((sc[1] - mx) * c), e2 = __builtin_amdgcn_exp2f((sc[2] - mx) * c);
                        float d = e0 + e1;
                        d += dppf<0x128>(d); d += e2; d += dppf<0x124>(d); d += dppf<0x122>(d); d += dppf<0x121>(d);
                        d = __builtin_amdgcn_rcpf(d);
                        const unsigned p0 = nosent1(pack2(e0 * d, e1 * d)), p1 = nosent1(pack2(e2 * d, dppf<0xF5>(e2) * d));
                        __builtin_amdgcn_raw_buffer_store_b32(p0, rwin, pa0, pb, 16); __builtin_amdgcn_raw_buffer_store_b32(p0, rwin, pa0, pb1, 16);
                        __builtin_amdgcn_raw_buffer_store_b32(p1, rwin, pa2, pb, 16);
                    }
                }
            } while (__builtin_expect(redo, 0));
            if (__builtin_expect(mb != 0, 0)) { mp0 -= mb; tk.sp -= mb; mb = 0; }
            if (s < 8) SST(8 + s);
            if (s >= 56) SST(s - 40);
            if (C + R == 1) {
                {
                    __builtin_amdgcn_s_sleep(DDMA);
                    unsigned l2 = fresh_lane(), s1;
                    asm volatile("v_mov_b32 %0, -1" : "=v"(s1));
                    const unsigned base = W_P + (unsigned)((s - 1) & 3) * 10u * PAIR + nh;
                    asm volatile("s_waitcnt vmcnt(3)" ::: "memory");
                    st128(((u32x4){s1, s1, s1, s1}), rwin, (l2 >> 5) * PAIR + (l2 & 31u) * 16u, base + (unsigned)(R * 4 + 2 * (hh >> 4)) * PAIR + (unsigned)(hh & 15) * 512u, 16);
                    if (R) __builtin_amdgcn_raw_buffer_store_b64((u32x2){s1, s1}, rwin, (l2 >> 5) * PAIR + (l2 & 16u) * 256u + (l2 & 8u) * 16u + (l2 & 7u) * 8u, base + 8u * PAIR + (unsigned)((hh >> 1) * 256 + (hh & 1) * 64), 16);
                    else __builtin_amdgcn_raw_buffer_store_b32(s1, rwin, two(), W_T + (unsigned)((s + 2) & 3) * 2u * PAIR + (unsigned)hh * 128u, 16);
                }
            } else if (TK) {
                if (s + 1 < NSEG) sched2(tk, s, sf);
            } else if (C == 2 && w == 4) {
                qrdy = max(qrdy, seg_done(MXQ, __builtin_amdgcn_readlane(sf, 0), __builtin_amdgcn_readlane(sf, 1)));
                if (__builtin_expect(qf <= s, 0) && s + 1 < NSEG) {
                    for (unsigned spins = 0; qrdy <= s;) {
                        asm volatile("" ::: "memory");
                        const int f = (int)__builtin_amdgcn_raw_buffer_load_b32(rwin, fqv, W_FQ + fb + (unsigned)hh * 128u, 16);
                        qrdy = seg_done(MXQ, __builtin_amdgcn_readlane(f, 0), __builtin_amdgcn_readlane(f, 1));
                        if (qrdy > s) break;
                        FC(fc2);
                        if (spin_fail(spins, rws)) { qrdy = NSEG; break; }
                        __builtin_amdgcn_s_sleep(2);
                    }
                    *(volatile LAS int*)(smem + L_S) = s + 1;
                }
                while (qf < min(min(s + 2, qrdy), NSEG - 1)) {
                    ++qf;
                    const unsigned so = O_QK + (unsigned)((qf * H_ + hh) * 16384), lo = (unsigned)(L_TOK + qf % 3 * 16384), l2 = fresh_lane();
#pragma unroll
                    for (int i = 0; i < 4; ++i) dma4k(rws, l2 * 16u, so + i * 4096u, lo + i * 4096u);
                }
            } else {
                if (C == 2 && s + 1 < NSEG) __builtin_amdgcn_s_sleep(DPT);
                const float scale = 0.08838834764831845f, l2e = 1.44269504088896340736f * (NORND ? scale : 1.f);
                const int tq = (w >> 1) & 1, qh = w & 1;
                f32x4 sc[3] = {z4, z4, z4};
                u32x4 kt[2][4], qf[4], kf[4];
                const unsigned l2 = fresh_lane(), kto = ((l2 & 8u) * 512u + (l2 & 51u) * 16u + (l2 & 4u) * 32u) ^ ((l2 & 16u) * 5u), qto = ((l2 & 55u) * 16u + qh * 128u) ^ ((l2 & 16u) * 8u);
#pragma unroll
                for (int ks = 0; ks < 4; ++ks) {
                    qf[ks] = *(const u32x4*)(tb + 8192 + (tq * 4 + ks) * 1024 + qto);
                    kt[0][ks] = *(const u32x4*)(tb + ks * 1024 + kto);
                    kt[1][ks] = *(const u32x4*)(tb + ks * 1024 + (kto ^ 64u));
                    SB();
                }
#pragma unroll
                for (int ks = 0; ks < 4; ++ks) {
                    sc[0] = mfma32(kt[0][ks], qf[ks], sc[0]); PIN1(sc[0]);
                    kf[ks] = ldtr2(ka + ks * 256, ka + ks * 256 + 128);
                    SB();
                    sc[1] = mfma32(kt[1][ks], qf[ks], sc[1]); PIN1(sc[1]);
                }
#pragma unroll
                for (int ks = 0; ks < 4; ++ks) sc[2] = mfma32(kf[ks], qf[ks], sc[2]);
                const bool hi = (l2 & 8u) != 0u;
                float v[6];
#pragma unroll
                for (int r = 0; r < 4; ++r) v[r] = hi ? sc[1][r] : sc[0][r];
                v[4] = hi ? sc[2][2] : sc[2][0]; v[5] = hi ? sc[2][3] : sc[2][1];
#pragma unroll
                for (int r = 0; r < 6; r += 2) {
                    if (NORND) break;
                    const unsigned b0 = pack2(v[r], v[r + 1]);
                    const unsigned b1 = pack2(lo2f(b0) * scale, hi2f(b0) * scale);
                    v[r] = lo2f(b1); v[r + 1] = hi2f(b1);
                }
                const int lim = 8 * (w & 3) + (int)(l2 & 7u) - 8 * (int)(l2 >> 4) - (int)((l2 & 8u) >> 1);
#pragma unroll
                for (int r = 0; r < 4; ++r) if (r > lim) v[r] = NORND ? -1000000000.0f / scale : bfr(-1000000000.0f);
                float mx = fmx(fmx(fmx(fmx(fmx(v[0], v[1]), v[2]), v[3]), v[4]), v[5]);
                mx = xmax3216(fmx(mx, ror8f(mx)));
                const float mb = -mx * l2e;
#pragma unroll
                for (int r = 0; r < 6; ++r) v[r] = __builtin_amdgcn_exp2f(fmaf(v[r], l2e, mb));
                float sum = fmaf(v[4] + v[5], 0.5f, (v[0] + v[1]) + (v[2] + v[3]));
                sum = xsum3216(sum + ror8f(sum));
                const float inv = __builtin_amdgcn_rcpf(sum);
                const unsigned p0 = pack2(v[0] * inv, v[1] * inv), p1 = nosent1(pack2(v[2] * inv, v[3] * inv)), p2 = nosent1(pack2(v[4] * inv, v[5] * inv));
                const unsigned base = s + 2 < NSEG ? O_PT + (unsigned)(s * PTSEG) * 2u : W_PL + fb + (s & 1) * 5u * PAIR;
                const __amdgpu_buffer_rsrc_t rp = s + 2 < NSEG ? rws : rwin;
                const unsigned oob = (l2 & 8u) << 27;
                st128(((u32x4){p0, p1, dppu<0x128>(p0), dppu<0x128>(p1)}), rp, (l2 * 16u) | oob, base + (unsigned)((hh * 2 + tq) * 1024 + qh * 128), 16);
                __builtin_amdgcn_raw_buffer_store_b64((u32x2){p2, dppu<0x128>(p2)}, rp, ((l2 & 7u) * 16u + (l2 >> 4) * 8u) | oob | (l2 & 32u) << 25, base + 65536u + (unsigned)((tq * 32 + hh) * 256 + qh * 128), 16);
            }
            tbo = tbo == 32768 ? 0 : tbo + 16384;
            if (__builtin_expect(budget <= 0, 0)) __builtin_amdgcn_raw_buffer_store_b32(1u, rws, 0u, 0u, 16);
            RT(5);
            RTS();
            MT(7);
            MTS();
        }
        };
        if (qt) {} else if (w == 0) steps(IC<0>()); else if (w == 1) steps(IC<1>()); else if (w == 2) steps(IC<2>()); else if (w == 3) steps(IC<3>()); else if (w < 7) steps(IC<4>()); else steps(IC<5>());
        __builtin_amdgcn_s_setprio(0);
    }
    SST(31);
    }
    } else
#pragma nounroll
    for (; lay < 2; ++lay) {
    fresh();
    SST(0);
    {
        LANES();
        const int G = bid < NWRK ? bid - 96 : bid == NWRK ? 80 : -1;
        const int fi = bid >= NWRK ? 255 - bid : bid < NREC ? 16 + G / 10 + 4 * (G % 10) : 99;
        const bool first = fi < 32;
        u32x2 tag = {0u, 0u};
        if (lay == 0) tag = get_tag();
        const int fl = min(lane, lay ? 13 : 15);
        const unsigned fv = w < 4 ? lane * 128u + 64u : (fl & 14) * 512u + (fl & 1) * 128u + (w & 3) * 256u, fs = lay ? W_FA : W_ST;
        auto ask = [&]() { asm volatile("" ::: "memory"); return __builtin_amdgcn_raw_buffer_load_b128(rwin, fv, fs, 16); };
        auto miss = [&](u32x4 f) { return __ballot(((f[0] ^ tag[0]) | (f[1] ^ tag[1]) | (f[2] ^ tag[0]) | (f[3] ^ tag[1])) != 0u); };
        if (G < 0 && w >= 4) for (unsigned spins = 0; spins < SPIN_CAP && miss(ask()); ++spins) __builtin_amdgcn_s_sleep(ASLP);
        BAR();
        if (G >= 0) {
            unsigned long long m = ~0ull;
            unsigned e = ~0u;
            a2_unit(rws, G, w, lane, lay == 0, [&](int r) {
                if (r == 6) { if (lay) e = __builtin_amdgcn_raw_buffer_load_b32(rwin, (lane & 1) * 128u, W_E + (E_A1 + 2 * (w & 3)) * 128u, 16); else if (DFA) __builtin_amdgcn_s_sleep(DFA); }
                if (r >= 7 && lay) { if (r == 7) await(E_A1 + 2 * (w & 3), 2, 0, e); }
                else for (unsigned spins = 0; spins < SPIN_CAP && (r == 6 ? m & 0x3FFFu : m); ++spins) {
                    if (spins) __builtin_amdgcn_s_sleep(ASLP);
                    m = miss(ask());
                }
                if (r == 7) SST(5);
            }, [&](int k) {
                if (k == 2 && w == 3 && blockIdx.x < 136) due(E_A2 + (blockIdx.x & 7), lay ? DA2 : DA20);
                if (k == 7 && YSLP) __builtin_amdgcn_s_sleep(YSLP);
                if (bid < NREC) SST(8 + k);
            });
            SST(6);
            VMWAIT();
            BAR0();
            if (tid == 0) __builtin_amdgcn_raw_buffer_store_b32(0u, rwin, 0u, W_FB + fb + (unsigned)((G >> 2) * 128 + (G & 3) * 4), 16);
            SST(7);
        }
        const RowTw tw = row_tw(lane);
        auto columns = [&](int all) {
            if (w == 7) await(E_A2 + fb / 128u, 8, all);
            if (w == 7 && all) for (unsigned spins = 0;;) {
                asm volatile("" ::: "memory");
                const u32x4 f = __builtin_amdgcn_raw_buffer_load_b128(rwin, (unsigned)min(lane, 20) * 128u, W_FB + fb, 16);
                if (!__any((f[0] | (lane < 20 ? f[1] | f[2] | f[3] : 0u)) != 0u)) break;
                if (spin_fail(spins, rws)) break;
                __builtin_amdgcn_s_sleep(ASLP);
            }
            BAR0();
            if (!all || !first) SST(3);
        };
        const __amdgpu_buffer_rsrc_t rx = mkrsrc(lay ? karg<0>() + OFF_X2 : karg<3>(), S_ * D_ * 2);
        if (first) {
            const unsigned fo = fb + (unsigned)((fi >> 2) * 128 + (fi & 3) * 4);
            a3_units<4>(rws, rx, fi * 8, 0, lay, smem, tw, w, lane, [&](int k) {
                if (k < 0) { if (G < 0) columns(0); } else if (fi < 16) SST(16 + k);
                if (k == 0 && w == 7 && fi < 4) due(E_A3 + fi, lay ? DA3 : DA30);
            }, [&](int) { if (tid == 0) __builtin_amdgcn_raw_buffer_store_b32(0u, rwin, 0u, W_H + fo, 16); });
            SST(4);
        }
                if (!first || (WK3 && bid >= NWRK)) {
            const bool two = bid >= NREC && bid < NREC + 32;
            const int u0 = two ? 32 + 16 * WK3 + (bid - NREC) / 8 + 4 * ((bid - NREC) % 8) : bid < NREC ? fi - 24 : bid < NWRK ? bid - NREC - 32 : 32 + fi;
            columns(two ? DFB - (32 + 16 * WK3) * DST + u0 * DST : DFB0);
            a3_units<8>(rws, rx, (16 + u0) * 16, two ? 512 : S_, lay, smem, tw, w, lane, [](int) {}, [&](int r) {
                if (tid == 0) __builtin_amdgcn_raw_buffer_store_b64((u32x2){0u, 0u}, rwin, 0u, W_H + fb + (unsigned)((r >> 5) * 128 + (r & 16) / 2), 16);
                if (r < 1536 && two) SST(2);
            });
            VMWAIT();
        }
        if (lay == 0 && bid < NREC) { config0(); config(); }
        SST(1);
        places();
    }
    const __amdgpu_buffer_rsrc_t rout = mkrsrc(lay ? karg<4>() : karg<0>() + OFF_X2, S_ * D_ * 2);
    if (bid < NREC) {
        LANES();
        const int o = bid - 96, hg = w & 3;
        const unsigned ztoff = (unsigned)((o * H_ + 8 * hg) * 64 + lane) * 16u;
        const unsigned zoff = zpair(o / 10, o % 10) + (unsigned)(lane * 16);
        const unsigned poff = (4u * (mc >> 1) + 2u * (hg >> 1) + (mc & 1u)) * PAIR + nh + (unsigned)((hg & 1) * 4096 + (fq >> 1) * 512 + (fr >> 2) * 128 + (fr & 1) * 64 + (fq & 1) * 32 + (fr & 2) * 8);
        const unsigned pmoff = (8u + (mc & 1u)) * PAIR + nh + (mc >> 1) * 4096u + (unsigned)(fq * 256 + (fr >> 3) * 128 + (fr & 2) * 32 + (fr & 4) * 8 + (fr & 1) * 16);
        const unsigned fzo = (unsigned)(8 * hg + (lane & 7)) * 128u + (unsigned)(o / 10) * 8192u + (lane & 8) * 512u;
        const unsigned moff = (o < 32 ? (unsigned)o * 256u : 4u * PAIR + (unsigned)(o - 32) * 256u) + nh + (unsigned)(lane * 4);
        const unsigned mc2 = o < 32 ? 2u * PAIR : 2048u;
        char* op = smem + 8192;
        SST(2);
        u32x8 zf[4];
        asm volatile("" : "={v[160:167]}"(zf[0]), "={v[168:175]}"(zf[1]), "={v[176:183]}"(zf[2]), "={v[184:191]}"(zf[3]));
        int zr = -1;
        const unsigned ra = (o < 32 ? (unsigned)fq * PAIR + (unsigned)o * 256u : (4u + (fq & 1)) * PAIR + (unsigned)(fq >> 1) * 2048u + (unsigned)(o - 32) * 256u) + nh + (unsigned)(fr * 16);
        auto zt_wait = [&](int s) {
            if (__builtin_expect(zr < s, 0)) for (unsigned spins = 0;;) {
                asm volatile("" ::: "memory");
                if (!__any((int)__builtin_amdgcn_raw_buffer_load_b32(rwin, fzo, W_FZ + fb, 16) < s && ((lane >> 3) & 1) == (int)((MXV >> s) & 1))) break;
                FC(fc2);
                if (spin_fail(spins, rws)) break;
                __builtin_amdgcn_s_sleep(ZPS);
            }
        };
        auto sent = [&](int s) {
            for (;;) {
                const u32x4 x = *(volatile LAS u32x4*)(smem + 4096);
                zr = __builtin_amdgcn_readfirstlane(x[0]);
                if (__builtin_amdgcn_readfirstlane(min((int)x[1], max((int)x[2], (int)x[0] - 1))) == s) break;
                __builtin_amdgcn_s_sleep(2);
            }
        };
        if (w < 4) {
            await(E_ZT + fb / 128u, 4, 0);
            for (int s = 0; s + 1 < NSEG; ++s) {
                const unsigned pb = W_P + (unsigned)(s & 3) * 10u * PAIR;
                zt_wait(s);
                if (s == 0) SST(1);
#define ZTL \
                    "buffer_load_dwordx4 v[160:163], %[zt], %[rs], %[so] offen sc1\n\t" \
                    "buffer_load_dwordx4 v[164:167], %[zt], %[rs], %[so] offen offset:1024 sc1\n\t" \
                    "buffer_load_dwordx4 v[168:171], %[zt], %[rs], %[so] offen offset:2048 sc1\n\t" \
                    "buffer_load_dwordx4 v[172:175], %[zt], %[rs], %[so] offen offset:3072 sc1\n\t" \
                    "buffer_load_dwordx4 v[176:179], %[zt], %[rs], %[s4] offen sc1\n\t" \
                    "buffer_load_dwordx4 v[180:183], %[zt], %[rs], %[s4] offen offset:1024 sc1\n\t" \
                    "buffer_load_dwordx4 v[184:187], %[zt], %[rs], %[s4] offen offset:2048 sc1\n\t" \
                    "buffer_load_dwordx4 v[188:191], %[zt], %[rs], %[s4] offen offset:3072 sc1\n\t"
                const unsigned zso = O_Z + (unsigned)(s * ZTSEG) * 2u;
                if (__builtin_expect(budget <= 0, 0)) __builtin_amdgcn_raw_buffer_store_b32(1u, rws, 0u, 0u, 16);
                RT(6);
                const unsigned spa = (unsigned)((s & 1) * 2048 + (w < 3 ? w * 512 : 0) + lane * 8);
#define OST(r)
                unsigned long long t0_, t1_, t2_, t3_, t4_ = 0, bad;
                int tries = budget;
                unsigned po = poff, pm = pmoff, x0 = 4u * PAIR, x1 = 4096u;
                const unsigned mb = W_MEM + (unsigned)((s + 1) & 3) * 6u * PAIR;
                if (w < 3) asm volatile("s_nop 4\n\t" ZTL AL
                    "s_barrier\n\t"
                    OST("%[t0]")
                    "v_mov_b64 v[96:97], 0\n\t"
                    "v_mov_b64 v[98:99], 0\n\t"
                    "s_barrier\n\t"
                    "1:\n\t"
                    "buffer_load_dwordx4 v[208:211], %[po], %[rw], %[pb] offen sc1\n\t"
                    "buffer_load_dwordx4 v[212:215], %[po], %[rw], %[pb] offen offset:1024 sc1\n\t"
                    "buffer_load_dwordx4 v[216:219], %[po], %[rw], %[pb] offen offset:2048 sc1\n\t"
                    "buffer_load_dwordx4 v[220:223], %[po], %[rw], %[pb] offen offset:3072 sc1\n\t"
                    OST("%[t1]")
                    "s_waitcnt vmcnt(3)\n\t"
                    OST("%[t3]")
                    "v_smfmac_f32_16x16x64_bf16 v[96:99], v[208:211], v[160:167], %[ix]\n\t"
                    "s_waitcnt vmcnt(2)\n\t"
                    "v_smfmac_f32_16x16x64_bf16 v[96:99], v[212:215], v[168:175], %[ix]\n\t"
                    "s_waitcnt vmcnt(1)\n\t"
                    "v_smfmac_f32_16x16x64_bf16 v[96:99], v[216:219], v[176:183], %[ix]\n\t"
                    "s_waitcnt vmcnt(0)\n\t"
                    "v_smfmac_f32_16x16x64_bf16 v[96:99], v[220:223], v[184:191], %[ix]\n\t"
                    OST("%[t2]")
                    "s_nop 7\n\t"
                    "v_pk_add_f32 v[96:97], v[96:97], v[98:99]\n\t"
                    "ds_write_b64 %[sp], v[96:97]\n\t"
                    "v_cmp_u_f32 vcc, v96, v97\n\t"
                    "s_cbranch_vccz 2f\n\t"
                    "v_max3_u32 v100, v208, v208, v209\n\t"
                    "v_max3_u32 v100, v100, v210, v211\n\t"
                    "v_max3_u32 v100, v100, v212, v213\n\t"
                    "v_max3_u32 v100, v100, v214, v215\n\t"
                    "v_max3_u32 v100, v100, v216, v217\n\t"
                    "v_max3_u32 v100, v100, v218, v219\n\t"
                    "v_max3_u32 v100, v100, v220, v221\n\t"
                    "v_max3_u32 v100, v100, v222, v223\n\t"
                    "v_cmp_eq_u32 vcc, -1, v100\n\t"
                    "s_cbranch_vccz 2f\n\t"
                    "s_sub_i32 %[tr], %[tr], 1\n\t"
                    "s_cmp_lt_i32 %[tr], 1\n\t"
                    "s_cbranch_scc1 2f\n\t"
                    "v_xor_b32 %[po], %[x0], %[po]\n\t"
                    "s_xor_b32 %[x0], %[x0], 0x14000\n\t"
                    "s_waitcnt lgkmcnt(0)\n\t"
                    "v_mov_b64 v[96:97], 0\n\t"
                    "v_mov_b64 v[98:99], 0\n\t"
                    "s_branch 1b\n\t"
                    "2:\n\t"
                    "s_waitcnt lgkmcnt(0)\n\t"
                    "s_barrier\n\t"
                    "s_sleep 3\n\t"
                    : [tr] "+&s"(tries), [po] "+&v"(po), [x0] "+&s"(x0), [t0] "=&s"(t0_), [t1] "=&s"(t1_), [t2] "=&s"(t2_), [t3] "=&s"(t3_), "=&{v[160:167]}"(zf[0]), "=&{v[168:175]}"(zf[1]), "=&{v[176:183]}"(zf[2]), "=&{v[184:191]}"(zf[3])
                    : [zt] "v"(ztoff), [rs] "s"(rws), [so] "s"(zso), [s4] "s"(zso + 4096u), [sp] "v"(spa), [ix] "v"(sidx), [rw] "s"(rwin), [pb] "s"(pb)
                    : "v96", "v97", "v98", "v99", "v100", "v208", "v209", "v210", "v211", "v212", "v213", "v214", "v215", "v216", "v217", "v218", "v219", "v220", "v221", "v222", "v223", "vcc", "scc", "memory");
                else asm volatile("s_nop 4\n\t" ZTL AL
                    "s_barrier\n\t"
                    "buffer_load_dwordx4 v[128:131], %[zo], %[rw], %[zb] offen sc1\n\t"
                    "buffer_load_dwordx4 v[132:135], %[zo], %[rw], %[zb] offen offset:1024 sc1\n\t"
                    "buffer_load_dwordx4 v[136:139], %[zo], %[rw], %[zb] offen offset:2048 sc1\n\t"
                    "buffer_load_dwordx4 v[140:143], %[zo], %[rw], %[zb] offen offset:3072 sc1\n\t"
                    "buffer_load_dwordx4 v[144:147], %[zo], %[rw], %[z4] offen sc1\n\t"
                    "buffer_load_dwordx4 v[148:151], %[zo], %[rw], %[z4] offen offset:1024 sc1\n\t"
                    "buffer_load_dwordx4 v[152:155], %[zo], %[rw], %[z4] offen offset:2048 sc1\n\t"
                    "buffer_load_dwordx4 v[156:159], %[zo], %[rw], %[z4] offen offset:3072 sc1\n\t"
                    OST("%[t0]")
                    "v_mov_b64 v[96:97], 0\n\t"
                    "v_mov_b64 v[98:99], 0\n\t"
                    "s_barrier\n\t"
                    "s_sleep %[fd]\n\t"
                    "buffer_load_dwordx4 v[208:211], %[po], %[rw], %[pb] offen sc1\n\t"
                    "buffer_load_dwordx4 v[212:215], %[po], %[rw], %[pb] offen offset:1024 sc1\n\t"
                    "buffer_load_dwordx4 v[216:219], %[po], %[rw], %[pb] offen offset:2048 sc1\n\t"
                    "buffer_load_dwordx4 v[220:223], %[po], %[rw], %[pb] offen offset:3072 sc1\n\t"
                    "buffer_load_dwordx4 v[224:227], %[pm], %[rw], %[pb] offen sc1\n\t"
                    "buffer_load_dwordx4 v[228:231], %[pm], %[rw], %[pb] offen offset:1024 sc1\n\t"
                    "buffer_load_dwordx4 v[232:235], %[pm], %[rw], %[pb] offen offset:2048 sc1\n\t"
                    "buffer_load_dwordx4 v[236:239], %[pm], %[rw], %[pb] offen offset:3072 sc1\n\t"
                    OST("%[t1]")
                    "s_waitcnt vmcnt(8)\n\t"
                    "v_max3_u32 v101, v129, v131, v133\n\t"
                    "v_max3_u32 v101, v101, v135, v137\n\t"
                    "v_max3_u32 v101, v101, v139, v141\n\t"
                    "v_max3_u32 v101, v101, v143, v145\n\t"
                    "v_max3_u32 v101, v101, v147, v149\n\t"
                    "v_max3_u32 v101, v101, v151, v153\n\t"
                    "v_max3_u32 v101, v101, v155, v157\n\t"
                    "v_max_u32 v101, v101, v159\n\t"
                    "v_cmp_eq_u32 vcc, -1, v101\n\t"
                    "s_cbranch_vccz 3f\n\t"
                    "buffer_load_dwordx4 v[128:131], %[zo], %[rw], %[zb] offen sc1\n\t"
                    "buffer_load_dwordx4 v[132:135], %[zo], %[rw], %[zb] offen offset:1024 sc1\n\t"
                    "buffer_load_dwordx4 v[136:139], %[zo], %[rw], %[zb] offen offset:2048 sc1\n\t"
                    "buffer_load_dwordx4 v[140:143], %[zo], %[rw], %[zb] offen offset:3072 sc1\n\t"
                    "buffer_load_dwordx4 v[144:147], %[zo], %[rw], %[z4] offen sc1\n\t"
                    "buffer_load_dwordx4 v[148:151], %[zo], %[rw], %[z4] offen offset:1024 sc1\n\t"
                    "buffer_load_dwordx4 v[152:155], %[zo], %[rw], %[z4] offen offset:2048 sc1\n\t"
                    "buffer_load_dwordx4 v[156:159], %[zo], %[rw], %[z4] offen offset:3072 sc1\n\t"
                    "s_waitcnt vmcnt(0)\n\t"
                    "v_max3_u32 v101, v129, v131, v133\n\t"
                    "v_max3_u32 v101, v101, v135, v137\n\t"
                    "v_max3_u32 v101, v101, v139, v141\n\t"
                    "v_max3_u32 v101, v101, v143, v145\n\t"
                    "v_max3_u32 v101, v101, v147, v149\n\t"
                    "v_max3_u32 v101, v101, v151, v153\n\t"
                    "v_max3_u32 v101, v101, v155, v157\n\t"
                    "v_max_u32 v101, v101, v159\n\t"
                    "3:\n\t"
                    "v_permlane16_swap_b32 v128, v129\n\t"
                    "v_permlane16_swap_b32 v130, v131\n\t"
                    "v_permlane16_swap_b32 v132, v133\n\t"
                    "v_permlane16_swap_b32 v134, v135\n\t"
                    "v_permlane16_swap_b32 v136, v137\n\t"
                    "v_permlane16_swap_b32 v138, v139\n\t"
                    "v_permlane16_swap_b32 v140, v141\n\t"
                    "v_permlane16_swap_b32 v142, v143\n\t"
                    "v_permlane16_swap_b32 v144, v145\n\t"
                    "v_permlane16_swap_b32 v146, v147\n\t"
                    "v_permlane16_swap_b32 v148, v149\n\t"
                    "v_permlane16_swap_b32 v150, v151\n\t"
                    "v_permlane16_swap_b32 v152, v153\n\t"
                    "v_permlane16_swap_b32 v154, v155\n\t"
                    "v_permlane16_swap_b32 v156, v157\n\t"
                    "v_permlane16_swap_b32 v158, v159\n\t"
                    "s_mov_b32 vcc_lo, 0x00ff00ff\n\t"
                    "s_mov_b32 vcc_hi, 0x00ff00ff\n\t"
                    "s_waitcnt vmcnt(7)\n\t"
                    OST("%[t3]")
                    "v_smfmac_f32_16x16x64_bf16 v[96:99], v[208:211], v[160:167], %[ix]\n\t"
                    "s_waitcnt vmcnt(6)\n\t"
                    "v_smfmac_f32_16x16x64_bf16 v[96:99], v[212:215], v[168:175], %[ix]\n\t"
                    "s_waitcnt vmcnt(5)\n\t"
                    "v_smfmac_f32_16x16x64_bf16 v[96:99], v[216:219], v[176:183], %[ix]\n\t"
                    "s_waitcnt vmcnt(4)\n\t"
                    "v_smfmac_f32_16x16x64_bf16 v[96:99], v[220:223], v[184:191], %[ix]\n\t"
                    OB(4)
                    "s_waitcnt vmcnt(3)\n\t"
                    "v_smfmac_f32_16x16x64_bf16 v[96:99], v[224:227], v[128:135], %[ix]\n\t"
                    OB(5)
                    "s_waitcnt vmcnt(2)\n\t"
                    "v_smfmac_f32_16x16x64_bf16 v[96:99], v[228:231], v[136:143], %[ix]\n\t"
                    OB(6)
                    "s_waitcnt vmcnt(1)\n\t"
                    "v_smfmac_f32_16x16x64_bf16 v[96:99], v[232:235], v[144:151], %[ix]\n\t"
                    "s_waitcnt vmcnt(0)\n\t"
                    "v_smfmac_f32_16x16x64_bf16 v[96:99], v[236:239], v[152:159], %[ix]\n\t"
                    OST("%[t2]")
                    "s_waitcnt lgkmcnt(0)\n\t"
                    "v_pk_add_f32 v[102:103], v[102:103], v[104:105]\n\t"
                    "v_pk_add_f32 v[102:103], v[102:103], v[106:107]\n\t"
                    "s_nop 4\n\t"
                    "6:\n\t"
                    "v_pk_add_f32 v[96:97], v[96:97], v[102:103]\n\t"
                    "v_pk_add_f32 v[96:97], v[96:97], v[98:99]\n\t"
                    "v_cmp_u_f32 %[bad], v96, v97\n\t"
                    "s_nop 0\n\t"
                    "v_cndmask_b32_dpp v98, v97, v96, vcc row_ror:8 row_mask:0xf bank_mask:0xf\n\t"
                    "s_not_b64 vcc, vcc\n\t"
                    "v_cndmask_b32_dpp v99, v96, v97, vcc row_ror:8 row_mask:0xf bank_mask:0xf\n\t"
                    "s_cmp_lg_u64 %[bad], 0\n\t"
                    "v_cvt_pk_bf16_f32 v98, v98, v99\n\t"
                    "s_cbranch_scc1 4f\n\t"
                    "2:\n\t"
                    "buffer_store_dword v98, %[mo], %[rw], %[m0] offen sc1\n\t"
                    "buffer_store_dword v98, %[mo], %[rw], %[m1] offen sc1\n\t"
                    "buffer_store_dword v98, %[mo], %[rw], %[m2] offen sc1\n\t"
                    "buffer_store_dword v98, %[mo], %[rw], %[m3] offen sc1\n\t"
                    OST("%[t4]")
                    "s_branch 5f\n\t"
                    "4:\n\t"
                    "v_min_u32 v98, -2, v98\n\t"
                    "v_max3_u32 v100, v101, v208, v209\n\t"
                    "v_max3_u32 v100, v100, v210, v211\n\t"
                    "v_max3_u32 v100, v100, v212, v213\n\t"
                    "v_max3_u32 v100, v100, v214, v215\n\t"
                    "v_max3_u32 v100, v100, v216, v217\n\t"
                    "v_max3_u32 v100, v100, v218, v219\n\t"
                    "v_max3_u32 v100, v100, v220, v221\n\t"
                    "v_max3_u32 v100, v100, v222, v223\n\t"
                    "v_max3_u32 v100, v100, v224, v225\n\t"
                    "v_max3_u32 v100, v100, v226, v227\n\t"
                    "v_max3_u32 v100, v100, v228, v229\n\t"
                    "v_max3_u32 v100, v100, v230, v231\n\t"
                    "v_max3_u32 v100, v100, v232, v233\n\t"
                    "v_max3_u32 v100, v100, v234, v235\n\t"
                    "v_max3_u32 v100, v100, v236, v237\n\t"
                    "v_max3_u32 v100, v100, v238, v239\n\t"
                    "v_cmp_eq_u32 %[bad], -1, v100\n\t"
                    "s_not_b64 vcc, vcc\n\t"
                    "s_cmp_eq_u64 %[bad], 0\n\t"
                    "s_cbranch_scc1 2b\n\t"
                    "s_sub_i32 %[tr], %[tr], 1\n\t"
                    "s_cmp_lt_i32 %[tr], 1\n\t"
                    "s_cbranch_scc1 2b\n\t"
                    "v_xor_b32 %[po], %[x0], %[po]\n\t"
                    "s_xor_b32 %[x0], %[x0], 0x14000\n\t"
                    "v_xor_b32 %[pm], %[x1], %[pm]\n\t"
                    "s_xor_b32 %[x1], %[x1], 0x5000\n\t"
                    "buffer_load_dwordx4 v[128:131], %[zo], %[rw], %[zb] offen sc1\n\t"
                    "buffer_load_dwordx4 v[132:135], %[zo], %[rw], %[zb] offen offset:1024 sc1\n\t"
                    "buffer_load_dwordx4 v[136:139], %[zo], %[rw], %[zb] offen offset:2048 sc1\n\t"
                    "buffer_load_dwordx4 v[140:143], %[zo], %[rw], %[zb] offen offset:3072 sc1\n\t"
                    "buffer_load_dwordx4 v[144:147], %[zo], %[rw], %[z4] offen sc1\n\t"
                    "buffer_load_dwordx4 v[148:151], %[zo], %[rw], %[z4] offen offset:1024 sc1\n\t"
                    "buffer_load_dwordx4 v[152:155], %[zo], %[rw], %[z4] offen offset:2048 sc1\n\t"
                    "buffer_load_dwordx4 v[156:159], %[zo], %[rw], %[z4] offen offset:3072 sc1\n\t"
                    "buffer_load_dwordx4 v[208:211], %[po], %[rw], %[pb] offen sc1\n\t"
                    "buffer_load_dwordx4 v[212:215], %[po], %[rw], %[pb] offen offset:1024 sc1\n\t"
                    "buffer_load_dwordx4 v[216:219], %[po], %[rw], %[pb] offen offset:2048 sc1\n\t"
                    "buffer_load_dwordx4 v[220:223], %[po], %[rw], %[pb] offen offset:3072 sc1\n\t"
                    "buffer_load_dwordx4 v[224:227], %[pm], %[rw], %[pb] offen sc1\n\t"
                    "buffer_load_dwordx4 v[228:231], %[pm], %[rw], %[pb] offen offset:1024 sc1\n\t"
                    "buffer_load_dwordx4 v[232:235], %[pm], %[rw], %[pb] offen offset:2048 sc1\n\t"
                    "buffer_load_dwordx4 v[236:239], %[pm], %[rw], %[pb] offen offset:3072 sc1\n\t"
                    "v_mov_b64 v[96:97], 0\n\t"
                    "v_mov_b64 v[98:99], 0\n\t"
                    "s_waitcnt vmcnt(0)\n\t"
                    "v_max3_u32 v101, v129, v131, v133\n\t"
                    "v_max3_u32 v101, v101, v135, v137\n\t"
                    "v_max3_u32 v101, v101, v139, v141\n\t"
                    "v_max3_u32 v101, v101, v143, v145\n\t"
                    "v_max3_u32 v101, v101, v147, v149\n\t"
                    "v_max3_u32 v101, v101, v151, v153\n\t"
                    "v_max3_u32 v101, v101, v155, v157\n\t"
                    "v_max_u32 v101, v101, v159\n\t"
                    "v_permlane16_swap_b32 v128, v129\n\t"
                    "v_permlane16_swap_b32 v130, v131\n\t"
                    "v_permlane16_swap_b32 v132, v133\n\t"
                    "v_permlane16_swap_b32 v134, v135\n\t"
                    "v_permlane16_swap_b32 v136, v137\n\t"
                    "v_permlane16_swap_b32 v138, v139\n\t"
                    "v_permlane16_swap_b32 v140, v141\n\t"
                    "v_permlane16_swap_b32 v142, v143\n\t"
                    "v_permlane16_swap_b32 v144, v145\n\t"
                    "v_permlane16_swap_b32 v146, v147\n\t"
                    "v_permlane16_swap_b32 v148, v149\n\t"
                    "v_permlane16_swap_b32 v150, v151\n\t"
                    "v_permlane16_swap_b32 v152, v153\n\t"
                    "v_permlane16_swap_b32 v154, v155\n\t"
                    "v_permlane16_swap_b32 v156, v157\n\t"
                    "v_permlane16_swap_b32 v158, v159\n\t"
                    "s_nop 1\n\t"
                    "v_smfmac_f32_16x16x64_bf16 v[96:99], v[208:211], v[160:167], %[ix]\n\t"
                    "v_smfmac_f32_16x16x64_bf16 v[96:99], v[212:215], v[168:175], %[ix]\n\t"
                    "v_smfmac_f32_16x16x64_bf16 v[96:99], v[216:219], v[176:183], %[ix]\n\t"
                    "v_smfmac_f32_16x16x64_bf16 v[96:99], v[220:223], v[184:191], %[ix]\n\t"
                    "v_smfmac_f32_16x16x64_bf16 v[96:99], v[224:227], v[128:135], %[ix]\n\t"
                    "v_smfmac_f32_16x16x64_bf16 v[96:99], v[228:231], v[136:143], %[ix]\n\t"
                    "v_smfmac_f32_16x16x64_bf16 v[96:99], v[232:235], v[144:151], %[ix]\n\t"
                    "v_smfmac_f32_16x16x64_bf16 v[96:99], v[236:239], v[152:159], %[ix]\n\t"
                    "s_nop 7\n\t"
                    "s_branch 6b\n\t"
                    "5:\n\t"
                    : [tr] "+&s"(tries), [po] "+&v"(po), [pm] "+&v"(pm), [x0] "+&s"(x0), [x1] "+&s"(x1), [bad] "=&s"(bad), [t0] "=&s"(t0_), [t1] "=&s"(t1_), [t2] "=&s"(t2_), [t3] "=&s"(t3_), [t4] "=&s"(t4_), "=&{v[160:167]}"(zf[0]), "=&{v[168:175]}"(zf[1]), "=&{v[176:183]}"(zf[2]), "=&{v[184:191]}"(zf[3])
                    : [zt] "v"(ztoff), [rs] "s"(rws), [so] "s"(zso), [s4] "s"(zso + 4096u), [zo] "v"(zoff), [sp] "v"(spa), [ix] "v"(sidx), [rw] "s"(rwin), [zb] "s"(zb), [z4] "s"(zb + 4096u), [pb] "s"(pb), [mo] "v"(moff), [fd] "n"(FDL),
                      [m0] "s"(mb), [m1] "s"(mb + PAIR), [m2] "s"(mb + mc2), [m3] "s"(mb + mc2 + PAIR)
                    : "v96", "v97", "v98", "v99", "v100", "v101", "v102", "v103", "v104", "v105", "v106", "v107", "v128", "v129", "v130", "v131", "v132", "v133", "v134", "v135", "v136", "v137", "v138", "v139", "v140", "v141", "v142", "v143", "v144", "v145", "v146", "v147", "v148", "v149", "v150", "v151", "v152", "v153", "v154", "v155", "v156", "v157", "v158", "v159", "v208", "v209", "v210", "v211", "v212", "v213", "v214", "v215", "v216", "v217", "v218", "v219", "v220", "v221", "v222", "v223", "v224", "v225", "v226", "v227", "v228", "v229", "v230", "v231", "v232", "v233", "v234", "v235", "v236", "v237", "v238", "v239", "vcc", "scc", "memory");
                budget = tries;
                if (w == 3) *(volatile LAS int*)(smem + 4100) = s;
                __builtin_amdgcn_s_sleep(DPS - 4);
                if (w == 3) {
                    st128(sent4(), rwin, ra, W_MEM + (unsigned)(s & 3) * 6u * PAIR, 16);
                    if (lane == 0) __builtin_amdgcn_raw_buffer_store_b64((u32x2){(unsigned)s - 1u, (unsigned)max(zr, s)}, rwin, 0u, W_GO + fb + (unsigned)o * 128u, 16);
                }
                sent(s);
                zb = zb == W_ZM + 2u * NOWN * PAIR ? W_ZM : zb + NOWN * PAIR;
            }
        } else if (w == 4) {
            const unsigned fo = (unsigned)lane * 128u + (unsigned)(o / 10) * 8192u;
            int zk = -1;
            if (lane < 3) *(volatile LAS int*)(smem + 4096 + lane * 4) = -1;
            for (int s = 0; s + 1 < NSEG; ++s) {
                asm volatile("s_waitcnt lgkmcnt(0)\n\ts_barrier\n\ts_barrier" ::: "memory");
                int f = 0;
                const bool look = zk < min(s + ZLA, NSEG - 1);
                if (look) { __builtin_amdgcn_s_sleep(6); f = (int)__builtin_amdgcn_raw_buffer_load_b32(rwin, fo, W_FZ + fb, 16); }
                SB();
                BAR0();
                if (look) for (int again = 0;; ++again) {
                    f = min(f, __builtin_amdgcn_update_dpp(f, f, 0xB1, 0xF, 0xF, false)); f = min(f, __builtin_amdgcn_update_dpp(f, f, 0x4E, 0xF, 0xF, false));
                    f = min(f, __builtin_amdgcn_update_dpp(f, f, 0x141, 0xF, 0xF, false)); f = min(f, __builtin_amdgcn_update_dpp(f, f, 0x140, 0xF, 0xF, false));
                    zk = seg_done(MXV, min(__builtin_amdgcn_readlane(f, 0), __builtin_amdgcn_readlane(f, 16)), min(__builtin_amdgcn_readlane(f, 32), __builtin_amdgcn_readlane(f, 48)));
                    *(volatile LAS int*)(smem + 4096) = zk;
                    if (zk > s || again) break;
                    __builtin_amdgcn_s_sleep(2);
                    f = (int)__builtin_amdgcn_raw_buffer_load_b32(rwin, fo, W_FZ + fb, 16);
                }
                *(volatile LAS int*)(smem + 4104) = s;
            }
        } else if (w < 7) {
            for (int s = 0; s < 3 * NSEG - 3; ++s) BAR0();
        } else {
            Tk tk = tk_init();
            __builtin_amdgcn_s_setprio(3);
            for (int s = -1; s + 1 < NSEG; ++s) {
                if (s >= 0) {
                    const unsigned ta0 = ck.ta, mm0 = ck.ma, tp = s ? tpp + lt + xs + M1 : tpp - M2 - XS0 - (lay ? PEN0 : PEN00);
                    wait_fine_bar(tp - DZP, ck);
                    wait_cyc_bar(cyc_at(ck, sx24(tp - ck.ta)));
                    clk_rate(ck, ta0, mm0);
                    asm volatile("s_barrier ; 7" ::: "memory");
                }
                if (s) sched(tk, max(s, 0));
                if (s < 0) continue;
                RTS();
            }
            __builtin_amdgcn_s_setprio(0);
            if (o < 2) due(E_OUT + o, (sx24(tpp + ML - 4u * (unsigned)__builtin_amdgcn_s_memrealtime()) >> 2) + DOUT);
        }
        {
            const int e = w >> 2;
            const unsigned ooff = (unsigned)((S_ - 2 * SEG + 8 * w + (lane >> 3)) * D_ + o * 16 + (lane & 7) * 2) * 2u;
            unsigned pl = W_PL + fb + e * 5u * PAIR, zs = W_ZM + (NSEG - 2 + e + lay * NSEG) % 3 * (NOWN * PAIR);
            asm volatile("" : "+s"(pl), "+s"(zs));
            if (e) {
                if (w < 7) __builtin_amdgcn_s_sleep(DPS);
                sent(NSEG - 2);
                zt_wait(NSEG - 1);
                const unsigned zl = O_Z + (unsigned)((NSEG - 1) * ZTSEG) * 2u;
#pragma unroll
                for (int q = 0; q < 4; ++q) zf[q] = cat8(__builtin_amdgcn_raw_buffer_load_b128(rws, ztoff + q * 2048u, zl, 16), __builtin_amdgcn_raw_buffer_load_b128(rws, ztoff + q * 2048u + 1024u, zl, 16));
            }
            if (DR0 && !e) __builtin_amdgcn_s_sleep(DR0);
            const unsigned xr = __builtin_amdgcn_raw_buffer_load_b32(rws, ooff, O_X1, 16);
            SB();
            if (w == 7) wait_fine_bar(tpp + ML, ck); else if (e) BAR0();
            f32x4 c[2];
            for (unsigned spins = 0;;) {
                asm volatile("" ::: "memory");
                u32x4 pt[16], ptm[2][2], zm[2];
#pragma unroll
                for (int q = 0; q < 16; ++q) pt[q] = __builtin_amdgcn_raw_buffer_load_b128(rwin, (unsigned)(16 * hg) * 1024u + lane * 16u + (q & 3) * 1024u, pl + (q >> 2) * 4096u, 16);
#pragma unroll
                for (int q = 0; q < 2; ++q) zm[q] = __builtin_amdgcn_raw_buffer_load_b128(rwin, zoff + hg * 2048u + q * 1024u, zs, 16);
#pragma unroll
                for (int q = 0; q < 4; ++q) ptm[q >> 1][q & 1] = __builtin_amdgcn_raw_buffer_load_b128(rwin, (unsigned)(hg * 2048 + lane * 16) + (q & 1) * 1024u, pl + 65536u + (q >> 1) * 8192u, 16);
                SB();
                if (!e && !spins) BAR0();
                unsigned m = max(smax(zm[0]), smax(zm[1]));
                #pragma unroll
                for (int q = 0; q < 16; ++q) { c[q & 1] = mfma32(half8(zf[q >> 2], q >> 1 & 1), pt[q], q < 2 ? z4 : c[q & 1]); m = max(m, smax(pt[q])); }
#pragma unroll
                for (int q = 0; q < 4; ++q) { c[q >> 1] = mfma32((u32x4){zm[q & 1][0], zm[q & 1][2], zm[q & 1][1], zm[q & 1][3]}, ptm[q >> 1][q & 1], c[q >> 1]); m = max(m, smax(ptm[q >> 1][q & 1])); }
                if (!__any(m == 0xFFFFFFFFu)) break;
                if (spin_fail(spins, rws)) break;
            }
#pragma unroll
            for (int t = 0; t < 2; ++t)
#pragma unroll
                for (int r = 0; r < 4; ++r) *(float*)(op + w * 2048 + (16 * t + fr) * 64 + pi16(4 * fq + r) * 4) = c[t][r];
            BAR();
            const char* src = op + e * 8192 + hg * 512 + lane * 8;
            const f32x2 y = (*(const f32x2*)src + *(const f32x2*)(src + 2048)) + (*(const f32x2*)(src + 4096) + *(const f32x2*)(src + 6144));
            __builtin_amdgcn_raw_buffer_store_b32(nosent(pack2(lo2f(xr) + bfr(y[0]), hi2f(xr) + bfr(y[1]))), rout, ooff, 0u, 16);
            __builtin_amdgcn_s_sleep(4);
            const u32x4 sv = sent4();
            st128(sv, rwin, zoff + hg * 2048u, zs, 16); st128(sv, rwin, zoff + hg * 2048u + 1024u, zs, 16);
            if (w == 3) st128(sv, rwin, ra, W_MEM + 18u * PAIR, 16);
            if (lay && o == 0 && w == 0) __builtin_amdgcn_raw_buffer_store_b64(get_tag(), rws, 0u, 8u, 16);
        }
        SST(23);
    } else if (bid < NREC + 30) {
        LANES();
        const int cg = (bid - NREC) % 10;
        const unsigned ztoff = (unsigned)((4 * cg * H_ + 4 * w) * 64 + lane) * 16u;
        const unsigned ptoff = (unsigned)(8 * w * 64 + lane) * 16u, ptmoff = 65536u + (unsigned)(4 * w * 16 + lane) * 16u;
        const int tk = (w & 3) * 8 + (lane >> 3);
        const unsigned ooff = (unsigned)(tk * D_ + cg * 64 + (lane & 7) * 8) * 2u;
        const unsigned gooff = W_GO + (unsigned)((4 * cg + (lane & 2 ? 3 : 0)) * 128 + (lane & 1) * 4);
        int rdy = -1, go = -1;
        auto ask = [&]() {
            asm volatile("" ::: "memory");
            return (int)__builtin_amdgcn_raw_buffer_load_b32(rwin, gooff, fb, 16);
        };
        auto look = [&](int f) {
            go = min(__builtin_amdgcn_readlane(f, 33), __builtin_amdgcn_readlane(f, 35));
            rdy = max(rdy, max(__builtin_amdgcn_readlane(f, 32), __builtin_amdgcn_readlane(f, 34)));
        };
        auto wait_rdy = [&](int& x, int v, int s) {
            for (unsigned spins = 0; x < v;) {
                look(ask());
                if (x >= v) break;
                if (spin_fail(spins, rws)) break;
                if (s >= NSEG - 5) __builtin_amdgcn_s_sleep(OSLP2); else __builtin_amdgcn_s_sleep(OSLP);
            }
        };
        if (w >= 4) wait_h(rws, rwin, fb, 3, hrdy);
        u32x4 zf[4][4];
        auto load_z = [&](int s) {
#pragma unroll
            for (int q = 0; q < 16; ++q) zf[q >> 2][q & 3] = __builtin_amdgcn_raw_buffer_load_b128(rws, ztoff + (q & 3) * 1024u, O_Z + (unsigned)(s * ZTSEG) * 2u + (q >> 2) * (H_ * 1024u), 16);
            SB();
        };
        wait_rdy(go, (bid - NREC) / 10, 0);
        load_z((bid - NREC) / 10);
        for (int s = (bid - NREC) / 10;; s += 3) {
            u32x4 xr, zx[4], pt[8], ptm[2];
            asm volatile("" : "=v"(xr));
            RT(0);
            wait_rdy(rdy, s, s);
            RT(1);
            if (s >= NSEG - 8 && cg == 0 && w == 0) due(E_OUT + NSEG - 1 - s, s + 3 < NSEG ? DOU3 : DOU2);
            const unsigned ptb = O_PT + (unsigned)(s * PTSEG) * 2u;
#pragma unroll
            for (int q = 0; q < 8; ++q) pt[q] = __builtin_amdgcn_raw_buffer_load_b128(rws, ptoff + q * 1024u, ptb, 16);
#pragma unroll
            for (int q = 0; q < 2; ++q) ptm[q] = __builtin_amdgcn_raw_buffer_load_b128(rws, ptmoff + q * 8192u, ptb, 16);
#pragma unroll
            for (int q = 0; q < 4; ++q) zx[q] = __builtin_amdgcn_raw_buffer_load_b128(rws, tid * 16u, O_Z2 + (unsigned)(s * NOWN + 4 * cg + q) * 8192u, 16);
            if (w >= 4) xr = __builtin_amdgcn_raw_buffer_load_b128(rws, ooff, O_X1 + (unsigned)(s * SEG * D_ * 2), 16);
            const int f = ask();
            SB();
            char* op = smem + (s & 1) * 65536;
            f32x4 c[8];
#pragma unroll
            for (int q = 0; q < 4; ++q)
#pragma unroll
                for (int u = 0; u < 8; ++u) c[u] = mfma32(zf[u >> 1][q], pt[2 * q + (u & 1)], q ? c[u] : z4);
#pragma unroll
            for (int u = 0; u < 8; ++u) {
                const int tq = u & 1, t = u >> 1;
                *(f32x4*)(op + w * 8192 + (16 * tq + fr) * 256 + (((t * 4 + fq) ^ (fr & 7)) << 4)) = mfma32((u32x4){zx[t][0], zx[t][2], zx[t][1], zx[t][3]}, ptm[tq], c[u]);
            }
            look(f);
            const bool pre = go >= s + 3 && s < NSEG - 14;
            if (pre) load_z(s + 3);
            BAR();
            if (lay == 0 && s >= 3 && s < NSEG - 11 && tid == 0) __builtin_amdgcn_raw_buffer_store_b32(0u, rwin, 0u, W_OUT + (unsigned)((s - 3) * 128 + cg * 4), 16);
            if (w >= 4) {
                f32x2 x[4];
#pragma unroll
                for (int q = 0; q < 4; ++q) {
                    const char* sp = op + tk * 256 + ((((lane & 6) * 2 + q) ^ (tk & 7)) << 4) + (lane & 1) * 8;
                    x[q] = *(const f32x2*)sp;
#pragma unroll
                    for (int ww = 1; ww < 8; ++ww) x[q] += *(const f32x2*)(sp + ww * 8192);
                }
                const u32x4 r = {pack2(lo2f(xr[0]) + bfr(x[0][0]), hi2f(xr[0]) + bfr(x[2][0])), pack2(lo2f(xr[1]) + bfr(x[1][0]), hi2f(xr[1]) + bfr(x[3][0])),
                                 pack2(lo2f(xr[2]) + bfr(x[0][1]), hi2f(xr[2]) + bfr(x[2][1])), pack2(lo2f(xr[3]) + bfr(x[1][1]), hi2f(xr[3]) + bfr(x[3][1]))};
                st128(((u32x4){nosent(r[0]), nosent(r[1]), nosent(r[2]), nosent(r[3])}), rout, ooff, (unsigned)(s * SEG * D_ * 2), 16);
            } else if (!pre) __builtin_amdgcn_s_sleep(ODL);
            if (lay == 0 && s >= NSEG - 14 && s < NSEG - 8) {
                VMWAIT();
                BAR0();
                if (tid == 0) __builtin_amdgcn_raw_buffer_store_b32(0u, rwin, 0u, W_OUT + (unsigned)(s * 128 + cg * 4), 16);
            }
            RT(2);
            RTS();
            if (s < 8) SST(8 + s);
            if (s >= 56) SST(s - 40);
            if (s + 3 >= NSEG - 2) break;
            if (!pre) { wait_rdy(go, s + 3, 0); load_z(s + 3); }
        }
    } else if (lay == 0 && bid < NWRK) {
        if (w == 0) for (unsigned spins = 0; (int)__builtin_amdgcn_raw_buffer_load_b32(rwin, 0u, W_E + (E_OUT + 3) * 128u, 16) == -1 && !spin_fail(spins, rws);) {
            __builtin_amdgcn_s_sleep(64);
            asm volatile("" ::: "memory");
        }
    } else if (lay == 0) {
        LANES();
        const RowTw tw = row_tw(lane);
        for (int u = (256 - bid) & 15; u < 256; u += 16) {
            if (w == 0) {
                if (u >= 224) await(E_OUT + (255 - u >> 2), 1, 0);
                else for (unsigned spins = 0;;) {
                    asm volatile("" ::: "memory");
                    const unsigned f = __builtin_amdgcn_raw_buffer_load_b32(rwin, (unsigned)min(lane, 9) * 4u, W_OUT + (unsigned)(u >> 2) * 128u, 16);
                    if (!__any(f != 0u)) break;
                    if (spin_fail(spins, rws)) break;
                    __builtin_amdgcn_s_sleep(WSLP);
                }
            }
            BAR0();
            SST(24);
            const unsigned fo = W_FA + (unsigned)((u >> 2) * 128 + (u & 3) * 4);
            a1_unit<4, 512>(rout, u * 8, u >= 224, rws, smem, tw, w, lane, [&](int k) {
                if (k == 0 && w == 0 && u >= 224 && !(u & 3)) due(E_A1 + (u - 224 >> 2), DA1);
                if (k >= 0) SST(8 + k);
            });
            SST(25);
            if (u >= 224) continue;
            VMWAIT();
            BAR0();
            if (tid == 0) __builtin_amdgcn_raw_buffer_store_b32(0u, rwin, 0u, fo, 16);
        }
    }
    if (lay == 0 && w == 0 && bid >= NREC && bid <= NWRK) await(E_OUT + 1, 1, bid < NREC + 32 ? -DOW : -60);
    SST(31);
    }
}

constexpr int L_DBG = 0;

extern "C" void kernel_launch(void* const* d_in, const int* in_sizes, int n_in, void* d_out, int out_size, void* d_ws, size_t ws_size,
                              hipStream_t stream) {
    char* ws = (char*)d_ws;
    hipFuncSetAttribute((const void*)b3_kernel, hipFuncAttributeMaxDynamicSharedMemorySize, L_END + L_DBG);
    char* win = (char*)(((uintptr_t)(ws + OFF_WIN) + 0x1FFFFFu) & ~(uintptr_t)0x1FFFFFu);
    RecArgs ra{(unsigned*)ws, win, (const u16*)d_in[4], (const u16*)d_in[0], (u16*)d_out, {(const u16*)d_in[5], (const u16*)d_in[6], (const u16*)d_in[7], (const u16*)d_in[8], (const u16*)d_in[1], (const u16*)d_in[2]}};
    b3_kernel<<<256, 512, L_END + L_DBG, stream>>>(ra);
}
```

```cpp
#define TD
#define ENC 7
#include <hip/hip_runtime.h>
#include <hip/hip_bf16.h>
#include <stdint.h>

typedef unsigned short u16;
typedef __attribute__((ext_vector_type(8))) __bf16 bf16x8;
typedef __attribute__((ext_vector_type(4))) short s16x4;
typedef __attribute__((ext_vector_type(4))) float f32x4;
typedef __attribute__((ext_vector_type(8))) float f32x8;
typedef __attribute__((ext_vector_type(16))) float f32x16;
typedef __attribute__((ext_vector_type(4))) unsigned u32x4;
typedef __attribute__((ext_vector_type(2))) unsigned u32x2;
typedef __attribute__((ext_vector_type(8))) unsigned u32x8;
typedef __attribute__((ext_vector_type(16))) __bf16 bf16x16;
typedef __attribute__((ext_vector_type(2))) unsigned short u16x2;
#define LAS __attribute__((address_space(3)))
template <int N> struct IC { static constexpr int v = N; };

constexpr int S_ = 2048, D_ = 640, DF = 321, H_ = 32, DH = 128, HD = 4096, NM = 8, SEG = 32, NSEG = 64;
constexpr int NQKV = 3 * HD;
constexpr int NOWN = 40;
constexpr int ZTR = NSEG;
constexpr size_t ZTSEG = (size_t)NOWN * H_ * 512;
constexpr unsigned PAIR = 16384, HALF = 8192;
constexpr unsigned W_MEM = 0, W_P = W_MEM + 24 * PAIR, W_T = W_P + 40 * PAIR, W_GO = W_T + 8 * PAIR, W_OUT = W_GO + PAIR, W_FQ = W_OUT + 8192, W_FZ = W_OUT + PAIR,
                   W_H = W_FZ + 2 * PAIR, W_FA = W_H + 8192, W_FB = W_GO + 8192, W_E = W_FB + 4096, W_PL = W_H + PAIR, W_FL = W_PL + 10 * PAIR - W_GO, W_PROBE = W_GO + 2 * W_FL,
                   W_ZM = 2u * 1024 * 1024, W_END = W_ZM + 120 * PAIR;
constexpr unsigned W_ST = W_PROBE + 2 * PAIR, W_ROT = W_ST + 8192, W_ARM = W_ROT + 128, W_CFG = W_ARM + 128;
static_assert(W_ROT + PAIR <= 2u * 1024 * 1024 && W_PROBE % (2 * PAIR) == 0, "window");
constexpr int PTSEG = 32768 + 8192;

#define WAUX 0
#define FPAD 512
constexpr int GF = 32768 + FPAD, YS = 12288 + FPAD;
static_assert(FPAD >= 16, "factors");
constexpr int NG = 81;
constexpr size_t al(size_t x) { return (x + 255) & ~(size_t)255; }
constexpr size_t OFF_CTL  = 0;
constexpr size_t OFF_WFT  = 4096 + 4 * 1024 * 1024;
constexpr size_t OFF_F    = OFF_WFT + al((size_t)4 * NG * S_ * 4);
constexpr size_t OFF_X1   = OFF_F + (size_t)NG * GF;
constexpr size_t OFF_X2   = OFF_X1 + al((size_t)S_ * D_ * 2);
constexpr size_t OFF_Y0   = OFF_X2 + al((size_t)S_ * D_ * 2);
constexpr size_t OFF_H    = OFF_Y0 + (size_t)NG * YS;
constexpr size_t OFF_ARM  = OFF_Y0 - 256 * D_ * 2, ARM_END = OFF_H + 8 * 40960;
constexpr size_t OFF_Z    = OFF_H + (size_t)(NSEG + 4) * 40960;
constexpr size_t OFF_Z2   = OFF_Z + al(ZTSEG * ZTR * 2);
constexpr size_t OFF_PT   = OFF_Z2 + al((size_t)NSEG * NOWN * 8192);
constexpr size_t OFF_QK   = OFF_PT + al((size_t)NSEG * PTSEG * 2);
constexpr size_t OFF_WIN  = OFF_QK + (size_t)NSEG * H_ * 16384;
constexpr size_t OFF_END  = OFF_WIN + (size_t)6 * 1024 * 1024;
static_assert(OFF_END <= (size_t)256 * 1024 * 1024, "workspace");

#define st128(v, r, vo, so, aux) do { const u32x4 d_ = (v); __builtin_amdgcn_raw_buffer_store_b128(d_, r, vo, so, aux); asm volatile("s_nop 1" :: "v"(d_)); } while (0)
__device__ __forceinline__ float bf2f(u16 v) { return __uint_as_float((unsigned)v << 16); }
__device__ __forceinline__ u16 f2bf(float f) { __bf16 b = (__bf16)f; return __builtin_bit_cast(u16, b); }
__device__ __forceinline__ float bfr(float f) { return (float)(__bf16)f; }
typedef __attribute__((ext_vector_type(2))) float f32x2;
typedef __attribute__((ext_vector_type(2))) __bf16 bf16x2;
__device__ __forceinline__ unsigned pack2(float a, float b) { return __builtin_bit_cast(unsigned, __builtin_convertvector((f32x2){a, b}, bf16x2)); }
typedef __attribute__((ext_vector_type(2))) _Float16 f16x2;
typedef __attribute__((ext_vector_type(8))) _Float16 f16x8;
__device__ __forceinline__ unsigned packh(float a, float b) { return __builtin_bit_cast(unsigned, __builtin_convertvector((f32x2){a, b}, f16x2)); }
__device__ __forceinline__ f32x4 mfmah(u32x4 a, u32x4 b) { return __builtin_amdgcn_mfma_f32_16x16x32_f16(__builtin_bit_cast(f16x8, a), __builtin_bit_cast(f16x8, b), (f32x4){0.f, 0.f, 0.f, 0.f}, 0, 0, 0); }
__device__ __forceinline__ float lo2f(unsigned v) { return __uint_as_float(v << 16); }
__device__ __forceinline__ float hi2f(unsigned v) { return __uint_as_float(v & 0xFFFF0000u); }
__device__ __forceinline__ float fmx(float a, float b) { return __builtin_elementwise_maximum(a, b); }
__device__ __forceinline__ f32x4 mfma32(u32x4 a, u32x4 b, f32x4 c) {
    return __builtin_amdgcn_mfma_f32_16x16x32_bf16(__builtin_bit_cast(bf16x8, a), __builtin_bit_cast(bf16x8, b), c, 0, 0, 0);
}
__device__ __forceinline__ f32x4 mfma16(u32x2 a, u32x2 b, f32x4 c) {
    return __builtin_amdgcn_mfma_f32_16x16x16bf16_1k(__builtin_bit_cast(s16x4, a), __builtin_bit_cast(s16x4, b), c, 0, 0, 0);
}
__device__ __forceinline__ f32x4 smfmac(u32x4 a, u32x8 b, f32x4 c, int idx) {
    return __builtin_amdgcn_smfmac_f32_16x16x64_bf16(__builtin_bit_cast(bf16x8, a), __builtin_bit_cast(bf16x16, b), c, idx, 0, 0);
}
__device__ __forceinline__ int pi16(int k) { return ((k & 3) << 2) | ((k >> 1) & 2) | (k >> 3); }
__device__ __forceinline__ float addswap32(float a, float b) {
    auto r = __builtin_amdgcn_permlane32_swap(__float_as_uint(a), __float_as_uint(b), false, false);
    return __uint_as_float(r[0]) + __uint_as_float(r[1]);
}
__device__ __forceinline__ float xmax3216(float v) {
    auto r = __builtin_amdgcn_permlane32_swap(__float_as_uint(v), __float_as_uint(v), false, false);
    v = fmx(__uint_as_float(r[0]), __uint_as_float(r[1]));
    auto q = __builtin_amdgcn_permlane16_swap(__float_as_uint(v), __float_as_uint(v), false, false);
    return fmx(__uint_as_float(q[0]), __uint_as_float(q[1]));
}
__device__ __forceinline__ float xsum3216(float v) {
    auto r = __builtin_amdgcn_permlane32_swap(__float_as_uint(v), __float_as_uint(v), false, false);
    v = __uint_as_float(r[0]) + __uint_as_float(r[1]);
    auto q = __builtin_amdgcn_permlane16_swap(__float_as_uint(v), __float_as_uint(v), false, false);
    return __uint_as_float(q[0]) + __uint_as_float(q[1]);
}
template <int CTRL> __device__ __forceinline__ float dppf(float v) { return __uint_as_float((unsigned)__builtin_amdgcn_update_dpp(0, (int)__float_as_uint(v), CTRL, 0xF, 0xF, false)); }
__device__ __forceinline__ float wsum(float v) {
    v += dppf<0xB1>(v); v += dppf<0x4E>(v); v += dppf<0x141>(v); v += dppf<0x140>(v);
    return xsum3216(v);
}
__device__ __forceinline__ unsigned ror8(unsigned v) { return (unsigned)__builtin_amdgcn_update_dpp(0, (int)v, 0x128, 0xF, 0xF, false); }
__device__ __forceinline__ float ror8f(float v) { return __uint_as_float(ror8(__float_as_uint(v))); }
__device__ __forceinline__ unsigned nosent(unsigned v) {
    u16x2 lim = {0xFFFE, 0xFFFE};
    return __builtin_bit_cast(unsigned, __builtin_elementwise_min(__builtin_bit_cast(u16x2, v), lim));
}
__device__ __forceinline__ unsigned pkmax(unsigned a, unsigned b) {
    return __builtin_bit_cast(unsigned, __builtin_elementwise_max(__builtin_bit_cast(u16x2, a), __builtin_bit_cast(u16x2, b)));
}
__device__ __forceinline__ bool has_sent(u32x4 v) {
    unsigned m = pkmax(pkmax(v.x, v.y), pkmax(v.z, v.w));
    return (m & 0xFFFFu) == 0xFFFFu || (m >> 16) == 0xFFFFu;
}

typedef f32x2 c32;
__device__ __forceinline__ c32 fma2(c32 a, c32 b, c32 c) { return __builtin_elementwise_fma(a, b, c); }
__device__ __forceinline__ c32 dup(float v) { return (c32){v, v}; }
template <int SG> __device__ __forceinline__ c32 addi(c32 a, c32 b) {
    c32 d;
    if (SG > 0) asm("v_pk_add_f32 %0, %1, %2 op_sel:[0,1] op_sel_hi:[1,0] neg_lo:[0,1]" : "=v"(d) : "v"(a), "v"(b));
    else asm("v_pk_add_f32 %0, %1, %2 op_sel:[0,1] op_sel_hi:[1,0] neg_hi:[0,1]" : "=v"(d) : "v"(a), "v"(b));
    return d;
}
template <int SG> __device__ __forceinline__ c32 ctw(c32 a, c32 w) {
    c32 d = a * w.xx;
    if (SG < 0) asm("v_pk_fma_f32 %0, %1, %2, %0 op_sel:[1,1,0] op_sel_hi:[0,1,1] neg_lo:[0,1,0]" : "+v"(d) : "v"(a), "v"(w));
    else asm("v_pk_fma_f32 %0, %1, %2, %0 op_sel:[1,1,0] op_sel_hi:[0,1,1] neg_hi:[0,1,0]" : "+v"(d) : "v"(a), "v"(w));
    return d;
}
__device__ __forceinline__ c32 cis(float rev) { return (c32){__builtin_amdgcn_cosf(rev), __builtin_amdgcn_sinf(rev)}; }
template <int N> __device__ __forceinline__ void cpow(c32 (&t)[N], float rev) {
    t[0] = cis(rev);
    asm volatile("s_nop 0" : "+v"(t[0]));
#pragma unroll
    for (int i = 1; i < N; ++i) { t[i] = ctw<-1>(t[(i - 1) / 2], t[i / 2]); asm volatile("" : "+v"(t[i])); }
}
#define SB() __builtin_amdgcn_sched_barrier(0)
#define SA() do { SB(); asm volatile(".p2align 3"); SB(); } while (0)
template <int SG, int I0, int N, class F> __device__ __forceinline__ void ctwn(c32 (&u)[N], F w) {
    c32 d[N];
    SA();
#pragma unroll
    for (int i = I0; i < N; ++i) asm("v_pk_mul_f32 %0, %1, %2 op_sel_hi:[1,0]" : "=v"(d[i]) : "v"(u[i]), "v"(w(i)));
    SB();
#pragma unroll
    for (int i = I0; i < N; ++i) {
        if (SG < 0) asm("v_pk_fma_f32 %0, %1, %2, %0 op_sel:[1,1,0] op_sel_hi:[0,1,1] neg_lo:[0,1,0]" : "+v"(d[i]) : "v"(u[i]), "v"(w(i)));
        else asm("v_pk_fma_f32 %0, %1, %2, %0 op_sel:[1,1,0] op_sel_hi:[0,1,1] neg_hi:[0,1,0]" : "+v"(d[i]) : "v"(u[i]), "v"(w(i)));
        u[i] = d[i];
    }
    SB();
}
template <int SG, int M, int A, int B, int CI, int N> __device__ __forceinline__ void dft4n(c32 (&u)[N]) {
    c32 t[M][4];
    SA();
#pragma unroll
    for (int i = 0; i < M; ++i) {
        const c32 a = u[A * i], b = u[A * i + B], c = u[A * i + 2 * B], d = u[A * i + 3 * B];
        t[i][0] = i == CI ? addi<SG>(a, c) : a + c; t[i][1] = i == CI ? addi<-SG>(a, c) : a - c; t[i][2] = b + d; t[i][3] = b - d;
        if (B == 4) SB();
    }
    SB();
#pragma unroll
    for (int i = 0; i < M; ++i) { u[A * i] = t[i][0] + t[i][2]; u[A * i + 2 * B] = t[i][0] - t[i][2]; u[A * i + B] = addi<SG>(t[i][1], t[i][3]); u[A * i + 3 * B] = addi<-SG>(t[i][1], t[i][3]); }
    SB();
}
template <int SG> __device__ __forceinline__ void dft8x2(c32 (&u)[16]) {
    dft4n<SG, 4, 1, 4, -1>(u);
    const float r = 0.70710678118654752440f;
    c32 o[16];
#pragma unroll
    for (int e = 0; e < 2; ++e) { u[6 + e] = addi<SG>(u[6 + e], u[6 + e]); u[14 + e] = addi<-SG>(u[14 + e], u[14 + e]); }
#pragma unroll
    for (int e = 0; e < 2; ++e) { o[e] = u[e] + u[2 + e]; o[8 + e] = u[e] - u[2 + e]; o[4 + e] = addi<SG>(u[8 + e], u[10 + e]); o[12 + e] = addi<-SG>(u[8 + e], u[10 + e]); }
    SB();
#pragma unroll
    for (int e = 0; e < 2; ++e) { o[2 + e] = fma2(u[6 + e], dup(r), u[4 + e]); o[10 + e] = fma2(u[6 + e], dup(-r), u[4 + e]); o[6 + e] = fma2(u[14 + e], dup(-r), u[12 + e]); o[14 + e] = fma2(u[14 + e], dup(r), u[12 + e]); }
    SB();
#pragma unroll
    for (int i = 0; i < 16; ++i) u[i] = o[i];
}
template <int SG> __device__ __forceinline__ void dft16(c32 (&u)[16]) {
    const float c1 = 0.92387953251128675613f, s1 = 0.38268343236508977173f, r = 0.70710678118654752440f;
    dft4n<SG, 4, 1, 4, -1>(u);
    const int ix[8] = {5, 9, 13, 6, 14, 7, 11, 15};
    const float wx[8] = {c1, r, s1, r, -r, s1, -r, -c1}, wy[8] = {s1, r, c1, r, r, c1, r, -s1};
    c32 d[8];
#pragma unroll
    for (int i = 0; i < 8; ++i) d[i] = u[ix[i]] * dup(wx[i]);
    SB();
#pragma unroll
    for (int i = 0; i < 8; ++i) u[ix[i]] = fma2(u[ix[i]].yx, (c32){-SG * wy[i], SG * wy[i]}, d[i]);
    SB();
    dft4n<SG, 4, 4, 1, 2>(u);
    c32 o[16];
#pragma unroll
    for (int i = 0; i < 16; ++i) o[(i >> 2) + 4 * (i & 3)] = u[i];
#pragma unroll
    for (int i = 0; i < 16; ++i) u[i] = o[i];
}
template <int SG> __device__ __forceinline__ void dft10(c32 (&u)[10]) {
    const float c1 = 0.30901699437494742410f, c2 = -0.80901699437494742410f, s1 = 0.95105651629515357212f, s2 = 0.58778525229247312917f;
    c32 x[2][5], t[2][4], a[2][5];
    SA();
#pragma unroll
    for (int i = 0; i < 5; ++i) { x[0][i] = u[2 * i] + u[(2 * i + 5) % 10]; x[1][i] = u[2 * i] - u[(2 * i + 5) % 10]; }
    SB();
#pragma unroll
    for (int e = 0; e < 2; ++e) { t[e][0] = x[e][1] + x[e][4]; t[e][1] = x[e][2] + x[e][3]; t[e][2] = x[e][1] - x[e][4]; t[e][3] = x[e][2] - x[e][3]; }
    SB();
#pragma unroll
    for (int e = 0; e < 2; ++e) { a[e][0] = t[e][0] + t[e][1]; a[e][1] = fma2(t[e][0], dup(c1), x[e][0]); a[e][2] = fma2(t[e][0], dup(c2), x[e][0]); a[e][3] = t[e][2] * dup(s1); a[e][4] = t[e][2] * dup(s2); }
    SB();
#pragma unroll
    for (int e = 0; e < 2; ++e) { a[e][0] += x[e][0]; a[e][1] = fma2(t[e][1], dup(c2), a[e][1]); a[e][2] = fma2(t[e][1], dup(c1), a[e][2]); a[e][3] = fma2(t[e][3], dup(s2), a[e][3]); a[e][4] = fma2(t[e][3], dup(-s1), a[e][4]); }
    SB();
#pragma unroll
    for (int e = 0; e < 2; ++e) {
        u[5 * e] = a[e][0];
        u[(6 + 5 * e) % 10] = addi<SG>(a[e][1], a[e][3]); u[(24 + 5 * e) % 10] = addi<-SG>(a[e][1], a[e][3]);
        u[(12 + 5 * e) % 10] = addi<SG>(a[e][2], a[e][4]); u[(18 + 5 * e) % 10] = addi<-SG>(a[e][2], a[e][4]);
    }
    SB();
}
template <int ST> __device__ __forceinline__ void lds16(c32 (&u)[16], const char* p) {
    const LAS char* q = (const LAS char*)p;
#pragma unroll
    for (int i = 0; i < 16; ++i) asm volatile("ds_read_b64 %0, %1 offset:%2" : "=v"(u[(i >> 2) + 4 * (i & 3)]) : "v"(q), "n"(((i >> 2) + 4 * (i & 3)) * ST) : "memory");
#pragma unroll
    for (int i = 0; i < 4; ++i) asm volatile("s_waitcnt lgkmcnt(%4)" : "+v"(u[i]), "+v"(u[i + 4]), "+v"(u[i + 8]), "+v"(u[i + 12]) : "n"(12 - 4 * i) : "memory");
}
__device__ __forceinline__ c32 unpack2(unsigned v) { return (c32){lo2f(v), hi2f(v)}; }
__device__ __forceinline__ float wsum2(c32 v) {
    float t = addswap32(v.x, v.y);
    auto q = __builtin_amdgcn_permlane16_swap(__float_as_uint(t), __float_as_uint(t), false, false);
    t = __uint_as_float(q[0]) + __uint_as_float(q[1]);
    t += dppf<0xB1>(t); t += dppf<0x4E>(t); t += dppf<0x141>(t); t += dppf<0x140>(t);
    return t;
}
__device__ __forceinline__ c32 halves(float t) { return (c32){__uint_as_float(__builtin_amdgcn_readlane(__float_as_uint(t), 0)), __uint_as_float(__builtin_amdgcn_readlane(__float_as_uint(t), 32))}; }
__device__ __forceinline__ void ln_rows(const c32 (&v)[10], unsigned (&o)[10]) {
    const float mean = wsum2(((v[0] + v[1]) + (v[2] + v[3])) + ((v[4] + v[5]) + (v[6] + v[7])) + (v[8] + v[9])) * (1.f / 640.f);
    const c32 m = halves(mean), mb = halves(bfr(mean));
    c32 d[10], e[10];
#pragma unroll
    for (int r = 0; r < 10; ++r) d[r] = v[r] - m;
    SB();
#pragma unroll
    for (int r = 0; r < 5; ++r) e[r] = d[r] * d[r];
#pragma unroll
    for (int r = 0; r < 5; ++r) d[r] = fma2(d[r + 5], d[r + 5], e[r]);
    SB();
#pragma unroll
    for (int r = 0; r < 10; ++r) { const c32 y = v[r] - mb; e[r] = unpack2(pack2(y.x, y.y)); }
    const float var = wsum2((d[0] + d[1]) + (d[2] + d[3]) + d[4]) * (1.f / 640.f);
    const c32 rs = halves(__builtin_amdgcn_rcpf(bfr(__builtin_amdgcn_sqrtf(bfr(bfr(var) + 1e-5f)))));
    SB();
#pragma unroll
    for (int r = 0; r < 10; ++r) e[r] *= rs;
    SB();
#pragma unroll
    for (int r = 0; r < 10; ++r) o[r] = pack2(e[r].x, e[r].y);
    SB();
}

__device__ __forceinline__ __amdgpu_buffer_rsrc_t mkrsrc(const void* p, unsigned bytes) {
    return __builtin_amdgcn_make_buffer_rsrc((void*)p, 0, bytes, 0x00020000);
}

constexpr unsigned O_F = (unsigned)OFF_F, O_Y0 = (unsigned)OFF_Y0;
__device__ __forceinline__ unsigned nosent1(unsigned v) { return min(v, 0xFFFFFFFEu); }
__device__ __forceinline__ u32x4 nosent4(u32x4 v) { v[1] = nosent1(v[1]); v[3] = nosent1(v[3]); return v; }
__device__ __forceinline__ unsigned smax(u32x4 v) { return max(v[1], v[3]); }
constexpr unsigned SPIN_CAP = 400000u;
constexpr int RW = 5920;
struct RowTw { c32 a[9], b, c; };
__device__ __forceinline__ RowTw row_tw(int lane) {
    RowTw t;
    t.b = cis((float)(lane & 31) * (-1.f / 64.f)); t.c = cis((float)(lane & 15) * (-1.f / 32.f));
    asm volatile("" : "+v"(t.b), "+v"(t.c));
    cpow(t.a, (float)lane * (-1.f / 640.f));
    return t;
}
template <int B> __device__ __forceinline__ void cswap(c32& x, c32& y) {
#pragma unroll
    for (int e = 0; e < 2; ++e) {
        auto r = B == 32 ? __builtin_amdgcn_permlane32_swap(__float_as_uint(x[e]), __float_as_uint(y[e]), false, false) : __builtin_amdgcn_permlane16_swap(__float_as_uint(x[e]), __float_as_uint(y[e]), false, false);
        x[e] = __uint_as_float(r[0]); y[e] = __uint_as_float(r[1]);
    }
}
template <int B> __device__ __forceinline__ void cswap5(c32 (&p)[5], c32 (&q)[5]) {
#pragma unroll
    for (int i = 0; i < 5; ++i) cswap<B>(p[i], q[i]);
    SB();
}
__device__ __forceinline__ void bfly5(c32 (&p)[5], c32 (&q)[5]) {
#pragma unroll
    for (int i = 0; i < 5; ++i) { const c32 t = p[i] - q[i]; p[i] += q[i]; q[i] = t; }
    SB();
}
constexpr unsigned L_X = 16 * RW;
__device__ __forceinline__ void row_x(__amdgpu_buffer_rsrc_t rx, int row, int w, int lane) {
    asm volatile("s_mov_b32 m0, %3\n\ts_nop 0\n\tbuffer_load_dwordx4 %0, %1, %2 offen sc1 lds\n\tbuffer_load_dwordx4 %0, %1, %2 offen offset:1024 sc1 lds\n\tbuffer_load_dwordx4 %0, %1, %2 offen offset:1536 sc1 lds"
                 :: "v"(lane * 16), "s"(rx), "s"(row * (D_ * 2)), "s"(L_X + w * 2560u) : "m0", "memory");
}
template <int M> __device__ __forceinline__ void row_get(u16 (&v)[20], int w, int lane) {
    asm volatile("s_waitcnt vmcnt(%0)" :: "n"(M) : "memory");
#pragma unroll
    for (int r = 0; r < 20; ++r) v[r] = *(const LAS u16*)(size_t)(L_X + w * 2560u + lane * 2u + 128u * (r >> 1) + 1280u * (r & 1));
    SB();
}
template <int NW, int NT, class FS> __device__ __forceinline__ void a1_unit(__amdgpu_buffer_rsrc_t rx, int row0, bool tail, __amdgpu_buffer_rsrc_t rws, char* sm, const RowTw& tw, int w, int lane, FS st_) {
    const unsigned fo = tail ? O_Y0 + 8192u + (unsigned)(row0 & 255) * 16u : O_F + (unsigned)row0 * 16u, gs = tail ? YS : GF;
    if (w < NW) {
        c32 u[10];
        unsigned h[10];
        u16 x[20];
        row_x(rx, row0 + 2 * w, w, lane);
        st_(-1);
        row_get<NW == 8 ? 8 : 0>(x, w, lane);
        if (tail) for (unsigned spins = 0; spins < SPIN_CAP; ++spins) {
            unsigned m = 0;
#pragma unroll
            for (int r = 0; r < 20; ++r) m = max(m, (unsigned)x[r]);
            if (!__any(m == 0xFFFFu)) break;
            row_x(rx, row0 + 2 * w, w, lane);
            row_get<0>(x, w, lane);
        }
#pragma unroll
        for (int r = 0; r < 10; ++r) u[r] = (c32){bf2f(x[2 * r]), bf2f(x[2 * r + 1])};
        st_(0);
        ln_rows(u, h);
        st_(1);
#pragma unroll
        for (int r = 0; r < 10; ++r) u[r] = unpack2(h[r]);
        dft10<-1>(u);
        char* mine = sm + w * RW;
        char* ex = mine + ((lane >> 5) + 10 * ((lane >> 4) & 1)) * 144 + (lane & 15) * 8;
        ctwn<-1, 1>(u, [&](int i) { return tw.a[i - 1]; });
        c32 p[5], q[5];
#pragma unroll
        for (int i = 0; i < 5; ++i) { p[i] = u[2 * i]; q[i] = u[2 * i + 1]; }
        cswap5<32>(p, q); bfly5(p, q); ctwn<-1, 0>(q, [&](int) { return tw.b; });
        cswap5<16>(p, q); bfly5(p, q);
#pragma unroll
        for (int i = 0; i < 5; ++i) *(c32*)(ex + i * 288) = p[i];
        ctwn<-1, 0>(q, [&](int) { return tw.c; });
#pragma unroll
        for (int i = 0; i < 5; ++i) *(c32*)(ex + i * 288 + 2880) = q[i];
        asm volatile("" ::: "memory");
        st_(2);
        if (lane < 40) {
            c32 v[16];
#pragma unroll
            for (int m = 0; m < 8; ++m) { const f32x4 t = *(const f32x4*)(mine + lane * 144 + m * 16); v[2 * m] = t.xy; v[2 * m + 1] = t.zw; }
            dft16<-1>(v);
            asm volatile("" ::: "memory");
#pragma unroll
            for (int m = 0; m < 16; ++m) *(c32*)(mine + lane * 8 + m * 320) = v[m];
        }
        st_(3);
    }
    asm volatile("s_waitcnt lgkmcnt(0)\n\ts_barrier" ::: "memory");
    st_(4);
    const int tid = w * 64 + lane, k0 = tid / (2 * NW) * 4 + (tid & 3), id = tid % (2 * NW);
    const char* X = sm + (id >> 2) * (2 * RW);
#pragma unroll
    for (int c = 0; c * NT < NG * 2 * NW; ++c) {
        const int k4 = k0 + c * (2 * NT / NW), k = min(k4, 320);
        if ((c + 1) * NT <= NG * 2 * NW || k4 < 4 * NG) {
            u32x4 o;
#pragma unroll
            for (int e = 0; e < 2; ++e) {
                const c32 zk = *(const c32*)(X + e * RW + k * 8) * dup(1.f / 32.f), zn = *(const c32*)(X + e * RW + (k == 0 ? 0 : 640 - k) * 8) * dup(1.f / 32.f);
                o[2 * e] = packh(zk.x + zn.x, zk.y - zn.y); o[2 * e + 1] = packh(zk.y + zn.y, zn.x - zk.x);
            }
            st128(nosent4(o), rws, (unsigned)(k4 >> 2) * gs + id * 16u, fo, 16);
        }
    }
}
template <int NW, class FS, class FD> __device__ __forceinline__ void a3_units(__amdgpu_buffer_rsrc_t rws, __amdgpu_buffer_rsrc_t rx, int row0, int step, int lay, char* sm, const RowTw& tw, int w, int lane, FS st_, FD done) {
    constexpr int NT = 512, NC = (NG * 2 * NW + NT - 1) / NT, GS = NW == 4 ? YS : GF;
    const int tid = w * 64 + lane, k0 = tid / (2 * NW) * 4 + (tid & 3), id = tid % (2 * NW);
    u32x4 fv[NC];
    unsigned sc[NC];
    u16 x[20];
    auto get = [&](int row) {
        const unsigned fo = (NW == 4 ? O_Y0 + lay * 4096u : O_F) + (unsigned)row * 16u;
#pragma unroll
        for (int c = 0; c < NC; ++c) fv[c] = __builtin_amdgcn_raw_buffer_load_b128(rws, (unsigned)((min(k0 + c * (2 * NT / NW), 320) >> 2) * GS + id * 16), fo, 16);
        __builtin_amdgcn_sched_barrier(0);
    };
    auto factors = [&]() {
#pragma unroll
        for (int c = 0; c < NC; ++c) { const int k = min(k0 + c * (2 * NT / NW), 320); sc[c] = __builtin_amdgcn_raw_buffer_load_b32(rws, (unsigned)((k >> 2) * YS + (k & 3) * 4), O_Y0 + 12288u, 16); }
    };
    factors();
    if (w < NW) row_x(rx, row0 + 2 * w, w, lane);
    st_(-1);
    for (unsigned spins = 0; spins < SPIN_CAP; ++spins) {
        asm volatile("" ::: "memory");
        if (!lay) factors();
        get(row0);
        if (NW == 8 || !__any(max(max(smax(fv[0]), smax(fv[1])), max(sc[0], sc[1])) == 0xFFFFFFFFu)) break;
    }
    for (int n = 0;; ++n, row0 += step, sm += n & 1 ? 8 * RW : -8 * RW) {
    char* X = sm + (id >> 2) * (2 * RW);
#pragma unroll
    for (int c = 0; c < NC; ++c) {
        const int k = k0 + c * (2 * NT / NW);
        const unsigned re = k == 0 || k == 320 ? 0xFFFFu : ~0u;
        if (c + 1 < NC || k <= 320)
#pragma unroll
            for (int e = 0; e < 2; ++e) {
                const c32 p = __builtin_convertvector(__builtin_bit_cast(f16x2, fv[c][2 * e] & re), c32) * dup(__uint_as_float(sc[c])), q = __builtin_convertvector(__builtin_bit_cast(f16x2, fv[c][2 * e + 1] & re), c32) * dup(__uint_as_float(sc[c]));
                *(c32*)(X + e * RW + k * 8) = addi<1>(p, q); *(c32*)(X + e * RW + (k == 0 ? 0 : 640 - k) * 8) = (c32){p.x + q.y, q.x - p.y};
            }
    }
    asm volatile("s_waitcnt vmcnt(0) lgkmcnt(0)\n\ts_barrier" ::: "memory");
    st_(0);
    if (NW == 8) {
        if (n) done(row0 - step);
        row_get<63>(x, w, lane);
        asm volatile("s_waitcnt lgkmcnt(0)" ::: "memory");
        if (row0 + step < S_) { row_x(rx, row0 + step + 2 * w, w, lane); get(row0 + step); }
    }
    if (w < NW) {
        char* mine = sm + w * RW;
        if (lane < 40) {
            c32 v[16];
            lds16<320>(v, mine + lane * 8);
            dft16<1>(v);
            asm volatile("" ::: "memory");
#pragma unroll
            for (int m = 0; m < 8; ++m) *(f32x4*)(mine + lane * 144 + m * 16) = (f32x4){v[2 * m].x, v[2 * m].y, v[2 * m + 1].x, v[2 * m + 1].y};
        }
        asm volatile("" ::: "memory");
        st_(1);
        const char* ex = mine + ((lane >> 5) + 10 * ((lane >> 4) & 1)) * 144 + (lane & 15) * 8;
        c32 u[10];
        c32 p[5], q[5];
#pragma unroll
        for (int i = 0; i < 5; ++i) q[i] = *(const c32*)(ex + i * 288 + 2880);
#pragma unroll
        for (int i = 0; i < 5; ++i) p[i] = *(const c32*)(ex + i * 288);
        SB();
        ctwn<1, 0>(q, [&](int) { return tw.c; }); bfly5(p, q); cswap5<16>(p, q);
        ctwn<1, 0>(q, [&](int) { return tw.b; }); bfly5(p, q); cswap5<32>(p, q);
#pragma unroll
        for (int i = 0; i < 5; ++i) { u[2 * i] = p[i]; u[2 * i + 1] = q[i]; }
        ctwn<1, 1>(u, [&](int i) { return tw.a[i - 1]; });
        if (NW == 4) row_get<63>(x, w, lane);
        dft10<1>(u);
        st_(2);
        asm volatile("" ::: "memory");
        char* st = mine + lane * 2;
        unsigned h[10];
#pragma unroll
        for (int r = 0; r < 10; ++r) {
            const c32 y = (c32){bf2f(x[2 * r]), bf2f(x[2 * r + 1])} + unpack2(pack2(u[r].x, u[r].y));
            const unsigned p = pack2(y.x, y.y);
            u[r] = unpack2(p);
            *(u16*)(st + r * 128) = (u16)p; *(u16*)(st + 1280 + r * 128) = (u16)(p >> 16);
        }
        st_(3);
        ln_rows(u, h);
        st_(4);
#pragma unroll
        for (int r = 0; r < 10; ++r) { *(u16*)(st + 2560 + r * 128) = (u16)h[r]; *(u16*)(st + 3840 + r * 128) = (u16)(h[r] >> 16); }
        asm volatile("" ::: "memory");
        u32x4 o[6];
#pragma unroll
        for (int c = 0; c < 6; ++c) o[c] = *(const u32x4*)(mine + (c < 3 ? 2560 : 0) + min(c % 3 * 64 + lane, 159) * 16);
#pragma unroll
        for (int c = 0; c < 3; ++c) {
            const int i = min(c * 64 + lane, 159), rr = i >= 80, ch = i - 80 * rr, row = (row0 + 2 * w + rr) & 31;
            st128(nosent4(o[c]), rws, (unsigned)((ch >> 3) * 4096 + row * 128 + (((ch & 7) ^ (row & 7)) << 4)) | (c == 2 ? (lane & 32u) << 25 : 0u), (unsigned)OFF_H + (unsigned)((row0 >> 5) + 4 - 4 * lay) * 40960u, 16);
        }
#pragma unroll
        for (int c = 0; c < 3; ++c) st128(o[3 + c], rws, (unsigned)(c * 64 + lane) * 16u | (c == 2 ? (lane & 32u) << 25 : 0u), (unsigned)OFF_X1 + (unsigned)(row0 + 2 * w) * (D_ * 2), 16);
    }
    if (NW == 4 || row0 + step >= S_) break;
    }
    st_(5);
    asm volatile("s_waitcnt vmcnt(0)\n\ts_barrier" ::: "memory");
    done(row0);
}

__device__ __forceinline__ unsigned xcc_id() { unsigned x; asm volatile("s_getreg_b32 %0, hwreg(HW_REG_XCC_ID)" : "=s"(x)); return x & 7u; }
__device__ __forceinline__ unsigned probe(__amdgpu_buffer_rsrc_t rw, unsigned base) {
    unsigned mn[4] = {~0u, ~0u, ~0u, ~0u};
#pragma unroll 1
    for (int r = 0; r < 2; ++r) {
#pragma unroll
        for (int i = 0; i < 4; ++i) {
            const unsigned long long t1 = __builtin_amdgcn_s_memtime();
            asm volatile("s_waitcnt lgkmcnt(0)" ::: "memory");
            unsigned v = __builtin_amdgcn_raw_buffer_load_b32(rw, i * HALF, base + r * 4096u, 16);
            asm volatile("s_waitcnt vmcnt(0)" : "+v"(v) :: "memory");
            const unsigned long long t2 = __builtin_amdgcn_s_memtime();
            mn[i] = min(mn[i], (unsigned)(t2 - t1) + v);
        }
    }
    return mn[1] + mn[3] < mn[0] + mn[2] ? (mn[3] < mn[1] ? 3u : 1u) : (mn[2] < mn[0] ? 2u : 0u);
}
__device__ __forceinline__ void filter_unit(const u16* wr, const u16* wi, int s0, __amdgpu_buffer_rsrc_t rws, u16* tf, int tid) {
    u32x4 v[6];
#pragma unroll
    for (int i = 0; i < 6; ++i) v[i] = ((const u32x4*)((i < 3 ? wr : wi) + (size_t)s0 * DF))[min(tid + 512 * (i % 3), 1283)];
    __builtin_amdgcn_sched_barrier(0);
#pragma unroll
    for (int i = 0; i < 6; ++i) ((u32x4*)tf)[min(tid + 512 * (i % 3), 1283) + i / 3 * 1284] = v[i];
    __syncthreads();
    const int k = tid >> 3, sq = (tid & 7) * 4;
    u16 t[6][8];
#pragma unroll
    for (int i = 0; i < 6; ++i)
#pragma unroll
        for (int j = 0; j < 8; ++j) t[i][j] = tf[((j & 1) * 32 + sq + (j >> 1)) * DF + k + 64 * i];
    __builtin_amdgcn_sched_barrier(0);
#pragma unroll
    for (int i = 0; i < 6; ++i) {
        u32x4 o;
#pragma unroll
        for (int j = 0; j < 4; ++j) o[j] = (unsigned)t[i][2 * j] | (unsigned)t[i][2 * j + 1] << 16;
        if (i < 5 || tid < 8) st128(o, rws, (unsigned)(k * S_ + sq) * 4u, (unsigned)OFF_WFT + (unsigned)(s0 * 4 + i * 64 * S_ * 4), 16);
    }
}

__device__ __forceinline__ void dma1k(__amdgpu_buffer_rsrc_t r, unsigned vo, unsigned so, unsigned lds) {
    asm volatile("s_mov_b32 m0, %3\n\ts_nop 0\n\tbuffer_load_dwordx4 %0, %1, %2 offen sc1 lds" :: "v"(vo), "s"(r), "s"(so), "s"(lds) : "m0", "memory");
}
template <class FW, class FS> __device__ __forceinline__ void a2_unit(__amdgpu_buffer_rsrc_t rws, int G, int w, int lane, bool late, FW rdy, FS st_) {
    const int fr = lane & 15, g = lane >> 4;
    const unsigned fo = O_F + (unsigned)(G * GF), yo = O_Y0 + (unsigned)(G * YS);
    const unsigned sw = (unsigned)(lane ^ (4 * (w & 1) + g)) * 16u;
    if (w >= 4) {
        const unsigned v = (w - 4) * 1024u, t = late ? fo + 28672u + v : yo + 8192u + v;
        rdy(6);
#pragma unroll
        for (int i = 0; i < 7; ++i) dma1k(rws, sw, fo + v + i * 4096u, v + i * 4096u);
        rdy(7);
        for (unsigned spins = 0; spins < SPIN_CAP; ++spins) {
            dma1k(rws, sw, t, v + 28672u);
            asm volatile("s_waitcnt vmcnt(0)" ::: "memory");
            if (late || !__any(smax(*(const LAS u32x4*)(size_t)(v + 28672u + lane * 16u)) == 0xFFFFFFFFu)) break;
        }
        asm volatile("s_barrier" ::: "memory");
    } else {
        const unsigned wl = 32768u + w * 8192u;
        c32 wf[4][8], t2[4][7];
        f32x4 tr, ti, tn;
        u32x4 mb[2], ma[2];
        float back;
        auto ldw = [&]() {
#pragma unroll
            for (int i = 0; i < 8; ++i) dma1k(rws, lane * 16u, (unsigned)OFF_WFT + (unsigned)(min(4 * G + w, 320) * 8192 + i * 1024), wl + i * 1024u);
        };
        auto cvw = [&]() {
            unsigned wv[4][8], m = 0;
            asm volatile("s_waitcnt vmcnt(0)" ::: "memory");
#pragma unroll
            for (int i = 0; i < 32; ++i) wv[i >> 3][i & 7] = *(const LAS unsigned*)(size_t)(wl + (fr + 32 * g) * 4u + ((i >> 3 & 1) * 16 + (i >> 4) * 128 + (i & 7) * 256) * 4u);
            SB();
#pragma unroll
            for (int i = 0; i < 32; ++i) m = pkmax(m, wv[i >> 3][i & 7] & 0x7F807F80u);
            float x = __uint_as_float(max(m << 16, m & 0xFFFF0000u));
            x = fmaxf(x, dppf<0xB1>(x)); x = fmaxf(x, dppf<0x4E>(x)); x = fmaxf(x, dppf<0x141>(x)); x = fmaxf(x, dppf<0x140>(x));
            const unsigned e = min(max(__float_as_uint(xmax3216(x)), 8u << 23), 246u << 23);
            back = __uint_as_float(e) * (1.f / 40.f);
#pragma unroll
            for (int i = 0; i < 32; ++i) { wf[i >> 3][i & 7] = unpack2(wv[i >> 3][i & 7]) * dup(__uint_as_float((251u << 23) - e)); asm volatile("" : "+v"(wf[i >> 3][i & 7])); }
        };
        rdy(8);
        ldw();
#pragma unroll
        for (int i = 0; i < 4; ++i) {
            const int s = 2 * g + (i & 1) + (i >> 1) * 8;
            cpow(t2[i], (float)(fr + 16 * s) * (-1.f / 2048.f));
            const c32 t = cis((float)((4 * g + i) * fr) * (-1.f / 256.f)), p = cis((float)(s * fr) * (1.f / 16.f)) * dup(0.25f);
            tr[i] = t.x; ti[i] = t.y;
            mb[0][i] = packh(p.x, p.y); mb[1][i] = packh(-p.y, p.x);
#pragma unroll
            for (int h = 0; h < 2; ++h) { const c32 q = cis((float)((4 * g + i) * (8 * h + (fr >> 1)) + 4 * (fr & 1)) * (1.f / 16.f)) * dup(0.25f); ma[h][i] = packh(q.x, q.y); }
        }
        tn = -ti;
        asm volatile("" : "+v"(tn), "+v"(tr), "+v"(ti), "+v"(mb[0]), "+v"(mb[1]), "+v"(ma[0]), "+v"(ma[1]));
        cvw();
        LAS u32x4* im = (LAS u32x4*)(size_t)((fr + 32 * g) * 128u + (unsigned)(w ^ (fr >> 1)) * 16u);
        LAS u32x4* im1 = (LAS u32x4*)((size_t)im ^ 64u);
        asm volatile("s_barrier" ::: "memory");
        st_(2);
        u32x4 x[4][2];
#pragma unroll
        for (int h = 0; h < 2; ++h)
#pragma unroll
            for (int i = 0; i < 4; ++i) x[i][h] = (h ? im1 : im)[(i & 1) * 128 + (i >> 1) * 1024];
        SB();
        if (late && lane == 0) __builtin_amdgcn_raw_buffer_store_b32(__float_as_uint(back), rws, 0u, yo + 12288u + w * 4u, 16);
        f32x4 c[8][2];
        auto cvt = [](float a, float b) { unsigned r; asm("v_cvt_pk_f16_f32 %0, %1, %2" : "=v"(r) : "v"(a), "v"(b)); return r; };
        auto two = [&](auto in) {
            u32x4 d = in(0), n;
            SA();
#pragma unroll
            for (int e = 0; e < 8; ++e) {
                asm volatile("s_nop 1" : "+v"(d));
                c[e][0] = mfmah(d, mb[0]);
                if (e < 7) n = in(e + 1);
                c[e][1] = mfmah(d, mb[1]);
                d = n;
                SB();
            }
            SA();
#pragma unroll
            for (int e = 0; e < 8; e += 2) {
                const f32x4 p0 = c[e][0] * tr, p1 = c[e][0] * ti, p2 = c[e + 1][0] * tr, p3 = c[e + 1][0] * ti;
                SB();
                c[e][0] = __builtin_elementwise_fma(c[e][1], tn, p0); c[e][1] = __builtin_elementwise_fma(c[e][1], tr, p1);
                c[e + 1][0] = __builtin_elementwise_fma(c[e + 1][1], tn, p2); c[e + 1][1] = __builtin_elementwise_fma(c[e + 1][1], tr, p3);
                SB();
            }
            auto mid = [&](int e) { return (u32x4){packh(c[e][0][0], c[e][1][0]), packh(c[e][0][1], c[e][1][1]), packh(c[e][0][2], c[e][1][2]), packh(c[e][0][3], c[e][1][3])}; };
            d = mid(0);
#pragma unroll
            for (int e = 0; e < 8; ++e) {
                SB();
                c[e][0] = mfmah(ma[0], d);
                if (e < 7) n = mid(e + 1);
                c[e][1] = mfmah(ma[1], d);
                d = n;
            }
            SB();
        };
        two([&](int e) { return (u32x4){x[0][e >> 2][e & 3], x[1][e >> 2][e & 3], x[2][e >> 2][e & 3], x[3][e >> 2][e & 3]}; });
        asm volatile("s_nop 7" : "+v"(c[0][0]), "+v"(c[0][1]), "+v"(c[1][0]), "+v"(c[1][1]), "+v"(c[2][0]), "+v"(c[2][1]), "+v"(c[3][0]), "+v"(c[3][1]), "+v"(c[4][0]), "+v"(c[4][1]), "+v"(c[5][0]), "+v"(c[5][1]), "+v"(c[6][0]), "+v"(c[6][1]), "+v"(c[7][0]), "+v"(c[7][1]));
        st_(3);
        c32 u[2][16];
#pragma unroll
        for (int h = 0; h < 2; ++h) {
#pragma unroll
            for (int e = 0; e < 8; ++e) { u[h][2 * e] = c[e][h].xy; u[h][2 * e + 1] = c[e][h].zw; }
            ctwn<-1, 2>(u[h], [&](int i) { return t2[2 * h + (i & 1)][(i >> 1) - 1]; });
            dft8x2<-1>(u[h]);
            ctwn<-1, 0>(u[h], [&](int i) { return wf[2 * h + (i & 1)][i >> 1]; });
            dft8x2<1>(u[h]);
            ctwn<1, 2>(u[h], [&](int i) { return t2[2 * h + (i & 1)][(i >> 1) - 1]; });
            if (h == 0) st_(4);
        }
        two([&](int e) { return (u32x4){cvt(u[0][2 * e].y, u[0][2 * e].x), cvt(u[0][2 * e + 1].y, u[0][2 * e + 1].x), cvt(u[1][2 * e].y, u[1][2 * e].x), cvt(u[1][2 * e + 1].y, u[1][2 * e + 1].x)}; });
#pragma unroll
        for (int h = 0; h < 2; ++h) {
            if (h) asm volatile("" : "+v"(c[4][0]), "+v"(c[7][0]), "+v"(c[7][1]));
            SA();
#pragma unroll
            for (int i = 0; i < 4; ++i) {
                u32x4 o;
#pragma unroll
                for (int j = 0; j < 4; ++j) { const f32x4 y = c[4 * h + j][i >> 1]; o[j] = cvt(y[2 * (i & 1) + 1], y[2 * (i & 1)]); }
                (h ? im1 : im)[(i & 1) * 128 + (i >> 1) * 1024] = o;
            }
        }
        st_(5);
    }
    asm volatile("s_waitcnt lgkmcnt(0)\n\ts_barrier" ::: "memory");
    st_(6);
    u32x4 o[4];
#pragma unroll
    for (int j = 0; j < 4; ++j) o[j] = *(const LAS u32x4*)(size_t)(w * 1024u + j * 8192u + sw);
    SB();
#pragma unroll
    for (int j = 0; j < 4; ++j) {
        st128(j ? o[j] : nosent4(o[j]), rws, lane * 16u, (j || w >= 4 ? fo + j * 8192u : late ? yo : yo + 4096u) + w * 1024u, 16);
        if (j == 0) st_(7);
    }
}

struct RecArgs {
    unsigned* ctl; char* win; const u16* mem0; const u16* xin; u16* out;
    const u16* W[6];
};
template <int I> __device__ __forceinline__ char* karg(int i = 0) {
    unsigned long long p;
    asm volatile("s_load_dwordx2 %0, %1, %2\n\ts_waitcnt lgkmcnt(0)" : "=s"(p) : "s"(__builtin_amdgcn_kernarg_segment_ptr()), "s"(I * 8 + i * 8) : "memory");
    return (char*)p;
}
constexpr unsigned O_H = (unsigned)OFF_H, O_X1 = (unsigned)OFF_X1, O_Z = (unsigned)OFF_Z, O_Z2 = (unsigned)OFF_Z2, O_PT = (unsigned)OFF_PT, O_QK = (unsigned)OFF_QK;
#define MXV 0x02A80000000AAAAAull
#define MXQ 0x888888000000ull
static_assert((MXV & 15) == 10 && __builtin_popcountll(MXV >> 32) >= 2 && __builtin_popcountll(MXQ) >= 2 && !(MXQ & 15) && !((MXV | MXQ) >> 63), "segment sets");
constexpr int NREC = 96 + NOWN, NQT = NREC + NOWN, NXT = NQT + 32, NWRK = NXT + 32;
__device__ __forceinline__ bool spin_fail(unsigned& spins, __amdgpu_buffer_rsrc_t rws) {
    if (++spins > SPIN_CAP) { __builtin_amdgcn_raw_buffer_store_b32(1u, rws, 0u, 0u, 16); return true; }
    return (spins & 1023u) == 0u && __builtin_amdgcn_readfirstlane(__builtin_amdgcn_raw_buffer_load_b32(rws, 0u, 0u, 16));
}
#define ALN 6
#define AL ".p2align " STR(ALN) "\n\t"
#define STR2(x) #x
#define STR(x) STR2(x)
#define TPRE 0
#define QP 2
#define BQN 7
#define BQ(n) ".if " #n "==" STR(BQN) "\n\ts_barrier\n\t.endif\n\t"
#define SST(k) do {} while (0)
#define LSEL 1
#define RT(k) do {} while (0)
#define RTS() do {} while (0)
#define B3ATTR
#define MTA(k)
#define MTRT() do {} while (0)
#define MT(k) do {} while (0)
#define MTS() do {} while (0)
#define FC(x) do {} while (0)
#define DQC 1130
#define DVC 1200
#define M1 44
#define MZ 40
#define MZP 20
#define M2 224
#define LTP (LT0 + 15)
#define LTZ 340
#define PEN 280
#define KSL 1
#define SVO 64
#define DZP 46
#define LT0 330
#define SAD 1
#define PEN2 80
#define PEN1 50
#define PEN0 60
#define DDM2 0
#define DRA 700
#define TSLP 1
#define PFS 0
#define PFOFF 1
#define OPR 2
#define BSPARSE 0
#define ZLAG 7
#define TKP 0
#define DPS 12
#define RZ 63
#define RQ 63
#define R2 63
#define DXT 3000
#define SSW 99
#define LTM 18
#define XS0 13
#define XSU 8
#define XSM 32
#define LTU 4
#define LTD 1
#define QUIET 0
#define QUAD 1
#define LTS 3
#define XSS 3
#define TKS 1
#define LT1 358
#define FDL 2
#define ZLA 3
#define DPT2 3
#define NORND 0
#define LTX 28
#define TKE 200
#define DVO 0
#define ZPS 4
#define ASLP 4
#define BADG 0
#define DW1 0
#define WLA 3
#define XSLP 30
#define DOUT 215
#define DA1 145
#define DA2 215
#define DA20 270
#define DFB 150
#define DA3 185
#define DA30 230
#define YSLP 0
#define QEU 22
#define QE2 30
#define PEN00 120
#define DQ0 930
#define DQ00 1080
#define DZ0 420
#define DZ00 430
#define DOU2 200
#define DFA 0
#define SRG 0x5A000000u
#define OBN 5
#define OB(n) ".if " #n "==" STR(OBN) "\n\ts_barrier\n\tds_read_b64 v[102:103], %[sp]\n\tds_read_b64 v[104:105], %[sp] offset:512\n\tds_read_b64 v[106:107], %[sp] offset:1024\n\t.endif\n\t"
#define VSLP 0
#define CPF 1
#define DST 4
#define XST 0
#define ODL 8
#define VTS 1
#define XF 5
#define XFS 32
#define DOU1 50
#define DR0 0
#define DFB0 120
#define TPF 21
#define DZF 160
#define DZF0 175
#define DV0 260
#define QTP 1
#define XDL 0
#define DOW 200
#define DOU3 300
#define NTM 31
#define WK3 0
#define QEV 8
#define HLA 8
#define PSLP 16
#define PFD 4
#define NPF 3
#define KSL2 1
#define KBQ 4
#define VB1 6
#define VB2 5
#define KR1 3
#define QB2 2
#define NB2 6
#define DZR 24
#define ML 470
#define MN 12
#define CF 190
#define DB 1500
#define DP 200
#define ZSL 1
#define NB1 3
#define DPT 3
#define DDMA 8
#define DZ2 800
#define QEARLY 0
#define OSLP 12
#define OSLP2 4
#define WSLP 32
#define DO0 12000
#define DW0 1200
#define DQT 9000
#define DQ4 700
#define ZEARLY 3
#define HSLP 4
#define RMAP 0
#define DIE 0
#define VMWAIT() do { asm volatile("" ::: "memory"); __builtin_amdgcn_s_waitcnt(0x0F70); asm volatile("" ::: "memory"); } while (0)
#define BAR() asm volatile("s_waitcnt lgkmcnt(0)\n\ts_barrier" ::: "memory")
#define PIN1(a) asm volatile("" : "+v"(a) :: "memory")
#define PIN2(a, b) asm volatile("" : "+v"(a), "+v"(b) :: "memory")
#define BAR0() asm volatile("s_barrier" ::: "memory")
__device__ __forceinline__ unsigned fresh_lane() { unsigned l; asm volatile("v_mbcnt_lo_u32_b32 %0, -1, 0\n\tv_mbcnt_hi_u32_b32 %0, -1, %0" : "=v"(l)); __builtin_assume(l < 64u); return l; }
__device__ __forceinline__ u32x4 sent4() { u32x2 a, b; asm volatile("v_mov_b64 %0, -1\n\tv_mov_b64 %1, -1" : "=v"(a), "=v"(b)); return (u32x4){a[0], a[1], b[0], b[1]}; }
__device__ __forceinline__ void wait_until(unsigned long long t) { while ((long long)(__builtin_amdgcn_s_memtime() - t) < 0) __builtin_amdgcn_s_sleep(1); }
struct Clk { unsigned ta, ma, cpq, qpc; };
__device__ __forceinline__ int sx24(unsigned v) { return (int)(v << 8) >> 8; }
#define WAIT_FINE AL \
        "s_memrealtime vcc\n\t" \
        "s_memtime s[100:101]\n\t" \
        "s_brev_b32 %[fi], -2\n\t" \
        "s_waitcnt lgkmcnt(0)\n\t" \
        "s_mov_b32 %[rp], vcc_lo\n\t" \
        "s_lshl_b32 vcc_hi, vcc_lo, 2\n\t" \
        "s_sub_u32 %[x], %[t], vcc_hi\n\t" \
        "s_bfe_i32 %[x], %[x], 0x180000\n\t" \
        "s_min_i32 %[x], %[x], 4000\n\t" \
        "s_max_i32 %[x], %[x], -4000\n\t" \
        "s_add_u32 %[t], vcc_hi, %[x]\n\t" \
        "s_mul_i32 %[x], %[x], %[cq]\n\t" \
        "s_ashr_i32 %[x], %[x], 8\n\t" \
        "s_sub_u32 %[tm], %[t], %[ta]\n\t" \
        "s_bfe_i32 %[tm], %[tm], 0x180000\n\t" \
        "s_min_i32 %[tm], %[tm], 1000000\n\t" \
        "s_max_i32 %[tm], %[tm], -1000000\n\t" \
        "s_mul_i32 %[tm], %[tm], %[cq]\n\t" \
        "s_ashr_i32 %[tm], %[tm], 8\n\t" \
        "s_add_u32 %[tm], %[tm], %[ma]\n\t" \
        "s_sub_u32 %[tm], %[tm], s100\n\t" \
        "s_lshr_b32 vcc_hi, %[cq], 4\n\t" \
        "s_sub_u32 vcc_hi, %[x], vcc_hi\n\t" \
        "s_max_i32 %[tm], %[tm], vcc_hi\n\t" \
        "s_min_i32 %[tm], %[tm], %[x]\n\t" \
        "s_add_u32 %[tm], %[tm], s100\n" \
        "1:\n\t" \
        "s_memrealtime vcc\n\t" \
        "s_memtime s[100:101]\n\t" \
        "s_waitcnt lgkmcnt(0)\n\t" \
        "s_cmp_eq_u32 vcc_lo, %[rp]\n\t" \
        "s_cbranch_scc1 2f\n\t" \
        "s_mov_b32 %[rp], vcc_lo\n\t" \
        "s_lshl_b32 %[x], vcc_lo, 2\n\t" \
        "s_sub_u32 %[x], %[t], %[x]\n\t" \
        "s_bfe_i32 %[x], %[x], 0x180000\n\t" \
        "s_mul_i32 %[x], %[x], %[cq]\n\t" \
        "s_ashr_i32 %[x], %[x], 8\n\t" \
        "s_add_u32 %[x], %[x], s100\n\t" \
        "s_sub_u32 s101, %[x], %[tm]\n\t" \
        "s_cmp_lt_i32 s101, %[fi]\n\t" \
        "s_cselect_b32 %[tm], %[x], %[tm]\n\t" \
        "s_mov_b32 %[fi], 0\n" \
        "2:\n\t" \
        "s_sub_u32 %[x], %[tm], s100\n\t" \
        "s_cmp_gt_i32 %[x], 260\n\t" \
        "s_cbranch_scc1 1b\n\t" \
        "s_cmp_eq_u32 %[fi], 0\n\t" \
        "s_cselect_b32 %[ta], %[t], %[ta]\n\t" \
        "s_cselect_b32 %[ma], %[tm], %[ma]\n" \
        "3:\n\t" \
        "s_memtime s[100:101]\n\t" \
        "s_waitcnt lgkmcnt(0)\n\t" \
        "s_sub_u32 %[x], %[tm], s100\n\t" \
        "s_cmp_gt_i32 %[x], 92\n\t" \
        "s_cbranch_scc1 3b\n\t" \
        "s_max_i32 %[x], %[x], 0\n\t" \
        "s_andn2_b32 %[x], %[x], 3\n\t" \
        "s_sub_u32 %[x], 104, %[x]\n\t" \
        "s_getpc_b64 vcc\n\t" \
        "s_add_u32 vcc_lo, vcc_lo, %[x]\n\t" \
        "s_addc_u32 vcc_hi, vcc_hi, 0\n\t" \
        "s_setpc_b64 vcc\n\t" \
        ".rept 23\n\t" \
        "s_nop 0\n\t" \
        ".endr\n\t"
#define WAIT_FINE_OUT [tm] "=&s"(tm), [rp] "=&s"(rp), [x] "=&s"(x), [fi] "=&s"(fi), [t] "+s"(t), [ta] "+s"(c.ta), [ma] "+s"(c.ma)
__device__ __forceinline__ void wait_fine_bar(unsigned t, Clk& c) {
    unsigned tm, rp, x, fi;
    asm volatile(WAIT_FINE "s_barrier" : WAIT_FINE_OUT : [cq] "s"(c.cpq) : "vcc", "scc", "s100", "s101", "memory");
}
__device__ __forceinline__ void wait_cyc_bar(unsigned tm) {
    unsigned x;
    asm volatile(AL
        "3:\n\t"
        "s_memtime s[100:101]\n\t"
        "s_waitcnt lgkmcnt(0)\n\t"
        "s_sub_u32 %0, %1, s100\n\t"
        "s_cmp_gt_i32 %0, 92\n\t"
        "s_cbranch_scc1 3b\n\t"
        "s_max_i32 %0, %0, 0\n\t"
        "s_andn2_b32 %0, %0, 3\n\t"
        "s_sub_u32 %0, 104, %0\n\t"
        "s_getpc_b64 vcc\n\t"
        "s_add_u32 vcc_lo, vcc_lo, %0\n\t"
        "s_addc_u32 vcc_hi, vcc_hi, 0\n\t"
        "s_setpc_b64 vcc\n\t"
        ".rept 23\n\t"
        "s_nop 0\n\t"
        ".endr\n\t"
        "s_barrier"
        : "=&s"(x) : "s"(tm) : "vcc", "scc", "s100", "s101", "memory");
}
__device__ __forceinline__ unsigned cyc_at(const Clk& c, int d) {
    d = min(d, 20000);
    asm volatile("" : "+s"(d));
    return c.ma + (unsigned)((max(d, -4000) * (int)c.cpq) >> 8);
}
__device__ __forceinline__ void clk_rate(Clk& c, unsigned ta0, unsigned ma0) {
    const int dq = sx24(c.ta - ta0);
    const float r = fminf(fmaxf((float)(c.ma - ma0) * __builtin_amdgcn_rcpf((float)dq), 1.f), 8.f);
    unsigned x, y;
    asm volatile("s_nop 1\n\tv_readfirstlane_b32 %0, %2\n\tv_readfirstlane_b32 %1, %3\n\ts_nop 3" : "=s"(x), "=s"(y)
                 : "v"((unsigned)(256.f * r + 0.5f)), "v"((unsigned)(65536.f * __builtin_amdgcn_rcpf(r))));
    c.cpq = dq > 200 ? x : c.cpq; c.qpc = dq > 200 ? y : c.qpc;
}
__device__ __forceinline__ Clk clk_init() { return Clk{4u * (unsigned)__builtin_amdgcn_s_memrealtime(), (unsigned)__builtin_amdgcn_s_memtime(), 1470u, 11413u}; }
__device__ __forceinline__ int read_sched(__amdgpu_buffer_rsrc_t rws, __amdgpu_buffer_rsrc_t rwin, int s, unsigned tg, unsigned vo, unsigned to, unsigned f, unsigned g0, int& lt, int& xs, unsigned& fc) {
    if (__builtin_expect(__any((f >> 24) != tg), 0)) {
        for (unsigned spins = 0; __any((f >> 24) != tg);) {
            FC(fc);
            if (spin_fail(spins, rws)) break;
            __builtin_amdgcn_s_sleep(TSLP);
            asm volatile("" ::: "memory");
            if ((f >> 24) != tg) f = __builtin_amdgcn_raw_buffer_load_b32(rwin, vo, to, 16);
        }
    }
    int d = (int)((f - g0) & 0xFFFFFFu);
    d = max(d, __builtin_amdgcn_update_dpp(0, d, 0xB1, 0xF, 0xF, true));
    d = max(d, __builtin_amdgcn_update_dpp(0, d, 0x4E, 0xF, 0xF, true));
    d = max(d, __builtin_amdgcn_update_dpp(0, d, 0x141, 0xF, 0xF, true));
    d = max(d, __builtin_amdgcn_update_dpp(0, d, 0x140, 0xF, 0xF, true));
    d = max(d, __builtin_amdgcn_update_dpp(0, d, 0x142, 0xA, 0xF, true));
    d = max(__builtin_amdgcn_readlane(d, 16), __builtin_amdgcn_readlane(d, 48));
    const int l0 = lt, x0 = xs;
    d -= 0x400000;
    auto sel = [](int c, int x, int y) { int r; asm("s_cmp_lt_i32 %1, 0\n\ts_cselect_b32 %0, %2, %3" : "=s"(r) : "s"(c), "s"(x), "s"(y) : "scc"); return r; };
    const int miss = l0 + min(x0 + LTM, LTX) - d, first = s - 1;
    lt = sel((s ^ 1) - 1, LT0, sel(miss | first, l0, min(max(d, l0 - 1 - ((l0 - d) >> LTS)), l0 + LTU)));
    xs = sel(first, x0, sel(miss, min(x0 + XSU, XSM), max(x0 - 1 - (x0 >> XSS), sel(s - XFS, XF, 0))));
    return 0x400000 + sel(miss | first, d + sel(first, sel((int)tg - 1, PEN00, PEN0), PEN), l0 + x0) + M1 + M2 + x0;
}
constexpr int QS = 272;
constexpr int TQS = 1152;
constexpr int L_Q = 10240, L_K = L_Q + 2 * TQS, L_S = L_K + 2 * TQS, L_TOK = L_S + 256, L_H = L_TOK + 49152, L_END = L_H + 2 * 40960;
typedef __attribute__((ext_vector_type(4))) short v4i16_t;
__device__ __forceinline__ u32x2 ldtr(const char* p) { return __builtin_bit_cast(u32x2, __builtin_amdgcn_ds_read_tr16_b64_v4i16((LAS v4i16_t*)p)); }
__device__ __forceinline__ u32x2 ldtr1(const LAS char* p) { return __builtin_bit_cast(u32x2, __builtin_amdgcn_ds_read_tr16_b64_v4i16((LAS v4i16_t*)p)); }
__device__ __forceinline__ u32x4 ldtr2(const LAS char* p0, const LAS char* p1) {
    const u32x2 x = __builtin_bit_cast(u32x2, __builtin_amdgcn_ds_read_tr16_b64_v4i16((LAS v4i16_t*)p0)), y = __builtin_bit_cast(u32x2, __builtin_amdgcn_ds_read_tr16_b64_v4i16((LAS v4i16_t*)p1));
    return (u32x4){x[0], x[1], y[0], y[1]};
}
__device__ __forceinline__ u32x4 ldtr2(const char* p0, const char* p1) { const u32x2 x = ldtr(p0), y = ldtr(p1); return (u32x4){x[0], x[1], y[0], y[1]}; }

__device__ __forceinline__ unsigned poll_lim(int& budget) {
    --budget;
    unsigned lim = budget > 0 ? 0xFFFFFFFEu : 0xFFFFFFFFu;
    asm volatile("" : "+s"(lim));
    return lim;
}
__device__ __forceinline__ bool mem_missing(unsigned lds, unsigned lim) {
    const LAS char* p = (const LAS char*)(size_t)(lds + fresh_lane() * 16u);
    unsigned m = 0;
#pragma unroll 1
    for (int h = 0; h < 2; ++h) {
        u32x4 v[5];
#pragma unroll
        for (int i = 0; i < 5; ++i) v[i] = *(const LAS u32x4*)(p + h * 5120 + i * 1024);
        SB();
#pragma unroll
        for (int i = 0; i < 5; ++i) m = max(max(m, max(v[i][0], v[i][1])), max(v[i][2], v[i][3]));
    }
    return __any(m > lim);
}
#define GATHER \
        "buffer_load_dwordx4 %[vo], %[rw], %[s0] offen sc1 lds\n\t" \
        "buffer_load_dwordx4 %[vo], %[rw], %[s0] offen offset:1024 sc1 lds\n\t" \
        "buffer_load_dwordx4 %[vo], %[rw], %[s0] offen offset:2048 sc1 lds\n\t" \
        "buffer_load_dwordx4 %[vo], %[rw], %[s0] offen offset:3072 sc1 lds\n\t" \
        "s_movk_i32 m0, 0x1000\n\t" \
        "s_nop 0\n\t" \
        "buffer_load_dwordx4 %[vo], %[rw], %[s4] offen sc1 lds\n\t" \
        "buffer_load_dwordx4 %[vo], %[rw], %[s4] offen offset:1024 sc1 lds\n\t" \
        "buffer_load_dwordx4 %[vo], %[rw], %[s4] offen offset:2048 sc1 lds\n\t" \
        "buffer_load_dwordx4 %[vo], %[rw], %[s4] offen offset:3072 sc1 lds\n\t" \
        "s_movk_i32 m0, 0x2000\n\t" \
        "s_nop 0\n\t" \
        "buffer_load_dwordx4 %[vo], %[rw], %[s1] offen sc1 lds\n\t" \
        "buffer_load_dwordx4 %[vo], %[rw], %[s1] offen offset:1024 sc1 lds"
#define GATHER_IN [vo] "v"(vo), [rw] "s"(rwin), [s0] "s"(so0), [s4] "s"(so0 + 4096u), [s1] "s"(so1)
__device__ __forceinline__ void gather_at(unsigned t, Clk& c, __amdgpu_buffer_rsrc_t rwin, unsigned vo, unsigned so0, unsigned so1) {
    unsigned tm, rp, x, fi;
    asm volatile("s_mov_b32 m0, 0\n\t" WAIT_FINE GATHER : WAIT_FINE_OUT : [cq] "s"(c.cpq), GATHER_IN : "vcc", "scc", "s100", "s101", "m0", "memory");
}
__device__ __forceinline__ void dma4k(__amdgpu_buffer_rsrc_t r, unsigned vo, unsigned so, unsigned lds) {
    asm volatile(
        "s_mov_b32 m0, %3\n\t"
        "s_nop 0\n\t"
        "buffer_load_dwordx4 %0, %1, %2 offen sc1 lds\n\t"
        "buffer_load_dwordx4 %0, %1, %2 offen offset:1024 sc1 lds\n\t"
        "buffer_load_dwordx4 %0, %1, %2 offen offset:2048 sc1 lds\n\t"
        "buffer_load_dwordx4 %0, %1, %2 offen offset:3072 sc1 lds"
        :: "v"(vo), "s"(r), "s"(so), "s"(lds) : "m0", "memory");
}
__device__ __forceinline__ int bs2(int c) { return (c & ~3) | ((c & 1) << 1) | ((c >> 1) & 1); }
__device__ __forceinline__ void get_mem0(u32x4 (&m)[2], int t) {
    const u16* mem0 = (const u16*)karg<2>();
#pragma unroll
    for (int i = 0; i < 2; ++i) {
        const int c = min(t + 512 * i, 639);
        const unsigned* src = (const unsigned*)(mem0 + ((c & 6) + ((c >> 3) & 1)) * D_ + (c >> 4) * 16 + (c & 1) * 2);
        m[i] = (u32x4){src[0], src[2], src[4], src[6]};
    }
    SB();
}
__device__ __forceinline__ void put_mem0(const u32x4 (&m)[2], __amdgpu_buffer_rsrc_t rwin, unsigned so0, unsigned so1, int t) { st128(m[0], rwin, t * 16u, so0, 16); st128(m[1], rwin, min(t, 127) * 16u, so1, 16); }
__device__ __forceinline__ f32x2 hadd(f32x4 c) { return (f32x2){c[0], c[1]} + (f32x2){c[2], c[3]}; }
__device__ __forceinline__ unsigned hpack(f32x4 c) { const f32x2 y = hadd(c); return pack2(y[0], y[1]); }
__device__ __forceinline__ u32x8 cat8(u32x4 lo, u32x4 hi) { return (u32x8){lo[0], lo[1], lo[2], lo[3], hi[0], hi[1], hi[2], hi[3]}; }
__device__ __forceinline__ u32x4 half8(const u32x8& v, int u) { return u ? (u32x4){v[4], v[5], v[6], v[7]} : (u32x4){v[0], v[1], v[2], v[3]}; }
template <int N, int ST> __device__ __forceinline__ void stage_h(__amdgpu_buffer_rsrc_t rws, unsigned so, unsigned lds, int lane) {
#pragma unroll
    for (int i = 0; i < N; ++i) dma1k(rws, lane * 16u, so + i * ST, lds + i * ST);
}
template <int N, int ST, int M> __device__ __forceinline__ void check_h(__amdgpu_buffer_rsrc_t rws, unsigned so, unsigned lds, int lane) {
    asm volatile("s_waitcnt vmcnt(%0)" :: "n"(M) : "memory");
    for (unsigned spins = 0; spins < SPIN_CAP; ++spins) {
        unsigned m = 0;
#pragma unroll
        for (int i = 0; i < N; ++i) m = max(m, smax(*(const LAS u32x4*)(size_t)(lds + lane * 16u + i * ST)));
        if (!__any(m == 0xFFFFFFFFu)) break;
        stage_h<N, ST>(rws, so, lds, lane);
        asm volatile("s_waitcnt vmcnt(0)" ::: "memory");
    }
}
__device__ __forceinline__ void wait_h(__amdgpu_buffer_rsrc_t rws, __amdgpu_buffer_rsrc_t rfl, unsigned fb, int seg, int& hrdy) {
    unsigned lim = hrdy ? 63u : (unsigned)min(seg + HLA, 63);
    if (__builtin_expect(hrdy <= seg, 0)) for (unsigned spins = 0;;) {
        asm volatile("" ::: "memory");
        const u32x4 f = __builtin_amdgcn_raw_buffer_load_b128(rfl, min(fresh_lane(), lim) * 128u, W_H + fb, 16);
        const unsigned long long miss = __ballot((f[0] | f[1] | f[2] | f[3]) != 0u);
        hrdy = miss ? __builtin_ctzll(miss) : (int)lim + 1;
        if (hrdy > seg) break;
        if (spin_fail(spins, rws)) break;
        lim = (unsigned)min(seg + HLA, 63);
        __builtin_amdgcn_s_sleep(HSLP);
    }
}
__device__ __forceinline__ u32x4 ask_h(__amdgpu_buffer_rsrc_t rfl, unsigned fb, unsigned lim) { return __builtin_amdgcn_raw_buffer_load_b128(rfl, min(fresh_lane(), lim) * 128u, W_H + fb, 16); }
__device__ __forceinline__ int see_h(u32x4 f) { const unsigned long long miss = __ballot((f[0] | f[1] | f[2] | f[3]) != 0u); return miss ? __builtin_ctzll(miss) : 64; }
template <int CTRL> __device__ __forceinline__ unsigned dppu(unsigned v) { return (unsigned)__builtin_amdgcn_mov_dpp((int)v, CTRL, 0xF, 0xF, true); }
template <int CTRL> __device__ __forceinline__ u32x4 ror4(u32x4 v) { return (u32x4){dppu<CTRL>(v[0]), dppu<CTRL>(v[1]), dppu<CTRL>(v[2]), dppu<CTRL>(v[3])}; }
__device__ __forceinline__ float xmax16(float v) {
    auto q = __builtin_amdgcn_permlane16_swap(__float_as_uint(v), __float_as_uint(v), false, false);
    return fmx(__uint_as_float(q[0]), __uint_as_float(q[1]));
}
__device__ __forceinline__ float xsum16(float v) {
    auto q = __builtin_amdgcn_permlane16_swap(__float_as_uint(v), __float_as_uint(v), false, false);
    return __uint_as_float(q[0]) + __uint_as_float(q[1]);
}
__device__ __forceinline__ float addswap16(float a, float b) {
    auto r = __builtin_amdgcn_permlane16_swap(__float_as_uint(a), __float_as_uint(b), false, false);
    return __uint_as_float(r[0]) + __uint_as_float(r[1]);
}
#define LANES() \
    const int lane = (int)fresh_lane(), tid = w * 64 + lane, fr = lane & 15, fq = lane >> 4; \
    const int sidx = (lane & 2) ? 0xEEEE : 0x4444; \
    const int ma0 = fq * 256 + (fr & 1) * 128 + (fr >> 1) * 16; \
    const int prow = 16 * ((fr >> 2) & 1) + 4 * (fr >> 3) + (fr & 3), hoff = fr * 128; \
    (void)tid; (void)sidx; (void)ma0; (void)prow; (void)hoff

__device__ __forceinline__ int seg_after(unsigned long long m, int j) { m = j >= 63 ? 0ull : m >> (j + 1); return m ? j + 1 + __builtin_ctzll(m) : NSEG; }
__device__ __forceinline__ int seg_done(unsigned long long m, int f0, int f1) { return min(seg_after(~m, f0), seg_after(m, f1)) - 1; }
template <int OFF> __device__ __forceinline__ void ldfrag(u32x4& d, const LAS char* p) { asm volatile("ds_read_b128 %0, %1 offset:%2" : "=v"(d) : "v"(p), "n"(OFF) : "memory"); }
template <int U, class FM, class FV> __device__ __forceinline__ void project(const u32x8 (&wt)[2][10], f32x4 (&tc)[2][2], u32x4 (&bb)[8], const LAS char* c0, const LAS char* c1, const LAS char* n0, const LAS char* n1, FM mid, FV vm) {
    constexpr int t = U >> 2, k = U & 3, v = (U + 7) % 40;
    if (U == 33) mid();
    vm(IC<U>());
    const LAS char* p = U < 33 ? (v >> 1 & 1 ? c1 : c0) : (v >> 1 & 1 ? n1 : n0);
    if (U < 2) asm volatile("s_nop 0\n\ts_waitcnt lgkmcnt(6)\n\tv_mfma_f32_16x16x32_bf16 %0, %3, %7, 0\n\tds_read_b128 %2, %5 offset:%6\n\tv_mfma_f32_16x16x32_bf16 %1, %4, %7, 0"
                            : "=&v"(tc[0][k & 1]), "=&v"(tc[1][k & 1]), "+v"(bb[(U + 7) & 7]) : "v"(half8(wt[0][t], k >> 1)), "v"(half8(wt[1][t], k >> 1)), "v"(p), "n"((v >> 2) * 4096 + (v & 1) * 2048), "v"(bb[U & 7]) : "memory");
    else asm volatile("s_waitcnt lgkmcnt(6)\n\tv_mfma_f32_16x16x32_bf16 %0, %3, %7, %0\n\tds_read_b128 %2, %5 offset:%6\n\tv_mfma_f32_16x16x32_bf16 %1, %4, %7, %1"
                      : "+v"(tc[0][k & 1]), "+v"(tc[1][k & 1]), "+v"(bb[(U + 7) & 7]) : "v"(half8(wt[0][t], k >> 1)), "v"(half8(wt[1][t], k >> 1)), "v"(p), "n"((v >> 2) * 4096 + (v & 1) * 2048), "v"(bb[U & 7]) : "memory");
    if constexpr (U < 39) project<U + 1>(wt, tc, bb, c0, c1, n0, n1, mid, vm);
    else asm volatile("s_nop 7" : "+v"(tc[0][0]), "+v"(tc[0][1]), "+v"(tc[1][0]), "+v"(tc[1][1]));
}
__device__ __forceinline__ void project_end(u32x4 (&bb)[8]) {
    asm volatile("s_waitcnt lgkmcnt(0)" : "+v"(bb[0]), "+v"(bb[1]), "+v"(bb[2]), "+v"(bb[3]), "+v"(bb[4]), "+v"(bb[5]), "+v"(bb[6]), "+v"(bb[7]) :: "memory");
}
__device__ __forceinline__ void project0(u32x4 (&bb)[8], const LAS char* c0, const LAS char* c1) {
    ldfrag<0>(bb[0], c0); ldfrag<2048>(bb[1], c0); ldfrag<0>(bb[2], c1); ldfrag<2048>(bb[3], c1);
    ldfrag<4096>(bb[4], c0); ldfrag<6144>(bb[5], c0); ldfrag<4096>(bb[6], c1); ldfrag<6144>(bb[7], c1);
    project_end(bb);
}
constexpr int WLB = 20992;
template <bool O, class F> __device__ __forceinline__ void load_w(u32x8 (&wt)[2][10], const char* W, int hh, int w, int c0, int cd, char* st, bool once, F gap) {
    const int lane = (int)fresh_lane(), fr = lane & 15, fq = lane >> 4, x = w & 3;
    const __amdgpu_buffer_rsrc_t r = mkrsrc(W, D_ * HD * 2);
    u32x4 raw[40];
    const unsigned vo = O ? (unsigned)(lane * 16) : (unsigned)(fq * 8192 + fr * 16);
    unsigned so = O ? (unsigned)(hh * 128 + 8 * x) * 1280u : (unsigned)(8 * x * 8192 + hh * 256);
    asm volatile("" : "+s"(so));
    auto fetch1 = [&](int p, auto aux) {
#pragma unroll
        for (int i = 5 * p; i < 5 * p + 5; ++i) {
            if (O) raw[i] = __builtin_amdgcn_raw_buffer_load_b128(r, vo + (i % 5 & 3) * 1024u, so + (unsigned)((32 * (i / 10) + 4 * (i / 5 & 1)) * 1280 + (i % 5 >> 2) * 4096), decltype(aux)::v);
            else raw[i] = __builtin_amdgcn_raw_buffer_load_b128(r, vo, so + (unsigned)(32 * (i >> 1) + 4 * (i & 1)) * 8192u, decltype(aux)::v);
        }
    };
    auto fetch = [&](int p) { if (WAUX && once) fetch1(p, IC<WAUX>()); else fetch1(p, IC<0>()); };
    gap(-1);
#pragma unroll
    for (int p = 0; p < WLA; ++p) { fetch(p); BAR0(); gap(p); }
    st += (w >> 2) * WLB;
    const int row = 4 * fq + (fr >> 2);
    int wo[5];
#pragma unroll
    for (int e = 0; e < 5; ++e) {
        const int id = 64 * e + lane, rr = id / 80, ch = id - 80 * rr;
        wo[e] = O ? (4 * x + rr) * 1312 + (ch >> 1) * 32 + (ch & 1) * 4 : (4 * x + fq) * 256 + ((fr ^ (2 * ((4 * x + fq) & 7))) * 16) + e * 4096;
    }
    const int r0 = O ? row * 1312 + x * 320 + (fr & 3) * 8 : row * 256 + (((c0 + ((fr >> 1) & 1)) ^ (2 * (row & 7))) * 16) + (fr & 1) * 8;
    const int r1 = O ? 0 : row * 256 + (((c0 + cd + ((fr >> 1) & 1)) ^ (2 * (row & 7))) * 16) + (fr & 1) * 8;
#pragma unroll
    for (int p = 0; p < 8; ++p) {
        char* b = st + (p & 1) * 2 * WLB;
#pragma unroll
        for (int e = 0; e < 5; ++e) {
            const u32x4 v = raw[5 * p + e];
            if (O) {
                unsigned* d = (unsigned*)(b + wo[e]);
                d[0] = __builtin_amdgcn_perm(v[2], v[0], 0x05040100); d[2] = __builtin_amdgcn_perm(v[3], v[1], 0x05040100);
                d[4] = __builtin_amdgcn_perm(v[2], v[0], 0x07060302); d[6] = __builtin_amdgcn_perm(v[3], v[1], 0x07060302);
            } else *(u32x4*)(b + wo[e]) = v;
        }
        BAR();
        if (p + WLA < 8) fetch(p + WLA);
        SB();
        gap(p + WLA);
#pragma unroll
        for (int e = 0; e < 10; ++e) {
            const int P = O ? p : 5 * p + (e >> 1), d = 2 * (P & 3);
            const u32x2 y = ldtr(b + (O ? r0 + e * 32 : (e & 1 ? r1 : r0) + (e >> 1) * 4096));
            u32x8& t = O ? wt[P >> 2][e] : wt[e & 1][P >> 2];
            t[d] = y[0]; t[d + 1] = y[1];
        }
    }
    BAR();
}

__global__ __launch_bounds__(512, 2) B3ATTR void b3_kernel(RecArgs a) {
    extern __shared__ __attribute__((aligned(16))) char smem[];
    char* memL = smem;
    char* tokL = smem + L_TOK;
    char* hL = smem + L_H;
    int w = __builtin_amdgcn_readfirstlane(threadIdx.x >> 6);
    __amdgpu_buffer_rsrc_t rwin = mkrsrc(a.win, W_END + 2 * PAIR);
    __amdgpu_buffer_rsrc_t rws = mkrsrc(a.ctl, (unsigned)OFF_WIN);
    asm volatile("" : "+s"(rwin), "+s"(rws));
    unsigned long long tref = 0;
    const unsigned long long tstart = __builtin_amdgcn_s_memtime();
    auto get_tag = [&]() __attribute__((always_inline)) {
        unsigned long long e;
        asm volatile("s_buffer_load_dwordx2 %0, %1, 8\n\ts_waitcnt lgkmcnt(0)" : "=s"(e) : "s"(rws) : "memory");
        e += 0x9E3779B97F4A7C15ull;
        return (u32x2){(unsigned)e, (unsigned)(e >> 32)};
    };
    auto role = [&](unsigned rot) {
        const unsigned vx = blockIdx.x + rot;
        const int j = blockIdx.x >> 3, x = vx & 3, hh = 8 * (blockIdx.x & 3) + (j & 7);
        int r;
        if (!(vx & 4)) r = j < 8 ? hh : j < 16 ? 32 + hh : j < 26 ? 80 + 10 * x + j : j < 30 ? 255 - 4 * (j - 26) - x : NREC + 2 + 2 * x + j;
        else r = j < 8 ? NQT + hh : j < 16 ? 64 + hh : j < 24 ? NREC - 16 + 8 * x + j : NXT + hh;
        return r;
    };
    int bid = role(xcc_id() - blockIdx.x + BADG * (blockIdx.x >> 3));
    unsigned nh = 0, nz = 0, mc = 0, qz = 0;
    auto hint = [&](unsigned vo, unsigned so, u32x2 f) __attribute__((always_inline)) {
        const u32x2 tag = get_tag();
        for (unsigned spins = 0; spins < SPIN_CAP && __any(f[1] != tag[1] || (f[0] ^ tag[0]) > 255u); ++spins) {
            __builtin_amdgcn_s_sleep(4);
            asm volatile("" ::: "memory");
            f = __builtin_amdgcn_raw_buffer_load_b64(rwin, vo, so, 16);
        }
        return f[0] & 255u;
    };
    auto tell = [&](unsigned so, unsigned v) __attribute__((always_inline)) {
        const u32x2 tag = get_tag();
        __builtin_amdgcn_raw_buffer_store_b64((u32x2){(tag[0] & ~255u) | v, tag[1]}, rwin, 0u, so, 16);
    };
    auto share = [&](unsigned v) __attribute__((always_inline)) {
        if (w == 0) *(volatile LAS unsigned*)(smem + L_S + 64) = v;
        BAR();
        v = __builtin_amdgcn_readfirstlane(*(volatile LAS unsigned*)(smem + L_S + 64));
        BAR();
        return v;
    };
    u32x2 cf0, cf1;
    auto config0 = [&]() __attribute__((always_inline)) { cf0 = __builtin_amdgcn_raw_buffer_load_b64(rwin, fresh_lane() * 8u, W_CFG, 16); cf1 = __builtin_amdgcn_raw_buffer_load_b64(rwin, 0u, W_ARM, 16); };
    auto config = [&]() __attribute__((always_inline)) {
        unsigned v = 0;
        if (w == 0) {
            const unsigned l = fresh_lane(), f = hint(l * 8u, W_CFG, cf0), rot = __builtin_amdgcn_readfirstlane(hint(0u, W_ARM, cf1));
            const unsigned long long ok = __ballot((f & 4u) && !((l + rot) & 4u)), h = __ballot(f & 1u), q = __ballot(((f >> 1) ^ (l + rot)) & 1u);
            v = rot;
#pragma unroll
            for (int p = 0; p < 2; ++p) {
                unsigned long long m = ok & (p ? 0xFF00FF00FF00FF00ull : 0x00FF00FF00FF00FFull);
                const bool hb = 2 * __builtin_popcountll(m & h) > __builtin_popcountll(m);
                m &= hb ? h : ~h;
                v |= (hb ? 8u : 0u) << p | (2 * __builtin_popcountll(m & q) > __builtin_popcountll(m) ? 32u : 0u) << p;
            }
            if (blockIdx.x == 0) __builtin_amdgcn_raw_buffer_store_b32(f | v << 8, rws, l * 4u, 64u, 0);
        }
        v = share(v);
        nh = (v >> 3 & 1u) * HALF; nz = (v >> 4 & 1u) * HALF;
        mc = ((blockIdx.x + v) & 3u) ^ (v >> 5 & 1u); qz = v >> 6 & 1u;
        return v;
    };
    auto zpair = [&](unsigned x, unsigned k) { return (((x >> 1) * 10u + k) * 2u + ((x & 1u) ^ qz)) * PAIR + nz; };
    unsigned mo, mo1, tro;
    auto two = [&]() { const unsigned l = fresh_lane(); return l < 4 ? (l >> 1) * PAIR + (l & 1) * 4096u + nh : 0x40000000u; };
    const u32x4 SENT = {0xFFFFFFFFu, 0xFFFFFFFFu, 0xFFFFFFFFu, 0xFFFFFFFFu};
    const f32x4 z4 = {0.f, 0.f, 0.f, 0.f};
    int hrdy = 0, budget = (int)SPIN_CAP;
    unsigned fc0 = 0, fc1 = 0, fc2 = 0;
    Clk ck = {0u, 0u, 1600u, 10486u};
    int lay = 0;
    unsigned fb = 0, zb = W_ZM, hb = 0;
    if (w == 0) __builtin_amdgcn_raw_buffer_store_b64(__builtin_bit_cast(u32x2, __builtin_amdgcn_s_memrealtime()), rws, 0u, 4096u + 524288u + blockIdx.x * 8u, 0);
    unsigned tpp = 0;
    int lt = LT1, xs = XS0;
    struct Tk { unsigned vo, tw, sl; const LAS char* sp; };
    auto tk_init = [&]() {
        const unsigned l = fresh_lane();
        Tk k = {l * 16u, two(), (l & 31u) * 128u + (l >> 5) * 4u, (const LAS char*)(memL + l * 64 + 4 + ((l >> 1) & 7) * 8)};
        PIN2(k.vo, k.tw); PIN2(k.sl, k.sp);
        ck = clk_init();
        tpp = ck.ta;
        return k;
    };
    auto sched2 = [&](Tk& k, int s, unsigned f) {
        const unsigned g0 = tpp - 0x400000u;
        tpp = g0 + (unsigned)read_sched(rws, rwin, s, (unsigned)(s + 64 * lay), k.sl, tro + (unsigned)(s & 3) * 2u * PAIR, f, g0, lt, xs, fc1);
        k.sl &= ~4u;
        RT(6);
    };
    auto sched = [&](Tk& k, int s) { sched2(k, s, __builtin_amdgcn_raw_buffer_load_b32(rwin, k.sl, tro + (unsigned)(s & 3) * 2u * PAIR, 16)); };
    auto own0 = [&](int i) { return W_ZM + (nz ^ HALF) + (i ? (64u + (bid >> 2)) * PAIR + (bid & 3) * 2048u : bid * PAIR); };
    auto fetch = [&](const Tk& k, int s, bool first) {
        const unsigned sb = (unsigned)(s & 3) * 6u * PAIR, ta0 = ck.ta, mm0 = ck.ma;
        if (__builtin_expect(first, 1)) {
            while ((unsigned)sx24(tpp - TKE - 4u * (unsigned)__builtin_amdgcn_s_memrealtime()) < 20000u) __builtin_amdgcn_s_sleep(1);
            gather_at(tpp, ck, rwin, k.vo, s ? mo + sb : own0(0), s ? mo1 + sb : own0(1));
        }
        __builtin_amdgcn_s_setprio(0);
        RT(1);
        clk_rate(ck, ta0, mm0);
        unsigned tb = ck.ma - DQC;
        asm volatile(AL "s_waitcnt vmcnt(%1) lgkmcnt(0)\n\ts_barrier\n\ts_waitcnt vmcnt(%2)\n\ts_barrier" : "+s"(tb) : "n"(10 - NB1), "n"(10 - NB2) : "memory");
        return tb;
    };
    unsigned mb = 0;
    auto again = [&](bool tk, unsigned vo, unsigned& sf, int s, unsigned lim, const LAS char*& p, const LAS char*& q) __attribute__((always_inline)) {
        const unsigned nb = mb == 0x10000u ? 0x14000u : 0x10000u;
        if (tk) {
            const unsigned c = (mc + 1u + (nb >> 14 & 1u)) & 3u, sb = W_MEM + (unsigned)(s & 3) * 6u * PAIR + nh, t = sb + (4u + (c & 1u)) * PAIR + (c >> 1) * 2048u;
            dma4k(rwin, vo, sb + c * PAIR, nb); dma4k(rwin, vo, sb + c * PAIR + 4096u, nb + 4096u);
            dma1k(rwin, vo, t, nb + 8192u); dma1k(rwin, vo, t + 1024u, nb + 9216u);
            asm volatile("s_waitcnt vmcnt(10)" : "+v"(sf) :: "memory");
        }
        const bool r = mem_missing(mb, lim);
        if (tk && !r) VMWAIT();
        if (r) { p += nb - mb; q += nb - mb; mb = nb; FC(fc0); }
        return r;
    };
    auto places = [&]() {
        mo = W_MEM + mc * PAIR + nh; mo1 = W_MEM + (4u + (mc & 1u)) * PAIR + nh + (mc >> 1) * 2048u;
        tro = W_T + (mc & 1u) * PAIR + nh + (mc >> 1) * 4096u;
    };
    auto fresh = [&]() {
        asm volatile("" : "+s"(bid), "+s"(w), "+s"(nh), "+s"(nz), "+s"(mc), "+s"(qz), "+s"(lay));
        fb = lay * W_FL; hb = O_H + (1 - lay) * (4u * 40960u); zb = W_ZM + lay * (NSEG % 3 * NOWN * PAIR);
        tref = 0; hrdy = 0; budget = (int)SPIN_CAP; ck = Clk{0u, 0u, 1600u, 10486u}; tpp = 0; lt = LT1; xs = XS0;
    };
    auto filter = [&]() __attribute__((always_inline)) {
        if ((bid >= 32 && bid < 64) || (bid >= NQT && bid < NXT)) {
            const unsigned u = bid < 64 ? bid : bid - NQT, l = fresh_lane();
            filter_unit((const u16*)karg<9>(), (const u16*)karg<10>(), u * 32, rws, (u16*)tokL, w * 64 + l);
            VMWAIT();
            BAR();
            if (w == 0) __builtin_amdgcn_raw_buffer_store_b64(get_tag(), rwin, (l & 1u) * 8u, W_ST + u * 128u + 64u, 16);
        }
    };
    auto settle = [&]() __attribute__((always_inline)) {
        if (TPF && w == 1 && (bid >> 5 == 1 || bid >= NQT)) for (unsigned i = 0; i < (unsigned)OFF_WIN; i += 64u << TPF) dma1k(rws, fresh_lane() << TPF, i, L_Q);
        const int b = role(config()); if (__builtin_expect(b != bid, 0)) { bid = b; filter(); } };
    auto warm = [&]() __attribute__((always_inline)) {
        unsigned long long pc;
        unsigned len;
        asm volatile("s_getpc_b64 %0\n1:\n\ts_mov_b32 %1, 2f-1b\n\t.subsection 1\n2:\n\t.subsection 0" : "=s"(pc), "=s"(len));
        if (CPF) dma1k(mkrsrc((const void*)pc, len), fresh_lane() * 16u, (blockIdx.x >> 3) * 1024u + w * 16384u, L_END - 8192 + w * 1024);
    };
    auto a1_done = [&]() __attribute__((always_inline)) {
        VMWAIT();
        BAR();
        if (w == 0) __builtin_amdgcn_raw_buffer_store_b64(get_tag(), rwin, 0u, W_ST + (blockIdx.x - 128u >> 1) * 128u + (blockIdx.x & 1u) * 8u, 16);
        SST(29);
    };
    auto due = [&](int i, int d) __attribute__((always_inline)) {
        __builtin_amdgcn_raw_buffer_store_b32(nosent1((unsigned)__builtin_amdgcn_s_memrealtime() + d), rwin, 0u, W_E + fb + i * 128u, 16);
    };
    auto await = [&](int i, int n, int d, unsigned f = ~0u) __attribute__((always_inline)) {
        if (__any(f == ~0u)) for (unsigned spins = 0;;) {
            asm volatile("" ::: "memory");
            f = __builtin_amdgcn_raw_buffer_load_b32(rwin, min(fresh_lane(), n - 1u) * 128u, W_E + i * 128u, 16);
            if (!__any(f == ~0u)) break;
            if (spin_fail(spins, rws)) break;
            __builtin_amdgcn_s_sleep(HSLP);
        }
        while (__any(f + d - (unsigned)__builtin_amdgcn_s_memrealtime() - 1u < 1000u)) {}
    };
    constexpr int E_OUT = 0, E_A1 = 8, E_A2 = 16, E_A3 = 24, E_ZT = 28;
    auto soon = [&](int j) __attribute__((always_inline)) {
        if (w == 0) { if (j >= 4) wait_h(rws, rwin, fb, j, hrdy); else await(E_A3 + fb / 128u, 4, 0); }
        BAR();
    };
    bool slow = false;
    auto gap = [&](int p) __attribute__((always_inline)) {
        if (p == 7) config0();
        if (slow) { if (p < 0) for (int i = (bid * 5 & 31) * XST; i > 0; i -= 16) __builtin_amdgcn_s_sleep(1); else if (p < 7) __builtin_amdgcn_s_sleep(XSLP); }
    };
    SST(27);
    {
        LANES();
        const unsigned u = blockIdx.x - 128u;
        if (blockIdx.x < 128) {
            const unsigned po = (blockIdx.x & 8u ? W_END : W_PROBE) + ((blockIdx.x >> 1 & 24u) | (blockIdx.x & 7u)) * 128u;
            const bool pr = bid >= 32 && bid < 64 && w == 7;
            if (pr) __builtin_amdgcn_raw_buffer_store_b32(0u, rwin, (lane & 3) * HALF + (lane >> 2 & 1) * 4096u, po, 16);
            warm();
            filter();
            if (blockIdx.x >= 64 && w == 7) tell(W_CFG + (blockIdx.x - 64u) * 8u, pr ? 4u | probe(rwin, po) : 0u);
            BAR0();
        } else a1_unit<8, 512>(mkrsrc(karg<3>(), S_ * D_ * 2), u * 16, false, rws, smem, row_tw(lane), w, lane, [&](int k) {
            if (k == 0 && u == 0 && w == 0) { tell(W_ROT, xcc_id()); __builtin_amdgcn_raw_buffer_store_b32(0u, rws, 0u, 0u, 16); }
            if (k == -1) {
                u32x4 sv = SENT;
                PIN1(sv);
#pragma unroll
                for (int i = 0; i < 4; ++i) {
                    static_assert(ARM_END - OFF_ARM <= 512 * 8192 && W_PROBE + W_END - W_ZM <= 512 * 8192, "arming");
                    const unsigned g = (i * 128u + u) * 8192u;
                    st128(sv, rwin, tid * 16u, g < W_PROBE ? g : g < W_PROBE + W_END - W_ZM ? g + W_ZM - W_PROBE : 0x40000000u, 16);
                    st128(sv, rws, tid * 16u, g < ARM_END - OFF_ARM ? (unsigned)OFF_ARM + g : 0x40000000u, 16);
                }
            }
        });
        SST(28);
        if (blockIdx.x >= 128) {
            if (w == 0) *(volatile LAS unsigned*)(smem + L_END - 16) = hint(0u, W_ROT, __builtin_amdgcn_raw_buffer_load_b64(rwin, 0u, W_ROT, 16));
            a1_done();
            const unsigned rot = __builtin_amdgcn_readfirstlane(*(volatile LAS unsigned*)(smem + L_END - 16));
            bid = role(rot);
            if (bid == 255 && w == 0) {
                const u32x2 tag = get_tag();
                for (unsigned spins = 0; spins < SPIN_CAP; ++spins) {
                    asm volatile("" ::: "memory");
                    const u32x4 g = __builtin_amdgcn_raw_buffer_load_b128(rwin, lane * 128u, W_ST, 16);
                    if (!__any(((g[0] ^ tag[0]) | (g[1] ^ tag[1]) | (g[2] ^ tag[0]) | (g[3] ^ tag[1])) != 0u)) break;
                    __builtin_amdgcn_s_sleep(4);
                }
                tell(W_ARM, rot);
            }
            SST(30);
        }
    }
    if (bid < 96 || (bid >= NQT && bid < NWRK)) {
    u32x8 wt[2][10];
#pragma nounroll
    for (int li = 0; li < 4; li += bid >= NXT ? 1 : 2) {
    lay = li >> 1;
    fresh();
    const bool xt = bid >= NXT, ph = li & 1;
    SST(0);
    if ((bid >= 32 && bid < 96) || xt) {
        const int hh = (xt ? bid - NXT : bid) & 31;
        if (li == 0 || ph) {
            u32x4 m0[2];
            get_mem0(m0, w * 64 + fresh_lane());
            wait_until(tstart + (bid < 64 ? DVO : xt ? DXT : DW1));
            const bool once = !slow && (NTM >> (xt ? 4 : bid >> 5) & 1);
            if (w < 4) load_w<false>(wt, karg<7>(), hh, w, 2 * w, 8, tokL, once, gap); else load_w<true>(wt, karg<8>(), hh, w, 0, 0, tokL, once, gap);
            SST(5);
            slow = true;
            if (li == 0) settle();
            if (bid < 64) put_mem0(m0, rwin, own0(0), own0(1), w * 64 + fresh_lane());
        }
        places();
        const bool vt = bid >= 64;
        const unsigned long long mv = xt ? MXV & (ph ? ~0xFFFFFFFFull : 0xFFFFFFFFull) : ~MXV;
        auto nextv = [&](int j) { return min(seg_after(mv, j), NSEG - 1); };
        const int jv1 = nextv(-1), jv2 = nextv(jv1);
        {
            LANES();
            VMWAIT();
            BAR0();
            if (tid < 8) *(unsigned*)(smem + L_S + tid * 4) = 0u;
            SST(1);
            if (vt) {
                soon(ph ? jv2 : jv1);
                SST(2);
                const unsigned so = hb + (unsigned)(jv1 * 40960 + w * 1024), lo = L_H + w * 1024;
                stage_h<5, 8192>(rws, so, lo, lane);
                BAR0();
                if (w < 4) check_h<5, 8192, 0>(rws, so, lo, lane);
                else { stage_h<10, 4096>(rws, hb + (unsigned)(jv2 * 40960 + (w - 4) * 1024), lo + 36864u, lane); check_h<5, 8192, 10>(rws, so, lo, lane); }
            }
        }
        auto steps = [&](auto cc) {
            constexpr int C = decltype(cc)::v & 1;
            constexpr bool TK = decltype(cc)::v == 3;
            LANES();
            Tk tk = {0u, 0u, 0x40000000u, nullptr};
            if (TK) { tk = tk_init(); __builtin_amdgcn_s_setprio(TKP); }
            const unsigned zi = (unsigned)(hh * 128 + fr * 8) * 2u;
            unsigned zo0 = zpair(w & 3, fq) + zi, zo1 = zo0 + 8 * PAIR, zo2 = zpair(w & 3, 8 + (fq >> 1)) + zi + (fq & 1) * 8;
            const LAS char* va = (const LAS char*)(smem + L_Q + (fr & 1) * 544 + ((fr >> 1) & 1) * 1152 + (16 * fq + 2 * (fr >> 2)) * 4);
            LAS char* vw = (LAS char*)(smem + L_Q + (fq & 1) * 544 + (fq >> 1) * 1152 + (16 * w + bs2(fr)) * 4);
            const LAS char* mp0 = (const LAS char*)(memL + ma0);
            PIN2(zo0, zo1); PIN1(zo2);
            if (C == 0) { PIN2(mp0, vw); } else PIN1(va);
            u32x4 za = SENT, zc = SENT;
            u32x2 ze = {0u, 0u};
            auto put_z2 = [&](int s) {
                const unsigned z2b = s < NSEG ? O_Z2 + (unsigned)s * (NOWN * 8192u) + (unsigned)(10 * (w & 3) * 8192 + hh * 256) : 0x40000000u, l2 = fresh_lane();
                st128(za, rws, (l2 >> 4) * 8192u + (l2 & 15u) * 16u, z2b, 16);
                st128(zc, rws, (l2 >> 4) * 8192u + (l2 & 15u) * 16u, z2b + 4u * 8192u, 16);
                __builtin_amdgcn_raw_buffer_store_b64(ze, rws, (l2 >> 5) * 8192u + (l2 & 15u) * 16u + ((l2 >> 4) & 1u) * 8u, z2b + 8u * 8192u, 16);
            };
            for (int s = 0; s < NSEG; ++s) {
                RT(0);
                bool redo = false;
                unsigned sf = 0;
                do {
                    unsigned lim = s > 0 ? poll_lim(budget) : ~0u;
                    unsigned long long sus;
                    asm volatile("" : "+s"(lim));
                    if (C == 0) {
                        asm volatile(
                            "v_mov_b64 v[200:201], 0\n\t"
                            "v_mov_b64 v[202:203], 0\n\t"
                            "v_mov_b64 v[204:205], 0\n\t"
                            "v_mov_b64 v[206:207], 0\n\t"
                            AL "s_setprio 2\n\t"
                            "s_waitcnt lgkmcnt(0)\n\t"
                            "s_barrier\n\t"
                            MTA(0)
                            "ds_read_b128 v[212:215], %[mp] offset:0\n\t"
                            "ds_read_b128 v[216:219], %[mp] offset:1024\n\t"
                            "ds_read_b128 v[220:223], %[mp] offset:2048\n\t"
                            "s_barrier\n\t"
                            "ds_read_b128 v[224:227], %[mp] offset:3072\n\t"
                            "ds_read_b128 v[228:231], %[mp] offset:4096\n\t"
                            "ds_read_b128 v[232:235], %[mp] offset:5120\n\t"
                            "s_waitcnt lgkmcnt(5)\n\t"
                            "v_smfmac_f32_16x16x64_bf16 v[200:203], v[212:215], %[w00], %[ix]\n\t"
                            "v_smfmac_f32_16x16x64_bf16 v[204:207], v[212:215], %[w10], %[ix]\n\t"
                            "s_waitcnt lgkmcnt(4)\n\t"
                            "v_smfmac_f32_16x16x64_bf16 v[200:203], v[216:219], %[w01], %[ix]\n\t"
                            "v_smfmac_f32_16x16x64_bf16 v[204:207], v[216:219], %[w11], %[ix]\n\t"
                            "s_barrier\n\t"
                            MTA(1)
                            "ds_read_b128 v[236:239], %[mp] offset:6144\n\t"
                            "ds_read_b128 v[240:243], %[mp] offset:7168\n\t"
                            "s_waitcnt lgkmcnt(5)\n\t"
                            "v_smfmac_f32_16x16x64_bf16 v[200:203], v[220:223], %[w02], %[ix]\n\t"
                            "ds_read_b128 v[244:247], %[mp] offset:8192\n\t"
                            "ds_read_b128 v[248:251], %[mp] offset:9216\n\t"
                            "s_waitcnt lgkmcnt(4)\n\t"
                            "v_smfmac_f32_16x16x64_bf16 v[200:203], v[224:227], %[w03], %[ix]\n\t"
                            "v_smfmac_f32_16x16x64_bf16 v[200:203], v[228:231], %[w04], %[ix]\n\t"
                            "v_smfmac_f32_16x16x64_bf16 v[200:203], v[232:235], %[w05], %[ix]\n\t"
                            "s_waitcnt lgkmcnt(3)\n\t"
                            "v_smfmac_f32_16x16x64_bf16 v[200:203], v[236:239], %[w06], %[ix]\n\t"
                            "s_waitcnt lgkmcnt(2)\n\t"
                            "v_smfmac_f32_16x16x64_bf16 v[200:203], v[240:243], %[w07], %[ix]\n\t"
                            "s_waitcnt lgkmcnt(1)\n\t"
                            "v_smfmac_f32_16x16x64_bf16 v[200:203], v[244:247], %[w08], %[ix]\n\t"
                            "s_waitcnt lgkmcnt(0)\n\t"
                            "v_smfmac_f32_16x16x64_bf16 v[200:203], v[248:251], %[w09], %[ix]\n\t"
                            "v_smfmac_f32_16x16x64_bf16 v[204:207], v[220:223], %[w12], %[ix]\n\t"
                            "s_nop 2\n\t"
                            "v_smfmac_f32_16x16x64_bf16 v[204:207], v[224:227], %[w13], %[ix]\n\t"
                            "s_nop 2\n\t"
                            "v_add_f32 v200, v200, v202\n\t"
                            "v_add_f32 v201, v201, v203\n\t"
                            "v_smfmac_f32_16x16x64_bf16 v[204:207], v[228:231], %[w14], %[ix]\n\t"
                            "v_cvt_pk_bf16_f32 v200, v200, v201\n\t"
                            "ds_write_b32 %[vw], v200\n\t"
                            MTA(2)
                            "v_smfmac_f32_16x16x64_bf16 v[204:207], v[232:235], %[w15], %[ix]\n\t"
                            "v_smfmac_f32_16x16x64_bf16 v[204:207], v[236:239], %[w16], %[ix]\n\t"
                            "v_smfmac_f32_16x16x64_bf16 v[204:207], v[240:243], %[w17], %[ix]\n\t"
                            "v_smfmac_f32_16x16x64_bf16 v[204:207], v[244:247], %[w18], %[ix]\n\t"
                            "s_waitcnt lgkmcnt(0)\n\t"
                            "s_barrier\n\t"
                            MTA(5)
                            "v_smfmac_f32_16x16x64_bf16 v[204:207], v[248:251], %[w19], %[ix]\n\t"
                            "s_nop 7\n\t"
                            "v_pk_add_f32 v[204:205], v[204:205], v[206:207]\n\t"
                            "v_cvt_pk_bf16_f32 v200, v204, v205\n\t"
                            "ds_write_b32 %[vw], v200 offset:256\n\t"
                            MTA(3)
                            "s_waitcnt vmcnt(0) lgkmcnt(0)\n\t"
                            "s_barrier\n\t"
                            "s_setprio 0\n\t"
                            "v_cmp_u_f32 %[sus], v204, v205\n\t"
                            : [sus] "=s"(sus) : [mp] "v"(mp0), [vw] "v"(vw), [ix] "v"(sidx),
                              [w00] "v"(wt[0][0]), [w01] "v"(wt[0][1]), [w02] "v"(wt[0][2]), [w03] "v"(wt[0][3]), [w04] "v"(wt[0][4]), [w05] "v"(wt[0][5]), [w06] "v"(wt[0][6]), [w07] "v"(wt[0][7]), [w08] "v"(wt[0][8]), [w09] "v"(wt[0][9]), [w10] "v"(wt[1][0]), [w11] "v"(wt[1][1]), [w12] "v"(wt[1][2]), [w13] "v"(wt[1][3]), [w14] "v"(wt[1][4]), [w15] "v"(wt[1][5]), [w16] "v"(wt[1][6]), [w17] "v"(wt[1][7]), [w18] "v"(wt[1][8]), [w19] "v"(wt[1][9])
                            : "v200", "v201", "v202", "v203", "v204", "v205", "v206", "v207", "v208", "v209", "v212", "v213", "v214", "v215", "v216", "v217", "v218", "v219", "v220", "v221", "v222", "v223", "v224", "v225", "v226", "v227", "v228", "v229", "v230", "v231", "v232", "v233", "v234", "v235", "v236", "v237", "v238", "v239", "v240", "v241", "v242", "v243", "v244", "v245", "v246", "v247", "v248", "v249", "v250", "v251", "vcc", "memory");
                        MT(4);
                    } else {
                        f32x16 ca, cb;
                        f32x8 cc;
                        asm volatile(".irp r,212,214,216,218,220,222,224,226,228,230,232,234,236,238,240,242,244,246,248,250\n\tv_mov_b64 v[\\r:\\r+1], 0\n\t.endr"
                                     : "={v[212:227]}"(ca), "={v[228:243]}"(cb), "={v[244:251]}"(cc));
                        unsigned sr = tro + (unsigned)(s & 3) * 2u * PAIR;
                        asm volatile("" : "+s"(sr));
                        if (TK) { fetch(tk, s, !redo); asm volatile("s_waitcnt vmcnt(0)\n\ts_barrier" ::: "memory"); RT(3); }
                        else asm volatile("s_waitcnt lgkmcnt(0)\n\ts_barrier\n\ts_barrier\n\ts_barrier" ::: "memory");
                        unsigned zs = zb;
                        asm volatile(
                            AL "s_setprio 1\n\t"
                            "s_waitcnt vmcnt(0) lgkmcnt(0)\n\t"
                            "s_barrier\n\t"
                            "ds_read_b64_tr_b16 v[192:193], %[va]\n\t"
                            "ds_read_b64_tr_b16 v[194:195], %[va] offset:32\n\t"
                            "s_waitcnt lgkmcnt(0)\n\t"
                            "v_smfmac_f32_16x16x64_bf16 v[212:215], v[192:195], %[w0], %[ix]\n\t"
                            "v_smfmac_f32_16x16x64_bf16 v[220:223], v[192:195], %[w2], %[ix]\n\t"
                            "v_smfmac_f32_16x16x64_bf16 v[216:219], v[192:195], %[w1], %[ix]\n\t"
                            "v_smfmac_f32_16x16x64_bf16 v[224:227], v[192:195], %[w3], %[ix]\n\t"
                            "s_barrier\n\t"
                            MTA(4)
                            "ds_read_b64_tr_b16 v[196:197], %[va] offset:256\n\t"
                            "v_smfmac_f32_16x16x64_bf16 v[228:231], v[192:195], %[w4], %[ix]\n\t"
                            "ds_read_b64_tr_b16 v[198:199], %[va] offset:288\n\t"
                            "v_smfmac_f32_16x16x64_bf16 v[236:239], v[192:195], %[w6], %[ix]\n\t"
                            "v_smfmac_f32_16x16x64_bf16 v[232:235], v[192:195], %[w5], %[ix]\n\t"
                            "v_smfmac_f32_16x16x64_bf16 v[240:243], v[192:195], %[w7], %[ix]\n\t"
                            "v_cmp_u_f32 %[sus], v212, v213\n\t"
                            "v_smfmac_f32_16x16x64_bf16 v[244:247], v[192:195], %[w8], %[ix]\n\t"
                            "v_smfmac_f32_16x16x64_bf16 v[248:251], v[192:195], %[w9], %[ix]\n\t"
                            "s_waitcnt lgkmcnt(0)\n\t"
                            "v_smfmac_f32_16x16x64_bf16 v[212:215], v[196:199], %[x0], %[ix]\n\t"
                            "s_nop 0\n\t"
                            "v_smfmac_f32_16x16x64_bf16 v[220:223], v[196:199], %[x2], %[ix]\n\t"
                            "s_cmp_lg_u64 %[sus], 0\n\t"
                            "s_cselect_b32 %[zs], 2.0, %[zs]\n\t"
                            "v_smfmac_f32_16x16x64_bf16 v[216:219], v[196:199], %[x1], %[ix]\n\t"
                            "s_nop 1\n\t"
                            "v_smfmac_f32_16x16x64_bf16 v[224:227], v[196:199], %[x3], %[ix]\n\t"
                            "v_add_f32 v212, v212, v214\n\t"
                            "v_smfmac_f32_16x16x64_bf16 v[228:231], v[196:199], %[x4], %[ix]\n\t"
                            "v_add_f32 v213, v213, v215\n\t"
                            "v_smfmac_f32_16x16x64_bf16 v[236:239], v[196:199], %[x6], %[ix]\n\t"
                            "v_add_f32 v220, v220, v222\n\t"
                            "v_smfmac_f32_16x16x64_bf16 v[232:235], v[196:199], %[x5], %[ix]\n\t"
                            "v_add_f32 v221, v221, v223\n\t"
                            "v_smfmac_f32_16x16x64_bf16 v[240:243], v[196:199], %[x7], %[ix]\n\t"
                            "v_cvt_pk_bf16_f32 v200, v212, v213\n\t"
                            "v_smfmac_f32_16x16x64_bf16 v[244:247], v[196:199], %[x8], %[ix]\n\t"
                            "v_cvt_pk_bf16_f32 v201, v220, v221\n\t"
                            "v_smfmac_f32_16x16x64_bf16 v[248:251], v[196:199], %[x9], %[ix]\n\t"
                            ".if %[tk]\n\tbuffer_load_dword %[sf], %[sl], %[rw], %[sr] offen sc1\n\t.endif\n\t"
                            "v_add_f32 v216, v216, v218\n\t"
                            "v_add_f32 v217, v217, v219\n\t"
                            "v_add_f32 v224, v224, v226\n\t"
                            "v_add_f32 v225, v225, v227\n\t"
                            "v_cvt_pk_bf16_f32 v202, v216, v217\n\t"
                            "v_cvt_pk_bf16_f32 v203, v224, v225\n\t"
                            "v_permlane32_swap_b32 v200, v201\n\t"
                            "v_pk_add_f32 v[228:229], v[228:229], v[230:231]\n\t"
                            "v_permlane32_swap_b32 v202, v203\n\t"
                            "v_min_u32 v201, -2, v201\n\t"
                            "v_pk_add_f32 v[236:237], v[236:237], v[238:239]\n\t"
                            "v_min_u32 v203, -2, v203\n\t"
                            "v_permlane16_swap_b32 v200, v202\n\t"
                            "v_pk_add_f32 v[232:233], v[232:233], v[234:235]\n\t"
                            "v_permlane16_swap_b32 v201, v203\n\t"
                            "v_pk_add_f32 v[240:241], v[240:241], v[242:243]\n\t"
                            MTA(5)
                            "buffer_store_dwordx4 v[200:203], %[z0], %[rw], %[zs] offen sc1\n\t"
                            "v_cvt_pk_bf16_f32 v204, v228, v229\n\t"
                            "v_cvt_pk_bf16_f32 v205, v236, v237\n\t"
                            "v_cvt_pk_bf16_f32 v206, v232, v233\n\t"
                            "v_cvt_pk_bf16_f32 v207, v240, v241\n\t"
                            "v_permlane32_swap_b32 v204, v205\n\t"
                            "v_pk_add_f32 v[244:245], v[244:245], v[246:247]\n\t"
                            "v_permlane32_swap_b32 v206, v207\n\t"
                            "v_min_u32 v205, -2, v205\n\t"
                            "v_pk_add_f32 v[248:249], v[248:249], v[250:251]\n\t"
                            "v_min_u32 v207, -2, v207\n\t"
                            "v_permlane16_swap_b32 v204, v206\n\t"
                            "v_cvt_pk_bf16_f32 v208, v244, v245\n\t"
                            "v_permlane16_swap_b32 v205, v207\n\t"
                            "v_cvt_pk_bf16_f32 v209, v248, v249\n\t"
                            "buffer_store_dwordx4 v[204:207], %[z1], %[rw], %[zs] offen sc1\n\t"
                            "s_nop 0\n\t"
                            "v_permlane32_swap_b32 v208, v209\n\t"
                            "v_min_u32 v209, -2, v209\n\t"
                            "buffer_store_dwordx2 v[208:209], %[z2], %[rw], %[zs] offen sc1\n\t"
                            ".if %[tk]\n\ts_setprio 0\n\ts_cmp_lg_u64 %[sus], 0\n\ts_cbranch_scc1 1f\n\ts_waitcnt vmcnt(3)\n1:\n\t.endif"
                            : [sf] "+&v"(sf), [zs] "+&s"(zs), [sus] "=&s"(sus), "=&{v[200:203]}"(za), "=&{v[204:207]}"(zc), "=&{v[208:209]}"(ze), "+{v[212:227]}"(ca), "+{v[228:243]}"(cb), "+{v[244:251]}"(cc)
                            : [va] "v"(va), [ix] "v"(sidx), [sl] "v"(tk.sl), [sr] "s"(sr), [rw] "s"(rwin), [z0] "v"(zo0), [z1] "v"(zo1), [z2] "v"(zo2), [tk] "n"(TK ? 1 : 0),
                              [w0] "v"(wt[0][0]), [w1] "v"(wt[0][1]), [w2] "v"(wt[0][2]), [w3] "v"(wt[0][3]), [w4] "v"(wt[0][4]), [w5] "v"(wt[0][5]), [w6] "v"(wt[0][6]), [w7] "v"(wt[0][7]), [w8] "v"(wt[0][8]), [w9] "v"(wt[0][9]),
                              [x0] "v"(wt[1][0]), [x1] "v"(wt[1][1]), [x2] "v"(wt[1][2]), [x3] "v"(wt[1][3]), [x4] "v"(wt[1][4]), [x5] "v"(wt[1][5]), [x6] "v"(wt[1][6]), [x7] "v"(wt[1][7]), [x8] "v"(wt[1][8]), [x9] "v"(wt[1][9])
                            : "v192", "v193", "v194", "v195", "v196", "v197", "v198", "v199", "scc", "memory");
                        RT(4);
                        MT(6);
                        tref = __builtin_amdgcn_s_memtime();
                        __builtin_amdgcn_s_setprio(TKP);
                    }
                    redo = false;
                    if (__builtin_expect(sus != 0, 0)) {
                        redo = again(TK, tk.vo, sf, s, lim, mp0, tk.sp);
                        if (!redo && C == 1) { st128(za, rwin, zo0, zb, 16); st128(zc, rwin, zo1, zb, 16); __builtin_amdgcn_raw_buffer_store_b64(ze, rwin, zo2, zb, 16); }
                    }
                } while (__builtin_expect(redo, 0));
                if (__builtin_expect(mb != 0, 0)) { mp0 -= mb; mb = 0; }
                if (s < 8) SST(8 + s);
                if (C == 0) {
                    if (s + 1 < NSEG) {
                        __builtin_amdgcn_s_sleep(DZR);
                        const unsigned zq = s == 0 ? 0x40000000u : zb == W_ZM ? W_ZM + 2u * NOWN * PAIR : zb - NOWN * PAIR;
                        unsigned s1;
                        asm volatile("v_mov_b32 %0, -1" : "=v"(s1));
                        st128(((u32x4){s1, s1, s1, s1}), rwin, zo0, zq, 16);
                        st128(((u32x4){s1, s1, s1, s1}), rwin, zo1, zq, 16);
                        __builtin_amdgcn_raw_buffer_store_b64((u32x2){s1, s1}, rwin, zo2, zq, 16);
                    }
                } else {
                    if (TK && s + 1 < NSEG) sched2(tk, s, sf);
                    if (!TK) wait_until(tref + DZ2);
                    if (s + 2 < NSEG) put_z2(s);
                }
                if (budget <= 0) __builtin_amdgcn_raw_buffer_store_b32(1u, rws, 0u, 0u, 16);
                zb = zb == W_ZM + 2u * NOWN * PAIR ? W_ZM : zb + NOWN * PAIR;
                RT(5);
                RTS();
                MT(7);
                MTS();
            }
        };
        if (!vt) {
            if (w < 4) steps(IC<0>()); else if (w < 7) steps(IC<1>()); else steps(IC<3>());
        } else {
            BAR();
            SST(3);
            LANES();
            const int ni = __builtin_popcountll(mv) + 1;
            if (w < 4) {
                const LAS char* c0 = (const LAS char*)(hL + hoff + ((fq ^ (fr & 7)) << 4));
                const LAS char* c1 = (const LAS char*)(hL + hoff + (((4 + fq) ^ (fr & 7)) << 4));
                const LAS char* n0 = c0 + 40960; const LAS char* n1 = c1 + 40960;
                u32x4 bb[8];
                project0(bb, c0, c1);
                for (int i = 1; i < ni; ++i) {
                    if (vt) MT(0);
                    f32x4 tc[2][2];
                    project<0>(wt, tc, bb, c0, c1, n0, n1,
                        [&]() {
                            MT(1);
                            if (__builtin_expect(i > 1, 1)) BAR();
                            MT(2);
                        }, [](auto) {});
                    { const LAS char* x = c0; c0 = n0; n0 = x; x = c1; c1 = n1; n1 = x; }
                    char* tb = tokL + (i & 1) * 32 * QS + (16 * w + 4 * fq) * 2;
#pragma unroll
                    for (int g = 0; g < 2; ++g)
#pragma unroll
                        for (int tt = 0; tt < 2; ++tt) *(u32x2*)(tb + (prow + 8 * tt) * QS + g * 128) = (u32x2){pack2(tc[g][tt][0], tc[g][tt][1]), pack2(tc[g][tt][2], tc[g][tt][3])};
                    asm volatile("" ::: "memory");
                    *(volatile LAS int*)(smem + L_S + 16 + w * 4) = i;
                    if (__builtin_expect(i == 1, 0)) { BAR(); if (VSLP) __builtin_amdgcn_s_sleep(VSLP); project0(bb, c0, c1); }
                    if (vt) { MT(3); const int s = i & 63; MTRT(); MTS(); }
                }
                project_end(bb);
            } else {
                __builtin_amdgcn_s_setprio(OPR);
                const unsigned hv = lane * 16u, fz_ = W_FZ + fb + (unsigned)(hh * 128 + xt * 4096 + (w - 4) * 8192);
                unsigned fp = jv1 - 1;
                u32x4 r = SENT;
                int jo = jv1, jh = jv2;
                check_h<10, 4096, 0>(rws, hb + (unsigned)(jv2 * 40960 + (w - 4) * 1024), L_H + 40960 + (w - 4) * 1024, lane);
                SST(16);
                auto rows = [&](int b) {
                    for (int t = 0; t < 10; ++t) dma1k(rws, hv, hb + (unsigned)(jh * 40960 + (w - 4) * 1024 + t * 4096), (unsigned)(L_H + ((b + 1) & 1) * 40960 + (w - 4) * 1024 + t * 4096));
                    VMWAIT();
                };
                for (int i = 1; i < ni; ++i, jo = nextv(jo)) {
                    const int b = i + (i >= ZLAG);
                    if (__builtin_expect(i == ZLAG, 0)) {
                        VMWAIT();
                        jh = nextv(jh);
                        wait_h(rws, rwin, fb, jh, hrdy);
                        BAR0();
                        rows(i);
                    }
                    jh = nextv(jh);
                    u32x4 hf;
                    asm volatile("" : "=v"(hf));
                    if (__builtin_expect(hrdy <= jh, 0)) asm volatile("buffer_load_dwordx4 %0, %1, %2, %3 offen sc1" : "+v"(hf) : "v"(min(lane, jh + 16) * 128), "s"(rwin), "s"(W_H + fb) : "memory");
                    if (b < ni) BAR();
                    if (i == 1) SST(17);
                    if (vt) MT(1);
                    for (;;) {
                        const u32x4 f = *(volatile LAS u32x4*)(smem + L_S + 16);
                        if (__builtin_amdgcn_readfirstlane(min(min(f[0], f[1]), min(f[2], f[3]))) >= (unsigned)i) break;
                        __builtin_amdgcn_s_sleep(1);
                    }
                    asm volatile("" ::: "memory");
                    u32x4 vf[2][4];
#pragma unroll
                    for (int ks = 0; ks < 4; ++ks)
#pragma unroll
                        for (int t = 0; t < 2; ++t) vf[t][ks] = *(const u32x4*)(tokL + (i & 1) * 32 * QS + (16 * t + fr) * QS + ks * 64 + fq * 16);
                    SB();
                    if (__builtin_expect(hrdy <= jh, 0)) { asm volatile("s_waitcnt vmcnt(0)" : "+v"(hf) :: "memory"); hrdy = min(see_h(hf), jh + 17); }
                    const bool ok = hrdy > jh;
                    if (__builtin_expect((i < ZLAG || !ok) && (int)fp > ZEARLY, 0)) { VMWAIT(); __builtin_amdgcn_raw_buffer_store_b32(fp, rwin, 0u, fz_, 16); }
                    if (__builtin_expect(jo == 0, 0) && w == 7) {
                        if (!(hh & 7)) due(E_ZT + (hh >> 3), lay ? DZF : DZF0);
                        __builtin_amdgcn_raw_buffer_store_b32(((4u * (unsigned)__builtin_amdgcn_s_memrealtime() + 4 * (lay ? DZF : DZF0) + DV0) & 0xFFFFFFu) | (unsigned)lay << 30, rwin, two(), W_T + hh * 128u + 4u, 16);
                    }
                    if (XDL && __builtin_expect(jo == 1, 0)) __builtin_amdgcn_s_sleep(XDL);
                    if (i == 1) SST(18);
                    if (vt) MT(0);
                    const unsigned ztb = O_Z + (unsigned)(jo * ZTSEG) * 2u + (unsigned)(10 * (w - 4) * H_ + hh) * 1024u;
                    f32x4 c[2][2];
                    unsigned hs = hb + (unsigned)(jh * 40960 + (w - 4) * 1024), hd = (unsigned)(L_H + ((b + 1) & 1) * 40960 + (w - 4) * 1024);
                    asm volatile("" : "+s"(hs), "+s"(hd));
#pragma unroll
                    for (int nt = 0; nt < 11; ++nt) {
                        auto& cc = c[nt & 1]; auto& cp = c[(nt + 1) & 1];
#pragma unroll
                        for (int ks = 0; ks < 4; ++ks) {
                            if (nt < 10) { cc[0] = mfma32(vf[0][ks], half8(wt[ks >> 1][nt], ks & 1), ks ? cc[0] : z4); PIN1(cc[0]); }
                            if (nt > 0 && ks >= 1 && ks < 3) { r[2 * ks - 2] = pack2(cp[ks - 1][0], cp[ks - 1][1]); PIN1(r); }
                            if (nt < 10) { cc[1] = mfma32(vf[1][ks], half8(wt[ks >> 1][nt], ks & 1), ks ? cc[1] : z4); PIN1(cc[1]); }
                            if (nt > 0 && ks >= 1 && ks < 3) { r[2 * ks - 1] = pack2(cp[ks - 1][2], cp[ks - 1][3]); PIN1(r); }
                            if (nt > 0 && ks == 2) st128(r, rws, lane * 16u, ztb + (unsigned)((nt - 1) * H_) * 1024u, 16);
                            if (nt < 5 && !(ks & 1) && __builtin_expect(ok, 1)) dma1k(rws, hv, hs + (2 * nt + (ks >> 1)) * 4096u, hd + (2 * nt + (ks >> 1)) * 4096u);
                            if (nt == 5 && ks == 0 && __builtin_expect(ok, 1)) {
                                asm volatile("s_waitcnt vmcnt(14)" ::: "memory");
                                __builtin_amdgcn_raw_buffer_store_b32(fp, rwin, 0u, fz_, 16);
                            }
                            SB();
                        }
                    }
                    if (i == 1) SST(19);
                    if (vt) MT(2);
                    fp = (unsigned)jo;
                    if (__builtin_expect(jo <= ZEARLY || i + 1 == ni || i + 1 == ZLAG || !ok, 0)) {
                        VMWAIT();
                        __builtin_amdgcn_raw_buffer_store_b32(fp, rwin, 0u, fz_, 16);
                    } else asm volatile("s_waitcnt vmcnt(7)" ::: "memory");
                    if (i == 1) SST(20);
                    if (__builtin_expect(!ok, 0)) { wait_h(rws, rwin, fb, jh, hrdy); rows(b); }
                    if (jo < 8) SST(8 + jo);
                    if (vt && (!BSPARSE || jo % 6 == 2)) { const int s = jo; RT(0); RTS(); }
                    if (vt) { MT(3); const int s = i & 63; MTRT(); MTS(); }
                }
            }
            __builtin_amdgcn_s_setprio(0);
            VMWAIT();
            BAR();
        }
    }
    if ((bid < 32 || bid >= NQT) && !ph) {
        LANES();
        const int hh = (bid < 32 ? bid : bid - NQT) & 31;
        if (lay == 0 || xt) {
            u32x4 m0[2];
            get_mem0(m0, tid);
            wait_until(tstart + (bid >= NQT ? DQT : DW1));
            load_w<false>(wt, karg<5>(w >> 2), hh, w, 4 * (w & 3), 2, tokL, !slow && (NTM >> (bid < 32 ? 0 : 3) & 1), gap);
            SST(5);
            if (!xt) settle();
            if (bid < 64) put_mem0(m0, rwin, own0(0), own0(1), tid);
        }
        places();
        const bool qt = bid >= NQT;
        const unsigned long long mq = xt ? MXQ : ~MXQ;
        auto nextq = [&](int j) { return min(seg_after(mq, j), NSEG - 1); };
        const int jq0 = qt ? nextq(1) : 0, jq1 = nextq(jq0);
        if (tid < 4) *(unsigned*)(smem + L_S + tid * 4) = 0u;
        SST(1);
        VMWAIT();
        soon(jq1);
        SST(2);
        const unsigned so1 = hb + (unsigned)(jq1 * 40960 + w * 1024), lo1 = L_H + 40960 + w * 1024;
        stage_h<5, 8192>(rws, hb + (unsigned)(jq0 * 40960 + w * 1024), lo1 - 40960u, lane);
        BAR0();
        stage_h<5, 8192>(rws, so1, lo1, lane);
        dma1k(rwin, lane * 16u, qt ? 0x40000000u : own0(0) + w * 1024u, w * 1024u); dma1k(rwin, lane * 16u, qt ? 0x40000000u : own0(1) + (w & 1) * 1024u, 8192u + (w & 1) * 1024u);
        check_h<5, 8192, 7>(rws, hb + (unsigned)(jq0 * 40960 + w * 1024), lo1 - 40960u, lane);
        BAR();
        SST(3);
        int qrdy = 0;
        {
            LANES();
            const unsigned to0 = (unsigned)((w ^ 4) >> 2) * 8192u + (unsigned)((w & 3) * 64 + (lane ^ ((fq & 1) * (w < 4 ? 8 : 5)))) * 16u, to1 = to0 + 4096u, hv = lane * 16u;
            const LAS char* c0 = (const LAS char*)(hL + hoff + ((fq ^ (fr & 7)) << 4));
            const LAS char* c1 = (const LAS char*)(hL + hoff + (((4 + fq) ^ (fr & 7)) << 4));
            u32x4 bb[8], g0, g1;
            f32x4 tc[2][2];
            auto pack = [&]() {
                g0 = (u32x4){pack2(tc[0][0][0], tc[0][0][1]), pack2(tc[0][0][2], tc[0][0][3]), pack2(tc[1][0][0], tc[1][0][1]), pack2(tc[1][0][2], tc[1][0][3])};
                g1 = (u32x4){pack2(tc[0][1][0], tc[0][1][1]), pack2(tc[0][1][2], tc[0][1][3]), pack2(tc[1][1][0], tc[1][1][1]), pack2(tc[1][1][2], tc[1][1][3])};
            };
            const LAS char* n0 = c0 + 40960; const LAS char* n1 = c1 + 40960;
            if (!qt) {
                project0(bb, c0, c1);
                for (int j = 0; j < 2; ++j) {
                    project<0>(wt, tc, bb, c0, c1, n0, n1, [&]() { if (j == 0) { check_h<5, 8192, 0>(rws, so1, lo1, lane); BAR(); } }, [](auto) {});
                    pack();
                    *(u32x4*)(tokL + j * 16384 + to0) = g0; *(u32x4*)(tokL + j * 16384 + to1) = g1;
                    c0 = n0; c1 = n1;
                    if (j == 0 && w == 7) __builtin_amdgcn_raw_buffer_store_b32(((4u * (unsigned)__builtin_amdgcn_s_memrealtime() + (lay ? DQ0 : DQ00)) & 0xFFFFFFu) | (unsigned)lay << 30, rwin, two(), W_T + hh * 128u, 16);
                }
                project_end(bb);
                VMWAIT();
            } else {
                const unsigned fq_ = W_FQ + fb + (unsigned)(hh * 128 + xt * 64), fv = tid == 0 ? 0u : 0x40000000u;
                const int jl = 63 - __builtin_clzll(mq);
                auto rounds = [&](auto ld) {
                constexpr bool LD = decltype(ld)::v;
                unsigned hs = 0x40000000u, hd = (unsigned)(L_H + 40960 + w * 1024);
                if (LD) __builtin_amdgcn_s_setprio(QTP);
                u32x4 bb[8];
                project0(bb, c0, c1);
                asm volatile("" : "=v"(g0), "=v"(g1));
                for (int jp = 1, j = jq0, jn = jq1;; jp = j, j = jn, jn = nextq(jn)) {
                    MT(0);
                    project<0>(wt, tc, bb, c0, c1, n0, n1,
                        [&]() {
                            unsigned fj = (unsigned)jp;
                            if (LD) {
                                if (__builtin_expect(hrdy <= nextq(jn), 0)) hrdy = min(see_h(g0), nextq(jn) + 17);
                                hs = hrdy > nextq(jn) ? hb + (unsigned)(nextq(jn) * 40960 + w * 1024) : 0x40000000u; hd = (unsigned)(2 * L_H + 40960 + 2 * w * 1024) - hd;
                                asm volatile("" : "+s"(hs), "+s"(hd));
                                PIN1(fj);
                            }
                            MT(1);
                            if (__builtin_expect(j == jq0, 0)) check_h<5, 8192, 0>(rws, so1, lo1, lane);
                            VMWAIT();
                            BAR();
                            MT(2);
                            if (LD && w == 0) __builtin_amdgcn_raw_buffer_store_b32(fj, rwin, fv, fq_, 16);
                        },
                        [&](auto uu) {
                            constexpr int u = decltype(uu)::v, t = (u + 6) % 40 / 2;
                            if (u == (LD ? QEU : QEV) && __builtin_expect(j <= QE2, 0)) {
                                asm volatile("s_waitcnt vmcnt(%0)\n\ts_barrier" :: "n"(LD ? 7 : 0) : "memory");
                                if (LD && w == 0) __builtin_amdgcn_raw_buffer_store_b32((unsigned)jp, rwin, fv, fq_, 16);
                            }
                            if (LD && u == QEU + 1 && __builtin_expect(hrdy <= nextq(jn), 0)) g0 = ask_h(rwin, fb, nextq(jn) + 16);
                            if (LD && !(u & 1) && t < 10 && (t < 3 || __builtin_expect(j != jq0, 1))) dma1k(rws, hv, hs + t * 4096u, hd + t * 4096u);
                        });
                    pack();
                    const unsigned qs = O_QK + (unsigned)(j * H_ + hh) * 16384u;
                    st128(g0, rws, to0, qs, 16);
                    st128(g1, rws, to1, qs, 16);
                    { const LAS char* x = c0; c0 = n0; n0 = x; x = c1; c1 = n1; n1 = x; }
                    if (__builtin_expect(j == jl, 0)) {
                        VMWAIT();
                        BAR0();
                        if (tid == 0) __builtin_amdgcn_raw_buffer_store_b32((unsigned)j, rwin, 0u, fq_, 16);
                        break;
                    }
                    if (LD && __builtin_expect(hrdy <= nextq(jn), 0)) {
                        wait_h(rws, rwin, fb, nextq(jn), hrdy);
                        hs = hb + (unsigned)(nextq(jn) * 40960 + w * 1024);
                        for (int t = 0; t < 3; ++t) dma1k(rws, hv, hs + t * 4096u, hd + t * 4096u);
                    }
                    MT(3);
                    if (j < 8) SST(8 + j);
                    if (!BSPARSE || j % 6 == 2) { const int s = j; RT(0); RTS(); MTRT(); MTS(); }
                }
                project_end(bb);
                };
                if (w < 4) rounds(IC<1>()); else rounds(IC<0>());
                VMWAIT();
                BAR();
            }
        }
        SST(4);
        auto steps = [&](auto cc) {
        constexpr int C = decltype(cc)::v >> 1, R = decltype(cc)::v & 1;
        LANES();
        const LAS char* ka = (const LAS char*)(smem + L_K + (fr & 1) * TQS + (4 * fq + (fr >> 2)) * 8);
        const LAS char* qa = (const LAS char*)(smem + L_Q + (fr & 1) * 544 + ((fr >> 1) & 1) * 1152 + (16 * fq + 2 * (fr >> 2)) * 4);
        LAS char* qw = (LAS char*)(C < 2 ? smem + L_Q + (fq & 1) * 544 + (fq >> 1) * 1152 + (32 * w + 8 * (fr >> 2) + bs2(fr & 3)) * 4
                                         : smem + L_K + (fq >> 1) * TQS + (32 * (w & 3) + fr) * 8 + (fq & 1) * 4);
        const LAS char* mp0 = (const LAS char*)(memL + ma0);
        const int z = 16 * (fr >> 3) + 4 * (fr & 3) + 2 * ((fr >> 2) & 1);
        const int ko = ((z >> 4) * 256 + fq * 16 + ((z & 15) ^ ((fq & 1) * 5))) * 16;
        unsigned pa0 = (4u * C + 2u * (hh >> 4)) * PAIR + (unsigned)((hh & 15) * 512 + R * 256 + lane * 4);
        unsigned pa2 = ((8u + (fr >> 3)) * PAIR + C * 4096u + (unsigned)((hh >> 1) * 256 + R * 128 + (hh & 1) * 64 + fq * 16 + (fr & 6) * 2)) | (fr & 1) << 30;
        PIN2(ka, qw); PIN1(mp0);
        constexpr bool TK = C == 2 && R == 1;
        Tk tk = {0u, 0u, 0x40000000u, nullptr};
        if (TK) { tk = tk_init(); __builtin_amdgcn_s_setprio(TKP); }
        if (C < 2) { PIN2(qa, pa0); PIN1(pa2); }
        unsigned fqv = w == 4 ? (lane & 1) * 64u : 0x40000000u;
        if (C == 2 && !TK) PIN1(fqv);
        int qf = 1, tbo = 0;
        for (int s = 0; s < NSEG; ++s) {
            RT(0);
            const char* tb = tokL + tbo;
            unsigned pb = __builtin_amdgcn_readfirstlane(s + 1 < NSEG ? W_P + (unsigned)(s & 3) * 10u * PAIR + nh : 0x40000000u);
            unsigned pb1 = pb + PAIR;
            asm volatile("" : "+s"(pb), "+s"(pb1));
            if (s == 0) BAR();
            u32x8 kx[2][2];
            auto keys = [&]() {
#pragma unroll
                for (int u = 0; u < 4; ++u) kx[u >> 1][u & 1] = cat8(*(const u32x4*)(tb + (ko ^ ((u >> 1) * 16)) + (u & 1) * 2048), *(const u32x4*)(tb + (ko ^ ((u >> 1) * 16)) + (u & 1) * 2048 + 1024));
                PIN2(kx[0][0], kx[0][1]); PIN2(kx[1][0], kx[1][1]);
            };
            if (C < 2) keys();
            bool redo = false;
            unsigned sf = 0;
            do {
                unsigned lim = s > 0 ? poll_lim(budget) : ~0u;
                unsigned long long sus;
                f32x4 sc;
                if (C < 2) {
                    f32x4 s0, s1, s2;
                    unsigned late;
                    asm volatile(
                        ".irp r,232,234,236,238,240,242,244,246,248,250\n\tv_mov_b64 v[\\r:\\r+1], 0\n\t.endr\n\t"
                        "s_cmp_lg_u32 %[p0], 2.0\n\t"
                        "s_cbranch_scc1 1f\n\t"
                        "s_mov_b32 %[lt], -1\n\ts_barrier\n\ts_barrier\n\ts_barrier\n\ts_branch 2f\n"
                        AL "1:\n\t"
                        "s_setprio 2\n\t"
                        "s_waitcnt lgkmcnt(0)\n\t"
                        "s_barrier\n\t"
                        MTA(0)
                        "ds_read_b128 v[208:211], %[mp] offset:0\n\t"
                        "ds_read_b128 v[212:215], %[mp] offset:1024\n\t"
                        "ds_read_b128 v[216:219], %[mp] offset:2048\n\t"
                        "s_barrier\n\t"
                        "ds_read_b128 v[220:223], %[mp] offset:3072\n\t"
                        "s_waitcnt lgkmcnt(3)\n\t"
                        "v_smfmac_f32_16x16x64_bf16 v[232:235], v[208:211], %[w00], %[ix]\n\t"
                        "v_smfmac_f32_16x16x64_bf16 v[236:239], v[208:211], %[w10], %[ix]\n\t"
                        "ds_read_b128 v[224:227], %[mp] offset:4096\n\t"
                        "s_waitcnt lgkmcnt(3)\n\t"
                        "v_smfmac_f32_16x16x64_bf16 v[232:235], v[212:215], %[w01], %[ix]\n\t"
                        "v_smfmac_f32_16x16x64_bf16 v[236:239], v[212:215], %[w11], %[ix]\n\t"
                        "s_barrier\n\t"
                        MTA(1)
                        "ds_read_b128 v[228:231], %[mp] offset:5120\n\t"
                        "s_waitcnt lgkmcnt(3)\n\t"
                        "v_smfmac_f32_16x16x64_bf16 v[232:235], v[216:219], %[w02], %[ix]\n\t"
                        "v_smfmac_f32_16x16x64_bf16 v[236:239], v[216:219], %[w12], %[ix]\n\t"
                        "ds_read_b128 v[208:211], %[mp] offset:6144\n\t"
                        "s_waitcnt lgkmcnt(3)\n\t"
                        "v_smfmac_f32_16x16x64_bf16 v[232:235], v[220:223], %[w03], %[ix]\n\t"
                        "v_smfmac_f32_16x16x64_bf16 v[236:239], v[220:223], %[w13], %[ix]\n\t"
                        "ds_read_b128 v[212:215], %[mp] offset:7168\n\t"
                        "s_waitcnt lgkmcnt(3)\n\t"
                        "v_smfmac_f32_16x16x64_bf16 v[232:235], v[224:227], %[w04], %[ix]\n\t"
                        "v_smfmac_f32_16x16x64_bf16 v[236:239], v[224:227], %[w14], %[ix]\n\t"
                        "ds_read_b128 v[216:219], %[mp] offset:8192\n\t"
                        "s_waitcnt lgkmcnt(3)\n\t"
                        "v_smfmac_f32_16x16x64_bf16 v[232:235], v[228:231], %[w05], %[ix]\n\t"
                        "v_smfmac_f32_16x16x64_bf16 v[236:239], v[228:231], %[w15], %[ix]\n\t"
                        "ds_read_b128 v[220:223], %[mp] offset:9216\n\t"
                        "s_waitcnt lgkmcnt(3)\n\t"
                        "v_smfmac_f32_16x16x64_bf16 v[232:235], v[208:211], %[w06], %[ix]\n\t"
                        "v_smfmac_f32_16x16x64_bf16 v[236:239], v[208:211], %[w16], %[ix]\n\t"
                        "ds_read_b32 v207, v250 offset:%[ls]\n\t"
                        "s_waitcnt lgkmcnt(3)\n\t"
                        "v_smfmac_f32_16x16x64_bf16 v[232:235], v[212:215], %[w07], %[ix]\n\t"
                        "v_smfmac_f32_16x16x64_bf16 v[236:239], v[212:215], %[w17], %[ix]\n\t"
                        "s_waitcnt lgkmcnt(0)\n\t"
                        "v_smfmac_f32_16x16x64_bf16 v[232:235], v[216:219], %[w08], %[ix]\n\t"
                        "v_readfirstlane_b32 %[lt], v207\n\t"
                        "v_smfmac_f32_16x16x64_bf16 v[232:235], v[220:223], %[w09], %[ix]\n\t"
                        "v_smfmac_f32_16x16x64_bf16 v[236:239], v[216:219], %[w18], %[ix]\n\t"
                        "v_smfmac_f32_16x16x64_bf16 v[236:239], v[220:223], %[w19], %[ix]\n\t"
                        "s_setprio " STR(QP) "\n\t"
                        "s_nop 5\n\t"
                        "v_add_f32 v232, v232, v234\n\t"
                        "v_add_f32 v233, v233, v235\n\t"
                        "v_add_f32 v236, v236, v238\n\t"
                        "v_add_f32 v237, v237, v239\n\t"
                        "v_cvt_pk_bf16_f32 v232, v232, v233\n\t"
                        "v_cvt_pk_bf16_f32 v236, v236, v237\n\t"
                        "ds_write2_b32 %[qw], v232, v236 offset1:4\n\t"
                        MTA(2)
                        "2:"
                        : [lt] "=&s"(late), "=&{v[240:243]}"(s0), "=&{v[244:247]}"(s1), "=&{v[248:251]}"(s2)
                        : [mp] "v"(mp0), [qw] "v"(qw), [ix] "v"(sidx), [ls] "n"(L_S), [p0] "s"(pb), [w00] "v"(wt[0][0]), [w10] "v"(wt[1][0]), [w01] "v"(wt[0][1]), [w11] "v"(wt[1][1]), [w02] "v"(wt[0][2]), [w12] "v"(wt[1][2]), [w03] "v"(wt[0][3]), [w13] "v"(wt[1][3]), [w04] "v"(wt[0][4]), [w14] "v"(wt[1][4]), [w05] "v"(wt[0][5]), [w15] "v"(wt[1][5]), [w06] "v"(wt[0][6]), [w16] "v"(wt[1][6]), [w07] "v"(wt[0][7]), [w17] "v"(wt[1][7]), [w08] "v"(wt[0][8]), [w18] "v"(wt[1][8]), [w09] "v"(wt[0][9]), [w19] "v"(wt[1][9])
                        : "v207", "v208", "v209", "v210", "v211", "v212", "v213", "v214", "v215", "v216", "v217", "v218", "v219", "v220", "v221", "v222", "v223", "v224", "v225", "v226", "v227", "v228", "v229", "v230", "v231", "v232", "v233", "v234", "v235", "v236", "v237", "v238", "v239", "scc", "memory");
                    if (__builtin_expect(late == (unsigned)s, 0)) keys();
#define TAIL(A0, A2, B0, B2, C0, C2) \
                        "s_cmp_lg_u32 %[p0], 2.0\n\ts_cbranch_scc1 1f\n\ts_barrier\n\ts_barrier\n\t" \
                        "ds_read_b64_tr_b16 v[232:233], %[ka]\n\ts_waitcnt lgkmcnt(0)\n\tv_lshlrev_b32 v232, 16, v232\n\tv_cmp_u_f32 vcc, v232, v232\n\ts_branch 3f\n\t" AL "1:\n\t" \
                        ".if " STR(BQN) "==0\n\t" \
                        "s_setprio 3\n\t" \
                        "s_waitcnt vmcnt(0) lgkmcnt(0)\n\t" \
                        "s_barrier\n\t" \
                        MTA(3) \
                        "ds_read_b64_tr_b16 v[208:209], %[qa]\n\t" \
                        "ds_read_b64_tr_b16 v[210:211], %[qa] offset:32\n\t" \
                        "ds_read_b64_tr_b16 v[212:213], %[qa] offset:256\n\t" \
                        "ds_read_b64_tr_b16 v[214:215], %[qa] offset:288\n\t" \
                        "ds_read_b64_tr_b16 v[216:217], %[ka] offset:0\n\t" \
                        "ds_read_b64_tr_b16 v[218:219], %[ka] offset:128\n\t" \
                        "ds_read_b64_tr_b16 v[220:221], %[ka] offset:256\n\t" \
                        "ds_read_b64_tr_b16 v[222:223], %[ka] offset:384\n\t" \
                        "ds_read_b64_tr_b16 v[224:225], %[ka] offset:512\n\t" \
                        "ds_read_b64_tr_b16 v[226:227], %[ka] offset:640\n\t" \
                        "s_waitcnt lgkmcnt(8)\n\t" \
                        "v_smfmac_f32_16x16x64_bf16 v[240:243], v[208:211], %[k00], %[ix]\n\t" \
                        "ds_read_b64_tr_b16 v[228:229], %[ka] offset:768\n\t" \
                        "s_waitcnt lgkmcnt(7)\n\t" \
                        "v_smfmac_f32_16x16x64_bf16 v[240:243], v[212:215], %[k01], %[ix]\n\t" \
                        "ds_read_b64_tr_b16 v[230:231], %[ka] offset:896\n\t" \
                        "v_smfmac_f32_16x16x64_bf16 v[244:247], v[208:211], %[k10], %[ix]\n\t" \
                        "v_smfmac_f32_16x16x64_bf16 v[244:247], v[212:215], %[k11], %[ix]\n\t" \
                        "s_nop 4\n\t" \
                        ".elseif " STR(TPRE) "==0\n\t" \
                        "s_waitcnt lgkmcnt(0)\n\t" \
                        "s_barrier\n\t" \
                        "ds_read_b64_tr_b16 v[208:209], %[qa]\n\t" \
                        "ds_read_b64_tr_b16 v[210:211], %[qa] offset:32\n\t" \
                        "ds_read_b64_tr_b16 v[212:213], %[qa] offset:256\n\t" \
                        "ds_read_b64_tr_b16 v[214:215], %[qa] offset:288\n\t" \
                        "s_setprio 3\n\t" \
                        "s_waitcnt vmcnt(0) lgkmcnt(0)\n\t" \
                        "s_barrier\n\t" \
                        MTA(3) \
                        "ds_read_b64_tr_b16 v[216:217], %[ka] offset:0\n\t" \
                        "ds_read_b64_tr_b16 v[218:219], %[ka] offset:128\n\t" \
                        "ds_read_b64_tr_b16 v[220:221], %[ka] offset:256\n\t" \
                        "ds_read_b64_tr_b16 v[222:223], %[ka] offset:384\n\t" \
                        "v_smfmac_f32_16x16x64_bf16 v[240:243], v[208:211], %[k00], %[ix]\n\t" \
                        "ds_read_b64_tr_b16 v[224:225], %[ka] offset:512\n\t" \
                        "ds_read_b64_tr_b16 v[226:227], %[ka] offset:640\n\t" \
                        "v_smfmac_f32_16x16x64_bf16 v[240:243], v[212:215], %[k01], %[ix]\n\t" \
                        "ds_read_b64_tr_b16 v[228:229], %[ka] offset:768\n\t" \
                        "ds_read_b64_tr_b16 v[230:231], %[ka] offset:896\n\t" \
                        "v_smfmac_f32_16x16x64_bf16 v[244:247], v[208:211], %[k10], %[ix]\n\t" \
                        "v_smfmac_f32_16x16x64_bf16 v[244:247], v[212:215], %[k11], %[ix]\n\t" \
                        "s_nop 3\n\t" \
                        ".else\n\t" \
                        "s_waitcnt lgkmcnt(0)\n\t" \
                        "s_barrier\n\t" \
                        "ds_read_b64_tr_b16 v[208:209], %[qa]\n\t" \
                        "ds_read_b64_tr_b16 v[210:211], %[qa] offset:32\n\t" \
                        "ds_read_b64_tr_b16 v[212:213], %[qa] offset:256\n\t" \
                        "ds_read_b64_tr_b16 v[214:215], %[qa] offset:288\n\t" \
                        "s_setprio 0\n\t" \
                        "s_waitcnt vmcnt(0) lgkmcnt(0)\n\t" \
                        "v_smfmac_f32_16x16x64_bf16 v[240:243], v[208:211], %[k00], %[ix]\n\t" \
                        "v_smfmac_f32_16x16x64_bf16 v[240:243], v[212:215], %[k01], %[ix]\n\t" \
                        "s_setprio 3\n\t" \
                        "s_barrier\n\t" \
                        MTA(3) \
                        "ds_read_b64_tr_b16 v[216:217], %[ka] offset:0\n\t" \
                        "ds_read_b64_tr_b16 v[218:219], %[ka] offset:128\n\t" \
                        "ds_read_b64_tr_b16 v[220:221], %[ka] offset:256\n\t" \
                        "ds_read_b64_tr_b16 v[222:223], %[ka] offset:384\n\t" \
                        "v_smfmac_f32_16x16x64_bf16 v[244:247], v[208:211], %[k10], %[ix]\n\t" \
                        "ds_read_b64_tr_b16 v[224:225], %[ka] offset:512\n\t" \
                        "ds_read_b64_tr_b16 v[226:227], %[ka] offset:640\n\t" \
                        "v_smfmac_f32_16x16x64_bf16 v[244:247], v[212:215], %[k11], %[ix]\n\t" \
                        "ds_read_b64_tr_b16 v[228:229], %[ka] offset:768\n\t" \
                        "ds_read_b64_tr_b16 v[230:231], %[ka] offset:896\n\t" \
                        ".endif\n\t" \
                        "v_add_f32 v232, " A0 ", " A2 "\n\t" \
                        "v_mul_f32 v236, 0x3e0293ee, v232\n\t" \
                        "v_exp_f32 v236, v236\n\t" \
                        "s_waitcnt lgkmcnt(4)\n\t" \
                        "v_smfmac_f32_16x16x64_bf16 v[248:251], v[208:211], v[216:223], %[ix]\n\t" \
                        "s_waitcnt lgkmcnt(0)\n\t" \
                        "v_smfmac_f32_16x16x64_bf16 v[248:251], v[212:215], v[224:231], %[ix]\n\t" \
                        "v_add_f32 v233, " B0 ", " B2 "\n\t" \
                        "v_mul_f32 v237, 0x3e0293ee, v233\n\t" \
                        "v_exp_f32 v237, v237\n\t" \
                        "s_nop 0\n\t" \
                        "v_add_f32 v239, v236, v237\n\t" \
                        "s_nop 1\n\t" \
                        "v_add_f32_dpp v239, v239, v239 row_ror:8 row_mask:0xf bank_mask:0xf\n\t" \
                        MTA(4) \
                        "v_add_f32 v234, " C0 ", " C2 "\n\t" \
                        "v_mul_f32 v238, 0x3e0293ee, v234\n\t" \
                        "v_exp_f32 v238, v238\n\t" \
                        "s_nop 0\n\t" \
                        "v_add_f32 v239, v239, v238\n\t" \
                        "v_mov_b32_dpp v235, v238 quad_perm:[1,1,3,3] row_mask:0xf bank_mask:0xf\n\t" \
                        "s_nop 0\n\t" \
                        "v_add_f32_dpp v239, v239, v239 row_ror:4 row_mask:0xf bank_mask:0xf\n\t" \
                        "s_nop 1\n\t" \
                        "v_add_f32_dpp v239, v239, v239 row_ror:2 row_mask:0xf bank_mask:0xf\n\t" \
                        "s_nop 1\n\t" \
                        "v_add_f32_dpp v239, v239, v239 row_ror:1 row_mask:0xf bank_mask:0xf\n\t" \
                        "v_rcp_f32 v207, v239\n\t" \
                        "v_add_u32 v239, 0xe8800000, v239\n\t" \
                        "v_mul_f32 v236, v236, v207\n\t" \
                        "v_cmp_lt_u32 vcc, %[rg], v239\n\t" \
                        "v_mul_f32 v237, v237, v207\n\t" \
                        "v_cvt_pk_bf16_f32 v236, v236, v237\n\t" \
                        "s_cbranch_vccnz 3f\n\t" \
                        MTA(5) \
                        "buffer_store_dword v236, %[pa], %[rw], %[p0] offen sc1\n\t" \
                        "v_mul_f32 v238, v238, v207\n\t" \
                        "v_mul_f32 v235, v235, v207\n\t" \
                        "buffer_store_dword v236, %[pa], %[rw], %[p1] offen sc1\n\t" \
                        "v_cvt_pk_bf16_f32 v238, v238, v235\n\t" \
                        "buffer_store_dword v238, %[pm], %[rw], %[p0] offen sc1\n\t" \
                        "3:\n\t" \
                        "s_mov_b64 %[sus], vcc\n\t" \
                        "s_setprio 0\n\t"
#define TAILIO : [sus] "=&s"(sus), "=&{v[232:235]}"(sc), "+{v[240:243]}"(s0), "+{v[244:247]}"(s1), "+{v[248:251]}"(s2) \
                        : [qa] "v"(qa), [ka] "v"(ka), [ix] "v"(sidx), [pa] "v"(pa0), [pm] "v"(pa2), [rw] "s"(rwin), [p0] "s"(pb), [p1] "s"(pb1), [rg] "s"(SRG), \
                          [k00] "v"(kx[0][0]), [k01] "v"(kx[0][1]), [k10] "v"(kx[1][0]), [k11] "v"(kx[1][1]) \
                        : "v207", "v208", "v209", "v210", "v211", "v212", "v213", "v214", "v215", "v216", "v217", "v218", "v219", "v220", "v221", "v222", "v223", "v224", "v225", "v226", "v227", "v228", "v229", "v230", "v231", "v236", "v237", "v238", "v239", "vcc", "scc", "memory"
                    if (R == 0) asm volatile(TAIL("v240", "v242", "v244", "v246", "v248", "v250") TAILIO);
                    else asm volatile(TAIL("v241", "v243", "v245", "v247", "v249", "v251") TAILIO);
                    RT(4);
                    MT(6);
                } else {
                    f32x8 a01;
                    asm volatile("v_mov_b64 v[200:201], 0\n\tv_mov_b64 v[202:203], 0\n\tv_mov_b64 v[204:205], 0\n\tv_mov_b64 v[206:207], 0" : "={v[200:207]}"(a01));
                    unsigned sr = TK ? tro + (unsigned)(s & 3) * 2u * PAIR : qrdy < min(s + 3, NSEG - 1) ? W_FQ + fb + (unsigned)hh * 128u : 0x40000000u;
                    unsigned tb = 0, tg = (unsigned)(s + 64 * lay) << 24, so = s > 0 ? W_T + (unsigned)(s & 3) * 2u * PAIR + (unsigned)hh * 128u : 0x40000000u;
                    asm volatile("" : "+s"(lim), "+s"(sr));
                    if (TK) { asm volatile("" : "+s"(tg), "+s"(so)); tb = fetch(tk, s, !redo && s >= lay); }
                    else asm volatile(AL "s_waitcnt lgkmcnt(0)\n\ts_barrier\n\ts_barrier\n\ts_waitcnt vmcnt(0)\n\ts_barrier" ::: "memory");
                    asm volatile(
                        ".if %[tk]\n\t"
                        "s_waitcnt vmcnt(0)\n\t"
                        "s_memtime s[100:101]\n\t"
                        "s_barrier\n\t"
                        "s_setprio 1\n\t"
                        "s_waitcnt lgkmcnt(0)\n\t"
                        "ds_read2st64_b32 v[208:209], %[sp] offset1:16\n\t"
                        "ds_read_b32 v210, %[sp] offset:6144\n\t"
                        "ds_read_b128 v[212:215], %[mp] offset:0\n\t"
                        "ds_read_b128 v[216:219], %[mp] offset:1024\n\t"
                        "ds_read_b128 v[220:223], %[mp] offset:2048\n\t"
                        "ds_read_b128 v[224:227], %[mp] offset:3072\n\t"
                        "s_sub_u32 %[tb], s100, %[tb]\n\t"
                        "s_mul_i32 %[tb], %[tb], %[qc]\n\t"
                        "s_lshr_b32 %[tb], %[tb], 16\n\t"
                        "s_add_u32 %[tb], %[tb], %[ta]\n\t"
                        "s_and_b32 %[tb], %[tb], 0xffffff\n\t"
                        "s_or_b32 %[tb], %[tb], %[tg]\n\t"
                        "v_mov_b32 v211, %[tb]\n\t"
                        "s_waitcnt lgkmcnt(4)\n\t"
                        "v_max3_u32 v208, v208, v209, v210\n\t"
                        "v_cmp_lt_u32 vcc, %[lim], v208\n\t"
                        "ds_read_b128 v[228:231], %[mp] offset:4096\n\t"
                        "s_cmp_eq_u64 vcc, 0\n\t"
                        "s_cselect_b32 %[so], %[so], 2.0\n\t"
                        "buffer_store_dword v211, %[tw], %[rw], %[so] offen sc1\n\t"
                        "ds_read_b128 v[232:235], %[mp] offset:5120\n\t"
                        ".else\n\t"
                        "s_setprio 1\n\t"
                        "ds_read_b128 v[212:215], %[mp] offset:0\n\t"
                        "ds_read_b128 v[216:219], %[mp] offset:1024\n\t"
                        "ds_read_b128 v[220:223], %[mp] offset:2048\n\t"
                        "ds_read_b128 v[224:227], %[mp] offset:3072\n\t"
                        "ds_read_b128 v[228:231], %[mp] offset:4096\n\t"
                        "ds_read_b128 v[232:235], %[mp] offset:5120\n\t"
                        ".endif\n\t"
                        ".if !%[tk]\n\tbuffer_load_dword %[sf], %[sl], %[rw], %[sr] offen sc1\n\t.endif\n\t"
                        MTA(4)
                        "s_waitcnt lgkmcnt(5)\n\t"
                        "v_smfmac_f32_16x16x64_bf16 v[200:203], v[212:215], %[w00], %[ix]\n\t"
                        "ds_read_b128 v[236:239], %[mp] offset:6144\n\t"
                        "v_smfmac_f32_16x16x64_bf16 v[204:207], v[212:215], %[w10], %[ix]\n\t"
                        "ds_read_b128 v[240:243], %[mp] offset:7168\n\t"
                        "s_waitcnt lgkmcnt(6)\n\t"
                        "v_smfmac_f32_16x16x64_bf16 v[200:203], v[216:219], %[w01], %[ix]\n\t"
                        "ds_read_b128 v[244:247], %[mp] offset:8192\n\t"
                        "v_smfmac_f32_16x16x64_bf16 v[204:207], v[216:219], %[w11], %[ix]\n\t"
                        "ds_read_b128 v[248:251], %[mp] offset:9216\n\t"
                        "s_waitcnt lgkmcnt(4)\n\t"
                        "v_smfmac_f32_16x16x64_bf16 v[200:203], v[220:223], %[w02], %[ix]\n\t"
                        "v_smfmac_f32_16x16x64_bf16 v[204:207], v[220:223], %[w12], %[ix]\n\t"
                        "v_smfmac_f32_16x16x64_bf16 v[200:203], v[224:227], %[w03], %[ix]\n\t"
                        "v_smfmac_f32_16x16x64_bf16 v[204:207], v[224:227], %[w13], %[ix]\n\t"
                        "v_smfmac_f32_16x16x64_bf16 v[200:203], v[228:231], %[w04], %[ix]\n\t"
                        "v_smfmac_f32_16x16x64_bf16 v[204:207], v[228:231], %[w14], %[ix]\n\t"
                        "v_smfmac_f32_16x16x64_bf16 v[200:203], v[232:235], %[w05], %[ix]\n\t"
                        "v_smfmac_f32_16x16x64_bf16 v[204:207], v[232:235], %[w15], %[ix]\n\t"
                        "s_waitcnt lgkmcnt(0)\n\t"
                        "v_smfmac_f32_16x16x64_bf16 v[200:203], v[236:239], %[w06], %[ix]\n\t"
                        "v_smfmac_f32_16x16x64_bf16 v[204:207], v[236:239], %[w16], %[ix]\n\t"
                        BQ(3)
                        "v_smfmac_f32_16x16x64_bf16 v[200:203], v[240:243], %[w07], %[ix]\n\t"
                        "v_smfmac_f32_16x16x64_bf16 v[204:207], v[240:243], %[w17], %[ix]\n\t"
                        BQ(5)
                        "v_smfmac_f32_16x16x64_bf16 v[200:203], v[244:247], %[w08], %[ix]\n\t"
                        "v_smfmac_f32_16x16x64_bf16 v[200:203], v[248:251], %[w09], %[ix]\n\t"
                        BQ(7)
                        "v_smfmac_f32_16x16x64_bf16 v[204:207], v[244:247], %[w18], %[ix]\n\t"
                        "v_smfmac_f32_16x16x64_bf16 v[204:207], v[248:251], %[w19], %[ix]\n\t"
                        BQ(9)
                        "s_nop 5\n\t"
                        "v_pk_add_f32 v[200:201], v[200:201], v[202:203]\n\t"
                        "v_cvt_pk_bf16_f32 v200, v200, v201\n\t"
                        "v_pk_add_f32 v[204:205], v[204:205], v[206:207]\n\t"
                        "v_cvt_pk_bf16_f32 v201, v204, v205\n\t"
                        "ds_write2_b32 %[qw], v200, v201 offset1:32\n\t"
                        ".if %[tk]\n\tbuffer_load_dword %[sf], %[sl], %[rw], %[sr] offen sc1\n\t.endif\n\t"
                        "v_cmp_u_f32 %[sus], v204, v205\n\t"
                        MTA(2)
                        "s_waitcnt lgkmcnt(0)\n\t"
                        "s_barrier\n\t"
                        "s_setprio 0\n\t"
                        ".if %[tk]\n\ts_cmp_lg_u64 %[sus], 0\n\ts_cbranch_scc1 1f\n\t.endif\n\ts_waitcnt vmcnt(0)\n1:"
                        : [sus] "=&s"(sus), [sf] "+&v"(sf), [tb] "+&s"(tb), [so] "+&s"(so), "+{v[200:207]}"(a01)
                        : [mp] "v"(mp0), [qw] "v"(qw), [ix] "v"(sidx), [sl] "v"(TK ? tk.sl : fqv), [tk] "n"(TK ? 1 : 0), [sr] "s"(sr), [rw] "s"(rwin),
                          [sp] "v"(TK ? tk.sp : mp0), [lim] "s"(lim), [qc] "s"(ck.qpc), [ta] "s"(ck.ta), [tg] "s"(tg), [tw] "v"(TK ? tk.tw : fqv), [w00] "v"(wt[0][0]), [w10] "v"(wt[1][0]), [w01] "v"(wt[0][1]), [w11] "v"(wt[1][1]), [w02] "v"(wt[0][2]), [w12] "v"(wt[1][2]), [w03] "v"(wt[0][3]), [w13] "v"(wt[1][3]), [w04] "v"(wt[0][4]), [w14] "v"(wt[1][4]), [w05] "v"(wt[0][5]), [w15] "v"(wt[1][5]), [w06] "v"(wt[0][6]), [w16] "v"(wt[1][6]), [w07] "v"(wt[0][7]), [w17] "v"(wt[1][7]), [w08] "v"(wt[0][8]), [w18] "v"(wt[1][8]), [w09] "v"(wt[0][9]), [w19] "v"(wt[1][9])
                        : "v208", "v209", "v210", "v211", "v212", "v213", "v214", "v215", "v216", "v217", "v218", "v219", "v220", "v221", "v222", "v223", "v224", "v225", "v226", "v227", "v228", "v229", "v230", "v231", "v232", "v233", "v234", "v235", "v236", "v237", "v238", "v239", "v240", "v241", "v242", "v243", "v244", "v245", "v246", "v247", "v248", "v249", "v250", "v251", "vcc", "scc", "s100", "s101", "memory");
                    MT(3);
                }
                redo = false;
                if (__builtin_expect(sus != 0, 0)) {
                    redo = again(TK, tk.vo, sf, s, lim, mp0, tk.sp);
                    if (!redo && C < 2) {
                        const float c = 0.08838834764831845f * 1.44269504088896340736f;
                        float mx = fmx(fmx(sc[0], sc[1]), sc[2]);
                        mx = fmx(mx, dppf<0x128>(mx)); mx = fmx(mx, dppf<0x124>(mx)); mx = fmx(mx, dppf<0x122>(mx)); mx = fmx(mx, dppf<0x121>(mx));
                        const float e0 = __builtin_amdgcn_exp2f((sc[0] - mx) * c), e1 = __builtin_amdgcn_exp2f((sc[1] - mx) * c), e2 = __builtin_amdgcn_exp2f((sc[2] - mx) * c);
                        float d = e0 + e1;
                        d += dppf<0x128>(d); d += e2; d += dppf<0x124>(d); d += dppf<0x122>(d); d += dppf<0x121>(d);
                        d = __builtin_amdgcn_rcpf(d);
                        const unsigned p0 = nosent1(pack2(e0 * d, e1 * d)), p1 = nosent1(pack2(e2 * d, dppf<0xF5>(e2) * d));
                        __builtin_amdgcn_raw_buffer_store_b32(p0, rwin, pa0, pb, 16); __builtin_amdgcn_raw_buffer_store_b32(p0, rwin, pa0, pb1, 16);
                        __builtin_amdgcn_raw_buffer_store_b32(p1, rwin, pa2, pb, 16);
                    }
                }
            } while (__builtin_expect(redo, 0));
            if (__builtin_expect(mb != 0, 0)) { mp0 -= mb; tk.sp -= mb; mb = 0; }
            if (s < 8) SST(8 + s);
            if (s >= 56) SST(s - 40);
            if (C + R == 1) {
                {
                    __builtin_amdgcn_s_sleep(DDMA);
                    unsigned l2 = fresh_lane(), s1;
                    asm volatile("v_mov_b32 %0, -1" : "=v"(s1));
                    const unsigned base = W_P + (unsigned)((s - 1) & 3) * 10u * PAIR + nh;
                    asm volatile("s_waitcnt vmcnt(3)" ::: "memory");
                    st128(((u32x4){s1, s1, s1, s1}), rwin, (l2 >> 5) * PAIR + (l2 & 31u) * 16u, base + (unsigned)(R * 4 + 2 * (hh >> 4)) * PAIR + (unsigned)(hh & 15) * 512u, 16);
                    if (R) __builtin_amdgcn_raw_buffer_store_b64((u32x2){s1, s1}, rwin, (l2 >> 5) * PAIR + (l2 & 16u) * 256u + (l2 & 8u) * 16u + (l2 & 7u) * 8u, base + 8u * PAIR + (unsigned)((hh >> 1) * 256 + (hh & 1) * 64), 16);
                    else __builtin_amdgcn_raw_buffer_store_b32(s1, rwin, two(), W_T + (unsigned)((s + 2) & 3) * 2u * PAIR + (unsigned)hh * 128u, 16);
                }
            } else if (TK) {
                if (s + 1 < NSEG) sched2(tk, s, sf);
            } else if (C == 2 && w == 4) {
                qrdy = max(qrdy, seg_done(MXQ, __builtin_amdgcn_readlane(sf, 0), __builtin_amdgcn_readlane(sf, 1)));
                if (__builtin_expect(qf <= s, 0) && s + 1 < NSEG) {
                    for (unsigned spins = 0; qrdy <= s;) {
                        asm volatile("" ::: "memory");
                        const int f = (int)__builtin_amdgcn_raw_buffer_load_b32(rwin, fqv, W_FQ + fb + (unsigned)hh * 128u, 16);
                        qrdy = seg_done(MXQ, __builtin_amdgcn_readlane(f, 0), __builtin_amdgcn_readlane(f, 1));
                        if (qrdy > s) break;
                        FC(fc2);
                        if (spin_fail(spins, rws)) { qrdy = NSEG; break; }
                        __builtin_amdgcn_s_sleep(2);
                    }
                    *(volatile LAS int*)(smem + L_S) = s + 1;
                }
                while (qf < min(min(s + 2, qrdy), NSEG - 1)) {
                    ++qf;
                    const unsigned so = O_QK + (unsigned)((qf * H_ + hh) * 16384), lo = (unsigned)(L_TOK + qf % 3 * 16384), l2 = fresh_lane();
#pragma unroll
                    for (int i = 0; i < 4; ++i) dma4k(rws, l2 * 16u, so + i * 4096u, lo + i * 4096u);
                }
            } else {
                if (C == 2 && s + 1 < NSEG) __builtin_amdgcn_s_sleep(DPT);
                const float scale = 0.08838834764831845f, l2e = 1.44269504088896340736f * (NORND ? scale : 1.f);
                const int tq = (w >> 1) & 1, qh = w & 1;
                f32x4 sc[3] = {z4, z4, z4};
                u32x4 kt[2][4], qf[4], kf[4];
                const unsigned l2 = fresh_lane(), kto = ((l2 & 8u) * 512u + (l2 & 51u) * 16u + (l2 & 4u) * 32u) ^ ((l2 & 16u) * 5u), qto = ((l2 & 55u) * 16u + qh * 128u) ^ ((l2 & 16u) * 8u);
#pragma unroll
                for (int ks = 0; ks < 4; ++ks) {
                    qf[ks] = *(const u32x4*)(tb + 8192 + (tq * 4 + ks) * 1024 + qto);
                    kt[0][ks] = *(const u32x4*)(tb + ks * 1024 + kto);
                    kt[1][ks] = *(const u32x4*)(tb + ks * 1024 + (kto ^ 64u));
                    SB();
                }
#pragma unroll
                for (int ks = 0; ks < 4; ++ks) {
                    sc[0] = mfma32(kt[0][ks], qf[ks], sc[0]); PIN1(sc[0]);
                    kf[ks] = ldtr2(ka + ks * 256, ka + ks * 256 + 128);
                    SB();
                    sc[1] = mfma32(kt[1][ks], qf[ks], sc[1]); PIN1(sc[1]);
                }
#pragma unroll
                for (int ks = 0; ks < 4; ++ks) sc[2] = mfma32(kf[ks], qf[ks], sc[2]);
                const bool hi = (l2 & 8u) != 0u;
                float v[6];
#pragma unroll
                for (int r = 0; r < 4; ++r) v[r] = hi ? sc[1][r] : sc[0][r];
                v[4] = hi ? sc[2][2] : sc[2][0]; v[5] = hi ? sc[2][3] : sc[2][1];
#pragma unroll
                for (int r = 0; r < 6; r += 2) {
                    if (NORND) break;
                    const unsigned b0 = pack2(v[r], v[r + 1]);
                    const unsigned b1 = pack2(lo2f(b0) * scale, hi2f(b0) * scale);
                    v[r] = lo2f(b1); v[r + 1] = hi2f(b1);
                }
                const int lim = 8 * (w & 3) + (int)(l2 & 7u) - 8 * (int)(l2 >> 4) - (int)((l2 & 8u) >> 1);
#pragma unroll
                for (int r = 0; r < 4; ++r) if (r > lim) v[r] = NORND ? -1000000000.0f / scale : bfr(-1000000000.0f);
                float mx = fmx(fmx(fmx(fmx(fmx(v[0], v[1]), v[2]), v[3]), v[4]), v[5]);
                mx = xmax3216(fmx(mx, ror8f(mx)));
                const float mb = -mx * l2e;
#pragma unroll
                for (int r = 0; r < 6; ++r) v[r] = __builtin_amdgcn_exp2f(fmaf(v[r], l2e, mb));
                float sum = fmaf(v[4] + v[5], 0.5f, (v[0] + v[1]) + (v[2] + v[3]));
                sum = xsum3216(sum + ror8f(sum));
                const float inv = __builtin_amdgcn_rcpf(sum);
                const unsigned p0 = pack2(v[0] * inv, v[1] * inv), p1 = nosent1(pack2(v[2] * inv, v[3] * inv)), p2 = nosent1(pack2(v[4] * inv, v[5] * inv));
                const unsigned base = s + 2 < NSEG ? O_PT + (unsigned)(s * PTSEG) * 2u : W_PL + fb + (s & 1) * 5u * PAIR;
                const __amdgpu_buffer_rsrc_t rp = s + 2 < NSEG ? rws : rwin;
                const unsigned oob = (l2 & 8u) << 27;
                st128(((u32x4){p0, p1, dppu<0x128>(p0), dppu<0x128>(p1)}), rp, (l2 * 16u) | oob, base + (unsigned)((hh * 2 + tq) * 1024 + qh * 128), 16);
                __builtin_amdgcn_raw_buffer_store_b64((u32x2){p2, dppu<0x128>(p2)}, rp, ((l2 & 7u) * 16u + (l2 >> 4) * 8u) | oob | (l2 & 32u) << 25, base + 65536u + (unsigned)((tq * 32 + hh) * 256 + qh * 128), 16);
            }
            tbo = tbo == 32768 ? 0 : tbo + 16384;
            if (__builtin_expect(budget <= 0, 0)) __builtin_amdgcn_raw_buffer_store_b32(1u, rws, 0u, 0u, 16);
            RT(5);
            RTS();
            MT(7);
            MTS();
        }
        };
        if (qt) {} else if (w == 0) steps(IC<0>()); else if (w == 1) steps(IC<1>()); else if (w == 2) steps(IC<2>()); else if (w == 3) steps(IC<3>()); else if (w < 7) steps(IC<4>()); else steps(IC<5>());
        __builtin_amdgcn_s_setprio(0);
    }
    SST(31);
    }
    } else
#pragma nounroll
    for (; lay < 2; ++lay) {
    fresh();
    SST(0);
    {
        LANES();
        const int G = bid < NWRK ? bid - 96 : bid == NWRK ? 80 : -1;
        const int fi = bid >= NWRK ? 255 - bid : bid < NREC ? 16 + G / 10 + 4 * (G % 10) : 99;
        const bool first = fi < 32;
        u32x2 tag = {0u, 0u};
        if (lay == 0) tag = get_tag();
        const int fl = min(lane, lay ? 13 : 15);
        const unsigned fv = w < 4 ? lane * 128u + 64u : (fl & 14) * 512u + (fl & 1) * 128u + (w & 3) * 256u, fs = lay ? W_FA : W_ST;
        auto ask = [&]() { asm volatile("" ::: "memory"); return __builtin_amdgcn_raw_buffer_load_b128(rwin, fv, fs, 16); };
        auto miss = [&](u32x4 f) { return __ballot(((f[0] ^ tag[0]) | (f[1] ^ tag[1]) | (f[2] ^ tag[0]) | (f[3] ^ tag[1])) != 0u); };
        if (G < 0 && w >= 4) for (unsigned spins = 0; spins < SPIN_CAP && miss(ask()); ++spins) __builtin_amdgcn_s_sleep(ASLP);
        BAR();
        if (G >= 0) {
            unsigned long long m = ~0ull;
            unsigned e = ~0u;
            a2_unit(rws, G, w, lane, lay == 0, [&](int r) {
                if (r == 6) { if (lay) e = __builtin_amdgcn_raw_buffer_load_b32(rwin, (lane & 1) * 128u, W_E + (E_A1 + 2 * (w & 3)) * 128u, 16); else if (DFA) __builtin_amdgcn_s_sleep(DFA); }
                if (r >= 7 && lay) { if (r == 7) await(E_A1 + 2 * (w & 3), 2, 0, e); }
                else for (unsigned spins = 0; spins < SPIN_CAP && (r == 6 ? m & 0x3FFFu : m); ++spins) {
                    if (spins) __builtin_amdgcn_s_sleep(ASLP);
                    m = miss(ask());
                }
                if (r == 7) SST(5);
            }, [&](int k) {
                if (k == 2 && w == 3 && blockIdx.x < 136) due(E_A2 + (blockIdx.x & 7), lay ? DA2 : DA20);
                if (k == 7 && YSLP) __builtin_amdgcn_s_sleep(YSLP);
                if (bid < NREC) SST(8 + k);
            });
            SST(6);
            VMWAIT();
            BAR0();
            if (tid == 0) __builtin_amdgcn_raw_buffer_store_b32(0u, rwin, 0u, W_FB + fb + (unsigned)((G >> 2) * 128 + (G & 3) * 4), 16);
            SST(7);
        }
        const RowTw tw = row_tw(lane);
        auto columns = [&](int all) {
            if (w == 7) await(E_A2 + fb / 128u, 8, all);
            if (w == 7 && all) for (unsigned spins = 0;;) {
                asm volatile("" ::: "memory");
                const u32x4 f = __builtin_amdgcn_raw_buffer_load_b128(rwin, (unsigned)min(lane, 20) * 128u, W_FB + fb, 16);
                if (!__any((f[0] | (lane < 20 ? f[1] | f[2] | f[3] : 0u)) != 0u)) break;
                if (spin_fail(spins, rws)) break;
                __builtin_amdgcn_s_sleep(ASLP);
            }
            BAR0();
            if (!all || !first) SST(3);
        };
        const __amdgpu_buffer_rsrc_t rx = mkrsrc(lay ? karg<0>() + OFF_X2 : karg<3>(), S_ * D_ * 2);
        if (first) {
            const unsigned fo = fb + (unsigned)((fi >> 2) * 128 + (fi & 3) * 4);
            a3_units<4>(rws, rx, fi * 8, 0, lay, smem, tw, w, lane, [&](int k) {
                if (k < 0) { if (G < 0) columns(0); } else if (fi < 16) SST(16 + k);
                if (k == 0 && w == 7 && fi < 4) due(E_A3 + fi, lay ? DA3 : DA30);
            }, [&](int) { if (tid == 0) __builtin_amdgcn_raw_buffer_store_b32(0u, rwin, 0u, W_H + fo, 16); });
            SST(4);
        }
                if (!first || (WK3 && bid >= NWRK)) {
            const bool two = bid >= NREC && bid < NREC + 32;
            const int u0 = two ? 32 + 16 * WK3 + (bid - NREC) / 8 + 4 * ((bid - NREC) % 8) : bid < NREC ? fi - 24 : bid < NWRK ? bid - NREC - 32 : 32 + fi;
            columns(two ? DFB - (32 + 16 * WK3) * DST + u0 * DST : DFB0);
            a3_units<8>(rws, rx, (16 + u0) * 16, two ? 512 : S_, lay, smem, tw, w, lane, [](int) {}, [&](int r) {
                if (tid == 0) __builtin_amdgcn_raw_buffer_store_b64((u32x2){0u, 0u}, rwin, 0u, W_H + fb + (unsigned)((r >> 5) * 128 + (r & 16) / 2), 16);
                if (r < 1536 && two) SST(2);
            });
            VMWAIT();
        }
        if (lay == 0 && bid < NREC) { config0(); config(); }
        SST(1);
        places();
    }
    const __amdgpu_buffer_rsrc_t rout = mkrsrc(lay ? karg<4>() : karg<0>() + OFF_X2, S_ * D_ * 2);
    if (bid < NREC) {
        LANES();
        const int o = bid - 96, hg = w & 3;
        const unsigned ztoff = (unsigned)((o * H_ + 8 * hg) * 64 + lane) * 16u;
        const unsigned zoff = zpair(o / 10, o % 10) + (unsigned)(lane * 16);
        const unsigned poff = (4u * (mc >> 1) + 2u * (hg >> 1) + (mc & 1u)) * PAIR + nh + (unsigned)((hg & 1) * 4096 + (fq >> 1) * 512 + (fr >> 2) * 128 + (fr & 1) * 64 + (fq & 1) * 32 + (fr & 2) * 8);
        const unsigned pmoff = (8u + (mc & 1u)) * PAIR + nh + (mc >> 1) * 4096u + (unsigned)(fq * 256 + (fr >> 3) * 128 + (fr & 2) * 32 + (fr & 4) * 8 + (fr & 1) * 16);
        const unsigned fzo = (unsigned)(8 * hg + (lane & 7)) * 128u + (unsigned)(o / 10) * 8192u + (lane & 8) * 512u;
        const unsigned moff = (o < 32 ? (unsigned)o * 256u : 4u * PAIR + (unsigned)(o - 32) * 256u) + nh + (unsigned)(lane * 4);
        const unsigned mc2 = o < 32 ? 2u * PAIR : 2048u;
        char* op = smem + 8192;
        SST(2);
        u32x8 zf[4];
        asm volatile("" : "={v[160:167]}"(zf[0]), "={v[168:175]}"(zf[1]), "={v[176:183]}"(zf[2]), "={v[184:191]}"(zf[3]));
        int zr = -1;
        const unsigned ra = (o < 32 ? (unsigned)fq * PAIR + (unsigned)o * 256u : (4u + (fq & 1)) * PAIR + (unsigned)(fq >> 1) * 2048u + (unsigned)(o - 32) * 256u) + nh + (unsigned)(fr * 16);
        auto zt_wait = [&](int s) {
            if (__builtin_expect(zr < s, 0)) for (unsigned spins = 0;;) {
                asm volatile("" ::: "memory");
                if (!__any((int)__builtin_amdgcn_raw_buffer_load_b32(rwin, fzo, W_FZ + fb, 16) < s && ((lane >> 3) & 1) == (int)((MXV >> s) & 1))) break;
                FC(fc2);
                if (spin_fail(spins, rws)) break;
                __builtin_amdgcn_s_sleep(ZPS);
            }
        };
        auto sent = [&](int s) {
            for (;;) {
                const u32x4 x = *(volatile LAS u32x4*)(smem + 4096);
                zr = __builtin_amdgcn_readfirstlane(x[0]);
                if (__builtin_amdgcn_readfirstlane(min((int)x[1], max((int)x[2], (int)x[0] - 1))) == s) break;
                __builtin_amdgcn_s_sleep(2);
            }
        };
        if (w < 4) {
            await(E_ZT + fb / 128u, 4, 0);
            for (int s = 0; s + 1 < NSEG; ++s) {
                const unsigned pb = W_P + (unsigned)(s & 3) * 10u * PAIR;
                zt_wait(s);
                if (s == 0) SST(1);
#define ZTL \
                    "buffer_load_dwordx4 v[160:163], %[zt], %[rs], %[so] offen sc1\n\t" \
                    "buffer_load_dwordx4 v[164:167], %[zt], %[rs], %[so] offen offset:1024 sc1\n\t" \
                    "buffer_load_dwordx4 v[168:171], %[zt], %[rs], %[so] offen offset:2048 sc1\n\t" \
                    "buffer_load_dwordx4 v[172:175], %[zt], %[rs], %[so] offen offset:3072 sc1\n\t" \
                    "buffer_load_dwordx4 v[176:179], %[zt], %[rs], %[s4] offen sc1\n\t" \
                    "buffer_load_dwordx4 v[180:183], %[zt], %[rs], %[s4] offen offset:1024 sc1\n\t" \
                    "buffer_load_dwordx4 v[184:187], %[zt], %[rs], %[s4] offen offset:2048 sc1\n\t" \
                    "buffer_load_dwordx4 v[188:191], %[zt], %[rs], %[s4] offen offset:3072 sc1\n\t"
                const unsigned zso = O_Z + (unsigned)(s * ZTSEG) * 2u;
                if (__builtin_expect(budget <= 0, 0)) __builtin_amdgcn_raw_buffer_store_b32(1u, rws, 0u, 0u, 16);
                RT(6);
                const unsigned spa = (unsigned)((s & 1) * 2048 + (w < 3 ? w * 512 : 0) + lane * 8);
#define OST(r)
                unsigned long long t0_, t1_, t2_, t3_, t4_ = 0, bad;
                int tries = budget;
                unsigned po = poff, pm = pmoff, x0 = 4u * PAIR, x1 = 4096u;
                const unsigned mb = W_MEM + (unsigned)((s + 1) & 3) * 6u * PAIR;
                if (w < 3) asm volatile("s_nop 4\n\t" ZTL AL
                    "s_barrier\n\t"
                    OST("%[t0]")
                    "v_mov_b64 v[96:97], 0\n\t"
                    "v_mov_b64 v[98:99], 0\n\t"
                    "s_barrier\n\t"
                    "1:\n\t"
                    "buffer_load_dwordx4 v[208:211], %[po], %[rw], %[pb] offen sc1\n\t"
                    "buffer_load_dwordx4 v[212:215], %[po], %[rw], %[pb] offen offset:1024 sc1\n\t"
                    "buffer_load_dwordx4 v[216:219], %[po], %[rw], %[pb] offen offset:2048 sc1\n\t"
                    "buffer_load_dwordx4 v[220:223], %[po], %[rw], %[pb] offen offset:3072 sc1\n\t"
                    OST("%[t1]")
                    "s_waitcnt vmcnt(3)\n\t"
                    OST("%[t3]")
                    "v_smfmac_f32_16x16x64_bf16 v[96:99], v[208:211], v[160:167], %[ix]\n\t"
                    "s_waitcnt vmcnt(2)\n\t"
                    "v_smfmac_f32_16x16x64_bf16 v[96:99], v[212:215], v[168:175], %[ix]\n\t"
                    "s_waitcnt vmcnt(1)\n\t"
                    "v_smfmac_f32_16x16x64_bf16 v[96:99], v[216:219], v[176:183], %[ix]\n\t"
                    "s_waitcnt vmcnt(0)\n\t"
                    "v_smfmac_f32_16x16x64_bf16 v[96:99], v[220:223], v[184:191], %[ix]\n\t"
                    OST("%[t2]")
                    "s_nop 7\n\t"
                    "v_pk_add_f32 v[96:97], v[96:97], v[98:99]\n\t"
                    "ds_write_b64 %[sp], v[96:97]\n\t"
                    "v_cmp_u_f32 vcc, v96, v97\n\t"
                    "s_cbranch_vccz 2f\n\t"
                    "v_max3_u32 v100, v208, v208, v209\n\t"
                    "v_max3_u32 v100, v100, v210, v211\n\t"
                    "v_max3_u32 v100, v100, v212, v213\n\t"
                    "v_max3_u32 v100, v100, v214, v215\n\t"
                    "v_max3_u32 v100, v100, v216, v217\n\t"
                    "v_max3_u32 v100, v100, v218, v219\n\t"
                    "v_max3_u32 v100, v100, v220, v221\n\t"
                    "v_max3_u32 v100, v100, v222, v223\n\t"
                    "v_cmp_eq_u32 vcc, -1, v100\n\t"
                    "s_cbranch_vccz 2f\n\t"
                    "s_sub_i32 %[tr], %[tr], 1\n\t"
                    "s_cmp_lt_i32 %[tr], 1\n\t"
                    "s_cbranch_scc1 2f\n\t"
                    "v_xor_b32 %[po], %[x0], %[po]\n\t"
                    "s_xor_b32 %[x0], %[x0], 0x14000\n\t"
                    "s_waitcnt lgkmcnt(0)\n\t"
                    "v_mov_b64 v[96:97], 0\n\t"
                    "v_mov_b64 v[98:99], 0\n\t"
                    "s_branch 1b\n\t"
                    "2:\n\t"
                    "s_waitcnt lgkmcnt(0)\n\t"
                    "s_barrier\n\t"
                    "s_sleep 3\n\t"
                    : [tr] "+&s"(tries), [po] "+&v"(po), [x0] "+&s"(x0), [t0] "=&s"(t0_), [t1] "=&s"(t1_), [t2] "=&s"(t2_), [t3] "=&s"(t3_), "=&{v[160:167]}"(zf[0]), "=&{v[168:175]}"(zf[1]), "=&{v[176:183]}"(zf[2]), "=&{v[184:191]}"(zf[3])
                    : [zt] "v"(ztoff), [rs] "s"(rws), [so] "s"(zso), [s4] "s"(zso + 4096u), [sp] "v"(spa), [ix] "v"(sidx), [rw] "s"(rwin), [pb] "s"(pb)
                    : "v96", "v97", "v98", "v99", "v100", "v208", "v209", "v210", "v211", "v212", "v213", "v214", "v215", "v216", "v217", "v218", "v219", "v220", "v221", "v222", "v223", "vcc", "scc", "memory");
                else asm volatile("s_nop 4\n\t" ZTL AL
                    "s_barrier\n\t"
                    "buffer_load_dwordx4 v[128:131], %[zo], %[rw], %[zb] offen sc1\n\t"
                    "buffer_load_dwordx4 v[132:135], %[zo], %[rw], %[zb] offen offset:1024 sc1\n\t"
                    "buffer_load_dwordx4 v[136:139], %[zo], %[rw], %[zb] offen offset:2048 sc1\n\t"
                    "buffer_load_dwordx4 v[140:143], %[zo], %[rw], %[zb] offen offset:3072 sc1\n\t"
                    "buffer_load_dwordx4 v[144:147], %[zo], %[rw], %[z4] offen sc1\n\t"
                    "buffer_load_dwordx4 v[148:151], %[zo], %[rw], %[z4] offen offset:1024 sc1\n\t"
                    "buffer_load_dwordx4 v[152:155], %[zo], %[rw], %[z4] offen offset:2048 sc1\n\t"
                    "buffer_load_dwordx4 v[156:159], %[zo], %[rw], %[z4] offen offset:3072 sc1\n\t"
                    OST("%[t0]")
                    "v_mov_b64 v[96:97], 0\n\t"
                    "v_mov_b64 v[98:99], 0\n\t"
                    "s_barrier\n\t"
                    "s_sleep %[fd]\n\t"
                    "buffer_load_dwordx4 v[208:211], %[po], %[rw], %[pb] offen sc1\n\t"
                    "buffer_load_dwordx4 v[212:215], %[po], %[rw], %[pb] offen offset:1024 sc1\n\t"
                    "buffer_load_dwordx4 v[216:219], %[po], %[rw], %[pb] offen offset:2048 sc1\n\t"
                    "buffer_load_dwordx4 v[220:223], %[po], %[rw], %[pb] offen offset:3072 sc1\n\t"
                    "buffer_load_dwordx4 v[224:227], %[pm], %[rw], %[pb] offen sc1\n\t"
                    "buffer_load_dwordx4 v[228:231], %[pm], %[rw], %[pb] offen offset:1024 sc1\n\t"
                    "buffer_load_dwordx4 v[232:235], %[pm], %[rw], %[pb] offen offset:2048 sc1\n\t"
                    "buffer_load_dwordx4 v[236:239], %[pm], %[rw], %[pb] offen offset:3072 sc1\n\t"
                    OST("%[t1]")
                    "s_waitcnt vmcnt(8)\n\t"
                    "v_max3_u32 v101, v129, v131, v133\n\t"
                    "v_max3_u32 v101, v101, v135, v137\n\t"
                    "v_max3_u32 v101, v101, v139, v141\n\t"
                    "v_max3_u32 v101, v101, v143, v145\n\t"
                    "v_max3_u32 v101, v101, v147, v149\n\t"
                    "v_max3_u32 v101, v101, v151, v153\n\t"
                    "v_max3_u32 v101, v101, v155, v157\n\t"
                    "v_max_u32 v101, v101, v159\n\t"
                    "v_cmp_eq_u32 vcc, -1, v101\n\t"
                    "s_cbranch_vccz 3f\n\t"
                    "buffer_load_dwordx4 v[128:131], %[zo], %[rw], %[zb] offen sc1\n\t"
                    "buffer_load_dwordx4 v[132:135], %[zo], %[rw], %[zb] offen offset:1024 sc1\n\t"
                    "buffer_load_dwordx4 v[136:139], %[zo], %[rw], %[zb] offen offset:2048 sc1\n\t"
                    "buffer_load_dwordx4 v[140:143], %[zo], %[rw], %[zb] offen offset:3072 sc1\n\t"
                    "buffer_load_dwordx4 v[144:147], %[zo], %[rw], %[z4] offen sc1\n\t"
                    "buffer_load_dwordx4 v[148:151], %[zo], %[rw], %[z4] offen offset:1024 sc1\n\t"
                    "buffer_load_dwordx4 v[152:155], %[zo], %[rw], %[z4] offen offset:2048 sc1\n\t"
                    "buffer_load_dwordx4 v[156:159], %[zo], %[rw], %[z4] offen offset:3072 sc1\n\t"
                    "s_waitcnt vmcnt(0)\n\t"
                    "v_max3_u32 v101, v129, v131, v133\n\t"
                    "v_max3_u32 v101, v101, v135, v137\n\t"
                    "v_max3_u32 v101, v101, v139, v141\n\t"
                    "v_max3_u32 v101, v101, v143, v145\n\t"
                    "v_max3_u32 v101, v101, v147, v149\n\t"
                    "v_max3_u32 v101, v101, v151, v153\n\t"
                    "v_max3_u32 v101, v101, v155, v157\n\t"
                    "v_max_u32 v101, v101, v159\n\t"
                    "3:\n\t"
                    "v_permlane16_swap_b32 v128, v129\n\t"
                    "v_permlane16_swap_b32 v130, v131\n\t"
                    "v_permlane16_swap_b32 v132, v133\n\t"
                    "v_permlane16_swap_b32 v134, v135\n\t"
                    "v_permlane16_swap_b32 v136, v137\n\t"
                    "v_permlane16_swap_b32 v138, v139\n\t"
                    "v_permlane16_swap_b32 v140, v141\n\t"
                    "v_permlane16_swap_b32 v142, v143\n\t"
                    "v_permlane16_swap_b32 v144, v145\n\t"
                    "v_permlane16_swap_b32 v146, v147\n\t"
                    "v_permlane16_swap_b32 v148, v149\n\t"
                    "v_permlane16_swap_b32 v150, v151\n\t"
                    "v_permlane16_swap_b32 v152, v153\n\t"
                    "v_permlane16_swap_b32 v154, v155\n\t"
                    "v_permlane16_swap_b32 v156, v157\n\t"
                    "v_permlane16_swap_b32 v158, v159\n\t"
                    "s_mov_b32 vcc_lo, 0x00ff00ff\n\t"
                    "s_mov_b32 vcc_hi, 0x00ff00ff\n\t"
                    "s_waitcnt vmcnt(7)\n\t"
                    OST("%[t3]")
                    "v_smfmac_f32_16x16x64_bf16 v[96:99], v[208:211], v[160:167], %[ix]\n\t"
                    "s_waitcnt vmcnt(6)\n\t"
                    "v_smfmac_f32_16x16x64_bf16 v[96:99], v[212:215], v[168:175], %[ix]\n\t"
                    "s_waitcnt vmcnt(5)\n\t"
                    "v_smfmac_f32_16x16x64_bf16 v[96:99], v[216:219], v[176:183], %[ix]\n\t"
                    "s_waitcnt vmcnt(4)\n\t"
                    "v_smfmac_f32_16x16x64_bf16 v[96:99], v[220:223], v[184:191], %[ix]\n\t"
                    OB(4)
                    "s_waitcnt vmcnt(3)\n\t"
                    "v_smfmac_f32_16x16x64_bf16 v[96:99], v[224:227], v[128:135], %[ix]\n\t"
                    OB(5)
                    "s_waitcnt vmcnt(2)\n\t"
                    "v_smfmac_f32_16x16x64_bf16 v[96:99], v[228:231], v[136:143], %[ix]\n\t"
                    OB(6)
                    "s_waitcnt vmcnt(1)\n\t"
                    "v_smfmac_f32_16x16x64_bf16 v[96:99], v[232:235], v[144:151], %[ix]\n\t"
                    "s_waitcnt vmcnt(0)\n\t"
                    "v_smfmac_f32_16x16x64_bf16 v[96:99], v[236:239], v[152:159], %[ix]\n\t"
                    OST("%[t2]")
                    "s_waitcnt lgkmcnt(0)\n\t"
                    "v_pk_add_f32 v[102:103], v[102:103], v[104:105]\n\t"
                    "v_pk_add_f32 v[102:103], v[102:103], v[106:107]\n\t"
                    "s_nop 4\n\t"
                    "6:\n\t"
                    "v_pk_add_f32 v[96:97], v[96:97], v[102:103]\n\t"
                    "v_pk_add_f32 v[96:97], v[96:97], v[98:99]\n\t"
                    "v_cmp_u_f32 %[bad], v96, v97\n\t"
                    "s_nop 0\n\t"
                    "v_cndmask_b32_dpp v98, v97, v96, vcc row_ror:8 row_mask:0xf bank_mask:0xf\n\t"
                    "s_not_b64 vcc, vcc\n\t"
                    "v_cndmask_b32_dpp v99, v96, v97, vcc row_ror:8 row_mask:0xf bank_mask:0xf\n\t"
                    "s_cmp_lg_u64 %[bad], 0\n\t"
                    "v_cvt_pk_bf16_f32 v98, v98, v99\n\t"
                    "s_cbranch_scc1 4f\n\t"
                    "2:\n\t"
                    "buffer_store_dword v98, %[mo], %[rw], %[m0] offen sc1\n\t"
                    "buffer_store_dword v98, %[mo], %[rw], %[m1] offen sc1\n\t"
                    "buffer_store_dword v98, %[mo], %[rw], %[m2] offen sc1\n\t"
                    "buffer_store_dword v98, %[mo], %[rw], %[m3] offen sc1\n\t"
                    OST("%[t4]")
                    "s_branch 5f\n\t"
                    "4:\n\t"
                    "v_min_u32 v98, -2, v98\n\t"
                    "v_max3_u32 v100, v101, v208, v209\n\t"
                    "v_max3_u32 v100, v100, v210, v211\n\t"
                    "v_max3_u32 v100, v100, v212, v213\n\t"
                    "v_max3_u32 v100, v100, v214, v215\n\t"
                    "v_max3_u32 v100, v100, v216, v217\n\t"
                    "v_max3_u32 v100, v100, v218, v219\n\t"
                    "v_max3_u32 v100, v100, v220, v221\n\t"
                    "v_max3_u32 v100, v100, v222, v223\n\t"
                    "v_max3_u32 v100, v100, v224, v225\n\t"
                    "v_max3_u32 v100, v100, v226, v227\n\t"
                    "v_max3_u32 v100, v100, v228, v229\n\t"
                    "v_max3_u32 v100, v100, v230, v231\n\t"
                    "v_max3_u32 v100, v100, v232, v233\n\t"
                    "v_max3_u32 v100, v100, v234, v235\n\t"
                    "v_max3_u32 v100, v100, v236, v237\n\t"
                    "v_max3_u32 v100, v100, v238, v239\n\t"
                    "v_cmp_eq_u32 %[bad], -1, v100\n\t"
                    "s_not_b64 vcc, vcc\n\t"
                    "s_cmp_eq_u64 %[bad], 0\n\t"
                    "s_cbranch_scc1 2b\n\t"
                    "s_sub_i32 %[tr], %[tr], 1\n\t"
                    "s_cmp_lt_i32 %[tr], 1\n\t"
                    "s_cbranch_scc1 2b\n\t"
                    "v_xor_b32 %[po], %[x0], %[po]\n\t"
                    "s_xor_b32 %[x0], %[x0], 0x14000\n\t"
                    "v_xor_b32 %[pm], %[x1], %[pm]\n\t"
                    "s_xor_b32 %[x1], %[x1], 0x5000\n\t"
                    "buffer_load_dwordx4 v[128:131], %[zo], %[rw], %[zb] offen sc1\n\t"
                    "buffer_load_dwordx4 v[132:135], %[zo], %[rw], %[zb] offen offset:1024 sc1\n\t"
                    "buffer_load_dwordx4 v[136:139], %[zo], %[rw], %[zb] offen offset:2048 sc1\n\t"
                    "buffer_load_dwordx4 v[140:143], %[zo], %[rw], %[zb] offen offset:3072 sc1\n\t"
                    "buffer_load_dwordx4 v[144:147], %[zo], %[rw], %[z4] offen sc1\n\t"
                    "buffer_load_dwordx4 v[148:151], %[zo], %[rw], %[z4] offen offset:1024 sc1\n\t"
                    "buffer_load_dwordx4 v[152:155], %[zo], %[rw], %[z4] offen offset:2048 sc1\n\t"
                    "buffer_load_dwordx4 v[156:159], %[zo], %[rw], %[z4] offen offset:3072 sc1\n\t"
                    "buffer_load_dwordx4 v[208:211], %[po], %[rw], %[pb] offen sc1\n\t"
                    "buffer_load_dwordx4 v[212:215], %[po], %[rw], %[pb] offen offset:1024 sc1\n\t"
                    "buffer_load_dwordx4 v[216:219], %[po], %[rw], %[pb] offen offset:2048 sc1\n\t"
                    "buffer_load_dwordx4 v[220:223], %[po], %[rw], %[pb] offen offset:3072 sc1\n\t"
                    "buffer_load_dwordx4 v[224:227], %[pm], %[rw], %[pb] offen sc1\n\t"
                    "buffer_load_dwordx4 v[228:231], %[pm], %[rw], %[pb] offen offset:1024 sc1\n\t"
                    "buffer_load_dwordx4 v[232:235], %[pm], %[rw], %[pb] offen offset:2048 sc1\n\t"
                    "buffer_load_dwordx4 v[236:239], %[pm], %[rw], %[pb] offen offset:3072 sc1\n\t"
                    "v_mov_b64 v[96:97], 0\n\t"
                    "v_mov_b64 v[98:99], 0\n\t"
                    "s_waitcnt vmcnt(0)\n\t"
                    "v_max3_u32 v101, v129, v131, v133\n\t"
                    "v_max3_u32 v101, v101, v135, v137\n\t"
                    "v_max3_u32 v101, v101, v139, v141\n\t"
                    "v_max3_u32 v101, v101, v143, v145\n\t"
                    "v_max3_u32 v101, v101, v147, v149\n\t"
                    "v_max3_u32 v101, v101, v151, v153\n\t"
                    "v_max3_u32 v101, v101, v155, v157\n\t"
                    "v_max_u32 v101, v101, v159\n\t"
                    "v_permlane16_swap_b32 v128, v129\n\t"
                    "v_permlane16_swap_b32 v130, v131\n\t"
                    "v_permlane16_swap_b32 v132, v133\n\t"
                    "v_permlane16_swap_b32 v134, v135\n\t"
                    "v_permlane16_swap_b32 v136, v137\n\t"
                    "v_permlane16_swap_b32 v138, v139\n\t"
                    "v_permlane16_swap_b32 v140, v141\n\t"
                    "v_permlane16_swap_b32 v142, v143\n\t"
                    "v_permlane16_swap_b32 v144, v145\n\t"
                    "v_permlane16_swap_b32 v146, v147\n\t"
                    "v_permlane16_swap_b32 v148, v149\n\t"
                    "v_permlane16_swap_b32 v150, v151\n\t"
                    "v_permlane16_swap_b32 v152, v153\n\t"
                    "v_permlane16_swap_b32 v154, v155\n\t"
                    "v_permlane16_swap_b32 v156, v157\n\t"
                    "v_permlane16_swap_b32 v158, v159\n\t"
                    "s_nop 1\n\t"
                    "v_smfmac_f32_16x16x64_bf16 v[96:99], v[208:211], v[160:167], %[ix]\n\t"
                    "v_smfmac_f32_16x16x64_bf16 v[96:99], v[212:215], v[168:175], %[ix]\n\t"
                    "v_smfmac_f32_16x16x64_bf16 v[96:99], v[216:219], v[176:183], %[ix]\n\t"
                    "v_smfmac_f32_16x16x64_bf16 v[96:99], v[220:223], v[184:191], %[ix]\n\t"
                    "v_smfmac_f32_16x16x64_bf16 v[96:99], v[224:227], v[128:135], %[ix]\n\t"
                    "v_smfmac_f32_16x16x64_bf16 v[96:99], v[228:231], v[136:143], %[ix]\n\t"
                    "v_smfmac_f32_16x16x64_bf16 v[96:99], v[232:235], v[144:151], %[ix]\n\t"
                    "v_smfmac_f32_16x16x64_bf16 v[96:99], v[236:239], v[152:159], %[ix]\n\t"
                    "s_nop 7\n\t"
                    "s_branch 6b\n\t"
                    "5:\n\t"
                    : [tr] "+&s"(tries), [po] "+&v"(po), [pm] "+&v"(pm), [x0] "+&s"(x0), [x1] "+&s"(x1), [bad] "=&s"(bad), [t0] "=&s"(t0_), [t1] "=&s"(t1_), [t2] "=&s"(t2_), [t3] "=&s"(t3_), [t4] "=&s"(t4_), "=&{v[160:167]}"(zf[0]), "=&{v[168:175]}"(zf[1]), "=&{v[176:183]}"(zf[2]), "=&{v[184:191]}"(zf[3])
                    : [zt] "v"(ztoff), [rs] "s"(rws), [so] "s"(zso), [s4] "s"(zso + 4096u), [zo] "v"(zoff), [sp] "v"(spa), [ix] "v"(sidx), [rw] "s"(rwin), [zb] "s"(zb), [z4] "s"(zb + 4096u), [pb] "s"(pb), [mo] "v"(moff), [fd] "n"(FDL),
                      [m0] "s"(mb), [m1] "s"(mb + PAIR), [m2] "s"(mb + mc2), [m3] "s"(mb + mc2 + PAIR)
                    : "v96", "v97", "v98", "v99", "v100", "v101", "v102", "v103", "v104", "v105", "v106", "v107", "v128", "v129", "v130", "v131", "v132", "v133", "v134", "v135", "v136", "v137", "v138", "v139", "v140", "v141", "v142", "v143", "v144", "v145", "v146", "v147", "v148", "v149", "v150", "v151", "v152", "v153", "v154", "v155", "v156", "v157", "v158", "v159", "v208", "v209", "v210", "v211", "v212", "v213", "v214", "v215", "v216", "v217", "v218", "v219", "v220", "v221", "v222", "v223", "v224", "v225", "v226", "v227", "v228", "v229", "v230", "v231", "v232", "v233", "v234", "v235", "v236", "v237", "v238", "v239", "vcc", "scc", "memory");
                budget = tries;
                if (w == 3) *(volatile LAS int*)(smem + 4100) = s;
                __builtin_amdgcn_s_sleep(DPS - 4);
                if (w == 3) {
                    st128(sent4(), rwin, ra, W_MEM + (unsigned)(s & 3) * 6u * PAIR, 16);
                    if (lane == 0) __builtin_amdgcn_raw_buffer_store_b64((u32x2){(unsigned)s - 1u, (unsigned)max(zr, s)}, rwin, 0u, W_GO + fb + (unsigned)o * 128u, 16);
                }
                sent(s);
                zb = zb == W_ZM + 2u * NOWN * PAIR ? W_ZM : zb + NOWN * PAIR;
            }
        } else if (w == 4) {
            const unsigned fo = (unsigned)lane * 128u + (unsigned)(o / 10) * 8192u;
            int zk = -1;
            if (lane < 3) *(volatile LAS int*)(smem + 4096 + lane * 4) = -1;
            for (int s = 0; s + 1 < NSEG; ++s) {
                asm volatile("s_waitcnt lgkmcnt(0)\n\ts_barrier\n\ts_barrier" ::: "memory");
                int f = 0;
                const bool look = zk < min(s + ZLA, NSEG - 1);
                if (look) { __builtin_amdgcn_s_sleep(6); f = (int)__builtin_amdgcn_raw_buffer_load_b32(rwin, fo, W_FZ + fb, 16); }
                SB();
                BAR0();
                if (look) for (int again = 0;; ++again) {
                    f = min(f, __builtin_amdgcn_update_dpp(f, f, 0xB1, 0xF, 0xF, false)); f = min(f, __builtin_amdgcn_update_dpp(f, f, 0x4E, 0xF, 0xF, false));
                    f = min(f, __builtin_amdgcn_update_dpp(f, f, 0x141, 0xF, 0xF, false)); f = min(f, __builtin_amdgcn_update_dpp(f, f, 0x140, 0xF, 0xF, false));
                    zk = seg_done(MXV, min(__builtin_amdgcn_readlane(f, 0), __builtin_amdgcn_readlane(f, 16)), min(__builtin_amdgcn_readlane(f, 32), __builtin_amdgcn_readlane(f, 48)));
                    *(volatile LAS int*)(smem + 4096) = zk;
                    if (zk > s || again) break;
                    __builtin_amdgcn_s_sleep(2);
                    f = (int)__builtin_amdgcn_raw_buffer_load_b32(rwin, fo, W_FZ + fb, 16);
                }
                *(volatile LAS int*)(smem + 4104) = s;
            }
        } else if (w < 7) {
            for (int s = 0; s < 3 * NSEG - 3; ++s) BAR0();
        } else {
            Tk tk = tk_init();
            __builtin_amdgcn_s_setprio(3);
            for (int s = -1; s + 1 < NSEG; ++s) {
                if (s >= 0) {
                    const unsigned ta0 = ck.ta, mm0 = ck.ma, tp = s ? tpp + lt + xs + M1 : tpp - M2 - XS0 - (lay ? PEN0 : PEN00);
                    wait_fine_bar(tp - DZP, ck);
                    wait_cyc_bar(cyc_at(ck, sx24(tp - ck.ta)));
                    clk_rate(ck, ta0, mm0);
                    asm volatile("s_barrier ; 7" ::: "memory");
                }
                if (s) sched(tk, max(s, 0));
                if (s < 0) continue;
                if (o == 0) __builtin_amdgcn_raw_buffer_store_b32(tpp, rws, 0u, 4096u + 540000u + (unsigned)(lay * 64 + s) * 4u, 0);
                RTS();
            }
            __builtin_amdgcn_s_setprio(0);
            if (o < 2) due(E_OUT + o, (sx24(tpp + ML - 4u * (unsigned)__builtin_amdgcn_s_memrealtime()) >> 2) + DOUT);
        }
        {
            const int e = w >> 2;
            const unsigned ooff = (unsigned)((S_ - 2 * SEG + 8 * w + (lane >> 3)) * D_ + o * 16 + (lane & 7) * 2) * 2u;
            unsigned pl = W_PL + fb + e * 5u * PAIR, zs = W_ZM + (NSEG - 2 + e + lay * NSEG) % 3 * (NOWN * PAIR);
            asm volatile("" : "+s"(pl), "+s"(zs));
            if (e) {
                if (w < 7) __builtin_amdgcn_s_sleep(DPS);
                sent(NSEG - 2);
                zt_wait(NSEG - 1);
                const unsigned zl = O_Z + (unsigned)((NSEG - 1) * ZTSEG) * 2u;
#pragma unroll
                for (int q = 0; q < 4; ++q) zf[q] = cat8(__builtin_amdgcn_raw_buffer_load_b128(rws, ztoff + q * 2048u, zl, 16), __builtin_amdgcn_raw_buffer_load_b128(rws, ztoff + q * 2048u + 1024u, zl, 16));
            }
            if (DR0 && !e) __builtin_amdgcn_s_sleep(DR0);
            const unsigned xr = __builtin_amdgcn_raw_buffer_load_b32(rws, ooff, O_X1, 16);
            SB();
            if (w == 7) wait_fine_bar(tpp + ML, ck); else if (e) BAR0();
            f32x4 c[2];
            for (unsigned spins = 0;;) {
                asm volatile("" ::: "memory");
                u32x4 pt[16], ptm[2][2], zm[2];
#pragma unroll
                for (int q = 0; q < 16; ++q) pt[q] = __builtin_amdgcn_raw_buffer_load_b128(rwin, (unsigned)(16 * hg) * 1024u + lane * 16u + (q & 3) * 1024u, pl + (q >> 2) * 4096u, 16);
#pragma unroll
                for (int q = 0; q < 2; ++q) zm[q] = __builtin_amdgcn_raw_buffer_load_b128(rwin, zoff + hg * 2048u + q * 1024u, zs, 16);
#pragma unroll
                for (int q = 0; q < 4; ++q) ptm[q >> 1][q & 1] = __builtin_amdgcn_raw_buffer_load_b128(rwin, (unsigned)(hg * 2048 + lane * 16) + (q & 1) * 1024u, pl + 65536u + (q >> 1) * 8192u, 16);
                SB();
                if (!e && !spins) BAR0();
                unsigned m = max(smax(zm[0]), smax(zm[1]));
                #pragma unroll
                for (int q = 0; q < 16; ++q) { c[q & 1] = mfma32(half8(zf[q >> 2], q >> 1 & 1), pt[q], q < 2 ? z4 : c[q & 1]); m = max(m, smax(pt[q])); }
#pragma unroll
                for (int q = 0; q < 4; ++q) { c[q >> 1] = mfma32((u32x4){zm[q & 1][0], zm[q & 1][2], zm[q & 1][1], zm[q & 1][3]}, ptm[q >> 1][q & 1], c[q >> 1]); m = max(m, smax(ptm[q >> 1][q & 1])); }
                if (!__any(m == 0xFFFFFFFFu)) break;
                if (spin_fail(spins, rws)) break;
            }
#pragma unroll
            for (int t = 0; t < 2; ++t)
#pragma unroll
                for (int r = 0; r < 4; ++r) *(float*)(op + w * 2048 + (16 * t + fr) * 64 + pi16(4 * fq + r) * 4) = c[t][r];
            BAR();
            const char* src = op + e * 8192 + hg * 512 + lane * 8;
            const f32x2 y = (*(const f32x2*)src + *(const f32x2*)(src + 2048)) + (*(const f32x2*)(src + 4096) + *(const f32x2*)(src + 6144));
            __builtin_amdgcn_raw_buffer_store_b32(nosent(pack2(lo2f(xr) + bfr(y[0]), hi2f(xr) + bfr(y[1]))), rout, ooff, 0u, 16);
            __builtin_amdgcn_s_sleep(4);
            const u32x4 sv = sent4();
            st128(sv, rwin, zoff + hg * 2048u, zs, 16); st128(sv, rwin, zoff + hg * 2048u + 1024u, zs, 16);
            if (w == 3) st128(sv, rwin, ra, W_MEM + 18u * PAIR, 16);
            if (lay && o == 0 && w == 0) __builtin_amdgcn_raw_buffer_store_b64(get_tag(), rws, 0u, 8u, 16);
        }
        SST(23);
    } else if (bid < NREC + 30) {
        LANES();
        const int cg = (bid - NREC) % 10;
        const unsigned ztoff = (unsigned)((4 * cg * H_ + 4 * w) * 64 + lane) * 16u;
        const unsigned ptoff = (unsigned)(8 * w * 64 + lane) * 16u, ptmoff = 65536u + (unsigned)(4 * w * 16 + lane) * 16u;
        const int tk = (w & 3) * 8 + (lane >> 3);
        const unsigned ooff = (unsigned)(tk * D_ + cg * 64 + (lane & 7) * 8) * 2u;
        const unsigned gooff = W_GO + (unsigned)((4 * cg + (lane & 2 ? 3 : 0)) * 128 + (lane & 1) * 4);
        int rdy = -1, go = -1;
        auto ask = [&]() {
            asm volatile("" ::: "memory");
            return (int)__builtin_amdgcn_raw_buffer_load_b32(rwin, gooff, fb, 16);
        };
        auto look = [&](int f) {
            go = min(__builtin_amdgcn_readlane(f, 33), __builtin_amdgcn_readlane(f, 35));
            rdy = max(rdy, max(__builtin_amdgcn_readlane(f, 32), __builtin_amdgcn_readlane(f, 34)));
        };
        auto wait_rdy = [&](int& x, int v, int s) {
            for (unsigned spins = 0; x < v;) {
                look(ask());
                if (x >= v) break;
                if (spin_fail(spins, rws)) break;
                if (s >= NSEG - 5) __builtin_amdgcn_s_sleep(OSLP2); else __builtin_amdgcn_s_sleep(OSLP);
            }
        };
        if (w >= 4) wait_h(rws, rwin, fb, 3, hrdy);
        u32x4 zf[4][4];
        auto load_z = [&](int s) {
#pragma unroll
            for (int q = 0; q < 16; ++q) zf[q >> 2][q & 3] = __builtin_amdgcn_raw_buffer_load_b128(rws, ztoff + (q & 3) * 1024u, O_Z + (unsigned)(s * ZTSEG) * 2u + (q >> 2) * (H_ * 1024u), 16);
            SB();
        };
        wait_rdy(go, (bid - NREC) / 10, 0);
        load_z((bid - NREC) / 10);
        for (int s = (bid - NREC) / 10;; s += 3) {
            u32x4 xr, zx[4], pt[8], ptm[2];
            asm volatile("" : "=v"(xr));
            RT(0);
            wait_rdy(rdy, s, s);
            RT(1);
            if (s >= NSEG - 8 && cg == 0 && w == 0) due(E_OUT + NSEG - 1 - s, s + 3 < NSEG ? DOU3 : DOU2);
            const unsigned ptb = O_PT + (unsigned)(s * PTSEG) * 2u;
#pragma unroll
            for (int q = 0; q < 8; ++q) pt[q] = __builtin_amdgcn_raw_buffer_load_b128(rws, ptoff + q * 1024u, ptb, 16);
#pragma unroll
            for (int q = 0; q < 2; ++q) ptm[q] = __builtin_amdgcn_raw_buffer_load_b128(rws, ptmoff + q * 8192u, ptb, 16);
#pragma unroll
            for (int q = 0; q < 4; ++q) zx[q] = __builtin_amdgcn_raw_buffer_load_b128(rws, tid * 16u, O_Z2 + (unsigned)(s * NOWN + 4 * cg + q) * 8192u, 16);
            if (w >= 4) xr = __builtin_amdgcn_raw_buffer_load_b128(rws, ooff, O_X1 + (unsigned)(s * SEG * D_ * 2), 16);
            const int f = ask();
            SB();
            char* op = smem + (s & 1) * 65536;
            f32x4 c[8];
#pragma unroll
            for (int q = 0; q < 4; ++q)
#pragma unroll
                for (int u = 0; u < 8; ++u) c[u] = mfma32(zf[u >> 1][q], pt[2 * q + (u & 1)], q ? c[u] : z4);
#pragma unroll
            for (int u = 0; u < 8; ++u) {
                const int tq = u & 1, t = u >> 1;
                *(f32x4*)(op + w * 8192 + (16 * tq + fr) * 256 + (((t * 4 + fq) ^ (fr & 7)) << 4)) = mfma32((u32x4){zx[t][0], zx[t][2], zx[t][1], zx[t][3]}, ptm[tq], c[u]);
            }
            look(f);
            const bool pre = go >= s + 3 && s < NSEG - 14;
            if (pre) load_z(s + 3);
            BAR();
            if (lay == 0 && s >= 3 && s < NSEG - 11 && tid == 0) __builtin_amdgcn_raw_buffer_store_b32(0u, rwin, 0u, W_OUT + (unsigned)((s - 3) * 128 + cg * 4), 16);
            if (w >= 4) {
                f32x2 x[4];
#pragma unroll
                for (int q = 0; q < 4; ++q) {
                    const char* sp = op + tk * 256 + ((((lane & 6) * 2 + q) ^ (tk & 7)) << 4) + (lane & 1) * 8;
                    x[q] = *(const f32x2*)sp;
#pragma unroll
                    for (int ww = 1; ww < 8; ++ww) x[q] += *(const f32x2*)(sp + ww * 8192);
                }
                const u32x4 r = {pack2(lo2f(xr[0]) + bfr(x[0][0]), hi2f(xr[0]) + bfr(x[2][0])), pack2(lo2f(xr[1]) + bfr(x[1][0]), hi2f(xr[1]) + bfr(x[3][0])),
                                 pack2(lo2f(xr[2]) + bfr(x[0][1]), hi2f(xr[2]) + bfr(x[2][1])), pack2(lo2f(xr[3]) + bfr(x[1][1]), hi2f(xr[3]) + bfr(x[3][1]))};
                st128(((u32x4){nosent(r[0]), nosent(r[1]), nosent(r[2]), nosent(r[3])}), rout, ooff, (unsigned)(s * SEG * D_ * 2), 16);
            } else if (!pre) __builtin_amdgcn_s_sleep(ODL);
            if (lay == 0 && s >= NSEG - 14 && s < NSEG - 8) {
                VMWAIT();
                BAR0();
                if (tid == 0) __builtin_amdgcn_raw_buffer_store_b32(0u, rwin, 0u, W_OUT + (unsigned)(s * 128 + cg * 4), 16);
            }
            RT(2);
            RTS();
            if (s < 8) SST(8 + s);
            if (s >= 56) SST(s - 40);
            if (s + 3 >= NSEG - 2) break;
            if (!pre) { wait_rdy(go, s + 3, 0); load_z(s + 3); }
        }
    } else if (lay == 0 && bid < NWRK) {
        if (w == 0) for (unsigned spins = 0; (int)__builtin_amdgcn_raw_buffer_load_b32(rwin, 0u, W_E + (E_OUT + 3) * 128u, 16) == -1 && !spin_fail(spins, rws);) {
            __builtin_amdgcn_s_sleep(64);
            asm volatile("" ::: "memory");
        }
    } else if (lay == 0) {
        LANES();
        const RowTw tw = row_tw(lane);
        for (int u = (256 - bid) & 15; u < 256; u += 16) {
            if (w == 0) {
                if (u >= 224) await(E_OUT + (255 - u >> 2), 1, 0);
                else for (unsigned spins = 0;;) {
                    asm volatile("" ::: "memory");
                    const unsigned f = __builtin_amdgcn_raw_buffer_load_b32(rwin, (unsigned)min(lane, 9) * 4u, W_OUT + (unsigned)(u >> 2) * 128u, 16);
                    if (!__any(f != 0u)) break;
                    if (spin_fail(spins, rws)) break;
                    __builtin_amdgcn_s_sleep(WSLP);
                }
            }
            BAR0();
            SST(24);
            const unsigned fo = W_FA + (unsigned)((u >> 2) * 128 + (u & 3) * 4);
            a1_unit<4, 512>(rout, u * 8, u >= 224, rws, smem, tw, w, lane, [&](int k) {
                if (k == 0 && w == 0 && u >= 224 && !(u & 3)) due(E_A1 + (u - 224 >> 2), DA1);
                if (k >= 0) SST(8 + k);
            });
            SST(25);
            if (u >= 224) continue;
            VMWAIT();
            BAR0();
            if (tid == 0) __builtin_amdgcn_raw_buffer_store_b32(0u, rwin, 0u, fo, 16);
        }
    }
    if (lay == 0 && w == 0 && bid >= NREC && bid <= NWRK) await(E_OUT + 1, 1, bid < NREC + 32 ? -DOW : -60);
    SST(31);
    }
    __syncthreads();
    if (w == 0) __builtin_amdgcn_raw_buffer_store_b64(__builtin_bit_cast(u32x2, __builtin_amdgcn_s_memrealtime()), rws, 0u, 4096u + 524288u + (1024u + bid) * 8u, 0);
}

constexpr int L_DBG = 0;

__global__ void enc_kernel(char* ws) {
    const unsigned long long* t = (const unsigned long long*)(ws + 4096 + 524288);
    const unsigned* g = (const unsigned*)(ws + 4096 + 540000);
    unsigned long long mn[5], mx[5];
    for (int i = 0; i < 5; ++i) { mn[i] = ~0ull; mx[i] = 0; for (int j = 0; j < 256; ++j) { mn[i] = t[i * 256 + j] < mn[i] ? t[i * 256 + j] : mn[i]; mx[i] = t[i * 256 + j] > mx[i] ? t[i * 256 + j] : mx[i]; } }
    auto d = [](unsigned a, unsigned b) { return (a - b) & 0xFFFFFFu; };
    unsigned v = 0;
    if (ENC == 1) v = (unsigned)(mx[4] - mn[0]);
    if (ENC == 2) v = 4u * (unsigned)(mn[1] - mn[0]);
    if (ENC == 3) v = d(g[0], 4u * (unsigned)mn[0]);
    if (ENC == 4) v = d(g[8], g[0]);
    if (ENC == 5) v = d(g[62], g[8]) / 4u;
    if (ENC == 6) v = d(g[64], g[62]);
    if (ENC == 7) v = d(g[72], g[64]);
    if (ENC == 8) v = d(g[126], g[72]) / 4u;
    if (ENC == 9) v = d(4u * (unsigned)mx[4], g[126]);
    const unsigned long long te = mn[0] + 40000ull + (v > 60000u ? 60000u : v);
    while (__builtin_amdgcn_s_memrealtime() < te) __builtin_amdgcn_s_sleep(1);
}
extern "C" void kernel_launch(void* const* d_in, const int* in_sizes, int n_in, void* d_out, int out_size, void* d_ws, size_t ws_size,
                              hipStream_t stream) {
    char* ws = (char*)d_ws;
    hipFuncSetAttribute((const void*)b3_kernel, hipFuncAttributeMaxDynamicSharedMemorySize, L_END + L_DBG);
    char* win = (char*)(((uintptr_t)(ws + OFF_WIN) + 0x1FFFFFu) & ~(uintptr_t)0x1FFFFFu);
    RecArgs ra{(unsigned*)ws, win, (const u16*)d_in[4], (const u16*)d_in[0], (u16*)d_out, {(const u16*)d_in[5], (const u16*)d_in[6], (const u16*)d_in[7], (const u16*)d_in[8], (const u16*)d_in[1], (const u16*)d_in[2]}};
    b3_kernel<<<256, 512, L_END + L_DBG, stream>>>(ra);
    enc_kernel<<<1, 1, 0, stream>>>(ws);
}
```

```cpp
#include <hip/hip_runtime.h>
#include <hip/hip_bf16.h>
#include <stdint.h>

typedef unsigned short u16;
typedef __attribute__((ext_vector_type(8))) __bf16 bf16x8;
typedef __attribute__((ext_vector_type(4))) short s16x4;
typedef __attribute__((ext_vector_type(4))) float f32x4;
typedef __attribute__((ext_vector_type(8))) float f32x8;
typedef __attribute__((ext_vector_type(16))) float f32x16;
typedef __attribute__((ext_vector_type(4))) unsigned u32x4;
typedef __attribute__((ext_vector_type(2))) unsigned u32x2;
typedef __attribute__((ext_vector_type(8))) unsigned u32x8;
typedef __attribute__((ext_vector_type(16))) __bf16 bf16x16;
typedef __attribute__((ext_vector_type(2))) unsigned short u16x2;
#define LAS __attribute__((address_space(3)))
template <int N> struct IC { static constexpr int v = N; };

constexpr int S_ = 2048, D_ = 640, DF = 321, H_ = 32, DH = 128, HD = 4096, NM = 8, SEG = 32, NSEG = 64;
constexpr int NQKV = 3 * HD;
constexpr int NOWN = 40;
constexpr int ZTR = NSEG;
constexpr size_t ZTSEG = (size_t)NOWN * H_ * 512;
constexpr unsigned PAIR = 16384, HALF = 8192;
constexpr unsigned W_MEM = 0, W_P = W_MEM + 24 * PAIR, W_T = W_P + 40 * PAIR, W_GO = W_T + 8 * PAIR, W_OUT = W_GO + PAIR, W_FQ = W_OUT + PAIR, W_FZ = W_FQ + PAIR,
                   W_H = W_FZ + 4 * PAIR, W_FA = W_H + 8192, W_FB = W_GO + 8192, W_E = W_FB + 4096, W_PL = W_H + PAIR, W_FL = W_PL + 10 * PAIR - W_GO, W_PROBE = W_GO + 2 * W_FL,
                   W_ZM = 2u * 1024 * 1024, W_END = W_ZM + 120 * PAIR;
constexpr unsigned W_ST = W_PROBE + 2 * PAIR, W_ROT = W_ST + 8192, W_ARM = W_ROT + 128, W_CFG = W_ARM + 128;
static_assert(W_ROT + PAIR <= 2u * 1024 * 1024 && W_PROBE % (2 * PAIR) == 0, "window");

#define WAUX 0
#define FPAD 512
constexpr int GF = 32768 + FPAD, YS = 12288 + FPAD;
static_assert(FPAD >= 16, "factors");
constexpr int NG = 81;
constexpr size_t al(size_t x) { return (x + 255) & ~(size_t)255; }
constexpr size_t OFF_CTL  = 0;
constexpr size_t OFF_WFT  = 4096 + 4 * 1024 * 1024;
constexpr size_t OFF_F    = OFF_WFT + al((size_t)4 * NG * S_ * 4);
constexpr size_t OFF_X1   = OFF_F + (size_t)NG * GF;
constexpr size_t OFF_X2   = OFF_X1 + al((size_t)S_ * D_ * 2);
constexpr size_t OFF_Y0   = OFF_X2 + al((size_t)S_ * D_ * 2);
constexpr size_t OFF_H    = OFF_Y0 + (size_t)NG * YS;
constexpr size_t OFF_ARM  = OFF_Y0 - 256 * D_ * 2, ARM_END = OFF_H + 8 * 40960;
constexpr size_t OFF_Z    = OFF_H + (size_t)(NSEG + 4) * 40960;
constexpr unsigned HS = 2097152 + 4352;
constexpr size_t OFF_QK   = (OFF_Z + ZTSEG * ZTR * 2 + 0x1FFFFF) & ~(size_t)0x1FFFFF, OFF_Z2 = OFF_QK + NSEG * 16384, OFF_PT = OFF_Z2 + NSEG * 10240;
constexpr size_t OFF_WIN  = OFF_QK + (size_t)H_ * 2097152;
constexpr size_t OFF_END  = OFF_WIN + (size_t)6 * 1024 * 1024;
static_assert(OFF_END <= (size_t)256 * 1024 * 1024, "workspace");

#define st128(v, r, vo, so, aux) do { const u32x4 d_ = (v); __builtin_amdgcn_raw_buffer_store_b128(d_, r, vo, so, aux); asm volatile("s_nop 1" :: "v"(d_)); } while (0)
__device__ __forceinline__ float bf2f(u16 v) { return __uint_as_float((unsigned)v << 16); }
__device__ __forceinline__ u16 f2bf(float f) { __bf16 b = (__bf16)f; return __builtin_bit_cast(u16, b); }
__device__ __forceinline__ float bfr(float f) { return (float)(__bf16)f; }
typedef __attribute__((ext_vector_type(2))) float f32x2;
typedef __attribute__((ext_vector_type(2))) __bf16 bf16x2;
__device__ __forceinline__ unsigned pack2(float a, float b) { return __builtin_bit_cast(unsigned, __builtin_convertvector((f32x2){a, b}, bf16x2)); }
typedef __attribute__((ext_vector_type(2))) _Float16 f16x2;
typedef __attribute__((ext_vector_type(8))) _Float16 f16x8;
__device__ __forceinline__ unsigned packh(float a, float b) { return __builtin_bit_cast(unsigned, __builtin_convertvector((f32x2){a, b}, f16x2)); }
__device__ __forceinline__ f32x4 mfmah(u32x4 a, u32x4 b) { return __builtin_amdgcn_mfma_f32_16x16x32_f16(__builtin_bit_cast(f16x8, a), __builtin_bit_cast(f16x8, b), (f32x4){0.f, 0.f, 0.f, 0.f}, 0, 0, 0); }
__device__ __forceinline__ float lo2f(unsigned v) { return __uint_as_float(v << 16); }
__device__ __forceinline__ float hi2f(unsigned v) { return __uint_as_float(v & 0xFFFF0000u); }
__device__ __forceinline__ float fmx(float a, float b) { return __builtin_elementwise_maximum(a, b); }
__device__ __forceinline__ f32x4 mfma32(u32x4 a, u32x4 b, f32x4 c) {
    return __builtin_amdgcn_mfma_f32_16x16x32_bf16(__builtin_bit_cast(bf16x8, a), __builtin_bit_cast(bf16x8, b), c, 0, 0, 0);
}
__device__ __forceinline__ f32x4 mfma16(u32x2 a, u32x2 b, f32x4 c) {
    return __builtin_amdgcn_mfma_f32_16x16x16bf16_1k(__builtin_bit_cast(s16x4, a), __builtin_bit_cast(s16x4, b), c, 0, 0, 0);
}
__device__ __forceinline__ f32x4 smfmac(u32x4 a, u32x8 b, f32x4 c, int idx) {
    return __builtin_amdgcn_smfmac_f32_16x16x64_bf16(__builtin_bit_cast(bf16x8, a), __builtin_bit_cast(bf16x16, b), c, idx, 0, 0);
}
__device__ __forceinline__ int pi16(int k) { return ((k & 3) << 2) | ((k >> 1) & 2) | (k >> 3); }
__device__ __forceinline__ float addswap32(float a, float b) {
    auto r = __builtin_amdgcn_permlane32_swap(__float_as_uint(a), __float_as_uint(b), false, false);
    return __uint_as_float(r[0]) + __uint_as_float(r[1]);
}
__device__ __forceinline__ float xmax3216(float v) {
    auto r = __builtin_amdgcn_permlane32_swap(__float_as_uint(v), __float_as_uint(v), false, false);
    v = fmx(__uint_as_float(r[0]), __uint_as_float(r[1]));
    auto q = __builtin_amdgcn_permlane16_swap(__float_as_uint(v), __float_as_uint(v), false, false);
    return fmx(__uint_as_float(q[0]), __uint_as_float(q[1]));
}
__device__ __forceinline__ float xsum3216(float v) {
    auto r = __builtin_amdgcn_permlane32_swap(__float_as_uint(v), __float_as_uint(v), false, false);
    v = __uint_as_float(r[0]) + __uint_as_float(r[1]);
    auto q = __builtin_amdgcn_permlane16_swap(__float_as_uint(v), __float_as_uint(v), false, false);
    return __uint_as_float(q[0]) + __uint_as_float(q[1]);
}
template <int CTRL> __device__ __forceinline__ float dppf(float v) { return __uint_as_float((unsigned)__builtin_amdgcn_update_dpp(0, (int)__float_as_uint(v), CTRL, 0xF, 0xF, false)); }
__device__ __forceinline__ float wsum(float v) {
    v += dppf<0xB1>(v); v += dppf<0x4E>(v); v += dppf<0x141>(v); v += dppf<0x140>(v);
    return xsum3216(v);
}
__device__ __forceinline__ unsigned ror8(unsigned v) { return (unsigned)__builtin_amdgcn_update_dpp(0, (int)v, 0x128, 0xF, 0xF, false); }
__device__ __forceinline__ float ror8f(float v) { return __uint_as_float(ror8(__float_as_uint(v))); }
__device__ __forceinline__ unsigned nosent(unsigned v) {
    u16x2 lim = {0xFFFE, 0xFFFE};
    return __builtin_bit_cast(unsigned, __builtin_elementwise_min(__builtin_bit_cast(u16x2, v), lim));
}
__device__ __forceinline__ unsigned pkmax(unsigned a, unsigned b) {
    return __builtin_bit_cast(unsigned, __builtin_elementwise_max(__builtin_bit_cast(u16x2, a), __builtin_bit_cast(u16x2, b)));
}
__device__ __forceinline__ bool has_sent(u32x4 v) {
    unsigned m = pkmax(pkmax(v.x, v.y), pkmax(v.z, v.w));
    return (m & 0xFFFFu) == 0xFFFFu || (m >> 16) == 0xFFFFu;
}

typedef f32x2 c32;
__device__ __forceinline__ c32 fma2(c32 a, c32 b, c32 c) { return __builtin_elementwise_fma(a, b, c); }
__device__ __forceinline__ c32 dup(float v) { return (c32){v, v}; }
template <int SG> __device__ __forceinline__ c32 addi(c32 a, c32 b) {
    c32 d;
    if (SG > 0) asm("v_pk_add_f32 %0, %1, %2 op_sel:[0,1] op_sel_hi:[1,0] neg_lo:[0,1]" : "=v"(d) : "v"(a), "v"(b));
    else asm("v_pk_add_f32 %0, %1, %2 op_sel:[0,1] op_sel_hi:[1,0] neg_hi:[0,1]" : "=v"(d) : "v"(a), "v"(b));
    return d;
}
template <int SG> __device__ __forceinline__ c32 ctw(c32 a, c32 w) {
    c32 d = a * w.xx;
    if (SG < 0) asm("v_pk_fma_f32 %0, %1, %2, %0 op_sel:[1,1,0] op_sel_hi:[0,1,1] neg_lo:[0,1,0]" : "+v"(d) : "v"(a), "v"(w));
    else asm("v_pk_fma_f32 %0, %1, %2, %0 op_sel:[1,1,0] op_sel_hi:[0,1,1] neg_hi:[0,1,0]" : "+v"(d) : "v"(a), "v"(w));
    return d;
}
__device__ __forceinline__ c32 cis(float rev) { return (c32){__builtin_amdgcn_cosf(rev), __builtin_amdgcn_sinf(rev)}; }
template <int N> __device__ __forceinline__ void cpow(c32 (&t)[N], float rev) {
    t[0] = cis(rev);
    asm volatile("s_nop 0" : "+v"(t[0]));
#pragma unroll
    for (int i = 1; i < N; ++i) { t[i] = ctw<-1>(t[(i - 1) / 2], t[i / 2]); asm volatile("" : "+v"(t[i])); }
}
#define SB() __builtin_amdgcn_sched_barrier(0)
#define SA() do { SB(); asm volatile(".p2align 3"); SB(); } while (0)
template <int SG, int I0, int N, class F> __device__ __forceinline__ void ctwn(c32 (&u)[N], F w) {
    c32 d[N];
    SA();
#pragma unroll
    for (int i = I0; i < N; ++i) asm("v_pk_mul_f32 %0, %1, %2 op_sel_hi:[1,0]" : "=v"(d[i]) : "v"(u[i]), "v"(w(i)));
    SB();
#pragma unroll
    for (int i = I0; i < N; ++i) {
        if (SG < 0) asm("v_pk_fma_f32 %0, %1, %2, %0 op_sel:[1,1,0] op_sel_hi:[0,1,1] neg_lo:[0,1,0]" : "+v"(d[i]) : "v"(u[i]), "v"(w(i)));
        else asm("v_pk_fma_f32 %0, %1, %2, %0 op_sel:[1,1,0] op_sel_hi:[0,1,1] neg_hi:[0,1,0]" : "+v"(d[i]) : "v"(u[i]), "v"(w(i)));
        u[i] = d[i];
    }
    SB();
}
template <int SG, int M, int A, int B, int CI, int N> __device__ __forceinline__ void dft4n(c32 (&u)[N]) {
    c32 t[M][4];
    SA();
#pragma unroll
    for (int i = 0; i < M; ++i) {
        const c32 a = u[A * i], b = u[A * i + B], c = u[A * i + 2 * B], d = u[A * i + 3 * B];
        t[i][0] = i == CI ? addi<SG>(a, c) : a + c; t[i][1] = i == CI ? addi<-SG>(a, c) : a - c; t[i][2] = b + d; t[i][3] = b - d;
        if (B == 4) SB();
    }
    SB();
#pragma unroll
    for (int i = 0; i < M; ++i) { u[A * i] = t[i][0] + t[i][2]; u[A * i + 2 * B] = t[i][0] - t[i][2]; u[A * i + B] = addi<SG>(t[i][1], t[i][3]); u[A * i + 3 * B] = addi<-SG>(t[i][1], t[i][3]); }
    SB();
}
template <int SG> __device__ __forceinline__ void dft8x2(c32 (&u)[16]) {
    dft4n<SG, 4, 1, 4, -1>(u);
    const float r = 0.70710678118654752440f;
    c32 o[16];
#pragma unroll
    for (int e = 0; e < 2; ++e) { u[6 + e] = addi<SG>(u[6 + e], u[6 + e]); u[14 + e] = addi<-SG>(u[14 + e], u[14 + e]); }
#pragma unroll
    for (int e = 0; e < 2; ++e) { o[e] = u[e] + u[2 + e]; o[8 + e] = u[e] - u[2 + e]; o[4 + e] = addi<SG>(u[8 + e], u[10 + e]); o[12 + e] = addi<-SG>(u[8 + e], u[10 + e]); }
    SB();
#pragma unroll
    for (int e = 0; e < 2; ++e) { o[2 + e] = fma2(u[6 + e], dup(r), u[4 + e]); o[10 + e] = fma2(u[6 + e], dup(-r), u[4 + e]); o[6 + e] = fma2(u[14 + e], dup(-r), u[12 + e]); o[14 + e] = fma2(u[14 + e], dup(r), u[12 + e]); }
    SB();
#pragma unroll
    for (int i = 0; i < 16; ++i) u[i] = o[i];
}
template <int SG> __device__ __forceinline__ void dft16(c32 (&u)[16]) {
    const float c1 = 0.92387953251128675613f, s1 = 0.38268343236508977173f, r = 0.70710678118654752440f;
    dft4n<SG, 4, 1, 4, -1>(u);
    const int ix[8] = {5, 9, 13, 6, 14, 7, 11, 15};
    const float wx[8] = {c1, r, s1, r, -r, s1, -r, -c1}, wy[8] = {s1, r, c1, r, r, c1, r, -s1};
    c32 d[8];
#pragma unroll
    for (int i = 0; i < 8; ++i) d[i] = u[ix[i]] * dup(wx[i]);
    SB();
#pragma unroll
    for (int i = 0; i < 8; ++i) u[ix[i]] = fma2(u[ix[i]].yx, (c32){-SG * wy[i], SG * wy[i]}, d[i]);
    SB();
    dft4n<SG, 4, 4, 1, 2>(u);
    c32 o[16];
#pragma unroll
    for (int i = 0; i < 16; ++i) o[(i >> 2) + 4 * (i & 3)] = u[i];
#pragma unroll
    for (int i = 0; i < 16; ++i) u[i] = o[i];
}
template <int SG> __device__ __forceinline__ void dft10(c32 (&u)[10]) {
    const float c1 = 0.30901699437494742410f, c2 = -0.80901699437494742410f, s1 = 0.95105651629515357212f, s2 = 0.58778525229247312917f;
    c32 x[2][5], t[2][4], a[2][5];
    SA();
#pragma unroll
    for (int i = 0; i < 5; ++i) { x[0][i] = u[2 * i] + u[(2 * i + 5) % 10]; x[1][i] = u[2 * i] - u[(2 * i + 5) % 10]; }
    SB();
#pragma unroll
    for (int e = 0; e < 2; ++e) { t[e][0] = x[e][1] + x[e][4]; t[e][1] = x[e][2] + x[e][3]; t[e][2] = x[e][1] - x[e][4]; t[e][3] = x[e][2] - x[e][3]; }
    SB();
#pragma unroll
    for (int e = 0; e < 2; ++e) { a[e][0] = t[e][0] + t[e][1]; a[e][1] = fma2(t[e][0], dup(c1), x[e][0]); a[e][2] = fma2(t[e][0], dup(c2), x[e][0]); a[e][3] = t[e][2] * dup(s1); a[e][4] = t[e][2] * dup(s2); }
    SB();
#pragma unroll
    for (int e = 0; e < 2; ++e) { a[e][0] += x[e][0]; a[e][1] = fma2(t[e][1], dup(c2), a[e][1]); a[e][2] = fma2(t[e][1], dup(c1), a[e][2]); a[e][3] = fma2(t[e][3], dup(s2), a[e][3]); a[e][4] = fma2(t[e][3], dup(-s1), a[e][4]); }
    SB();
#pragma unroll
    for (int e = 0; e < 2; ++e) {
        u[5 * e] = a[e][0];
        u[(6 + 5 * e) % 10] = addi<SG>(a[e][1], a[e][3]); u[(24 + 5 * e) % 10] = addi<-SG>(a[e][1], a[e][3]);
        u[(12 + 5 * e) % 10] = addi<SG>(a[e][2], a[e][4]); u[(18 + 5 * e) % 10] = addi<-SG>(a[e][2], a[e][4]);
    }
    SB();
}
template <int ST> __device__ __forceinline__ void lds16(c32 (&u)[16], const char* p) {
    const LAS char* q = (const LAS char*)p;
#pragma unroll
    for (int i = 0; i < 16; ++i) asm volatile("ds_read_b64 %0, %1 offset:%2" : "=v"(u[(i >> 2) + 4 * (i & 3)]) : "v"(q), "n"(((i >> 2) + 4 * (i & 3)) * ST) : "memory");
#pragma unroll
    for (int i = 0; i < 4; ++i) asm volatile("s_waitcnt lgkmcnt(%4)" : "+v"(u[i]), "+v"(u[i + 4]), "+v"(u[i + 8]), "+v"(u[i + 12]) : "n"(12 - 4 * i) : "memory");
}
__device__ __forceinline__ c32 unpack2(unsigned v) { return (c32){lo2f(v), hi2f(v)}; }
__device__ __forceinline__ float wsum2(c32 v) {
    float t = addswap32(v.x, v.y);
    auto q = __builtin_amdgcn_permlane16_swap(__float_as_uint(t), __float_as_uint(t), false, false);
    t = __uint_as_float(q[0]) + __uint_as_float(q[1]);
    t += dppf<0xB1>(t); t += dppf<0x4E>(t); t += dppf<0x141>(t); t += dppf<0x140>(t);
    return t;
}
__device__ __forceinline__ c32 halves(float t) { return (c32){__uint_as_float(__builtin_amdgcn_readlane(__float_as_uint(t), 0)), __uint_as_float(__builtin_amdgcn_readlane(__float_as_uint(t), 32))}; }
__device__ __forceinline__ void ln_rows(const c32 (&v)[10], unsigned (&o)[10]) {
    const float mean = wsum2(((v[0] + v[1]) + (v[2] + v[3])) + ((v[4] + v[5]) + (v[6] + v[7])) + (v[8] + v[9])) * (1.f / 640.f);
    const c32 m = halves(mean), mb = halves(bfr(mean));
    c32 d[10], e[10];
#pragma unroll
    for (int r = 0; r < 10; ++r) d[r] = v[r] - m;
    SB();
#pragma unroll
    for (int r = 0; r < 5; ++r) e[r] = d[r] * d[r];
#pragma unroll
    for (int r = 0; r < 5; ++r) d[r] = fma2(d[r + 5], d[r + 5], e[r]);
    SB();
#pragma unroll
    for (int r = 0; r < 10; ++r) { const c32 y = v[r] - mb; e[r] = unpack2(pack2(y.x, y.y)); }
    const float var = wsum2((d[0] + d[1]) + (d[2] + d[3]) + d[4]) * (1.f / 640.f);
    const c32 rs = halves(__builtin_amdgcn_rcpf(bfr(__builtin_amdgcn_sqrtf(bfr(bfr(var) + 1e-5f)))));
    SB();
#pragma unroll
    for (int r = 0; r < 10; ++r) e[r] *= rs;
    SB();
#pragma unroll
    for (int r = 0; r < 10; ++r) o[r] = pack2(e[r].x, e[r].y);
    SB();
}

__device__ __forceinline__ __amdgpu_buffer_rsrc_t mkrsrc(const void* p, unsigned bytes) {
    return __builtin_amdgcn_make_buffer_rsrc((void*)p, 0, bytes, 0x00020000);
}

constexpr unsigned O_F = (unsigned)OFF_F, O_Y0 = (unsigned)OFF_Y0;
__device__ __forceinline__ unsigned nosent1(unsigned v) { return min(v, 0xFFFFFFFEu); }
__device__ __forceinline__ u32x4 nosent4(u32x4 v) { v[1] = nosent1(v[1]); v[3] = nosent1(v[3]); return v; }
__device__ __forceinline__ unsigned smax(u32x4 v) { return max(v[1], v[3]); }
constexpr unsigned SPIN_CAP = 400000u;
constexpr int RW = 5920;
struct RowTw { c32 a[9], b, c; };
__device__ __forceinline__ RowTw row_tw(int lane) {
    RowTw t;
    t.b = cis((float)(lane & 31) * (-1.f / 64.f)); t.c = cis((float)(lane & 15) * (-1.f / 32.f));
    asm volatile("" : "+v"(t.b), "+v"(t.c));
    cpow(t.a, (float)lane * (-1.f / 640.f));
    return t;
}
template <int B> __device__ __forceinline__ void cswap(c32& x, c32& y) {
#pragma unroll
    for (int e = 0; e < 2; ++e) {
        auto r = B == 32 ? __builtin_amdgcn_permlane32_swap(__float_as_uint(x[e]), __float_as_uint(y[e]), false, false) : __builtin_amdgcn_permlane16_swap(__float_as_uint(x[e]), __float_as_uint(y[e]), false, false);
        x[e] = __uint_as_float(r[0]); y[e] = __uint_as_float(r[1]);
    }
}
template <int B> __device__ __forceinline__ void cswap5(c32 (&p)[5], c32 (&q)[5]) {
#pragma unroll
    for (int i = 0; i < 5; ++i) cswap<B>(p[i], q[i]);
    SB();
}
__device__ __forceinline__ void bfly5(c32 (&p)[5], c32 (&q)[5]) {
#pragma unroll
    for (int i = 0; i < 5; ++i) { const c32 t = p[i] - q[i]; p[i] += q[i]; q[i] = t; }
    SB();
}
constexpr unsigned L_X = 16 * RW;
__device__ __forceinline__ void row_x(__amdgpu_buffer_rsrc_t rx, int row, int w, int lane) {
    asm volatile("s_mov_b32 m0, %3\n\ts_nop 0\n\tbuffer_load_dwordx4 %0, %1, %2 offen sc1 lds\n\tbuffer_load_dwordx4 %0, %1, %2 offen offset:1024 sc1 lds\n\tbuffer_load_dwordx4 %0, %1, %2 offen offset:1536 sc1 lds"
                 :: "v"(lane * 16), "s"(rx), "s"(row * (D_ * 2)), "s"(L_X + w * 2560u) : "m0", "memory");
}
template <int M> __device__ __forceinline__ void row_get(u16 (&v)[20], int w, int lane) {
    asm volatile("s_waitcnt vmcnt(%0)" :: "n"(M) : "memory");
#pragma unroll
    for (int r = 0; r < 20; ++r) v[r] = *(const LAS u16*)(size_t)(L_X + w * 2560u + lane * 2u + 128u * (r >> 1) + 1280u * (r & 1));
    SB();
}
template <int NW, int NT, class FS> __device__ __forceinline__ void a1_unit(__amdgpu_buffer_rsrc_t rx, int row0, bool tail, __amdgpu_buffer_rsrc_t rws, char* sm, const RowTw& tw, int w, int lane, FS st_) {
    const unsigned fo = tail ? O_Y0 + 8192u + (unsigned)(row0 & 255) * 16u : O_F + (unsigned)row0 * 16u, gs = tail ? YS : GF;
    if (w < NW) {
        c32 u[10];
        unsigned h[10];
        u16 x[20];
        row_x(rx, row0 + 2 * w, w, lane);
        st_(-1);
        row_get<NW == 8 ? 8 : 0>(x, w, lane);
        if (tail) for (unsigned spins = 0; spins < SPIN_CAP; ++spins) {
            unsigned m = 0;
#pragma unroll
            for (int r = 0; r < 20; ++r) m = max(m, (unsigned)x[r]);
            if (!__any(m == 0xFFFFu)) break;
            row_x(rx, row0 + 2 * w, w, lane);
            row_get<0>(x, w, lane);
        }
#pragma unroll
        for (int r = 0; r < 10; ++r) u[r] = (c32){bf2f(x[2 * r]), bf2f(x[2 * r + 1])};
        st_(0);
        ln_rows(u, h);
        st_(1);
#pragma unroll
        for (int r = 0; r < 10; ++r) u[r] = unpack2(h[r]);
        dft10<-1>(u);
        char* mine = sm + w * RW;
        char* ex = mine + ((lane >> 5) + 10 * ((lane >> 4) & 1)) * 144 + (lane & 15) * 8;
        ctwn<-1, 1>(u, [&](int i) { return tw.a[i - 1]; });
        c32 p[5], q[5];
#pragma unroll
        for (int i = 0; i < 5; ++i) { p[i] = u[2 * i]; q[i] = u[2 * i + 1]; }
        cswap5<32>(p, q); bfly5(p, q); ctwn<-1, 0>(q, [&](int) { return tw.b; });
        cswap5<16>(p, q); bfly5(p, q);
#pragma unroll
        for (int i = 0; i < 5; ++i) *(c32*)(ex + i * 288) = p[i];
        ctwn<-1, 0>(q, [&](int) { return tw.c; });
#pragma unroll
        for (int i = 0; i < 5; ++i) *(c32*)(ex + i * 288 + 2880) = q[i];
        asm volatile("" ::: "memory");
        st_(2);
        if (lane < 40) {
            c32 v[16];
#pragma unroll
            for (int m = 0; m < 8; ++m) { const f32x4 t = *(const f32x4*)(mine + lane * 144 + m * 16); v[2 * m] = t.xy; v[2 * m + 1] = t.zw; }
            dft16<-1>(v);
            asm volatile("" ::: "memory");
#pragma unroll
            for (int m = 0; m < 16; ++m) *(c32*)(mine + lane * 8 + m * 320) = v[m];
        }
        st_(3);
    }
    asm volatile("s_waitcnt lgkmcnt(0)\n\ts_barrier" ::: "memory");
    st_(4);
    const int tid = w * 64 + lane, k0 = tid / (2 * NW) * 4 + (tid & 3), id = tid % (2 * NW);
    const char* X = sm + (id >> 2) * (2 * RW);
#pragma unroll
    for (int c = 0; c * NT < NG * 2 * NW; ++c) {
        const int k4 = k0 + c * (2 * NT / NW), k = min(k4, 320);
        if ((c + 1) * NT <= NG * 2 * NW || k4 < 4 * NG) {
            u32x4 o;
#pragma unroll
            for (int e = 0; e < 2; ++e) {
                const c32 zk = *(const c32*)(X + e * RW + k * 8) * dup(1.f / 32.f), zn = *(const c32*)(X + e * RW + (k == 0 ? 0 : 640 - k) * 8) * dup(1.f / 32.f);
                o[2 * e] = packh(zk.x + zn.x, zk.y - zn.y); o[2 * e + 1] = packh(zk.y + zn.y, zn.x - zk.x);
            }
            st128(nosent4(o), rws, (unsigned)(k4 >> 2) * gs + id * 16u, fo, 16);
        }
    }
}
template <int NW, class FS, class FD> __device__ __forceinline__ void a3_units(__amdgpu_buffer_rsrc_t rws, __amdgpu_buffer_rsrc_t rx, int row0, int step, int lay, char* sm, const RowTw& tw, int w, int lane, FS st_, FD done) {
    constexpr int NT = 512, NC = (NG * 2 * NW + NT - 1) / NT, GS = NW == 4 ? YS : GF;
    const int tid = w * 64 + lane, k0 = tid / (2 * NW) * 4 + (tid & 3), id = tid % (2 * NW);
    u32x4 fv[NC];
    unsigned sc[NC];
    u16 x[20];
    auto get = [&](int row) {
        const unsigned fo = (NW == 4 ? O_Y0 + lay * 4096u : O_F) + (unsigned)row * 16u;
#pragma unroll
        for (int c = 0; c < NC; ++c) fv[c] = __builtin_amdgcn_raw_buffer_load_b128(rws, (unsigned)((min(k0 + c * (2 * NT / NW), 320) >> 2) * GS + id * 16), fo, 16);
        __builtin_amdgcn_sched_barrier(0);
    };
    auto factors = [&]() {
#pragma unroll
        for (int c = 0; c < NC; ++c) { const int k = min(k0 + c * (2 * NT / NW), 320); sc[c] = __builtin_amdgcn_raw_buffer_load_b32(rws, (unsigned)((k >> 2) * YS + (k & 3) * 4), O_Y0 + 12288u, 16); }
    };
    factors();
    if (w < NW) row_x(rx, row0 + 2 * w, w, lane);
    st_(-1);
    for (unsigned spins = 0; spins < SPIN_CAP; ++spins) {
        asm volatile("" ::: "memory");
        if (!lay) factors();
        get(row0);
        if (NW == 8 || !__any(max(max(smax(fv[0]), smax(fv[1])), max(sc[0], sc[1])) == 0xFFFFFFFFu)) break;
    }
    for (int n = 0;; ++n, row0 += step, sm += n & 1 ? 8 * RW : -8 * RW) {
    char* X = sm + (id >> 2) * (2 * RW);
#pragma unroll
    for (int c = 0; c < NC; ++c) {
        const int k = k0 + c * (2 * NT / NW);
        const unsigned re = k == 0 || k == 320 ? 0xFFFFu : ~0u;
        if (c + 1 < NC || k <= 320)
#pragma unroll
            for (int e = 0; e < 2; ++e) {
                const c32 p = __builtin_convertvector(__builtin_bit_cast(f16x2, fv[c][2 * e] & re), c32) * dup(__uint_as_float(sc[c])), q = __builtin_convertvector(__builtin_bit_cast(f16x2, fv[c][2 * e + 1] & re), c32) * dup(__uint_as_float(sc[c]));
                *(c32*)(X + e * RW + k * 8) = addi<1>(p, q); *(c32*)(X + e * RW + (k == 0 ? 0 : 640 - k) * 8) = (c32){p.x + q.y, q.x - p.y};
            }
    }
    asm volatile("s_waitcnt vmcnt(0) lgkmcnt(0)\n\ts_barrier" ::: "memory");
    st_(0);
    if (NW == 8) {
        if (n) done(row0 - step);
        row_get<63>(x, w, lane);
        asm volatile("s_waitcnt lgkmcnt(0)" ::: "memory");
        if (row0 + step < S_) { row_x(rx, row0 + step + 2 * w, w, lane); get(row0 + step); }
    }
    if (w < NW) {
        char* mine = sm + w * RW;
        if (lane < 40) {
            c32 v[16];
            lds16<320>(v, mine + lane * 8);
            dft16<1>(v);
            asm volatile("" ::: "memory");
#pragma unroll
            for (int m = 0; m < 8; ++m) *(f32x4*)(mine + lane * 144 + m * 16) = (f32x4){v[2 * m].x, v[2 * m].y, v[2 * m + 1].x, v[2 * m + 1].y};
        }
        asm volatile("" ::: "memory");
        st_(1);
        const char* ex = mine + ((lane >> 5) + 10 * ((lane >> 4) & 1)) * 144 + (lane & 15) * 8;
        c32 u[10];
        c32 p[5], q[5];
#pragma unroll
        for (int i = 0; i < 5; ++i) q[i] = *(const c32*)(ex + i * 288 + 2880);
#pragma unroll
        for (int i = 0; i < 5; ++i) p[i] = *(const c32*)(ex + i * 288);
        SB();
        ctwn<1, 0>(q, [&](int) { return tw.c; }); bfly5(p, q); cswap5<16>(p, q);
        ctwn<1, 0>(q, [&](int) { return tw.b; }); bfly5(p, q); cswap5<32>(p, q);
#pragma unroll
        for (int i = 0; i < 5; ++i) { u[2 * i] = p[i]; u[2 * i + 1] = q[i]; }
        ctwn<1, 1>(u, [&](int i) { return tw.a[i - 1]; });
        if (NW == 4) row_get<63>(x, w, lane);
        dft10<1>(u);
        st_(2);
        asm volatile("" ::: "memory");
        char* st = mine + lane * 2;
        unsigned h[10];
#pragma unroll
        for (int r = 0; r < 10; ++r) {
            const c32 y = (c32){bf2f(x[2 * r]), bf2f(x[2 * r + 1])} + unpack2(pack2(u[r].x, u[r].y));
            const unsigned p = pack2(y.x, y.y);
            u[r] = unpack2(p);
            *(u16*)(st + r * 128) = (u16)p; *(u16*)(st + 1280 + r * 128) = (u16)(p >> 16);
        }
        st_(3);
        ln_rows(u, h);
        st_(4);
#pragma unroll
        for (int r = 0; r < 10; ++r) { *(u16*)(st + 2560 + r * 128) = (u16)h[r]; *(u16*)(st + 3840 + r * 128) = (u16)(h[r] >> 16); }
        asm volatile("" ::: "memory");
        u32x4 o[6];
#pragma unroll
        for (int c = 0; c < 6; ++c) o[c] = *(const u32x4*)(mine + (c < 3 ? 2560 : 0) + min(c % 3 * 64 + lane, 159) * 16);
#pragma unroll
        for (int c = 0; c < 3; ++c) {
            const int i = min(c * 64 + lane, 159), rr = i >= 80, ch = i - 80 * rr, row = (row0 + 2 * w + rr) & 31;
            st128(nosent4(o[c]), rws, (unsigned)((ch >> 3) * 4096 + row * 128 + (((ch & 7) ^ (row & 7)) << 4)) | (c == 2 ? (lane & 32u) << 25 : 0u), (unsigned)OFF_H + (unsigned)((row0 >> 5) + 4 - 4 * lay) * 40960u, 16);
        }
#pragma unroll
        for (int c = 0; c < 3; ++c) st128(o[3 + c], rws, (unsigned)(c * 64 + lane) * 16u | (c == 2 ? (lane & 32u) << 25 : 0u), (unsigned)OFF_X1 + (unsigned)(row0 + 2 * w) * (D_ * 2), 16);
    }
    if (NW == 4 || row0 + step >= S_) break;
    }
    st_(5);
    asm volatile("s_waitcnt vmcnt(0)\n\ts_barrier" ::: "memory");
    done(row0);
}

__device__ __forceinline__ unsigned xcc_id() { unsigned x; asm volatile("s_getreg_b32 %0, hwreg(HW_REG_XCC_ID)" : "=s"(x)); return x & 7u; }
__device__ __forceinline__ unsigned probe(__amdgpu_buffer_rsrc_t rw, unsigned base) {
    unsigned mn[4] = {~0u, ~0u, ~0u, ~0u};
#pragma unroll 1
    for (int r = 0; r < 2; ++r) {
#pragma unroll
        for (int i = 0; i < 4; ++i) {
            const unsigned long long t1 = __builtin_amdgcn_s_memtime();
            asm volatile("s_waitcnt lgkmcnt(0)" ::: "memory");
            unsigned v = __builtin_amdgcn_raw_buffer_load_b32(rw, i * HALF, base + r * 4096u, 16);
            asm volatile("s_waitcnt vmcnt(0)" : "+v"(v) :: "memory");
            const unsigned long long t2 = __builtin_amdgcn_s_memtime();
            mn[i] = min(mn[i], (unsigned)(t2 - t1) + v);
        }
    }
    return mn[1] + mn[3] < mn[0] + mn[2] ? (mn[3] < mn[1] ? 3u : 1u) : (mn[2] < mn[0] ? 2u : 0u);
}
__device__ __forceinline__ void filter_unit(const u16* wr, const u16* wi, int s0, __amdgpu_buffer_rsrc_t rws, u16* tf, int tid) {
    u32x4 v[6];
#pragma unroll
    for (int i = 0; i < 6; ++i) v[i] = ((const u32x4*)((i < 3 ? wr : wi) + (size_t)s0 * DF))[min(tid + 512 * (i % 3), 1283)];
    __builtin_amdgcn_sched_barrier(0);
#pragma unroll
    for (int i = 0; i < 6; ++i) ((u32x4*)tf)[min(tid + 512 * (i % 3), 1283) + i / 3 * 1284] = v[i];
    __syncthreads();
    const int k = tid >> 3, sq = (tid & 7) * 4;
    u16 t[6][8];
#pragma unroll
    for (int i = 0; i < 6; ++i)
#pragma unroll
        for (int j = 0; j < 8; ++j) t[i][j] = tf[((j & 1) * 32 + sq + (j >> 1)) * DF + k + 64 * i];
    __builtin_amdgcn_sched_barrier(0);
#pragma unroll
    for (int i = 0; i < 6; ++i) {
        u32x4 o;
#pragma unroll
        for (int j = 0; j < 4; ++j) o[j] = (unsigned)t[i][2 * j] | (unsigned)t[i][2 * j + 1] << 16;
        if (i < 5 || tid < 8) st128(o, rws, (unsigned)(k * S_ + sq) * 4u, (unsigned)OFF_WFT + (unsigned)(s0 * 4 + i * 64 * S_ * 4), 16);
    }
}

__device__ __forceinline__ void dma1k(__amdgpu_buffer_rsrc_t r, unsigned vo, unsigned so, unsigned lds) {
    asm volatile("s_mov_b32 m0, %3\n\ts_nop 0\n\tbuffer_load_dwordx4 %0, %1, %2 offen sc1 lds" :: "v"(vo), "s"(r), "s"(so), "s"(lds) : "m0", "memory");
}
template <class FW, class FS> __device__ __forceinline__ void a2_unit(__amdgpu_buffer_rsrc_t rws, int G, int w, int lane, bool late, FW rdy, FS st_) {
    const int fr = lane & 15, g = lane >> 4;
    const unsigned fo = O_F + (unsigned)(G * GF), yo = O_Y0 + (unsigned)(G * YS);
    const unsigned sw = (unsigned)(lane ^ (4 * (w & 1) + g)) * 16u;
    if (w >= 4) {
        const unsigned v = (w - 4) * 1024u, t = late ? fo + 28672u + v : yo + 8192u + v;
        rdy(6);
#pragma unroll
        for (int i = 0; i < 7; ++i) dma1k(rws, sw, fo + v + i * 4096u, v + i * 4096u);
        rdy(7);
        for (unsigned spins = 0; spins < SPIN_CAP; ++spins) {
            dma1k(rws, sw, t, v + 28672u);
            asm volatile("s_waitcnt vmcnt(0)" ::: "memory");
            if (late || !__any(smax(*(const LAS u32x4*)(size_t)(v + 28672u + lane * 16u)) == 0xFFFFFFFFu)) break;
        }
        asm volatile("s_barrier" ::: "memory");
    } else {
        const unsigned wl = 32768u + w * 8192u;
        c32 wf[4][8], t2[4][7];
        f32x4 tr, ti, tn;
        u32x4 mb[2], ma[2];
        float back;
        auto ldw = [&]() {
#pragma unroll
            for (int i = 0; i < 8; ++i) dma1k(rws, lane * 16u, (unsigned)OFF_WFT + (unsigned)(min(4 * G + w, 320) * 8192 + i * 1024), wl + i * 1024u);
        };
        auto cvw = [&]() {
            unsigned wv[4][8], m = 0;
            asm volatile("s_waitcnt vmcnt(0)" ::: "memory");
#pragma unroll
            for (int i = 0; i < 32; ++i) wv[i >> 3][i & 7] = *(const LAS unsigned*)(size_t)(wl + (fr + 32 * g) * 4u + ((i >> 3 & 1) * 16 + (i >> 4) * 128 + (i & 7) * 256) * 4u);
            SB();
#pragma unroll
            for (int i = 0; i < 32; ++i) m = pkmax(m, wv[i >> 3][i & 7] & 0x7F807F80u);
            float x = __uint_as_float(max(m << 16, m & 0xFFFF0000u));
            x = fmaxf(x, dppf<0xB1>(x)); x = fmaxf(x, dppf<0x4E>(x)); x = fmaxf(x, dppf<0x141>(x)); x = fmaxf(x, dppf<0x140>(x));
            const unsigned e = min(max(__float_as_uint(xmax3216(x)), 8u << 23), 246u << 23);
            back = __uint_as_float(e) * (1.f / 40.f);
#pragma unroll
            for (int i = 0; i < 32; ++i) { wf[i >> 3][i & 7] = unpack2(wv[i >> 3][i & 7]) * dup(__uint_as_float((251u << 23) - e)); asm volatile("" : "+v"(wf[i >> 3][i & 7])); }
        };
        rdy(8);
        ldw();
#pragma unroll
        for (int i = 0; i < 4; ++i) {
            const int s = 2 * g + (i & 1) + (i >> 1) * 8;
            cpow(t2[i], (float)(fr + 16 * s) * (-1.f / 2048.f));
            const c32 t = cis((float)((4 * g + i) * fr) * (-1.f / 256.f)), p = cis((float)(s * fr) * (1.f / 16.f)) * dup(0.25f);
            tr[i] = t.x; ti[i] = t.y;
            mb[0][i] = packh(p.x, p.y); mb[1][i] = packh(-p.y, p.x);
#pragma unroll
            for (int h = 0; h < 2; ++h) { const c32 q = cis((float)((4 * g + i) * (8 * h + (fr >> 1)) + 4 * (fr & 1)) * (1.f / 16.f)) * dup(0.25f); ma[h][i] = packh(q.x, q.y); }
        }
        tn = -ti;
        asm volatile("" : "+v"(tn), "+v"(tr), "+v"(ti), "+v"(mb[0]), "+v"(mb[1]), "+v"(ma[0]), "+v"(ma[1]));
        cvw();
        LAS u32x4* im = (LAS u32x4*)(size_t)((fr + 32 * g) * 128u + (unsigned)(w ^ (fr >> 1)) * 16u);
        LAS u32x4* im1 = (LAS u32x4*)((size_t)im ^ 64u);
        asm volatile("s_barrier" ::: "memory");
        st_(2);
        u32x4 x[4][2];
#pragma unroll
        for (int h = 0; h < 2; ++h)
#pragma unroll
            for (int i = 0; i < 4; ++i) x[i][h] = (h ? im1 : im)[(i & 1) * 128 + (i >> 1) * 1024];
        SB();
        if (late && lane == 0) __builtin_amdgcn_raw_buffer_store_b32(__float_as_uint(back), rws, 0u, yo + 12288u + w * 4u, 16);
        f32x4 c[8][2];
        auto cvt = [](float a, float b) { unsigned r; asm("v_cvt_pk_f16_f32 %0, %1, %2" : "=v"(r) : "v"(a), "v"(b)); return r; };
        auto two = [&](auto in) {
            u32x4 d = in(0), n;
            SA();
#pragma unroll
            for (int e = 0; e < 8; ++e) {
                asm volatile("s_nop 1" : "+v"(d));
                c[e][0] = mfmah(d, mb[0]);
                if (e < 7) n = in(e + 1);
                c[e][1] = mfmah(d, mb[1]);
                d = n;
                SB();
            }
            SA();
#pragma unroll
            for (int e = 0; e < 8; e += 2) {
                const f32x4 p0 = c[e][0] * tr, p1 = c[e][0] * ti, p2 = c[e + 1][0] * tr, p3 = c[e + 1][0] * ti;
                SB();
                c[e][0] = __builtin_elementwise_fma(c[e][1], tn, p0); c[e][1] = __builtin_elementwise_fma(c[e][1], tr, p1);
                c[e + 1][0] = __builtin_elementwise_fma(c[e + 1][1], tn, p2); c[e + 1][1] = __builtin_elementwise_fma(c[e + 1][1], tr, p3);
                SB();
            }
            auto mid = [&](int e) { return (u32x4){packh(c[e][0][0], c[e][1][0]), packh(c[e][0][1], c[e][1][1]), packh(c[e][0][2], c[e][1][2]), packh(c[e][0][3], c[e][1][3])}; };
            d = mid(0);
#pragma unroll
            for (int e = 0; e < 8; ++e) {
                SB();
                c[e][0] = mfmah(ma[0], d);
                if (e < 7) n = mid(e + 1);
                c[e][1] = mfmah(ma[1], d);
                d = n;
            }
            SB();
        };
        two([&](int e) { return (u32x4){x[0][e >> 2][e & 3], x[1][e >> 2][e & 3], x[2][e >> 2][e & 3], x[3][e >> 2][e & 3]}; });
        asm volatile("s_nop 7" : "+v"(c[0][0]), "+v"(c[0][1]), "+v"(c[1][0]), "+v"(c[1][1]), "+v"(c[2][0]), "+v"(c[2][1]), "+v"(c[3][0]), "+v"(c[3][1]), "+v"(c[4][0]), "+v"(c[4][1]), "+v"(c[5][0]), "+v"(c[5][1]), "+v"(c[6][0]), "+v"(c[6][1]), "+v"(c[7][0]), "+v"(c[7][1]));
        st_(3);
        c32 u[2][16];
#pragma unroll
        for (int h = 0; h < 2; ++h) {
#pragma unroll
            for (int e = 0; e < 8; ++e) { u[h][2 * e] = c[e][h].xy; u[h][2 * e + 1] = c[e][h].zw; }
            ctwn<-1, 2>(u[h], [&](int i) { return t2[2 * h + (i & 1)][(i >> 1) - 1]; });
            dft8x2<-1>(u[h]);
            ctwn<-1, 0>(u[h], [&](int i) { return wf[2 * h + (i & 1)][i >> 1]; });
            dft8x2<1>(u[h]);
            ctwn<1, 2>(u[h], [&](int i) { return t2[2 * h + (i & 1)][(i >> 1) - 1]; });
            if (h == 0) st_(4);
        }
        two([&](int e) { return (u32x4){cvt(u[0][2 * e].y, u[0][2 * e].x), cvt(u[0][2 * e + 1].y, u[0][2 * e + 1].x), cvt(u[1][2 * e].y, u[1][2 * e].x), cvt(u[1][2 * e + 1].y, u[1][2 * e + 1].x)}; });
#pragma unroll
        for (int h = 0; h < 2; ++h) {
            if (h) asm volatile("" : "+v"(c[4][0]), "+v"(c[7][0]), "+v"(c[7][1]));
            SA();
#pragma unroll
            for (int i = 0; i < 4; ++i) {
                u32x4 o;
#pragma unroll
                for (int j = 0; j < 4; ++j) { const f32x4 y = c[4 * h + j][i >> 1]; o[j] = cvt(y[2 * (i & 1) + 1], y[2 * (i & 1)]); }
                (h ? im1 : im)[(i & 1) * 128 + (i >> 1) * 1024] = o;
            }
        }
        st_(5);
    }
    asm volatile("s_waitcnt lgkmcnt(0)\n\ts_barrier" ::: "memory");
    st_(6);
    u32x4 o[4];
#pragma unroll
    for (int j = 0; j < 4; ++j) o[j] = *(const LAS u32x4*)(size_t)(w * 1024u + j * 8192u + sw);
    SB();
#pragma unroll
    for (int j = 0; j < 4; ++j) {
        st128(j ? o[j] : nosent4(o[j]), rws, lane * 16u, (j || w >= 4 ? fo + j * 8192u : late ? yo : yo + 4096u) + w * 1024u, 16);
        if (j == 0) st_(7);
    }
}

struct RecArgs {
    unsigned* ctl; char* win; const u16* mem0; const u16* xin; u16* out;
    const u16* W[6];
};
template <int I> __device__ __forceinline__ char* karg(int i = 0) {
    unsigned long long p;
    asm volatile("s_load_dwordx2 %0, %1, %2\n\ts_waitcnt lgkmcnt(0)" : "=s"(p) : "s"(__builtin_amdgcn_kernarg_segment_ptr()), "s"(I * 8 + i * 8) : "memory");
    return (char*)p;
}
constexpr unsigned O_H = (unsigned)OFF_H, O_X1 = (unsigned)OFF_X1, O_Z = (unsigned)OFF_Z, O_Z2 = (unsigned)OFF_Z2, O_PT = (unsigned)OFF_PT, O_QK = (unsigned)OFF_QK;
#define MXV 0x2A000000000AAAAAull
#define MXQ 0x888888000000ull
static_assert((MXV & 15) == 10 && __builtin_popcountll(MXV >> 32) >= 2 && __builtin_popcountll(MXQ) >= 2 && !(MXQ & 15) && !((MXV | MXQ) >> 63), "segment sets");
constexpr int NREC = 96 + NOWN, NQT = NREC + NOWN, NXT = NQT + 32, NWRK = NXT + 32;
__device__ __forceinline__ bool spin_fail(unsigned& spins, __amdgpu_buffer_rsrc_t rws) {
    if (++spins > SPIN_CAP) { __builtin_amdgcn_raw_buffer_store_b32(1u, rws, 0u, 0u, 16); return true; }
    return (spins & 1023u) == 0u && __builtin_amdgcn_readfirstlane(__builtin_amdgcn_raw_buffer_load_b32(rws, 0u, 0u, 16));
}
#define ALN 6
#define AL ".p2align " STR(ALN) "\n\t"
#define STR2(x) #x
#define STR(x) STR2(x)
#define TPRE 0
#define QP 2
#define BQN 7
#define BQ(n) ".if " #n "==" STR(BQN) "\n\ts_barrier\n\t.endif\n\t"
#define SST(k) do {} while (0)
#define LSEL 1
#define RT(k) do {} while (0)
#define RTS() do {} while (0)
#define B3ATTR
#define MTA(k)
#define MTRT() do {} while (0)
#define MT(k) do {} while (0)
#define MTS() do {} while (0)
#define FC(x) do {} while (0)
#define DQC 1130
#define DVC 1200
#define M1 44
#define MZ 40
#define MZP 20
#define M2 220
#define LTP (LT0 + 15)
#define LTZ 340
#define PEN 280
#define KSL 1
#define SVO 64
#define DZP 46
#define LT0 330
#define SAD 1
#define PEN2 80
#define PEN1 50
#define PEN0 60
#define DDM2 0
#define DRA 700
#define TSLP 1
#define PFS 0
#define PFOFF 1
#define OPR 2
#define BSPARSE 0
#define ZLAG 7
#define TKP 0
#define DPS 12
#define RZ 63
#define RQ 63
#define R2 63
#define DXT 3000
#define SSW 99
#define LTM 18
#define XS0 13
#define XSU 8
#define XSM 32
#define LTU 4
#define LTD 1
#define QUIET 0
#define QUAD 1
#define LTS 3
#define XSS 3
#define TKS 1
#define LT1 358
#define FDL 2
#define ZLA 3
#define DPT2 3
#define NORND 0
#define LTX 28
#define TKE 200
#define DVO 0
#define ZPS 4
#define ASLP 4
#define BADG 0
#define DW1 0
#define WLA 3
#define XSLP 30
#define DOUT 215
#define DA1 145
#define DA2 215
#define DA20 270
#define DFB 150
#define DA3 185
#define DA30 230
#define YSLP 0
#define QEU 22
#define QE2 30
#define PEN00 120
#define DQ0 930
#define DQ00 1160
#define DZ0 420
#define DZ00 430
#define DOU2 200
#define DFA 0
#define SRG 0x5A000000u
#define OBN 5
#define OB(n) ".if " #n "==" STR(OBN) "\n\ts_barrier\n\tds_read_b64 v[102:103], %[sp]\n\tds_read_b64 v[104:105], %[sp] offset:512\n\tds_read_b64 v[106:107], %[sp] offset:1024\n\t.endif\n\t"
#define VSLP 0
#define CPF 1
#define DST 4
#define XSL2 3
#define ODL 8
#define VTS 1
#define XF 4
#define XFM 0xFFFFFFFFull
#define DOU1 50
#define DR0 0
#define DFB0 120
#define TPF 21
#define DZF 160
#define DZF0 175
#define DV0 260
#define QTP 1
#define XDL 0
#define DOW 200
#define DOU3 300
#define NTM 31
#define WK3 0
#define QEV 8
#define QNB 0
#define QDO 6
#define WPF 0
#define QCU 4
#define HLA 8
#define PSLP 16
#define PFD 4
#define NPF 3
#define KSL2 1
#define KBQ 4
#define VB1 6
#define VB2 5
#define KR1 3
#define QB2 2
#define NB2 6
#define DZR 24
#define ML 470
#define MN 12
#define CF 190
#define DB 1500
#define DP 200
#define ZSL 1
#define NB1 3
#define DPT 3
#define DDMA 8
#define DZ2 800
#define QEARLY 0
#define OSLP 12
#define OSLP2 4
#define WSLP 32
#define DO0 12000
#define DW0 1200
#define DQT 9000
#define DQ4 700
#define ZEARLY 3
#define HSLP 4
#define RMAP 0
#define DIE 0
#define VMWAIT() do { asm volatile("" ::: "memory"); __builtin_amdgcn_s_waitcnt(0x0F70); asm volatile("" ::: "memory"); } while (0)
#define BAR() asm volatile("s_waitcnt lgkmcnt(0)\n\ts_barrier" ::: "memory")
#define PIN1(a) asm volatile("" : "+v"(a) :: "memory")
#define PIN2(a, b) asm volatile("" : "+v"(a), "+v"(b) :: "memory")
#define BAR0() asm volatile("s_barrier" ::: "memory")
__device__ __forceinline__ unsigned fresh_lane() { unsigned l; asm volatile("v_mbcnt_lo_u32_b32 %0, -1, 0\n\tv_mbcnt_hi_u32_b32 %0, -1, %0" : "=v"(l)); __builtin_assume(l < 64u); return l; }
__device__ __forceinline__ u32x4 sent4() { u32x2 a, b; asm volatile("v_mov_b64 %0, -1\n\tv_mov_b64 %1, -1" : "=v"(a), "=v"(b)); return (u32x4){a[0], a[1], b[0], b[1]}; }
__device__ __forceinline__ void wait_until(unsigned long long t) { while ((long long)(__builtin_amdgcn_s_memtime() - t) < 0) __builtin_amdgcn_s_sleep(1); }
struct Clk { unsigned ta, ma, cpq, qpc; };
__device__ __forceinline__ int sx24(unsigned v) { return (int)(v << 8) >> 8; }
#define WAIT_FINE AL \
        "s_memrealtime vcc\n\t" \
        "s_memtime s[100:101]\n\t" \
        "s_brev_b32 %[fi], -2\n\t" \
        "s_waitcnt lgkmcnt(0)\n\t" \
        "s_mov_b32 %[rp], vcc_lo\n\t" \
        "s_lshl_b32 vcc_hi, vcc_lo, 2\n\t" \
        "s_sub_u32 %[x], %[t], vcc_hi\n\t" \
        "s_bfe_i32 %[x], %[x], 0x180000\n\t" \
        "s_min_i32 %[x], %[x], 4000\n\t" \
        "s_max_i32 %[x], %[x], -4000\n\t" \
        "s_add_u32 %[t], vcc_hi, %[x]\n\t" \
        "s_mul_i32 %[x], %[x], %[cq]\n\t" \
        "s_ashr_i32 %[x], %[x], 8\n\t" \
        "s_sub_u32 %[tm], %[t], %[ta]\n\t" \
        "s_bfe_i32 %[tm], %[tm], 0x180000\n\t" \
        "s_min_i32 %[tm], %[tm], 1000000\n\t" \
        "s_max_i32 %[tm], %[tm], -1000000\n\t" \
        "s_mul_i32 %[tm], %[tm], %[cq]\n\t" \
        "s_ashr_i32 %[tm], %[tm], 8\n\t" \
        "s_add_u32 %[tm], %[tm], %[ma]\n\t" \
        "s_sub_u32 %[tm], %[tm], s100\n\t" \
        "s_lshr_b32 vcc_hi, %[cq], 4\n\t" \
        "s_sub_u32 vcc_hi, %[x], vcc_hi\n\t" \
        "s_max_i32 %[tm], %[tm], vcc_hi\n\t" \
        "s_min_i32 %[tm], %[tm], %[x]\n\t" \
        "s_add_u32 %[tm], %[tm], s100\n" \
        "1:\n\t" \
        "s_memrealtime vcc\n\t" \
        "s_memtime s[100:101]\n\t" \
        "s_waitcnt lgkmcnt(0)\n\t" \
        "s_cmp_eq_u32 vcc_lo, %[rp]\n\t" \
        "s_cbranch_scc1 2f\n\t" \
        "s_mov_b32 %[rp], vcc_lo\n\t" \
        "s_lshl_b32 %[x], vcc_lo, 2\n\t" \
        "s_sub_u32 %[x], %[t], %[x]\n\t" \
        "s_bfe_i32 %[x], %[x], 0x180000\n\t" \
        "s_mul_i32 %[x], %[x], %[cq]\n\t" \
        "s_ashr_i32 %[x], %[x], 8\n\t" \
        "s_add_u32 %[x], %[x], s100\n\t" \
        "s_sub_u32 s101, %[x], %[tm]\n\t" \
        "s_cmp_lt_i32 s101, %[fi]\n\t" \
        "s_cselect_b32 %[tm], %[x], %[tm]\n\t" \
        "s_mov_b32 %[fi], 0\n" \
        "2:\n\t" \
        "s_sub_u32 %[x], %[tm], s100\n\t" \
        "s_cmp_gt_i32 %[x], 260\n\t" \
        "s_cbranch_scc1 1b\n\t" \
        "s_cmp_eq_u32 %[fi], 0\n\t" \
        "s_cselect_b32 %[ta], %[t], %[ta]\n\t" \
        "s_cselect_b32 %[ma], %[tm], %[ma]\n" \
        "3:\n\t" \
        "s_memtime s[100:101]\n\t" \
        "s_waitcnt lgkmcnt(0)\n\t" \
        "s_sub_u32 %[x], %[tm], s100\n\t" \
        "s_cmp_gt_i32 %[x], 92\n\t" \
        "s_cbranch_scc1 3b\n\t" \
        "s_max_i32 %[x], %[x], 0\n\t" \
        "s_andn2_b32 %[x], %[x], 3\n\t" \
        "s_sub_u32 %[x], 104, %[x]\n\t" \
        "s_getpc_b64 vcc\n\t" \
        "s_add_u32 vcc_lo, vcc_lo, %[x]\n\t" \
        "s_addc_u32 vcc_hi, vcc_hi, 0\n\t" \
        "s_setpc_b64 vcc\n\t" \
        ".rept 23\n\t" \
        "s_nop 0\n\t" \
        ".endr\n\t"
#define WAIT_FINE_OUT [tm] "=&s"(tm), [rp] "=&s"(rp), [x] "=&s"(x), [fi] "=&s"(fi), [t] "+s"(t), [ta] "+s"(c.ta), [ma] "+s"(c.ma)
__device__ __forceinline__ void wait_fine_bar(unsigned t, Clk& c) {
    unsigned tm, rp, x, fi;
    asm volatile(WAIT_FINE "s_barrier" : WAIT_FINE_OUT : [cq] "s"(c.cpq) : "vcc", "scc", "s100", "s101", "memory");
}
__device__ __forceinline__ void wait_cyc_bar(unsigned tm) {
    unsigned x;
    asm volatile(AL
        "3:\n\t"
        "s_memtime s[100:101]\n\t"
        "s_waitcnt lgkmcnt(0)\n\t"
        "s_sub_u32 %0, %1, s100\n\t"
        "s_cmp_gt_i32 %0, 92\n\t"
        "s_cbranch_scc1 3b\n\t"
        "s_max_i32 %0, %0, 0\n\t"
        "s_andn2_b32 %0, %0, 3\n\t"
        "s_sub_u32 %0, 104, %0\n\t"
        "s_getpc_b64 vcc\n\t"
        "s_add_u32 vcc_lo, vcc_lo, %0\n\t"
        "s_addc_u32 vcc_hi, vcc_hi, 0\n\t"
        "s_setpc_b64 vcc\n\t"
        ".rept 23\n\t"
        "s_nop 0\n\t"
        ".endr\n\t"
        "s_barrier"
        : "=&s"(x) : "s"(tm) : "vcc", "scc", "s100", "s101", "memory");
}
__device__ __forceinline__ unsigned cyc_at(const Clk& c, int d) {
    d = min(d, 20000);
    asm volatile("" : "+s"(d));
    return c.ma + (unsigned)((max(d, -4000) * (int)c.cpq) >> 8);
}
__device__ __forceinline__ void clk_rate(Clk& c, unsigned ta0, unsigned ma0) {
    const int dq = sx24(c.ta - ta0);
    const float r = fminf(fmaxf((float)(c.ma - ma0) * __builtin_amdgcn_rcpf((float)dq), 1.f), 8.f);
    unsigned x, y;
    asm volatile("s_nop 1\n\tv_readfirstlane_b32 %0, %2\n\tv_readfirstlane_b32 %1, %3\n\ts_nop 3" : "=s"(x), "=s"(y)
                 : "v"((unsigned)(256.f * r + 0.5f)), "v"((unsigned)(65536.f * __builtin_amdgcn_rcpf(r))));
    c.cpq = dq > 200 ? x : c.cpq; c.qpc = dq > 200 ? y : c.qpc;
}
__device__ __forceinline__ Clk clk_init() { return Clk{4u * (unsigned)__builtin_amdgcn_s_memrealtime(), (unsigned)__builtin_amdgcn_s_memtime(), 1470u, 11413u}; }
__device__ __forceinline__ int read_sched(__amdgpu_buffer_rsrc_t rws, __amdgpu_buffer_rsrc_t rwin, int s, unsigned tg, unsigned vo, unsigned to, unsigned f, unsigned g0, int& lt, int& xs, unsigned& fc) {
    if (__builtin_expect(__any((f >> 24) != tg), 0)) {
        for (unsigned spins = 0; __any((f >> 24) != tg);) {
            FC(fc);
            if (spin_fail(spins, rws)) break;
            __builtin_amdgcn_s_sleep(TSLP);
            asm volatile("" ::: "memory");
            if ((f >> 24) != tg) f = __builtin_amdgcn_raw_buffer_load_b32(rwin, vo, to, 16);
        }
    }
    int d = (int)((f - g0) & 0xFFFFFFu);
    d = max(d, __builtin_amdgcn_update_dpp(0, d, 0xB1, 0xF, 0xF, true));
    d = max(d, __builtin_amdgcn_update_dpp(0, d, 0x4E, 0xF, 0xF, true));
    d = max(d, __builtin_amdgcn_update_dpp(0, d, 0x141, 0xF, 0xF, true));
    d = max(d, __builtin_amdgcn_update_dpp(0, d, 0x140, 0xF, 0xF, true));
    d = max(d, __builtin_amdgcn_update_dpp(0, d, 0x142, 0xA, 0xF, true));
    d = max(__builtin_amdgcn_readlane(d, 16), __builtin_amdgcn_readlane(d, 48));
    const int l0 = lt, x0 = xs;
    d -= 0x400000;
    auto sel = [](int c, int x, int y) { int r; asm("s_cmp_lt_i32 %1, 0\n\ts_cselect_b32 %0, %2, %3" : "=s"(r) : "s"(c), "s"(x), "s"(y) : "scc"); return r; };
    const int miss = l0 + min(x0 + LTM, LTX) - d, first = s - 1;
    lt = sel((s ^ 1) - 1, LT0, sel(miss | first, l0, min(max(d, l0 - 1 - ((l0 - d) >> LTS)), l0 + LTU)));
    xs = sel(first, x0, sel(miss, min(x0 + XSU, XSM), max(x0 - 1 - (x0 >> XSS), XF & -(int)(XFM >> s & 1))));
    return 0x400000 + sel(miss | first, d + sel(first, sel((int)tg - 1, PEN00, PEN0), PEN), l0 + x0) + M1 + M2 + x0;
}
constexpr int QS = 272;
constexpr int TQS = 1152;
constexpr int L_Q = 10240, L_K = L_Q + 2 * TQS, L_S = L_K + 2 * TQS, L_TOK = L_S + 256, L_H = L_TOK + 49152, L_END = L_H + 2 * 40960;
typedef __attribute__((ext_vector_type(4))) short v4i16_t;
__device__ __forceinline__ u32x2 ldtr(const char* p) { return __builtin_bit_cast(u32x2, __builtin_amdgcn_ds_read_tr16_b64_v4i16((LAS v4i16_t*)p)); }
__device__ __forceinline__ u32x2 ldtr1(const LAS char* p) { return __builtin_bit_cast(u32x2, __builtin_amdgcn_ds_read_tr16_b64_v4i16((LAS v4i16_t*)p)); }
__device__ __forceinline__ u32x4 ldtr2(const LAS char* p0, const LAS char* p1) {
    const u32x2 x = __builtin_bit_cast(u32x2, __builtin_amdgcn_ds_read_tr16_b64_v4i16((LAS v4i16_t*)p0)), y = __builtin_bit_cast(u32x2, __builtin_amdgcn_ds_read_tr16_b64_v4i16((LAS v4i16_t*)p1));
    return (u32x4){x[0], x[1], y[0], y[1]};
}
__device__ __forceinline__ u32x4 ldtr2(const char* p0, const char* p1) { const u32x2 x = ldtr(p0), y = ldtr(p1); return (u32x4){x[0], x[1], y[0], y[1]}; }

__device__ __forceinline__ unsigned poll_lim(int& budget) {
    --budget;
    unsigned lim = budget > 0 ? 0xFFFFFFFEu : 0xFFFFFFFFu;
    asm volatile("" : "+s"(lim));
    return lim;
}
__device__ __forceinline__ bool mem_missing(unsigned lds, unsigned lim) {
    const LAS char* p = (const LAS char*)(size_t)(lds + fresh_lane() * 16u);
    unsigned m = 0;
#pragma unroll 1
    for (int h = 0; h < 2; ++h) {
        u32x4 v[5];
#pragma unroll
        for (int i = 0; i < 5; ++i) v[i] = *(const LAS u32x4*)(p + h * 5120 + i * 1024);
        SB();
#pragma unroll
        for (int i = 0; i < 5; ++i) m = max(max(m, max(v[i][0], v[i][1])), max(v[i][2], v[i][3]));
    }
    return __any(m > lim);
}
#define GATHER \
        "buffer_load_dwordx4 %[vo], %[rw], %[s0] offen sc1 lds\n\t" \
        "buffer_load_dwordx4 %[vo], %[rw], %[s0] offen offset:1024 sc1 lds\n\t" \
        "buffer_load_dwordx4 %[vo], %[rw], %[s0] offen offset:2048 sc1 lds\n\t" \
        "buffer_load_dwordx4 %[vo], %[rw], %[s0] offen offset:3072 sc1 lds\n\t" \
        "s_movk_i32 m0, 0x1000\n\t" \
        "s_nop 0\n\t" \
        "buffer_load_dwordx4 %[vo], %[rw], %[s4] offen sc1 lds\n\t" \
        "buffer_load_dwordx4 %[vo], %[rw], %[s4] offen offset:1024 sc1 lds\n\t" \
        "buffer_load_dwordx4 %[vo], %[rw], %[s4] offen offset:2048 sc1 lds\n\t" \
        "buffer_load_dwordx4 %[vo], %[rw], %[s4] offen offset:3072 sc1 lds\n\t" \
        "s_movk_i32 m0, 0x2000\n\t" \
        "s_nop 0\n\t" \
        "buffer_load_dwordx4 %[vo], %[rw], %[s1] offen sc1 lds\n\t" \
        "buffer_load_dwordx4 %[vo], %[rw], %[s1] offen offset:1024 sc1 lds"
#define GATHER_IN [vo] "v"(vo), [rw] "s"(rwin), [s0] "s"(so0), [s4] "s"(so0 + 4096u), [s1] "s"(so1)
__device__ __forceinline__ void gather_at(unsigned t, Clk& c, __amdgpu_buffer_rsrc_t rwin, unsigned vo, unsigned so0, unsigned so1) {
    unsigned tm, rp, x, fi;
    asm volatile("s_mov_b32 m0, 0\n\t" WAIT_FINE GATHER : WAIT_FINE_OUT : [cq] "s"(c.cpq), GATHER_IN : "vcc", "scc", "s100", "s101", "m0", "memory");
}
__device__ __forceinline__ void dma4k(__amdgpu_buffer_rsrc_t r, unsigned vo, unsigned so, unsigned lds) {
    asm volatile(
        "s_mov_b32 m0, %3\n\t"
        "s_nop 0\n\t"
        "buffer_load_dwordx4 %0, %1, %2 offen sc1 lds\n\t"
        "buffer_load_dwordx4 %0, %1, %2 offen offset:1024 sc1 lds\n\t"
        "buffer_load_dwordx4 %0, %1, %2 offen offset:2048 sc1 lds\n\t"
        "buffer_load_dwordx4 %0, %1, %2 offen offset:3072 sc1 lds"
        :: "v"(vo), "s"(r), "s"(so), "s"(lds) : "m0", "memory");
}
__device__ __forceinline__ int bs2(int c) { return (c & ~3) | ((c & 1) << 1) | ((c >> 1) & 1); }
__device__ __forceinline__ void get_mem0(u32x4 (&m)[2], int t) {
    const u16* mem0 = (const u16*)karg<2>();
#pragma unroll
    for (int i = 0; i < 2; ++i) {
        const int c = min(t + 512 * i, 639);
        const unsigned* src = (const unsigned*)(mem0 + ((c & 6) + ((c >> 3) & 1)) * D_ + (c >> 4) * 16 + (c & 1) * 2);
        m[i] = (u32x4){src[0], src[2], src[4], src[6]};
    }
    SB();
}
__device__ __forceinline__ void put_mem0(const u32x4 (&m)[2], __amdgpu_buffer_rsrc_t rwin, unsigned so0, unsigned so1, int t) { st128(m[0], rwin, t * 16u, so0, 16); st128(m[1], rwin, min(t, 127) * 16u, so1, 16); }
__device__ __forceinline__ f32x2 hadd(f32x4 c) { return (f32x2){c[0], c[1]} + (f32x2){c[2], c[3]}; }
__device__ __forceinline__ unsigned hpack(f32x4 c) { const f32x2 y = hadd(c); return pack2(y[0], y[1]); }
__device__ __forceinline__ u32x8 cat8(u32x4 lo, u32x4 hi) { return (u32x8){lo[0], lo[1], lo[2], lo[3], hi[0], hi[1], hi[2], hi[3]}; }
__device__ __forceinline__ u32x4 half8(const u32x8& v, int u) { return u ? (u32x4){v[4], v[5], v[6], v[7]} : (u32x4){v[0], v[1], v[2], v[3]}; }
template <int N, int ST> __device__ __forceinline__ void stage_h(__amdgpu_buffer_rsrc_t rws, unsigned so, unsigned lds, int lane) {
#pragma unroll
    for (int i = 0; i < N; ++i) dma1k(rws, lane * 16u, so + i * ST, lds + i * ST);
}
template <int N, int ST, int M> __device__ __forceinline__ void check_h(__amdgpu_buffer_rsrc_t rws, unsigned so, unsigned lds, int lane) {
    asm volatile("s_waitcnt vmcnt(%0)" :: "n"(M) : "memory");
    for (unsigned spins = 0; spins < SPIN_CAP; ++spins) {
        unsigned m = 0;
#pragma unroll
        for (int i = 0; i < N; ++i) m = max(m, smax(*(const LAS u32x4*)(size_t)(lds + lane * 16u + i * ST)));
        if (!__any(m == 0xFFFFFFFFu)) break;
        stage_h<N, ST>(rws, so, lds, lane);
        asm volatile("s_waitcnt vmcnt(0)" ::: "memory");
    }
}
__device__ __forceinline__ void wait_h(__amdgpu_buffer_rsrc_t rws, __amdgpu_buffer_rsrc_t rfl, unsigned fb, int seg, int& hrdy) {
    unsigned lim = hrdy ? 63u : (unsigned)min(seg + HLA, 63);
    if (__builtin_expect(hrdy <= seg, 0)) for (unsigned spins = 0;;) {
        asm volatile("" ::: "memory");
        const u32x4 f = __builtin_amdgcn_raw_buffer_load_b128(rfl, min(fresh_lane(), lim) * 128u, W_H + fb, 16);
        const unsigned long long miss = __ballot((f[0] | f[1] | f[2] | f[3]) != 0u);
        hrdy = miss ? __builtin_ctzll(miss) : (int)lim + 1;
        if (hrdy > seg) break;
        if (spin_fail(spins, rws)) break;
        lim = (unsigned)min(seg + HLA, 63);
        __builtin_amdgcn_s_sleep(HSLP);
    }
}
__device__ __forceinline__ u32x4 ask_h(__amdgpu_buffer_rsrc_t rfl, unsigned fb, unsigned lim) { return __builtin_amdgcn_raw_buffer_load_b128(rfl, min(fresh_lane(), lim) * 128u, W_H + fb, 16); }
__device__ __forceinline__ int see_h(u32x4 f) { const unsigned long long miss = __ballot((f[0] | f[1] | f[2] | f[3]) != 0u); return miss ? __builtin_ctzll(miss) : 64; }
template <int CTRL> __device__ __forceinline__ unsigned dppu(unsigned v) { return (unsigned)__builtin_amdgcn_mov_dpp((int)v, CTRL, 0xF, 0xF, true); }
template <int CTRL> __device__ __forceinline__ u32x4 ror4(u32x4 v) { return (u32x4){dppu<CTRL>(v[0]), dppu<CTRL>(v[1]), dppu<CTRL>(v[2]), dppu<CTRL>(v[3])}; }
__device__ __forceinline__ float xmax16(float v) {
    auto q = __builtin_amdgcn_permlane16_swap(__float_as_uint(v), __float_as_uint(v), false, false);
    return fmx(__uint_as_float(q[0]), __uint_as_float(q[1]));
}
__device__ __forceinline__ float xsum16(float v) {
    auto q = __builtin_amdgcn_permlane16_swap(__float_as_uint(v), __float_as_uint(v), false, false);
    return __uint_as_float(q[0]) + __uint_as_float(q[1]);
}
__device__ __forceinline__ float addswap16(float a, float b) {
    auto r = __builtin_amdgcn_permlane16_swap(__float_as_uint(a), __float_as_uint(b), false, false);
    return __uint_as_float(r[0]) + __uint_as_float(r[1]);
}
#define LANES() \
    const int lane = (int)fresh_lane(), tid = w * 64 + lane, fr = lane & 15, fq = lane >> 4; \
    const int sidx = (lane & 2) ? 0xEEEE : 0x4444; \
    const int ma0 = fq * 256 + (fr & 1) * 128 + (fr >> 1) * 16; \
    const int prow = 16 * ((fr >> 2) & 1) + 4 * (fr >> 3) + (fr & 3), hoff = fr * 128; \
    (void)tid; (void)sidx; (void)ma0; (void)prow; (void)hoff

__device__ __forceinline__ int seg_after(unsigned long long m, int j) { m = j >= 63 ? 0ull : m >> (j + 1); return m ? j + 1 + __builtin_ctzll(m) : NSEG; }
__device__ __forceinline__ int seg_done(unsigned long long m, int f0, int f1) { return min(seg_after(~m, f0), seg_after(m, f1)) - 1; }
template <int OFF> __device__ __forceinline__ void ldfrag(u32x4& d, const LAS char* p) { asm volatile("ds_read_b128 %0, %1 offset:%2" : "=v"(d) : "v"(p), "n"(OFF) : "memory"); }
template <int U, class FM, class FV> __device__ __forceinline__ void project(const u32x8 (&wt)[2][10], f32x4 (&tc)[2][2], u32x4 (&bb)[8], const LAS char* c0, const LAS char* c1, const LAS char* n0, const LAS char* n1, FM mid, FV vm) {
    constexpr int t = U >> 2, k = U & 3, v = (U + 7) % 40;
    if (U == 33) mid();
    vm(IC<U>());
    const LAS char* p = U < 33 ? (v >> 1 & 1 ? c1 : c0) : (v >> 1 & 1 ? n1 : n0);
    if (U < 2) asm volatile("s_nop 0\n\ts_waitcnt lgkmcnt(6)\n\tv_mfma_f32_16x16x32_bf16 %0, %3, %7, 0\n\tds_read_b128 %2, %5 offset:%6\n\tv_mfma_f32_16x16x32_bf16 %1, %4, %7, 0"
                            : "=&v"(tc[0][k & 1]), "=&v"(tc[1][k & 1]), "+v"(bb[(U + 7) & 7]) : "v"(half8(wt[0][t], k >> 1)), "v"(half8(wt[1][t], k >> 1)), "v"(p), "n"((v >> 2) * 4096 + (v & 1) * 2048), "v"(bb[U & 7]) : "memory");
    else asm volatile("s_waitcnt lgkmcnt(6)\n\tv_mfma_f32_16x16x32_bf16 %0, %3, %7, %0\n\tds_read_b128 %2, %5 offset:%6\n\tv_mfma_f32_16x16x32_bf16 %1, %4, %7, %1"
                      : "+v"(tc[0][k & 1]), "+v"(tc[1][k & 1]), "+v"(bb[(U + 7) & 7]) : "v"(half8(wt[0][t], k >> 1)), "v"(half8(wt[1][t], k >> 1)), "v"(p), "n"((v >> 2) * 4096 + (v & 1) * 2048), "v"(bb[U & 7]) : "memory");
    if constexpr (U < 39) project<U + 1>(wt, tc, bb, c0, c1, n0, n1, mid, vm);
    else asm volatile("s_nop 7" : "+v"(tc[0][0]), "+v"(tc[0][1]), "+v"(tc[1][0]), "+v"(tc[1][1]));
}
__device__ __forceinline__ void project_end(u32x4 (&bb)[8]) {
    asm volatile("s_waitcnt lgkmcnt(0)" : "+v"(bb[0]), "+v"(bb[1]), "+v"(bb[2]), "+v"(bb[3]), "+v"(bb[4]), "+v"(bb[5]), "+v"(bb[6]), "+v"(bb[7]) :: "memory");
}
__device__ __forceinline__ void project0(u32x4 (&bb)[8], const LAS char* c0, const LAS char* c1) {
    ldfrag<0>(bb[0], c0); ldfrag<2048>(bb[1], c0); ldfrag<0>(bb[2], c1); ldfrag<2048>(bb[3], c1);
    ldfrag<4096>(bb[4], c0); ldfrag<6144>(bb[5], c0); ldfrag<4096>(bb[6], c1); ldfrag<6144>(bb[7], c1);
    project_end(bb);
}
constexpr int WLB = 20992;
template <bool O, class F> __device__ __forceinline__ void load_w(u32x8 (&wt)[2][10], const char* W, int hh, int w, int c0, int cd, char* st, bool once, F gap) {
    const int lane = (int)fresh_lane(), fr = lane & 15, fq = lane >> 4, x = w & 3;
    const __amdgpu_buffer_rsrc_t r = mkrsrc(W, D_ * HD * 2);
    u32x4 raw[40];
    const unsigned vo = O ? (unsigned)(lane * 16) : (unsigned)(fq * 8192 + fr * 16);
    unsigned so = O ? (unsigned)(hh * 128 + 8 * x) * 1280u : (unsigned)(8 * x * 8192 + hh * 256);
    asm volatile("" : "+s"(so));
    auto fetch1 = [&](int p, auto aux) {
#pragma unroll
        for (int i = 5 * p; i < 5 * p + 5; ++i) {
            if (O) raw[i] = __builtin_amdgcn_raw_buffer_load_b128(r, vo + (i % 5 & 3) * 1024u, so + (unsigned)((32 * (i / 10) + 4 * (i / 5 & 1)) * 1280 + (i % 5 >> 2) * 4096), decltype(aux)::v);
            else raw[i] = __builtin_amdgcn_raw_buffer_load_b128(r, vo, so + (unsigned)(32 * (i >> 1) + 4 * (i & 1)) * 8192u, decltype(aux)::v);
        }
    };
    auto fetch = [&](int p) { if (WAUX && once) fetch1(p, IC<WAUX>()); else fetch1(p, IC<0>()); };
    gap(-1);
#pragma unroll
    for (int p = 0; p < WLA; ++p) { fetch(p); BAR0(); gap(p); }
    st += (w >> 2) * WLB;
    const int row = 4 * fq + (fr >> 2);
    int wo[5];
#pragma unroll
    for (int e = 0; e < 5; ++e) {
        const int id = 64 * e + lane, rr = id / 80, ch = id - 80 * rr;
        wo[e] = O ? (4 * x + rr) * 1312 + (ch >> 1) * 32 + (ch & 1) * 4 : (4 * x + fq) * 256 + ((fr ^ (2 * ((4 * x + fq) & 7))) * 16) + e * 4096;
    }
    const int r0 = O ? row * 1312 + x * 320 + (fr & 3) * 8 : row * 256 + (((c0 + ((fr >> 1) & 1)) ^ (2 * (row & 7))) * 16) + (fr & 1) * 8;
    const int r1 = O ? 0 : row * 256 + (((c0 + cd + ((fr >> 1) & 1)) ^ (2 * (row & 7))) * 16) + (fr & 1) * 8;
#pragma unroll
    for (int p = 0; p < 8; ++p) {
        char* b = st + (p & 1) * 2 * WLB;
#pragma unroll
        for (int e = 0; e < 5; ++e) {
            const u32x4 v = raw[5 * p + e];
            if (O) {
                unsigned* d = (unsigned*)(b + wo[e]);
                d[0] = __builtin_amdgcn_perm(v[2], v[0], 0x05040100); d[2] = __builtin_amdgcn_perm(v[3], v[1], 0x05040100);
                d[4] = __builtin_amdgcn_perm(v[2], v[0], 0x07060302); d[6] = __builtin_amdgcn_perm(v[3], v[1], 0x07060302);
            } else *(u32x4*)(b + wo[e]) = v;
        }
        BAR();
        if (p + WLA < 8) fetch(p + WLA);
        SB();
        gap(p + WLA);
#pragma unroll
        for (int e = 0; e < 10; ++e) {
            const int P = O ? p : 5 * p + (e >> 1), d = 2 * (P & 3);
            const u32x2 y = ldtr(b + (O ? r0 + e * 32 : (e & 1 ? r1 : r0) + (e >> 1) * 4096));
            u32x8& t = O ? wt[P >> 2][e] : wt[e & 1][P >> 2];
            t[d] = y[0]; t[d + 1] = y[1];
        }
    }
    BAR();
}

__global__ __launch_bounds__(512, 2) B3ATTR void b3_kernel(RecArgs a) {
    extern __shared__ __attribute__((aligned(16))) char smem[];
    char* memL = smem;
    char* tokL = smem + L_TOK;
    char* hL = smem + L_H;
    int w = __builtin_amdgcn_readfirstlane(threadIdx.x >> 6);
    __amdgpu_buffer_rsrc_t rwin = mkrsrc(a.win, W_END + 2 * PAIR);
    __amdgpu_buffer_rsrc_t rws = mkrsrc(a.ctl, (unsigned)OFF_WIN);
    asm volatile("" : "+s"(rwin), "+s"(rws));
    unsigned long long tref = 0;
    const unsigned long long tstart = __builtin_amdgcn_s_memtime();
    auto get_tag = [&]() __attribute__((always_inline)) {
        unsigned long long e;
        asm volatile("s_buffer_load_dwordx2 %0, %1, 8\n\ts_waitcnt lgkmcnt(0)" : "=s"(e) : "s"(rws) : "memory");
        e += 0x9E3779B97F4A7C15ull;
        return (u32x2){(unsigned)e, (unsigned)(e >> 32)};
    };
    auto role = [&](unsigned rot) {
        const unsigned vx = blockIdx.x + rot;
        const int j = blockIdx.x >> 3, x = vx & 3, hh = 8 * (blockIdx.x & 3) + (j & 7);
        int r;
        if (!(vx & 4)) r = j < 8 ? hh : j < 16 ? 32 + hh : j < 26 ? 80 + 10 * x + j : j < 30 ? 255 - 4 * (j - 26) - x : NREC + 2 + 2 * x + j;
        else r = j < 8 ? NQT + hh : j < 16 ? 64 + hh : j < 24 ? NREC - 16 + 8 * x + j : NXT + hh;
        return r;
    };
    int bid = role(xcc_id() - blockIdx.x + BADG * (blockIdx.x >> 3));
    if (WPF && (bid < 32 || (bid >= 64 && bid < 96))) {
        const unsigned l = threadIdx.x & 63u, hh = bid & 31, x = w & 3;
        const bool o = bid >= 64 && w >= 4;
        const __amdgpu_buffer_rsrc_t r = mkrsrc(a.W[(bid >> 5) + (w >> 2)], D_ * HD * 2);
#pragma unroll 1
        for (unsigned i = 0; i < WPF; ++i)
            dma1k(r, o ? l * 16u : (l >> 4) * 8192u + (l & 15u) * 16u, o ? (hh * 128u + 8u * x) * 1280u + i * 1024u + i / 10u * 30720u : (8u * x + 32u * (i >> 1) + 4u * (i & 1u)) * 8192u + hh * 256u, L_END - 16384 + w * 1024);
    }
    unsigned nh = 0, nz = 0, mc = 0, qz = 0;
    auto hint = [&](unsigned vo, unsigned so, u32x2 f) __attribute__((always_inline)) {
        const u32x2 tag = get_tag();
        for (unsigned spins = 0; spins < SPIN_CAP && __any(f[1] != tag[1] || (f[0] ^ tag[0]) > 255u); ++spins) {
            __builtin_amdgcn_s_sleep(4);
            asm volatile("" ::: "memory");
            f = __builtin_amdgcn_raw_buffer_load_b64(rwin, vo, so, 16);
        }
        return f[0] & 255u;
    };
    auto tell = [&](unsigned so, unsigned v) __attribute__((always_inline)) {
        const u32x2 tag = get_tag();
        __builtin_amdgcn_raw_buffer_store_b64((u32x2){(tag[0] & ~255u) | v, tag[1]}, rwin, 0u, so, 16);
    };
    auto share = [&](unsigned v) __attribute__((always_inline)) {
        if (w == 0) *(volatile LAS unsigned*)(smem + L_S + 64) = v;
        BAR();
        v = __builtin_amdgcn_readfirstlane(*(volatile LAS unsigned*)(smem + L_S + 64));
        BAR();
        return v;
    };
    u32x2 cf0, cf1;
    auto config0 = [&]() __attribute__((always_inline)) { cf0 = __builtin_amdgcn_raw_buffer_load_b64(rwin, fresh_lane() * 8u, W_CFG, 16); cf1 = __builtin_amdgcn_raw_buffer_load_b64(rwin, 0u, W_ARM, 16); };
    auto config = [&]() __attribute__((always_inline)) {
        unsigned v = 0;
        if (w == 0) {
            const unsigned l = fresh_lane(), f = hint(l * 8u, W_CFG, cf0), rot = __builtin_amdgcn_readfirstlane(hint(0u, W_ARM, cf1));
            const unsigned long long ok = __ballot((f & 4u) && !((l + rot) & 4u)), h = __ballot(f & 1u), q = __ballot(((f >> 1) ^ (l + rot)) & 1u);
            v = rot;
#pragma unroll
            for (int p = 0; p < 2; ++p) {
                unsigned long long m = ok & (p ? 0xFF00FF00FF00FF00ull : 0x00FF00FF00FF00FFull);
                const bool hb = 2 * __builtin_popcountll(m & h) > __builtin_popcountll(m);
                m &= hb ? h : ~h;
                v |= (hb ? 8u : 0u) << p | (2 * __builtin_popcountll(m & q) > __builtin_popcountll(m) ? 32u : 0u) << p;
            }
        }
        v = share(v);
        nh = (v >> 3 & 1u) * HALF; nz = (v >> 4 & 1u) * HALF;
        mc = ((blockIdx.x + v) & 3u) ^ (v >> 5 & 1u); qz = v >> 6 & 1u;
        return v;
    };
    auto zpair = [&](unsigned x, unsigned k) { return (((x >> 1) * 10u + k) * 2u + ((x & 1u) ^ qz)) * PAIR + nz; };
    unsigned mo, mo1, tro;
    auto two = [&]() { const unsigned l = fresh_lane(); return l < 4 ? (l >> 1) * PAIR + (l & 1) * 4096u + nh : 0x40000000u; };
    const u32x4 SENT = {0xFFFFFFFFu, 0xFFFFFFFFu, 0xFFFFFFFFu, 0xFFFFFFFFu};
    const f32x4 z4 = {0.f, 0.f, 0.f, 0.f};
    int hrdy = 0, budget = (int)SPIN_CAP;
    unsigned fc0 = 0, fc1 = 0, fc2 = 0;
    Clk ck = {0u, 0u, 1600u, 10486u};
    int lay = 0;
    unsigned fb = 0, zb = W_ZM, hb = 0;
    unsigned tpp = 0;
    int lt = LT1, xs = XS0;
    struct Tk { unsigned vo, tw, sl; const LAS char* sp; };
    auto tk_init = [&]() {
        const unsigned l = fresh_lane();
        Tk k = {l * 16u, two(), (l & 31u) * 128u + (l >> 5) * 4u, (const LAS char*)(memL + l * 64 + 4 + ((l >> 1) & 7) * 8)};
        PIN2(k.vo, k.tw); PIN2(k.sl, k.sp);
        ck = clk_init();
        tpp = ck.ta;
        return k;
    };
    auto sched2 = [&](Tk& k, int s, unsigned f) {
        const unsigned g0 = tpp - 0x400000u;
        tpp = g0 + (unsigned)read_sched(rws, rwin, s, (unsigned)(s + 64 * lay), k.sl, tro + (unsigned)(s & 3) * 2u * PAIR, f, g0, lt, xs, fc1);
        k.sl &= ~4u;
        RT(6);
    };
    auto sched = [&](Tk& k, int s) { sched2(k, s, __builtin_amdgcn_raw_buffer_load_b32(rwin, k.sl, tro + (unsigned)(s & 3) * 2u * PAIR, 16)); };
    auto own0 = [&](int i) { return W_ZM + (nz ^ HALF) + (i ? (64u + (bid >> 2)) * PAIR + (bid & 3) * 2048u : bid * PAIR); };
    auto fetch = [&](const Tk& k, int s, bool first) {
        const unsigned sb = (unsigned)(s & 3) * 6u * PAIR, ta0 = ck.ta, mm0 = ck.ma;
        if (__builtin_expect(first, 1)) {
            while ((unsigned)sx24(tpp - TKE - 4u * (unsigned)__builtin_amdgcn_s_memrealtime()) < 20000u) __builtin_amdgcn_s_sleep(1);
            gather_at(tpp, ck, rwin, k.vo, s ? mo + sb : own0(0), s ? mo1 + sb : own0(1));
        }
        __builtin_amdgcn_s_setprio(0);
        RT(1);
        clk_rate(ck, ta0, mm0);
        unsigned tb = ck.ma - DQC;
        asm volatile(AL "s_waitcnt vmcnt(%1) lgkmcnt(0)\n\ts_barrier\n\ts_waitcnt vmcnt(%2)\n\ts_barrier" : "+s"(tb) : "n"(10 - NB1), "n"(10 - NB2) : "memory");
        return tb;
    };
    unsigned mb = 0;
    auto again = [&](bool tk, unsigned vo, unsigned& sf, int s, unsigned lim, const LAS char*& p, const LAS char*& q) __attribute__((always_inline)) {
        const unsigned nb = mb == 0x10000u ? 0x14000u : 0x10000u;
        if (tk) {
            const unsigned c = (mc + 1u + (nb >> 14 & 1u)) & 3u, sb = W_MEM + (unsigned)(s & 3) * 6u * PAIR + nh, t = sb + (4u + (c & 1u)) * PAIR + (c >> 1) * 2048u;
            dma4k(rwin, vo, sb + c * PAIR, nb); dma4k(rwin, vo, sb + c * PAIR + 4096u, nb + 4096u);
            dma1k(rwin, vo, t, nb + 8192u); dma1k(rwin, vo, t + 1024u, nb + 9216u);
            asm volatile("s_waitcnt vmcnt(10)" : "+v"(sf) :: "memory");
        }
        const bool r = mem_missing(mb, lim);
        if (tk && !r) VMWAIT();
        if (r) { p += nb - mb; q += nb - mb; mb = nb; FC(fc0); }
        return r;
    };
    auto places = [&]() {
        mo = W_MEM + mc * PAIR + nh; mo1 = W_MEM + (4u + (mc & 1u)) * PAIR + nh + (mc >> 1) * 2048u;
        tro = W_T + (mc & 1u) * PAIR + nh + (mc >> 1) * 4096u;
    };
    auto fresh = [&]() {
        asm volatile("" : "+s"(bid), "+s"(w), "+s"(nh), "+s"(nz), "+s"(mc), "+s"(qz), "+s"(lay));
        fb = lay * W_FL; hb = O_H + (1 - lay) * (4u * 40960u); zb = W_ZM + lay * (NSEG % 3 * NOWN * PAIR);
        tref = 0; hrdy = 0; budget = (int)SPIN_CAP; ck = Clk{0u, 0u, 1600u, 10486u}; tpp = 0; lt = LT1; xs = XS0;
    };
    auto filter = [&]() __attribute__((always_inline)) {
        if ((bid >= 32 && bid < 64) || (bid >= NQT && bid < NXT)) {
            const unsigned u = bid < 64 ? bid : bid - NQT, l = fresh_lane();
            filter_unit((const u16*)karg<9>(), (const u16*)karg<10>(), u * 32, rws, (u16*)tokL, w * 64 + l);
            VMWAIT();
            BAR();
            if (w == 0) __builtin_amdgcn_raw_buffer_store_b64(get_tag(), rwin, (l & 1u) * 8u, W_ST + u * 128u + 64u, 16);
        }
    };
    auto settle = [&]() __attribute__((always_inline)) {
        if (TPF && w == 1 && (bid >> 5 == 1 || bid >= NQT)) for (unsigned i = 0; i < (unsigned)OFF_WIN; i += 64u << TPF) dma1k(rws, fresh_lane() << TPF, i, L_Q);
        const int b = role(config()); if (__builtin_expect(b != bid, 0)) { bid = b; filter(); } };
    auto warm = [&]() __attribute__((always_inline)) {
        unsigned long long pc;
        unsigned len;
        asm volatile("s_getpc_b64 %0\n1:\n\ts_mov_b32 %1, 2f-1b\n\t.subsection 1\n2:\n\t.subsection 0" : "=s"(pc), "=s"(len));
        if (CPF) dma1k(mkrsrc((const void*)pc, len), fresh_lane() * 16u, (blockIdx.x >> 3) * 1024u + w * 16384u, L_END - 8192 + w * 1024);
    };
    auto a1_done = [&]() __attribute__((always_inline)) {
        VMWAIT();
        BAR();
        if (w == 0) __builtin_amdgcn_raw_buffer_store_b64(get_tag(), rwin, 0u, W_ST + (blockIdx.x - 128u >> 1) * 128u + (blockIdx.x & 1u) * 8u, 16);
        SST(29);
    };
    auto due = [&](int i, int d) __attribute__((always_inline)) {
        __builtin_amdgcn_raw_buffer_store_b32(nosent1((unsigned)__builtin_amdgcn_s_memrealtime() + d), rwin, 0u, W_E + fb + i * 128u, 16);
    };
    auto await = [&](int i, int n, int d, unsigned f = ~0u) __attribute__((always_inline)) {
        if (__any(f == ~0u)) for (unsigned spins = 0;;) {
            asm volatile("" ::: "memory");
            f = __builtin_amdgcn_raw_buffer_load_b32(rwin, min(fresh_lane(), n - 1u) * 128u, W_E + i * 128u, 16);
            if (!__any(f == ~0u)) break;
            if (spin_fail(spins, rws)) break;
            __builtin_amdgcn_s_sleep(HSLP);
        }
        while (__any(f + d - (unsigned)__builtin_amdgcn_s_memrealtime() - 1u < 1000u)) {}
    };
    constexpr int E_OUT = 0, E_A1 = 8, E_A2 = 16, E_A3 = 24, E_ZT = 28;
    auto soon = [&](int j) __attribute__((always_inline)) {
        if (w == 0) { if (j >= 4) wait_h(rws, rwin, fb, j, hrdy); else await(E_A3 + fb / 128u, 4, 0); }
        BAR();
    };
    int slow = 0;
    auto gap = [&](int p) __attribute__((always_inline)) {
        if (p == 7) config0();
        if (p >= 0 && p < 7) for (int i = slow; i > 0; --i) __builtin_amdgcn_s_sleep(XSLP);
    };
    SST(27);
    {
        LANES();
        const unsigned u = blockIdx.x - 128u;
        if (blockIdx.x < 128) {
            const unsigned po = (blockIdx.x & 8u ? W_END : W_PROBE) + ((blockIdx.x >> 1 & 24u) | (blockIdx.x & 7u)) * 128u;
            const bool pr = bid >= 32 && bid < 64 && w == 7;
            if (pr) __builtin_amdgcn_raw_buffer_store_b32(0u, rwin, (lane & 3) * HALF + (lane >> 2 & 1) * 4096u, po, 16);
            warm();
            filter();
            if (blockIdx.x >= 64 && w == 7) tell(W_CFG + (blockIdx.x - 64u) * 8u, pr ? 4u | probe(rwin, po) : 0u);
            BAR0();
        } else a1_unit<8, 512>(mkrsrc(karg<3>(), S_ * D_ * 2), u * 16, false, rws, smem, row_tw(lane), w, lane, [&](int k) {
            if (k == 0 && u == 0 && w == 0) { tell(W_ROT, xcc_id()); __builtin_amdgcn_raw_buffer_store_b32(0u, rws, 0u, 0u, 16); }
            if (k == -1) {
                u32x4 sv = SENT;
                PIN1(sv);
#pragma unroll
                for (int i = 0; i < 4; ++i) {
                    static_assert(ARM_END - OFF_ARM <= 512 * 8192 && W_PROBE + W_END - W_ZM <= 512 * 8192, "arming");
                    const unsigned g = (i * 128u + u) * 8192u;
                    st128(sv, rwin, tid * 16u, g < W_PROBE ? g : g < W_PROBE + W_END - W_ZM ? g + W_ZM - W_PROBE : 0x40000000u, 16);
                    st128(sv, rws, tid * 16u, g < ARM_END - OFF_ARM ? (unsigned)OFF_ARM + g : 0x40000000u, 16);
                }
            }
        });
        SST(28);
        if (blockIdx.x >= 128) {
            if (w == 0) *(volatile LAS unsigned*)(smem + L_END - 16) = hint(0u, W_ROT, __builtin_amdgcn_raw_buffer_load_b64(rwin, 0u, W_ROT, 16));
            a1_done();
            const unsigned rot = __builtin_amdgcn_readfirstlane(*(volatile LAS unsigned*)(smem + L_END - 16));
            bid = role(rot);
            if (bid == 255 && w == 0) {
                const u32x2 tag = get_tag();
                for (unsigned spins = 0; spins < SPIN_CAP; ++spins) {
                    asm volatile("" ::: "memory");
                    const u32x4 g = __builtin_amdgcn_raw_buffer_load_b128(rwin, lane * 128u, W_ST, 16);
                    if (!__any(((g[0] ^ tag[0]) | (g[1] ^ tag[1]) | (g[2] ^ tag[0]) | (g[3] ^ tag[1])) != 0u)) break;
                    __builtin_amdgcn_s_sleep(4);
                }
                tell(W_ARM, rot);
            }
            SST(30);
        }
    }
    if (bid < 96 || (bid >= NQT && bid < NWRK)) {
    u32x8 wt[2][10];
#pragma nounroll
    for (int li = 0; li < 4; li += bid >= NXT ? 1 : 2) {
    lay = li >> 1;
    fresh();
    const bool xt = bid >= NXT, ph = li & 1;
    SST(0);
    if ((bid >= 32 && bid < 96) || xt) {
        const int hh = (xt ? bid - NXT : bid) & 31;
        if (li == 0 || ph) {
            u32x4 m0[2];
            get_mem0(m0, w * 64 + fresh_lane());
            wait_until(tstart + (bid < 64 ? DVO : xt ? DXT : DW1));
            const bool once = !slow && (NTM >> (xt ? 4 : bid >> 5) & 1);
            if (w < 4) load_w<false>(wt, karg<7>(), hh, w, 2 * w, 8, tokL, once, gap); else load_w<true>(wt, karg<8>(), hh, w, 0, 0, tokL, once, gap);
            SST(5);
            slow = 1;
            if (li == 0) settle();
            if (bid < 64) put_mem0(m0, rwin, own0(0), own0(1), w * 64 + fresh_lane());
        }
        places();
        const bool vt = bid >= 64;
        const unsigned long long mv = xt ? MXV & (ph ? ~0xFFFFFFFFull : 0xFFFFFFFFull) : ~MXV;
        auto nextv = [&](int j) { return min(seg_after(mv, j), NSEG - 1); };
        const int jv1 = nextv(-1), jv2 = nextv(jv1);
        {
            LANES();
            VMWAIT();
            BAR0();
            if (tid < 8) *(unsigned*)(smem + L_S + tid * 4) = 0u;
            SST(1);
            if (vt) {
                soon(ph ? jv2 : jv1);
                SST(2);
                const unsigned so = hb + (unsigned)(jv1 * 40960 + w * 1024), lo = L_H + w * 1024;
                stage_h<5, 8192>(rws, so, lo, lane);
                BAR0();
                if (w < 4) check_h<5, 8192, 0>(rws, so, lo, lane);
                else { stage_h<10, 4096>(rws, hb + (unsigned)(jv2 * 40960 + (w - 4) * 1024), lo + 36864u, lane); check_h<5, 8192, 10>(rws, so, lo, lane); }
            }
        }
        auto steps = [&](auto cc) {
            constexpr int C = decltype(cc)::v & 1;
            constexpr bool TK = decltype(cc)::v == 3;
            LANES();
            Tk tk = {0u, 0u, 0x40000000u, nullptr};
            if (TK) { tk = tk_init(); __builtin_amdgcn_s_setprio(TKP); }
            const unsigned zi = (unsigned)(hh * 128 + fr * 8) * 2u;
            unsigned zo0 = zpair(w & 3, fq) + zi, zo1 = zo0 + 8 * PAIR, zo2 = zpair(w & 3, 8 + (fq >> 1)) + zi + (fq & 1) * 8;
            const LAS char* va = (const LAS char*)(smem + L_Q + (fr & 1) * 544 + ((fr >> 1) & 1) * 1152 + (16 * fq + 2 * (fr >> 2)) * 4);
            LAS char* vw = (LAS char*)(smem + L_Q + (fq & 1) * 544 + (fq >> 1) * 1152 + (16 * w + bs2(fr)) * 4);
            const LAS char* mp0 = (const LAS char*)(memL + ma0);
            PIN2(zo0, zo1); PIN1(zo2);
            if (C == 0) { PIN2(mp0, vw); } else PIN1(va);
            u32x4 za = SENT, zc = SENT;
            u32x2 ze = {0u, 0u};
            auto put_z2 = [&](int s) {
                const unsigned z2b = O_Z2 + (unsigned)(hh * HS + s * 10240 + (w & 3) * 2560), l2 = fresh_lane();
                st128(za, rws, l2 * 16u, z2b, 16);
                st128(zc, rws, l2 * 16u, z2b + 1024u, 16);
                __builtin_amdgcn_raw_buffer_store_b64(ze, rws, (l2 >> 5) * 256u + (l2 & 15u) * 16u + ((l2 >> 4) & 1u) * 8u, z2b + 2048u, 16);
            };
            for (int s = 0; s < NSEG; ++s) {
                RT(0);
                bool redo = false;
                unsigned sf = 0;
                do {
                    unsigned lim = s > 0 ? poll_lim(budget) : ~0u;
                    unsigned long long sus;
                    asm volatile("" : "+s"(lim));
                    if (C == 0) {
                        asm volatile(
                            "v_mov_b64 v[200:201], 0\n\t"
                            "v_mov_b64 v[202:203], 0\n\t"
                            "v_mov_b64 v[204:205], 0\n\t"
                            "v_mov_b64 v[206:207], 0\n\t"
                            AL "s_setprio 2\n\t"
                            "s_waitcnt lgkmcnt(0)\n\t"
                            "s_barrier\n\t"
                            MTA(0)
                            "ds_read_b128 v[212:215], %[mp] offset:0\n\t"
                            "ds_read_b128 v[216:219], %[mp] offset:1024\n\t"
                            "ds_read_b128 v[220:223], %[mp] offset:2048\n\t"
                            "s_barrier\n\t"
                            "ds_read_b128 v[224:227], %[mp] offset:3072\n\t"
                            "ds_read_b128 v[228:231], %[mp] offset:4096\n\t"
                            "ds_read_b128 v[232:235], %[mp] offset:5120\n\t"
                            "s_waitcnt lgkmcnt(5)\n\t"
                            "v_smfmac_f32_16x16x64_bf16 v[200:203], v[212:215], %[w00], %[ix]\n\t"
                            "v_smfmac_f32_16x16x64_bf16 v[204:207], v[212:215], %[w10], %[ix]\n\t"
                            "s_waitcnt lgkmcnt(4)\n\t"
                            "v_smfmac_f32_16x16x64_bf16 v[200:203], v[216:219], %[w01], %[ix]\n\t"
                            "v_smfmac_f32_16x16x64_bf16 v[204:207], v[216:219], %[w11], %[ix]\n\t"
                            "s_barrier\n\t"
                            MTA(1)
                            "ds_read_b128 v[236:239], %[mp] offset:6144\n\t"
                            "ds_read_b128 v[240:243], %[mp] offset:7168\n\t"
                            "s_waitcnt lgkmcnt(5)\n\t"
                            "v_smfmac_f32_16x16x64_bf16 v[200:203], v[220:223], %[w02], %[ix]\n\t"
                            "ds_read_b128 v[244:247], %[mp] offset:8192\n\t"
                            "ds_read_b128 v[248:251], %[mp] offset:9216\n\t"
                            "s_waitcnt lgkmcnt(4)\n\t"
                            "v_smfmac_f32_16x16x64_bf16 v[200:203], v[224:227], %[w03], %[ix]\n\t"
                            "v_smfmac_f32_16x16x64_bf16 v[200:203], v[228:231], %[w04], %[ix]\n\t"
                            "v_smfmac_f32_16x16x64_bf16 v[200:203], v[232:235], %[w05], %[ix]\n\t"
                            "s_waitcnt lgkmcnt(3)\n\t"
                            "v_smfmac_f32_16x16x64_bf16 v[200:203], v[236:239], %[w06], %[ix]\n\t"
                            "s_waitcnt lgkmcnt(2)\n\t"
                            "v_smfmac_f32_16x16x64_bf16 v[200:203], v[240:243], %[w07], %[ix]\n\t"
                            "s_waitcnt lgkmcnt(1)\n\t"
                            "v_smfmac_f32_16x16x64_bf16 v[200:203], v[244:247], %[w08], %[ix]\n\t"
                            "s_waitcnt lgkmcnt(0)\n\t"
                            "v_smfmac_f32_16x16x64_bf16 v[200:203], v[248:251], %[w09], %[ix]\n\t"
                            "v_smfmac_f32_16x16x64_bf16 v[204:207], v[220:223], %[w12], %[ix]\n\t"
                            "s_nop 2\n\t"
                            "v_smfmac_f32_16x16x64_bf16 v[204:207], v[224:227], %[w13], %[ix]\n\t"
                            "s_nop 2\n\t"
                            "v_add_f32 v200, v200, v202\n\t"
                            "v_add_f32 v201, v201, v203\n\t"
                            "v_smfmac_f32_16x16x64_bf16 v[204:207], v[228:231], %[w14], %[ix]\n\t"
                            "v_cvt_pk_bf16_f32 v200, v200, v201\n\t"
                            "ds_write_b32 %[vw], v200\n\t"
                            MTA(2)
                            "v_smfmac_f32_16x16x64_bf16 v[204:207], v[232:235], %[w15], %[ix]\n\t"
                            "v_smfmac_f32_16x16x64_bf16 v[204:207], v[236:239], %[w16], %[ix]\n\t"
                            "v_smfmac_f32_16x16x64_bf16 v[204:207], v[240:243], %[w17], %[ix]\n\t"
                            "v_smfmac_f32_16x16x64_bf16 v[204:207], v[244:247], %[w18], %[ix]\n\t"
                            "s_waitcnt lgkmcnt(0)\n\t"
                            "s_barrier\n\t"
                            MTA(5)
                            "v_smfmac_f32_16x16x64_bf16 v[204:207], v[248:251], %[w19], %[ix]\n\t"
                            "s_nop 7\n\t"
                            "v_pk_add_f32 v[204:205], v[204:205], v[206:207]\n\t"
                            "v_cvt_pk_bf16_f32 v200, v204, v205\n\t"
                            "ds_write_b32 %[vw], v200 offset:256\n\t"
                            MTA(3)
                            "s_waitcnt vmcnt(0) lgkmcnt(0)\n\t"
                            "s_barrier\n\t"
                            "s_setprio 0\n\t"
                            "v_cmp_u_f32 %[sus], v204, v205\n\t"
                            : [sus] "=s"(sus) : [mp] "v"(mp0), [vw] "v"(vw), [ix] "v"(sidx),
                              [w00] "v"(wt[0][0]), [w01] "v"(wt[0][1]), [w02] "v"(wt[0][2]), [w03] "v"(wt[0][3]), [w04] "v"(wt[0][4]), [w05] "v"(wt[0][5]), [w06] "v"(wt[0][6]), [w07] "v"(wt[0][7]), [w08] "v"(wt[0][8]), [w09] "v"(wt[0][9]), [w10] "v"(wt[1][0]), [w11] "v"(wt[1][1]), [w12] "v"(wt[1][2]), [w13] "v"(wt[1][3]), [w14] "v"(wt[1][4]), [w15] "v"(wt[1][5]), [w16] "v"(wt[1][6]), [w17] "v"(wt[1][7]), [w18] "v"(wt[1][8]), [w19] "v"(wt[1][9])
                            : "v200", "v201", "v202", "v203", "v204", "v205", "v206", "v207", "v208", "v209", "v212", "v213", "v214", "v215", "v216", "v217", "v218", "v219", "v220", "v221", "v222", "v223", "v224", "v225", "v226", "v227", "v228", "v229", "v230", "v231", "v232", "v233", "v234", "v235", "v236", "v237", "v238", "v239", "v240", "v241", "v242", "v243", "v244", "v245", "v246", "v247", "v248", "v249", "v250", "v251", "vcc", "memory");
                        MT(4);
                    } else {
                        f32x16 ca, cb;
                        f32x8 cc;
                        asm volatile(".irp r,212,214,216,218,220,222,224,226,228,230,232,234,236,238,240,242,244,246,248,250\n\tv_mov_b64 v[\\r:\\r+1], 0\n\t.endr"
                                     : "={v[212:227]}"(ca), "={v[228:243]}"(cb), "={v[244:251]}"(cc));
                        unsigned sr = tro + (unsigned)(s & 3) * 2u * PAIR;
                        asm volatile("" : "+s"(sr));
                        if (TK) { fetch(tk, s, !redo); asm volatile("s_waitcnt vmcnt(0)\n\ts_barrier" ::: "memory"); RT(3); }
                        else asm volatile("s_waitcnt lgkmcnt(0)\n\ts_barrier\n\ts_barrier\n\ts_barrier" ::: "memory");
                        unsigned zs = zb;
                        asm volatile(
                            AL "s_setprio 1\n\t"
                            "s_waitcnt vmcnt(0) lgkmcnt(0)\n\t"
                            "s_barrier\n\t"
                            "ds_read_b64_tr_b16 v[192:193], %[va]\n\t"
                            "ds_read_b64_tr_b16 v[194:195], %[va] offset:32\n\t"
                            "s_waitcnt lgkmcnt(0)\n\t"
                            "v_smfmac_f32_16x16x64_bf16 v[212:215], v[192:195], %[w0], %[ix]\n\t"
                            "v_smfmac_f32_16x16x64_bf16 v[220:223], v[192:195], %[w2], %[ix]\n\t"
                            "v_smfmac_f32_16x16x64_bf16 v[216:219], v[192:195], %[w1], %[ix]\n\t"
                            "v_smfmac_f32_16x16x64_bf16 v[224:227], v[192:195], %[w3], %[ix]\n\t"
                            "s_barrier\n\t"
                            MTA(4)
                            "ds_read_b64_tr_b16 v[196:197], %[va] offset:256\n\t"
                            "v_smfmac_f32_16x16x64_bf16 v[228:231], v[192:195], %[w4], %[ix]\n\t"
                            "ds_read_b64_tr_b16 v[198:199], %[va] offset:288\n\t"
                            "v_smfmac_f32_16x16x64_bf16 v[236:239], v[192:195], %[w6], %[ix]\n\t"
                            "v_smfmac_f32_16x16x64_bf16 v[232:235], v[192:195], %[w5], %[ix]\n\t"
                            "v_smfmac_f32_16x16x64_bf16 v[240:243], v[192:195], %[w7], %[ix]\n\t"
                            "v_cmp_u_f32 %[sus], v212, v213\n\t"
                            "v_smfmac_f32_16x16x64_bf16 v[244:247], v[192:195], %[w8], %[ix]\n\t"
                            "v_smfmac_f32_16x16x64_bf16 v[248:251], v[192:195], %[w9], %[ix]\n\t"
                            "s_waitcnt lgkmcnt(0)\n\t"
                            "v_smfmac_f32_16x16x64_bf16 v[212:215], v[196:199], %[x0], %[ix]\n\t"
                            "s_nop 0\n\t"
                            "v_smfmac_f32_16x16x64_bf16 v[220:223], v[196:199], %[x2], %[ix]\n\t"
                            "s_cmp_lg_u64 %[sus], 0\n\t"
                            "s_cselect_b32 %[zs], 2.0, %[zs]\n\t"
                            "v_smfmac_f32_16x16x64_bf16 v[216:219], v[196:199], %[x1], %[ix]\n\t"
                            "s_nop 1\n\t"
                            "v_smfmac_f32_16x16x64_bf16 v[224:227], v[196:199], %[x3], %[ix]\n\t"
                            "v_add_f32 v212, v212, v214\n\t"
                            "v_smfmac_f32_16x16x64_bf16 v[228:231], v[196:199], %[x4], %[ix]\n\t"
                            "v_add_f32 v213, v213, v215\n\t"
                            "v_smfmac_f32_16x16x64_bf16 v[236:239], v[196:199], %[x6], %[ix]\n\t"
                            "v_add_f32 v220, v220, v222\n\t"
                            "v_smfmac_f32_16x16x64_bf16 v[232:235], v[196:199], %[x5], %[ix]\n\t"
                            "v_add_f32 v221, v221, v223\n\t"
                            "v_smfmac_f32_16x16x64_bf16 v[240:243], v[196:199], %[x7], %[ix]\n\t"
                            "v_cvt_pk_bf16_f32 v200, v212, v213\n\t"
                            "v_smfmac_f32_16x16x64_bf16 v[244:247], v[196:199], %[x8], %[ix]\n\t"
                            "v_cvt_pk_bf16_f32 v201, v220, v221\n\t"
                            "v_smfmac_f32_16x16x64_bf16 v[248:251], v[196:199], %[x9], %[ix]\n\t"
                            ".if %[tk]\n\tbuffer_load_dword %[sf], %[sl], %[rw], %[sr] offen sc1\n\t.endif\n\t"
                            "v_add_f32 v216, v216, v218\n\t"
                            "v_add_f32 v217, v217, v219\n\t"
                            "v_add_f32 v224, v224, v226\n\t"
                            "v_add_f32 v225, v225, v227\n\t"
                            "v_cvt_pk_bf16_f32 v202, v216, v217\n\t"
                            "v_cvt_pk_bf16_f32 v203, v224, v225\n\t"
                            "v_permlane32_swap_b32 v200, v201\n\t"
                            "v_pk_add_f32 v[228:229], v[228:229], v[230:231]\n\t"
                            "v_permlane32_swap_b32 v202, v203\n\t"
                            "v_min_u32 v201, -2, v201\n\t"
                            "v_pk_add_f32 v[236:237], v[236:237], v[238:239]\n\t"
                            "v_min_u32 v203, -2, v203\n\t"
                            "v_permlane16_swap_b32 v200, v202\n\t"
                            "v_pk_add_f32 v[232:233], v[232:233], v[234:235]\n\t"
                            "v_permlane16_swap_b32 v201, v203\n\t"
                            "v_pk_add_f32 v[240:241], v[240:241], v[242:243]\n\t"
                            MTA(5)
                            "buffer_store_dwordx4 v[200:203], %[z0], %[rw], %[zs] offen sc1\n\t"
                            "v_cvt_pk_bf16_f32 v204, v228, v229\n\t"
                            "v_cvt_pk_bf16_f32 v205, v236, v237\n\t"
                            "v_cvt_pk_bf16_f32 v206, v232, v233\n\t"
                            "v_cvt_pk_bf16_f32 v207, v240, v241\n\t"
                            "v_permlane32_swap_b32 v204, v205\n\t"
                            "v_pk_add_f32 v[244:245], v[244:245], v[246:247]\n\t"
                            "v_permlane32_swap_b32 v206, v207\n\t"
                            "v_min_u32 v205, -2, v205\n\t"
                            "v_pk_add_f32 v[248:249], v[248:249], v[250:251]\n\t"
                            "v_min_u32 v207, -2, v207\n\t"
                            "v_permlane16_swap_b32 v204, v206\n\t"
                            "v_cvt_pk_bf16_f32 v208, v244, v245\n\t"
                            "v_permlane16_swap_b32 v205, v207\n\t"
                            "v_cvt_pk_bf16_f32 v209, v248, v249\n\t"
                            "buffer_store_dwordx4 v[204:207], %[z1], %[rw], %[zs] offen sc1\n\t"
                            "s_nop 0\n\t"
                            "v_permlane32_swap_b32 v208, v209\n\t"
                            "v_min_u32 v209, -2, v209\n\t"
                            "buffer_store_dwordx2 v[208:209], %[z2], %[rw], %[zs] offen sc1\n\t"
                            ".if %[tk]\n\ts_setprio 0\n\ts_cmp_lg_u64 %[sus], 0\n\ts_cbranch_scc1 1f\n\ts_waitcnt vmcnt(3)\n1:\n\t.endif"
                            : [sf] "+&v"(sf), [zs] "+&s"(zs), [sus] "=&s"(sus), "=&{v[200:203]}"(za), "=&{v[204:207]}"(zc), "=&{v[208:209]}"(ze), "+{v[212:227]}"(ca), "+{v[228:243]}"(cb), "+{v[244:251]}"(cc)
                            : [va] "v"(va), [ix] "v"(sidx), [sl] "v"(tk.sl), [sr] "s"(sr), [rw] "s"(rwin), [z0] "v"(zo0), [z1] "v"(zo1), [z2] "v"(zo2), [tk] "n"(TK ? 1 : 0),
                              [w0] "v"(wt[0][0]), [w1] "v"(wt[0][1]), [w2] "v"(wt[0][2]), [w3] "v"(wt[0][3]), [w4] "v"(wt[0][4]), [w5] "v"(wt[0][5]), [w6] "v"(wt[0][6]), [w7] "v"(wt[0][7]), [w8] "v"(wt[0][8]), [w9] "v"(wt[0][9]),
                              [x0] "v"(wt[1][0]), [x1] "v"(wt[1][1]), [x2] "v"(wt[1][2]), [x3] "v"(wt[1][3]), [x4] "v"(wt[1][4]), [x5] "v"(wt[1][5]), [x6] "v"(wt[1][6]), [x7] "v"(wt[1][7]), [x8] "v"(wt[1][8]), [x9] "v"(wt[1][9])
                            : "v192", "v193", "v194", "v195", "v196", "v197", "v198", "v199", "scc", "memory");
                        RT(4);
                        MT(6);
                        tref = __builtin_amdgcn_s_memtime();
                        __builtin_amdgcn_s_setprio(TKP);
                    }
                    redo = false;
                    if (__builtin_expect(sus != 0, 0)) {
                        redo = again(TK, tk.vo, sf, s, lim, mp0, tk.sp);
                        if (!redo && C == 1) { st128(za, rwin, zo0, zb, 16); st128(zc, rwin, zo1, zb, 16); __builtin_amdgcn_raw_buffer_store_b64(ze, rwin, zo2, zb, 16); }
                    }
                } while (__builtin_expect(redo, 0));
                if (__builtin_expect(mb != 0, 0)) { mp0 -= mb; mb = 0; }
                if (s < 8) SST(8 + s);
                if (C == 0) {
                    if (s + 1 < NSEG) {
                        __builtin_amdgcn_s_sleep(DZR);
                        const unsigned zq = s == 0 ? 0x40000000u : zb == W_ZM ? W_ZM + 2u * NOWN * PAIR : zb - NOWN * PAIR;
                        unsigned s1;
                        asm volatile("v_mov_b32 %0, -1" : "=v"(s1));
                        st128(((u32x4){s1, s1, s1, s1}), rwin, zo0, zq, 16);
                        st128(((u32x4){s1, s1, s1, s1}), rwin, zo1, zq, 16);
                        __builtin_amdgcn_raw_buffer_store_b64((u32x2){s1, s1}, rwin, zo2, zq, 16);
                    }
                } else {
                    if (TK && s + 1 < NSEG) sched2(tk, s, sf);
                    if (!TK) wait_until(tref + DZ2);
                    if (s + 2 < NSEG) put_z2(s);
                }
                if (budget <= 0) __builtin_amdgcn_raw_buffer_store_b32(1u, rws, 0u, 0u, 16);
                zb = zb == W_ZM + 2u * NOWN * PAIR ? W_ZM : zb + NOWN * PAIR;
                RT(5);
                RTS();
                MT(7);
                MTS();
            }
        };
        if (!vt) {
            if (w < 4) steps(IC<0>()); else if (w < 7) steps(IC<1>()); else steps(IC<3>());
        } else {
            BAR();
            SST(3);
            LANES();
            const int ni = __builtin_popcountll(mv) + 1;
            if (w < 4) {
                const LAS char* c0 = (const LAS char*)(hL + hoff + ((fq ^ (fr & 7)) << 4));
                const LAS char* c1 = (const LAS char*)(hL + hoff + (((4 + fq) ^ (fr & 7)) << 4));
                const LAS char* n0 = c0 + 40960; const LAS char* n1 = c1 + 40960;
                u32x4 bb[8];
                project0(bb, c0, c1);
                for (int i = 1; i < ni; ++i) {
                    if (vt) MT(0);
                    f32x4 tc[2][2];
                    project<0>(wt, tc, bb, c0, c1, n0, n1,
                        [&]() {
                            MT(1);
                            if (__builtin_expect(i > 1, 1)) BAR();
                            MT(2);
                        }, [](auto) {});
                    { const LAS char* x = c0; c0 = n0; n0 = x; x = c1; c1 = n1; n1 = x; }
                    char* tb = tokL + (i & 1) * 32 * QS + (16 * w + 4 * fq) * 2;
#pragma unroll
                    for (int g = 0; g < 2; ++g)
#pragma unroll
                        for (int tt = 0; tt < 2; ++tt) *(u32x2*)(tb + (prow + 8 * tt) * QS + g * 128) = (u32x2){pack2(tc[g][tt][0], tc[g][tt][1]), pack2(tc[g][tt][2], tc[g][tt][3])};
                    asm volatile("" ::: "memory");
                    *(volatile LAS int*)(smem + L_S + 16 + w * 4) = i;
                    if (__builtin_expect(i == 1, 0)) { BAR(); if (VSLP) __builtin_amdgcn_s_sleep(VSLP); project0(bb, c0, c1); }
                    if (vt) { MT(3); const int s = i & 63; MTRT(); MTS(); }
                }
                project_end(bb);
            } else {
                __builtin_amdgcn_s_setprio(OPR);
                const unsigned hv = lane * 16u, fz_ = W_FZ + fb + nh + (unsigned)(hh * 128 + xt * 4096) + (w - 4) * PAIR;
                unsigned fp = jv1 - 1;
                u32x4 r = SENT;
                int jo = jv1, jh = jv2;
                check_h<10, 4096, 0>(rws, hb + (unsigned)(jv2 * 40960 + (w - 4) * 1024), L_H + 40960 + (w - 4) * 1024, lane);
                SST(16);
                auto rows = [&](int b) {
                    for (int t = 0; t < 10; ++t) dma1k(rws, hv, hb + (unsigned)(jh * 40960 + (w - 4) * 1024 + t * 4096), (unsigned)(L_H + ((b + 1) & 1) * 40960 + (w - 4) * 1024 + t * 4096));
                    VMWAIT();
                };
                for (int i = 1; i < ni; ++i, jo = nextv(jo)) {
                    const int b = i + (i >= ZLAG);
                    if (__builtin_expect(i == ZLAG, 0)) {
                        VMWAIT();
                        jh = nextv(jh);
                        wait_h(rws, rwin, fb, jh, hrdy);
                        BAR0();
                        rows(i);
                    }
                    jh = nextv(jh);
                    u32x4 hf;
                    asm volatile("" : "=v"(hf));
                    if (__builtin_expect(hrdy <= jh, 0)) asm volatile("buffer_load_dwordx4 %0, %1, %2, %3 offen sc1" : "+v"(hf) : "v"(min(lane, jh + 16) * 128), "s"(rwin), "s"(W_H + fb) : "memory");
                    if (b < ni) BAR();
                    if (i == 1) SST(17);
                    if (vt) MT(1);
                    for (;;) {
                        const u32x4 f = *(volatile LAS u32x4*)(smem + L_S + 16);
                        if (__builtin_amdgcn_readfirstlane(min(min(f[0], f[1]), min(f[2], f[3]))) >= (unsigned)i) break;
                        __builtin_amdgcn_s_sleep(1);
                    }
                    asm volatile("" ::: "memory");
                    u32x4 vf[2][4];
#pragma unroll
                    for (int ks = 0; ks < 4; ++ks)
#pragma unroll
                        for (int t = 0; t < 2; ++t) vf[t][ks] = *(const u32x4*)(tokL + (i & 1) * 32 * QS + (16 * t + fr) * QS + ks * 64 + fq * 16);
                    SB();
                    if (__builtin_expect(hrdy <= jh, 0)) { asm volatile("s_waitcnt vmcnt(0)" : "+v"(hf) :: "memory"); hrdy = min(see_h(hf), jh + 17); }
                    const bool ok = hrdy > jh;
                    if (__builtin_expect((i < ZLAG || !ok) && (int)fp > ZEARLY, 0)) { VMWAIT(); __builtin_amdgcn_raw_buffer_store_b32(fp, rwin, 0u, fz_, 16); }
                    if (__builtin_expect(jo == 0, 0) && w == 7) {
                        if (!(hh & 7)) due(E_ZT + (hh >> 3), lay ? DZF : DZF0);
                        __builtin_amdgcn_raw_buffer_store_b32(((4u * (unsigned)__builtin_amdgcn_s_memrealtime() + 4 * (lay ? DZF : DZF0) + DV0) & 0xFFFFFFu) | (unsigned)lay << 30, rwin, two(), W_T + hh * 128u + 4u, 16);
                    }
                    if (XDL && __builtin_expect(jo == 1, 0)) __builtin_amdgcn_s_sleep(XDL);
                    if (i == 1) SST(18);
                    if (vt) MT(0);
                    const unsigned ztb = O_Z + (unsigned)(jo * ZTSEG) * 2u + (unsigned)(10 * (w - 4) * H_ + hh) * 1024u;
                    f32x4 c[2][2];
                    unsigned hs = hb + (unsigned)(jh * 40960 + (w - 4) * 1024), hd = (unsigned)(L_H + ((b + 1) & 1) * 40960 + (w - 4) * 1024);
                    asm volatile("" : "+s"(hs), "+s"(hd));
#pragma unroll
                    for (int nt = 0; nt < 11; ++nt) {
                        auto& cc = c[nt & 1]; auto& cp = c[(nt + 1) & 1];
#pragma unroll
                        for (int ks = 0; ks < 4; ++ks) {
                            if (nt < 10) { cc[0] = mfma32(vf[0][ks], half8(wt[ks >> 1][nt], ks & 1), ks ? cc[0] : z4); PIN1(cc[0]); }
                            if (nt > 0 && ks >= 1 && ks < 3) { r[2 * ks - 2] = pack2(cp[ks - 1][0], cp[ks - 1][1]); PIN1(r); }
                            if (nt < 10) { cc[1] = mfma32(vf[1][ks], half8(wt[ks >> 1][nt], ks & 1), ks ? cc[1] : z4); PIN1(cc[1]); }
                            if (nt > 0 && ks >= 1 && ks < 3) { r[2 * ks - 1] = pack2(cp[ks - 1][2], cp[ks - 1][3]); PIN1(r); }
                            if (nt > 0 && ks == 2) st128(r, rws, lane * 16u, ztb + (unsigned)((nt - 1) * H_) * 1024u, 16);
                            if (nt < 5 && !(ks & 1) && __builtin_expect(ok, 1)) dma1k(rws, hv, hs + (2 * nt + (ks >> 1)) * 4096u, hd + (2 * nt + (ks >> 1)) * 4096u);
                            if (nt == 5 && ks == 0 && __builtin_expect(ok, 1)) {
                                asm volatile("s_waitcnt vmcnt(14)" ::: "memory");
                                __builtin_amdgcn_raw_buffer_store_b32(fp, rwin, 0u, fz_, 16);
                            }
                            SB();
                        }
                    }
                    if (i == 1) SST(19);
                    if (vt) MT(2);
                    fp = (unsigned)jo;
                    if (__builtin_expect(jo <= ZEARLY || i + 1 == ni || i + 1 == ZLAG || !ok, 0)) {
                        VMWAIT();
                        __builtin_amdgcn_raw_buffer_store_b32(fp, rwin, 0u, fz_, 16);
                    } else asm volatile("s_waitcnt vmcnt(7)" ::: "memory");
                    if (i == 1) SST(20);
                    if (__builtin_expect(!ok, 0)) { wait_h(rws, rwin, fb, jh, hrdy); rows(b); }
                    if (jo < 8) SST(8 + jo);
                    if (vt && (!BSPARSE || jo % 6 == 2)) { const int s = jo; RT(0); RTS(); }
                    if (vt) { MT(3); const int s = i & 63; MTRT(); MTS(); }
                }
            }
            __builtin_amdgcn_s_setprio(0);
            VMWAIT();
            BAR();
        }
    }
    if ((bid < 32 || bid >= NQT) && !ph) {
        LANES();
        const int hh = (bid < 32 ? bid : bid - NQT) & 31;
        if (lay == 0 || xt) {
            u32x4 m0[2];
            get_mem0(m0, tid);
            wait_until(tstart + (bid >= NQT ? DQT : DW1));
            load_w<false>(wt, karg<5>(w >> 2), hh, w, 4 * (w & 3), 2, tokL, !slow && (NTM >> (bid < 32 ? 0 : 3) & 1), gap);
            SST(5);
            slow = XSL2;
            if (!xt) settle();
            if (bid < 64) put_mem0(m0, rwin, own0(0), own0(1), tid);
        }
        places();
        const bool qt = bid >= NQT;
        const unsigned long long mq = xt ? MXQ : ~MXQ;
        auto nextq = [&](int j) { return min(seg_after(mq, j), NSEG - 1); };
        const int jq0 = qt ? nextq(1) : 0, jq1 = nextq(jq0);
        if (tid < 4) *(unsigned*)(smem + L_S + tid * 4) = 0u;
        SST(1);
        VMWAIT();
        soon(jq1);
        SST(2);
        const unsigned so1 = hb + (unsigned)(jq1 * 40960 + w * 1024), lo1 = L_H + 40960 + w * 1024;
        stage_h<5, 8192>(rws, hb + (unsigned)(jq0 * 40960 + w * 1024), lo1 - 40960u, lane);
        BAR0();
        stage_h<5, 8192>(rws, so1, lo1, lane);
        dma1k(rwin, lane * 16u, qt ? 0x40000000u : own0(0) + w * 1024u, w * 1024u); dma1k(rwin, lane * 16u, qt ? 0x40000000u : own0(1) + (w & 1) * 1024u, 8192u + (w & 1) * 1024u);
        check_h<5, 8192, 7>(rws, hb + (unsigned)(jq0 * 40960 + w * 1024), lo1 - 40960u, lane);
        BAR();
        SST(3);
        int qrdy = 0;
        {
            LANES();
            const unsigned to0 = (unsigned)((w ^ 4) >> 2) * 8192u + (unsigned)((w & 3) * 64 + (lane ^ ((fq & 1) * (w < 4 ? 8 : 5)))) * 16u, to1 = to0 + 4096u, hv = lane * 16u;
            const LAS char* c0 = (const LAS char*)(hL + hoff + ((fq ^ (fr & 7)) << 4));
            const LAS char* c1 = (const LAS char*)(hL + hoff + (((4 + fq) ^ (fr & 7)) << 4));
            u32x4 bb[8], g0, g1;
            f32x4 tc[2][2];
            auto pack = [&]() {
                g0 = (u32x4){pack2(tc[0][0][0], tc[0][0][1]), pack2(tc[0][0][2], tc[0][0][3]), pack2(tc[1][0][0], tc[1][0][1]), pack2(tc[1][0][2], tc[1][0][3])};
                g1 = (u32x4){pack2(tc[0][1][0], tc[0][1][1]), pack2(tc[0][1][2], tc[0][1][3]), pack2(tc[1][1][0], tc[1][1][1]), pack2(tc[1][1][2], tc[1][1][3])};
            };
            const LAS char* n0 = c0 + 40960; const LAS char* n1 = c1 + 40960;
            if (!qt) {
                project0(bb, c0, c1);
                for (int j = 0; j < 2; ++j) {
                    project<0>(wt, tc, bb, c0, c1, n0, n1, [&]() { if (j == 0) { check_h<5, 8192, 0>(rws, so1, lo1, lane); BAR(); } }, [](auto) {});
                    pack();
                    *(u32x4*)(tokL + j * 16384 + to0) = g0; *(u32x4*)(tokL + j * 16384 + to1) = g1;
                    c0 = n0; c1 = n1;
                    if (j == 0 && w == 7) __builtin_amdgcn_raw_buffer_store_b32(((4u * (unsigned)__builtin_amdgcn_s_memrealtime() + (lay ? DQ0 : DQ00)) & 0xFFFFFFu) | (unsigned)lay << 30, rwin, two(), W_T + hh * 128u, 16);
                }
                project_end(bb);
                VMWAIT();
            } else {
                const unsigned fq_ = W_FQ + fb + nh + (unsigned)(hh * 128 + xt * 4096), fv = tid == 0 ? 0u : 0x40000000u;
                const int jl = 63 - __builtin_clzll(mq);
                auto rounds = [&](auto ld) {
                constexpr bool LD = decltype(ld)::v;
                unsigned hs = 0x40000000u, hd = (unsigned)(L_H + 40960 + w * 1024);
                if (LD) __builtin_amdgcn_s_setprio(QTP);
                u32x4 bb[8];
                project0(bb, c0, c1);
                asm volatile("" : "=v"(g0), "=v"(g1));
                for (int jp = 1, j = jq0, jn = jq1;; jp = j, j = jn, jn = nextq(jn)) {
                    MT(0);
                    project<0>(wt, tc, bb, c0, c1, n0, n1,
                        [&]() {
                            unsigned fj = (unsigned)jp;
                            if (LD) {
                                if (__builtin_expect(hrdy <= nextq(jn), 0)) hrdy = min(see_h(g0), nextq(jn) + 17);
                                hs = hrdy > nextq(jn) ? hb + (unsigned)(nextq(jn) * 40960 + w * 1024) : 0x40000000u; hd = (unsigned)(2 * L_H + 40960 + 2 * w * 1024) - hd;
                                asm volatile("" : "+s"(hs), "+s"(hd));
                                PIN1(fj);
                            }
                            MT(1);
                            if (__builtin_expect(j == jq0, 0)) check_h<5, 8192, 0>(rws, so1, lo1, lane);
                            VMWAIT();
                            if (QNB) BAR0(); else BAR();
                            MT(2);
                            if (LD && w == 0) __builtin_amdgcn_raw_buffer_store_b32(fj, rwin, fv, fq_, 16);
                        },
                        [&](auto uu) {
                            constexpr int u = decltype(uu)::v, t = (u + QDO) % 40 / 2;
                            if (u == (LD ? QEU : QEV) && __builtin_expect(j <= QE2, 0)) {
                                asm volatile("s_waitcnt vmcnt(%0)\n\ts_barrier" :: "n"(LD ? 10 - QDO / 2 : 0) : "memory");
                                if (LD && w == 0) __builtin_amdgcn_raw_buffer_store_b32((unsigned)jp, rwin, fv, fq_, 16);
                            }
                            if (LD && u == QEU + 1 && __builtin_expect(hrdy <= nextq(jn), 0)) g0 = ask_h(rwin, fb, nextq(jn) + 16);
                            if (LD && !(u & 1) && t < 10 && (t < QDO / 2 || __builtin_expect(j != jq0, 1))) dma1k(rws, hv, hs + t * 4096u, hd + t * 4096u);
                        });
                    pack();
                    const unsigned qs = O_QK + (unsigned)(hh * HS + j * 16384);
                    st128(g0, rws, to0, qs, 16);
                    st128(g1, rws, to1, qs, 16);
                    { const LAS char* x = c0; c0 = n0; n0 = x; x = c1; c1 = n1; n1 = x; }
                    if (__builtin_expect(j == jl, 0)) {
                        VMWAIT();
                        BAR0();
                        if (tid == 0) __builtin_amdgcn_raw_buffer_store_b32((unsigned)j, rwin, 0u, fq_, 16);
                        break;
                    }
                    if (LD && __builtin_expect(hrdy <= nextq(jn), 0)) {
                        wait_h(rws, rwin, fb, nextq(jn), hrdy);
                        hs = hb + (unsigned)(nextq(jn) * 40960 + w * 1024);
                        for (int t = 0; t < QDO / 2; ++t) dma1k(rws, hv, hs + t * 4096u, hd + t * 4096u);
                    }
                    MT(3);
                    if (j < 8) SST(8 + j);
                    if (!BSPARSE || j % 6 == 2) { const int s = j; RT(0); RTS(); MTRT(); MTS(); }
                }
                project_end(bb);
                };
                if (w < 4) rounds(IC<1>()); else rounds(IC<0>());
                VMWAIT();
                BAR();
            }
        }
        SST(4);
        auto steps = [&](auto cc) {
        constexpr int C = decltype(cc)::v >> 1, R = decltype(cc)::v & 1;
        LANES();
        const LAS char* ka = (const LAS char*)(smem + L_K + (fr & 1) * TQS + (4 * fq + (fr >> 2)) * 8);
        const LAS char* qa = (const LAS char*)(smem + L_Q + (fr & 1) * 544 + ((fr >> 1) & 1) * 1152 + (16 * fq + 2 * (fr >> 2)) * 4);
        LAS char* qw = (LAS char*)(C < 2 ? smem + L_Q + (fq & 1) * 544 + (fq >> 1) * 1152 + (32 * w + 8 * (fr >> 2) + bs2(fr & 3)) * 4
                                         : smem + L_K + (fq >> 1) * TQS + (32 * (w & 3) + fr) * 8 + (fq & 1) * 4);
        const LAS char* mp0 = (const LAS char*)(memL + ma0);
        const int z = 16 * (fr >> 3) + 4 * (fr & 3) + 2 * ((fr >> 2) & 1);
        const int ko = ((z >> 4) * 256 + fq * 16 + ((z & 15) ^ ((fq & 1) * 5))) * 16;
        unsigned pa0 = (4u * C + 2u * (hh >> 4)) * PAIR + (unsigned)((hh & 15) * 512 + R * 256 + lane * 4);
        unsigned pa2 = ((8u + (fr >> 3)) * PAIR + C * 4096u + (unsigned)((hh >> 1) * 256 + R * 128 + (hh & 1) * 64 + fq * 16 + (fr & 6) * 2)) | (fr & 1) << 30;
        PIN2(ka, qw); PIN1(mp0);
        constexpr bool TK = C == 2 && R == 1;
        Tk tk = {0u, 0u, 0x40000000u, nullptr};
        if (TK) { tk = tk_init(); __builtin_amdgcn_s_setprio(TKP); }
        if (C < 2) { PIN2(qa, pa0); PIN1(pa2); }
        unsigned fqv = w == 4 ? (lane & 1) * 4096u + nh : 0x40000000u;
        if (C == 2 && !TK) PIN1(fqv);
        int qf = 1, tbo = 0;
        for (int s = 0; s < NSEG; ++s) {
            RT(0);
            const char* tb = tokL + tbo;
            unsigned pb = __builtin_amdgcn_readfirstlane(s + 1 < NSEG ? W_P + (unsigned)(s & 3) * 10u * PAIR + nh : 0x40000000u);
            unsigned pb1 = pb + PAIR;
            asm volatile("" : "+s"(pb), "+s"(pb1));
            if (s == 0) BAR();
            u32x8 kx[2][2];
            auto keys = [&]() {
#pragma unroll
                for (int u = 0; u < 4; ++u) kx[u >> 1][u & 1] = cat8(*(const u32x4*)(tb + (ko ^ ((u >> 1) * 16)) + (u & 1) * 2048), *(const u32x4*)(tb + (ko ^ ((u >> 1) * 16)) + (u & 1) * 2048 + 1024));
                PIN2(kx[0][0], kx[0][1]); PIN2(kx[1][0], kx[1][1]);
            };
            if (C < 2) keys();
            bool redo = false;
            unsigned sf = 0;
            do {
                unsigned lim = s > 0 ? poll_lim(budget) : ~0u;
                unsigned long long sus;
                f32x4 sc;
                if (C < 2) {
                    f32x4 s0, s1, s2;
                    unsigned late;
                    asm volatile(
                        ".irp r,232,234,236,238,240,242,244,246,248,250\n\tv_mov_b64 v[\\r:\\r+1], 0\n\t.endr\n\t"
                        "s_cmp_lg_u32 %[p0], 2.0\n\t"
                        "s_cbranch_scc1 1f\n\t"
                        "s_mov_b32 %[lt], -1\n\ts_barrier\n\ts_barrier\n\ts_barrier\n\ts_branch 2f\n"
                        AL "1:\n\t"
                        "s_setprio 2\n\t"
                        "s_waitcnt lgkmcnt(0)\n\t"
                        "s_barrier\n\t"
                        MTA(0)
                        "ds_read_b128 v[208:211], %[mp] offset:0\n\t"
                        "ds_read_b128 v[212:215], %[mp] offset:1024\n\t"
                        "ds_read_b128 v[216:219], %[mp] offset:2048\n\t"
                        "s_barrier\n\t"
                        "ds_read_b128 v[220:223], %[mp] offset:3072\n\t"
                        "s_waitcnt lgkmcnt(3)\n\t"
                        "v_smfmac_f32_16x16x64_bf16 v[232:235], v[208:211], %[w00], %[ix]\n\t"
                        "v_smfmac_f32_16x16x64_bf16 v[236:239], v[208:211], %[w10], %[ix]\n\t"
                        "ds_read_b128 v[224:227], %[mp] offset:4096\n\t"
                        "s_waitcnt lgkmcnt(3)\n\t"
                        "v_smfmac_f32_16x16x64_bf16 v[232:235], v[212:215], %[w01], %[ix]\n\t"
                        "v_smfmac_f32_16x16x64_bf16 v[236:239], v[212:215], %[w11], %[ix]\n\t"
                        "s_barrier\n\t"
                        MTA(1)
                        "ds_read_b128 v[228:231], %[mp] offset:5120\n\t"
                        "s_waitcnt lgkmcnt(3)\n\t"
                        "v_smfmac_f32_16x16x64_bf16 v[232:235], v[216:219], %[w02], %[ix]\n\t"
                        "v_smfmac_f32_16x16x64_bf16 v[236:239], v[216:219], %[w12], %[ix]\n\t"
                        "ds_read_b128 v[208:211], %[mp] offset:6144\n\t"
                        "s_waitcnt lgkmcnt(3)\n\t"
                        "v_smfmac_f32_16x16x64_bf16 v[232:235], v[220:223], %[w03], %[ix]\n\t"
                        "v_smfmac_f32_16x16x64_bf16 v[236:239], v[220:223], %[w13], %[ix]\n\t"
                        "ds_read_b128 v[212:215], %[mp] offset:7168\n\t"
                        "s_waitcnt lgkmcnt(3)\n\t"
                        "v_smfmac_f32_16x16x64_bf16 v[232:235], v[224:227], %[w04], %[ix]\n\t"
                        "v_smfmac_f32_16x16x64_bf16 v[236:239], v[224:227], %[w14], %[ix]\n\t"
                        "ds_read_b128 v[216:219], %[mp] offset:8192\n\t"
                        "s_waitcnt lgkmcnt(3)\n\t"
                        "v_smfmac_f32_16x16x64_bf16 v[232:235], v[228:231], %[w05], %[ix]\n\t"
                        "v_smfmac_f32_16x16x64_bf16 v[236:239], v[228:231], %[w15], %[ix]\n\t"
                        "ds_read_b128 v[220:223], %[mp] offset:9216\n\t"
                        "s_waitcnt lgkmcnt(3)\n\t"
                        "v_smfmac_f32_16x16x64_bf16 v[232:235], v[208:211], %[w06], %[ix]\n\t"
                        "v_smfmac_f32_16x16x64_bf16 v[236:239], v[208:211], %[w16], %[ix]\n\t"
                        "ds_read_b32 v207, v250 offset:%[ls]\n\t"
                        "s_waitcnt lgkmcnt(3)\n\t"
                        "v_smfmac_f32_16x16x64_bf16 v[232:235], v[212:215], %[w07], %[ix]\n\t"
                        "v_smfmac_f32_16x16x64_bf16 v[236:239], v[212:215], %[w17], %[ix]\n\t"
                        "s_waitcnt lgkmcnt(0)\n\t"
                        "v_smfmac_f32_16x16x64_bf16 v[232:235], v[216:219], %[w08], %[ix]\n\t"
                        "v_readfirstlane_b32 %[lt], v207\n\t"
                        "v_smfmac_f32_16x16x64_bf16 v[232:235], v[220:223], %[w09], %[ix]\n\t"
                        "v_smfmac_f32_16x16x64_bf16 v[236:239], v[216:219], %[w18], %[ix]\n\t"
                        "v_smfmac_f32_16x16x64_bf16 v[236:239], v[220:223], %[w19], %[ix]\n\t"
                        "s_setprio " STR(QP) "\n\t"
                        "s_nop 5\n\t"
                        "v_add_f32 v232, v232, v234\n\t"
                        "v_add_f32 v233, v233, v235\n\t"
                        "v_add_f32 v236, v236, v238\n\t"
                        "v_add_f32 v237, v237, v239\n\t"
                        "v_cvt_pk_bf16_f32 v232, v232, v233\n\t"
                        "v_cvt_pk_bf16_f32 v236, v236, v237\n\t"
                        "ds_write2_b32 %[qw], v232, v236 offset1:4\n\t"
                        MTA(2)
                        "2:"
                        : [lt] "=&s"(late), "=&{v[240:243]}"(s0), "=&{v[244:247]}"(s1), "=&{v[248:251]}"(s2)
                        : [mp] "v"(mp0), [qw] "v"(qw), [ix] "v"(sidx), [ls] "n"(L_S), [p0] "s"(pb), [w00] "v"(wt[0][0]), [w10] "v"(wt[1][0]), [w01] "v"(wt[0][1]), [w11] "v"(wt[1][1]), [w02] "v"(wt[0][2]), [w12] "v"(wt[1][2]), [w03] "v"(wt[0][3]), [w13] "v"(wt[1][3]), [w04] "v"(wt[0][4]), [w14] "v"(wt[1][4]), [w05] "v"(wt[0][5]), [w15] "v"(wt[1][5]), [w06] "v"(wt[0][6]), [w16] "v"(wt[1][6]), [w07] "v"(wt[0][7]), [w17] "v"(wt[1][7]), [w08] "v"(wt[0][8]), [w18] "v"(wt[1][8]), [w09] "v"(wt[0][9]), [w19] "v"(wt[1][9])
                        : "v207", "v208", "v209", "v210", "v211", "v212", "v213", "v214", "v215", "v216", "v217", "v218", "v219", "v220", "v221", "v222", "v223", "v224", "v225", "v226", "v227", "v228", "v229", "v230", "v231", "v232", "v233", "v234", "v235", "v236", "v237", "v238", "v239", "scc", "memory");
                    if (__builtin_expect(late == (unsigned)s, 0)) keys();
#define TAIL(A0, A2, B0, B2, C0, C2) \
                        "s_cmp_lg_u32 %[p0], 2.0\n\ts_cbranch_scc1 1f\n\ts_barrier\n\ts_barrier\n\t" \
                        "ds_read_b64_tr_b16 v[232:233], %[ka]\n\ts_waitcnt lgkmcnt(0)\n\tv_lshlrev_b32 v232, 16, v232\n\tv_cmp_u_f32 vcc, v232, v232\n\ts_branch 3f\n\t" AL "1:\n\t" \
                        ".if " STR(BQN) "==0\n\t" \
                        "s_setprio 3\n\t" \
                        "s_waitcnt vmcnt(0) lgkmcnt(0)\n\t" \
                        "s_barrier\n\t" \
                        MTA(3) \
                        "ds_read_b64_tr_b16 v[208:209], %[qa]\n\t" \
                        "ds_read_b64_tr_b16 v[210:211], %[qa] offset:32\n\t" \
                        "ds_read_b64_tr_b16 v[212:213], %[qa] offset:256\n\t" \
                        "ds_read_b64_tr_b16 v[214:215], %[qa] offset:288\n\t" \
                        "ds_read_b64_tr_b16 v[216:217], %[ka] offset:0\n\t" \
                        "ds_read_b64_tr_b16 v[218:219], %[ka] offset:128\n\t" \
                        "ds_read_b64_tr_b16 v[220:221], %[ka] offset:256\n\t" \
                        "ds_read_b64_tr_b16 v[222:223], %[ka] offset:384\n\t" \
                        "ds_read_b64_tr_b16 v[224:225], %[ka] offset:512\n\t" \
                        "ds_read_b64_tr_b16 v[226:227], %[ka] offset:640\n\t" \
                        "s_waitcnt lgkmcnt(8)\n\t" \
                        "v_smfmac_f32_16x16x64_bf16 v[240:243], v[208:211], %[k00], %[ix]\n\t" \
                        "ds_read_b64_tr_b16 v[228:229], %[ka] offset:768\n\t" \
                        "s_waitcnt lgkmcnt(7)\n\t" \
                        "v_smfmac_f32_16x16x64_bf16 v[240:243], v[212:215], %[k01], %[ix]\n\t" \
                        "ds_read_b64_tr_b16 v[230:231], %[ka] offset:896\n\t" \
                        "v_smfmac_f32_16x16x64_bf16 v[244:247], v[208:211], %[k10], %[ix]\n\t" \
                        "v_smfmac_f32_16x16x64_bf16 v[244:247], v[212:215], %[k11], %[ix]\n\t" \
                        "s_nop 4\n\t" \
                        ".elseif " STR(TPRE) "==0\n\t" \
                        "s_waitcnt lgkmcnt(0)\n\t" \
                        "s_barrier\n\t" \
                        "ds_read_b64_tr_b16 v[208:209], %[qa]\n\t" \
                        "ds_read_b64_tr_b16 v[210:211], %[qa] offset:32\n\t" \
                        "ds_read_b64_tr_b16 v[212:213], %[qa] offset:256\n\t" \
                        "ds_read_b64_tr_b16 v[214:215], %[qa] offset:288\n\t" \
                        "s_setprio 3\n\t" \
                        "s_waitcnt vmcnt(0) lgkmcnt(0)\n\t" \
                        "s_barrier\n\t" \
                        MTA(3) \
                        "ds_read_b64_tr_b16 v[216:217], %[ka] offset:0\n\t" \
                        "ds_read_b64_tr_b16 v[218:219], %[ka] offset:128\n\t" \
                        "ds_read_b64_tr_b16 v[220:221], %[ka] offset:256\n\t" \
                        "ds_read_b64_tr_b16 v[222:223], %[ka] offset:384\n\t" \
                        "v_smfmac_f32_16x16x64_bf16 v[240:243], v[208:211], %[k00], %[ix]\n\t" \
                        "ds_read_b64_tr_b16 v[224:225], %[ka] offset:512\n\t" \
                        "ds_read_b64_tr_b16 v[226:227], %[ka] offset:640\n\t" \
                        "v_smfmac_f32_16x16x64_bf16 v[240:243], v[212:215], %[k01], %[ix]\n\t" \
                        "ds_read_b64_tr_b16 v[228:229], %[ka] offset:768\n\t" \
                        "ds_read_b64_tr_b16 v[230:231], %[ka] offset:896\n\t" \
                        "v_smfmac_f32_16x16x64_bf16 v[244:247], v[208:211], %[k10], %[ix]\n\t" \
                        "v_smfmac_f32_16x16x64_bf16 v[244:247], v[212:215], %[k11], %[ix]\n\t" \
                        "s_nop 3\n\t" \
                        ".else\n\t" \
                        "s_waitcnt lgkmcnt(0)\n\t" \
                        "s_barrier\n\t" \
                        "ds_read_b64_tr_b16 v[208:209], %[qa]\n\t" \
                        "ds_read_b64_tr_b16 v[210:211], %[qa] offset:32\n\t" \
                        "ds_read_b64_tr_b16 v[212:213], %[qa] offset:256\n\t" \
                        "ds_read_b64_tr_b16 v[214:215], %[qa] offset:288\n\t" \
                        "s_setprio 0\n\t" \
                        "s_waitcnt vmcnt(0) lgkmcnt(0)\n\t" \
                        "v_smfmac_f32_16x16x64_bf16 v[240:243], v[208:211], %[k00], %[ix]\n\t" \
                        "v_smfmac_f32_16x16x64_bf16 v[240:243], v[212:215], %[k01], %[ix]\n\t" \
                        "s_setprio 3\n\t" \
                        "s_barrier\n\t" \
                        MTA(3) \
                        "ds_read_b64_tr_b16 v[216:217], %[ka] offset:0\n\t" \
                        "ds_read_b64_tr_b16 v[218:219], %[ka] offset:128\n\t" \
                        "ds_read_b64_tr_b16 v[220:221], %[ka] offset:256\n\t" \
                        "ds_read_b64_tr_b16 v[222:223], %[ka] offset:384\n\t" \
                        "v_smfmac_f32_16x16x64_bf16 v[244:247], v[208:211], %[k10], %[ix]\n\t" \
                        "ds_read_b64_tr_b16 v[224:225], %[ka] offset:512\n\t" \
                        "ds_read_b64_tr_b16 v[226:227], %[ka] offset:640\n\t" \
                        "v_smfmac_f32_16x16x64_bf16 v[244:247], v[212:215], %[k11], %[ix]\n\t" \
                        "ds_read_b64_tr_b16 v[228:229], %[ka] offset:768\n\t" \
                        "ds_read_b64_tr_b16 v[230:231], %[ka] offset:896\n\t" \
                        ".endif\n\t" \
                        "v_add_f32 v232, " A0 ", " A2 "\n\t" \
                        "v_mul_f32 v236, 0x3e0293ee, v232\n\t" \
                        "v_exp_f32 v236, v236\n\t" \
                        "s_waitcnt lgkmcnt(4)\n\t" \
                        "v_smfmac_f32_16x16x64_bf16 v[248:251], v[208:211], v[216:223], %[ix]\n\t" \
                        "s_waitcnt lgkmcnt(0)\n\t" \
                        "v_smfmac_f32_16x16x64_bf16 v[248:251], v[212:215], v[224:231], %[ix]\n\t" \
                        "v_add_f32 v233, " B0 ", " B2 "\n\t" \
                        "v_mul_f32 v237, 0x3e0293ee, v233\n\t" \
                        "v_exp_f32 v237, v237\n\t" \
                        "s_nop 0\n\t" \
                        "v_add_f32 v239, v236, v237\n\t" \
                        "s_nop 1\n\t" \
                        "v_add_f32_dpp v239, v239, v239 row_ror:8 row_mask:0xf bank_mask:0xf\n\t" \
                        MTA(4) \
                        "v_add_f32 v234, " C0 ", " C2 "\n\t" \
                        "v_mul_f32 v238, 0x3e0293ee, v234\n\t" \
                        "v_exp_f32 v238, v238\n\t" \
                        "s_nop 0\n\t" \
                        "v_add_f32 v239, v239, v238\n\t" \
                        "v_mov_b32_dpp v235, v238 quad_perm:[1,1,3,3] row_mask:0xf bank_mask:0xf\n\t" \
                        "s_nop 0\n\t" \
                        "v_add_f32_dpp v239, v239, v239 row_ror:4 row_mask:0xf bank_mask:0xf\n\t" \
                        "s_nop 1\n\t" \
                        "v_add_f32_dpp v239, v239, v239 row_ror:2 row_mask:0xf bank_mask:0xf\n\t" \
                        "s_nop 1\n\t" \
                        "v_add_f32_dpp v239, v239, v239 row_ror:1 row_mask:0xf bank_mask:0xf\n\t" \
                        "v_rcp_f32 v207, v239\n\t" \
                        "v_add_u32 v239, 0xe8800000, v239\n\t" \
                        "v_mul_f32 v236, v236, v207\n\t" \
                        "v_cmp_lt_u32 vcc, %[rg], v239\n\t" \
                        "v_mul_f32 v237, v237, v207\n\t" \
                        "v_cvt_pk_bf16_f32 v236, v236, v237\n\t" \
                        "s_cbranch_vccnz 3f\n\t" \
                        MTA(5) \
                        "buffer_store_dword v236, %[pa], %[rw], %[p0] offen sc1\n\t" \
                        "v_mul_f32 v238, v238, v207\n\t" \
                        "v_mul_f32 v235, v235, v207\n\t" \
                        "buffer_store_dword v236, %[pa], %[rw], %[p1] offen sc1\n\t" \
                        "v_cvt_pk_bf16_f32 v238, v238, v235\n\t" \
                        "buffer_store_dword v238, %[pm], %[rw], %[p0] offen sc1\n\t" \
                        "3:\n\t" \
                        "s_mov_b64 %[sus], vcc\n\t" \
                        "s_setprio 0\n\t"
#define TAILIO : [sus] "=&s"(sus), "=&{v[232:235]}"(sc), "+{v[240:243]}"(s0), "+{v[244:247]}"(s1), "+{v[248:251]}"(s2) \
                        : [qa] "v"(qa), [ka] "v"(ka), [ix] "v"(sidx), [pa] "v"(pa0), [pm] "v"(pa2), [rw] "s"(rwin), [p0] "s"(pb), [p1] "s"(pb1), [rg] "s"(SRG), \
                          [k00] "v"(kx[0][0]), [k01] "v"(kx[0][1]), [k10] "v"(kx[1][0]), [k11] "v"(kx[1][1]) \
                        : "v207", "v208", "v209", "v210", "v211", "v212", "v213", "v214", "v215", "v216", "v217", "v218", "v219", "v220", "v221", "v222", "v223", "v224", "v225", "v226", "v227", "v228", "v229", "v230", "v231", "v236", "v237", "v238", "v239", "vcc", "scc", "memory"
                    if (R == 0) asm volatile(TAIL("v240", "v242", "v244", "v246", "v248", "v250") TAILIO);
                    else asm volatile(TAIL("v241", "v243", "v245", "v247", "v249", "v251") TAILIO);
                    RT(4);
                    MT(6);
                } else {
                    f32x8 a01;
                    asm volatile("v_mov_b64 v[200:201], 0\n\tv_mov_b64 v[202:203], 0\n\tv_mov_b64 v[204:205], 0\n\tv_mov_b64 v[206:207], 0" : "={v[200:207]}"(a01));
                    unsigned sr = TK ? tro + (unsigned)(s & 3) * 2u * PAIR : qrdy < min(s + 3, NSEG - 1) ? W_FQ + fb + (unsigned)hh * 128u : 0x40000000u;
                    unsigned tb = 0, tg = (unsigned)(s + 64 * lay) << 24, so = s > 0 ? W_T + (unsigned)(s & 3) * 2u * PAIR + (unsigned)hh * 128u : 0x40000000u;
                    asm volatile("" : "+s"(lim), "+s"(sr));
                    if (TK) { asm volatile("" : "+s"(tg), "+s"(so)); tb = fetch(tk, s, !redo && s >= lay); }
                    else asm volatile(AL "s_waitcnt lgkmcnt(0)\n\ts_barrier\n\ts_barrier\n\ts_waitcnt vmcnt(0)\n\ts_barrier" ::: "memory");
                    asm volatile(
                        ".if %[tk]\n\t"
                        "s_waitcnt vmcnt(0)\n\t"
                        "s_memtime s[100:101]\n\t"
                        "s_barrier\n\t"
                        "s_setprio 1\n\t"
                        "s_waitcnt lgkmcnt(0)\n\t"
                        "ds_read2st64_b32 v[208:209], %[sp] offset1:16\n\t"
                        "ds_read_b32 v210, %[sp] offset:6144\n\t"
                        "ds_read_b128 v[212:215], %[mp] offset:0\n\t"
                        "ds_read_b128 v[216:219], %[mp] offset:1024\n\t"
                        "ds_read_b128 v[220:223], %[mp] offset:2048\n\t"
                        "ds_read_b128 v[224:227], %[mp] offset:3072\n\t"
                        "s_sub_u32 %[tb], s100, %[tb]\n\t"
                        "s_mul_i32 %[tb], %[tb], %[qc]\n\t"
                        "s_lshr_b32 %[tb], %[tb], 16\n\t"
                        "s_add_u32 %[tb], %[tb], %[ta]\n\t"
                        "s_and_b32 %[tb], %[tb], 0xffffff\n\t"
                        "s_or_b32 %[tb], %[tb], %[tg]\n\t"
                        "v_mov_b32 v211, %[tb]\n\t"
                        "s_waitcnt lgkmcnt(4)\n\t"
                        "v_max3_u32 v208, v208, v209, v210\n\t"
                        "v_cmp_lt_u32 vcc, %[lim], v208\n\t"
                        "ds_read_b128 v[228:231], %[mp] offset:4096\n\t"
                        "s_cmp_eq_u64 vcc, 0\n\t"
                        "s_cselect_b32 %[so], %[so], 2.0\n\t"
                        "buffer_store_dword v211, %[tw], %[rw], %[so] offen sc1\n\t"
                        "ds_read_b128 v[232:235], %[mp] offset:5120\n\t"
                        ".else\n\t"
                        "s_setprio 1\n\t"
                        "ds_read_b128 v[212:215], %[mp] offset:0\n\t"
                        "ds_read_b128 v[216:219], %[mp] offset:1024\n\t"
                        "ds_read_b128 v[220:223], %[mp] offset:2048\n\t"
                        "ds_read_b128 v[224:227], %[mp] offset:3072\n\t"
                        "ds_read_b128 v[228:231], %[mp] offset:4096\n\t"
                        "ds_read_b128 v[232:235], %[mp] offset:5120\n\t"
                        ".endif\n\t"
                        ".if !%[tk]\n\tbuffer_load_dword %[sf], %[sl], %[rw], %[sr] offen sc1\n\t.endif\n\t"
                        MTA(4)
                        "s_waitcnt lgkmcnt(5)\n\t"
                        "v_smfmac_f32_16x16x64_bf16 v[200:203], v[212:215], %[w00], %[ix]\n\t"
                        "ds_read_b128 v[236:239], %[mp] offset:6144\n\t"
                        "v_smfmac_f32_16x16x64_bf16 v[204:207], v[212:215], %[w10], %[ix]\n\t"
                        "ds_read_b128 v[240:243], %[mp] offset:7168\n\t"
                        "s_waitcnt lgkmcnt(6)\n\t"
                        "v_smfmac_f32_16x16x64_bf16 v[200:203], v[216:219], %[w01], %[ix]\n\t"
                        "ds_read_b128 v[244:247], %[mp] offset:8192\n\t"
                        "v_smfmac_f32_16x16x64_bf16 v[204:207], v[216:219], %[w11], %[ix]\n\t"
                        "ds_read_b128 v[248:251], %[mp] offset:9216\n\t"
                        "s_waitcnt lgkmcnt(4)\n\t"
                        "v_smfmac_f32_16x16x64_bf16 v[200:203], v[220:223], %[w02], %[ix]\n\t"
                        "v_smfmac_f32_16x16x64_bf16 v[204:207], v[220:223], %[w12], %[ix]\n\t"
                        "v_smfmac_f32_16x16x64_bf16 v[200:203], v[224:227], %[w03], %[ix]\n\t"
                        "v_smfmac_f32_16x16x64_bf16 v[204:207], v[224:227], %[w13], %[ix]\n\t"
                        "v_smfmac_f32_16x16x64_bf16 v[200:203], v[228:231], %[w04], %[ix]\n\t"
                        "v_smfmac_f32_16x16x64_bf16 v[204:207], v[228:231], %[w14], %[ix]\n\t"
                        "v_smfmac_f32_16x16x64_bf16 v[200:203], v[232:235], %[w05], %[ix]\n\t"
                        "v_smfmac_f32_16x16x64_bf16 v[204:207], v[232:235], %[w15], %[ix]\n\t"
                        "s_waitcnt lgkmcnt(0)\n\t"
                        "v_smfmac_f32_16x16x64_bf16 v[200:203], v[236:239], %[w06], %[ix]\n\t"
                        "v_smfmac_f32_16x16x64_bf16 v[204:207], v[236:239], %[w16], %[ix]\n\t"
                        BQ(3)
                        "v_smfmac_f32_16x16x64_bf16 v[200:203], v[240:243], %[w07], %[ix]\n\t"
                        "v_smfmac_f32_16x16x64_bf16 v[204:207], v[240:243], %[w17], %[ix]\n\t"
                        BQ(5)
                        "v_smfmac_f32_16x16x64_bf16 v[200:203], v[244:247], %[w08], %[ix]\n\t"
                        "v_smfmac_f32_16x16x64_bf16 v[200:203], v[248:251], %[w09], %[ix]\n\t"
                        BQ(7)
                        "v_smfmac_f32_16x16x64_bf16 v[204:207], v[244:247], %[w18], %[ix]\n\t"
                        "v_smfmac_f32_16x16x64_bf16 v[204:207], v[248:251], %[w19], %[ix]\n\t"
                        BQ(9)
                        "s_nop 5\n\t"
                        "v_pk_add_f32 v[200:201], v[200:201], v[202:203]\n\t"
                        "v_cvt_pk_bf16_f32 v200, v200, v201\n\t"
                        "v_pk_add_f32 v[204:205], v[204:205], v[206:207]\n\t"
                        "v_cvt_pk_bf16_f32 v201, v204, v205\n\t"
                        "ds_write2_b32 %[qw], v200, v201 offset1:32\n\t"
                        ".if %[tk]\n\tbuffer_load_dword %[sf], %[sl], %[rw], %[sr] offen sc1\n\t.endif\n\t"
                        "v_cmp_u_f32 %[sus], v204, v205\n\t"
                        MTA(2)
                        "s_waitcnt lgkmcnt(0)\n\t"
                        "s_barrier\n\t"
                        "s_setprio 0\n\t"
                        ".if %[tk]\n\ts_cmp_lg_u64 %[sus], 0\n\ts_cbranch_scc1 1f\n\t.endif\n\ts_waitcnt vmcnt(0)\n1:"
                        : [sus] "=&s"(sus), [sf] "+&v"(sf), [tb] "+&s"(tb), [so] "+&s"(so), "+{v[200:207]}"(a01)
                        : [mp] "v"(mp0), [qw] "v"(qw), [ix] "v"(sidx), [sl] "v"(TK ? tk.sl : fqv), [tk] "n"(TK ? 1 : 0), [sr] "s"(sr), [rw] "s"(rwin),
                          [sp] "v"(TK ? tk.sp : mp0), [lim] "s"(lim), [qc] "s"(ck.qpc), [ta] "s"(ck.ta), [tg] "s"(tg), [tw] "v"(TK ? tk.tw : fqv), [w00] "v"(wt[0][0]), [w10] "v"(wt[1][0]), [w01] "v"(wt[0][1]), [w11] "v"(wt[1][1]), [w02] "v"(wt[0][2]), [w12] "v"(wt[1][2]), [w03] "v"(wt[0][3]), [w13] "v"(wt[1][3]), [w04] "v"(wt[0][4]), [w14] "v"(wt[1][4]), [w05] "v"(wt[0][5]), [w15] "v"(wt[1][5]), [w06] "v"(wt[0][6]), [w16] "v"(wt[1][6]), [w07] "v"(wt[0][7]), [w17] "v"(wt[1][7]), [w08] "v"(wt[0][8]), [w18] "v"(wt[1][8]), [w09] "v"(wt[0][9]), [w19] "v"(wt[1][9])
                        : "v208", "v209", "v210", "v211", "v212", "v213", "v214", "v215", "v216", "v217", "v218", "v219", "v220", "v221", "v222", "v223", "v224", "v225", "v226", "v227", "v228", "v229", "v230", "v231", "v232", "v233", "v234", "v235", "v236", "v237", "v238", "v239", "v240", "v241", "v242", "v243", "v244", "v245", "v246", "v247", "v248", "v249", "v250", "v251", "vcc", "scc", "s100", "s101", "memory");
                    MT(3);
                }
                redo = false;
                if (__builtin_expect(sus != 0, 0)) {
                    redo = again(TK, tk.vo, sf, s, lim, mp0, tk.sp);
                    if (!redo && C < 2) {
                        const float c = 0.08838834764831845f * 1.44269504088896340736f;
                        float mx = fmx(fmx(sc[0], sc[1]), sc[2]);
                        mx = fmx(mx, dppf<0x128>(mx)); mx = fmx(mx, dppf<0x124>(mx)); mx = fmx(mx, dppf<0x122>(mx)); mx = fmx(mx, dppf<0x121>(mx));
                        const float e0 = __builtin_amdgcn_exp2f((sc[0] - mx) * c), e1 = __builtin_amdgcn_exp2f((sc[1] - mx) * c), e2 = __builtin_amdgcn_exp2f((sc[2] - mx) * c);
                        float d = e0 + e1;
                        d += dppf<0x128>(d); d += e2; d += dppf<0x124>(d); d += dppf<0x122>(d); d += dppf<0x121>(d);
                        d = __builtin_amdgcn_rcpf(d);
                        const unsigned p0 = nosent1(pack2(e0 * d, e1 * d)), p1 = nosent1(pack2(e2 * d, dppf<0xF5>(e2) * d));
                        __builtin_amdgcn_raw_buffer_store_b32(p0, rwin, pa0, pb, 16); __builtin_amdgcn_raw_buffer_store_b32(p0, rwin, pa0, pb1, 16);
                        __builtin_amdgcn_raw_buffer_store_b32(p1, rwin, pa2, pb, 16);
                    }
                }
            } while (__builtin_expect(redo, 0));
            if (__builtin_expect(mb != 0, 0)) { mp0 -= mb; tk.sp -= mb; mb = 0; }
            if (s < 8) SST(8 + s);
            if (s >= 56) SST(s - 40);
            if (C + R == 1) {
                {
                    __builtin_amdgcn_s_sleep(DDMA);
                    unsigned l2 = fresh_lane(), s1;
                    asm volatile("v_mov_b32 %0, -1" : "=v"(s1));
                    const unsigned base = W_P + (unsigned)((s - 1) & 3) * 10u * PAIR + nh;
                    asm volatile("s_waitcnt vmcnt(3)" ::: "memory");
                    st128(((u32x4){s1, s1, s1, s1}), rwin, (l2 >> 5) * PAIR + (l2 & 31u) * 16u, base + (unsigned)(R * 4 + 2 * (hh >> 4)) * PAIR + (unsigned)(hh & 15) * 512u, 16);
                    if (R) __builtin_amdgcn_raw_buffer_store_b64((u32x2){s1, s1}, rwin, (l2 >> 5) * PAIR + (l2 & 16u) * 256u + (l2 & 8u) * 16u + (l2 & 7u) * 8u, base + 8u * PAIR + (unsigned)((hh >> 1) * 256 + (hh & 1) * 64), 16);
                    else __builtin_amdgcn_raw_buffer_store_b32(s1, rwin, two(), W_T + (unsigned)((s + 2) & 3) * 2u * PAIR + (unsigned)hh * 128u, 16);
                }
            } else if (TK) {
                if (s + 1 < NSEG) sched2(tk, s, sf);
            } else if (C == 2 && w == 4) {
                qrdy = max(qrdy, seg_done(MXQ, __builtin_amdgcn_readlane(sf, 0), __builtin_amdgcn_readlane(sf, 1)));
                if (__builtin_expect(qf <= s, 0) && s + 1 < NSEG) {
                    for (unsigned spins = 0; qrdy <= s;) {
                        asm volatile("" ::: "memory");
                        const int f = (int)__builtin_amdgcn_raw_buffer_load_b32(rwin, fqv, W_FQ + fb + (unsigned)hh * 128u, 16);
                        qrdy = seg_done(MXQ, __builtin_amdgcn_readlane(f, 0), __builtin_amdgcn_readlane(f, 1));
                        if (qrdy > s) break;
                        FC(fc2);
                        if (spin_fail(spins, rws)) { qrdy = NSEG; break; }
                        __builtin_amdgcn_s_sleep(2);
                    }
                    *(volatile LAS int*)(smem + L_S) = s + 1;
                }
                for (const int qe = min(min(qf <= s && qrdy < s + QCU ? s + 1 : s + 2, qrdy), NSEG - 1); qf < qe;) {
                    ++qf;
                    const unsigned so = O_QK + (unsigned)(hh * HS + qf * 16384), lo = (unsigned)(L_TOK + qf % 3 * 16384), l2 = fresh_lane();
#pragma unroll
                    for (int i = 0; i < 4; ++i) dma4k(rws, l2 * 16u, so + i * 4096u, lo + i * 4096u);
                }
            } else {
                if (C == 2 && s + 1 < NSEG) __builtin_amdgcn_s_sleep(DPT);
                const float scale = 0.08838834764831845f, l2e = 1.44269504088896340736f * (NORND ? scale : 1.f);
                const int tq = (w >> 1) & 1, qh = w & 1;
                f32x4 sc[3] = {z4, z4, z4};
                u32x4 kt[2][4], qf[4], kf[4];
                const unsigned l2 = fresh_lane(), kto = ((l2 & 8u) * 512u + (l2 & 51u) * 16u + (l2 & 4u) * 32u) ^ ((l2 & 16u) * 5u), qto = ((l2 & 55u) * 16u + qh * 128u) ^ ((l2 & 16u) * 8u);
#pragma unroll
                for (int ks = 0; ks < 4; ++ks) {
                    qf[ks] = *(const u32x4*)(tb + 8192 + (tq * 4 + ks) * 1024 + qto);
                    kt[0][ks] = *(const u32x4*)(tb + ks * 1024 + kto);
                    kt[1][ks] = *(const u32x4*)(tb + ks * 1024 + (kto ^ 64u));
                    SB();
                }
#pragma unroll
                for (int ks = 0; ks < 4; ++ks) {
                    sc[0] = mfma32(kt[0][ks], qf[ks], sc[0]); PIN1(sc[0]);
                    kf[ks] = ldtr2(ka + ks * 256, ka + ks * 256 + 128);
                    SB();
                    sc[1] = mfma32(kt[1][ks], qf[ks], sc[1]); PIN1(sc[1]);
                }
#pragma unroll
                for (int ks = 0; ks < 4; ++ks) sc[2] = mfma32(kf[ks], qf[ks], sc[2]);
                const bool hi = (l2 & 8u) != 0u;
                float v[6];
#pragma unroll
                for (int r = 0; r < 4; ++r) v[r] = hi ? sc[1][r] : sc[0][r];
                v[4] = hi ? sc[2][2] : sc[2][0]; v[5] = hi ? sc[2][3] : sc[2][1];
#pragma unroll
                for (int r = 0; r < 6; r += 2) {
                    if (NORND) break;
                    const unsigned b0 = pack2(v[r], v[r + 1]);
                    const unsigned b1 = pack2(lo2f(b0) * scale, hi2f(b0) * scale);
                    v[r] = lo2f(b1); v[r + 1] = hi2f(b1);
                }
                const int lim = 8 * (w & 3) + (int)(l2 & 7u) - 8 * (int)(l2 >> 4) - (int)((l2 & 8u) >> 1);
#pragma unroll
                for (int r = 0; r < 4; ++r) if (r > lim) v[r] = NORND ? -1000000000.0f / scale : bfr(-1000000000.0f);
                float mx = fmx(fmx(fmx(fmx(fmx(v[0], v[1]), v[2]), v[3]), v[4]), v[5]);
                mx = xmax3216(fmx(mx, ror8f(mx)));
                const float mb = -mx * l2e;
#pragma unroll
                for (int r = 0; r < 6; ++r) v[r] = __builtin_amdgcn_exp2f(fmaf(v[r], l2e, mb));
                float sum = fmaf(v[4] + v[5], 0.5f, (v[0] + v[1]) + (v[2] + v[3]));
                sum = xsum3216(sum + ror8f(sum));
                const float inv = __builtin_amdgcn_rcpf(sum);
                const unsigned p0 = pack2(v[0] * inv, v[1] * inv), p1 = nosent1(pack2(v[2] * inv, v[3] * inv)), p2 = nosent1(pack2(v[4] * inv, v[5] * inv));
                const unsigned base = s + 2 < NSEG ? O_PT + (unsigned)(hh * HS + s * 2560) : W_PL + fb + (s & 1) * 5u * PAIR + hh * 2048u;
                const __amdgpu_buffer_rsrc_t rp = s + 2 < NSEG ? rws : rwin;
                const unsigned oob = (l2 & 8u) << 27;
                st128(((u32x4){p0, p1, dppu<0x128>(p0), dppu<0x128>(p1)}), rp, (l2 * 16u) | oob, base + (unsigned)(tq * 1024 + qh * 128), 16);
                __builtin_amdgcn_raw_buffer_store_b64((u32x2){p2, dppu<0x128>(p2)}, rp, ((l2 & 7u) * 16u + (l2 >> 4) * 8u) | oob | (l2 & 32u) << 25, base + (s + 2 < NSEG ? 2048u + tq * 256u : 65536u + tq * 8192u - hh * 1792u) + qh * 128u, 16);
            }
            tbo = tbo == 32768 ? 0 : tbo + 16384;
            if (__builtin_expect(budget <= 0, 0)) __builtin_amdgcn_raw_buffer_store_b32(1u, rws, 0u, 0u, 16);
            RT(5);
            RTS();
            MT(7);
            MTS();
        }
        };
        if (qt) {} else if (w == 0) steps(IC<0>()); else if (w == 1) steps(IC<1>()); else if (w == 2) steps(IC<2>()); else if (w == 3) steps(IC<3>()); else if (w < 7) steps(IC<4>()); else steps(IC<5>());
        __builtin_amdgcn_s_setprio(0);
    }
    SST(31);
    }
    } else
#pragma nounroll
    for (; lay < 2; ++lay) {
    fresh();
    SST(0);
    {
        LANES();
        const int G = bid < NWRK ? bid - 96 : bid == NWRK ? 80 : -1;
        const int fi = bid >= NWRK ? 255 - bid : bid < NREC ? 16 + G / 10 + 4 * (G % 10) : 99;
        const bool first = fi < 32;
        u32x2 tag = {0u, 0u};
        if (lay == 0) tag = get_tag();
        const int fl = min(lane, lay ? 13 : 15);
        const unsigned fv = w < 4 ? lane * 128u + 64u : (fl & 14) * 512u + (fl & 1) * 128u + (w & 3) * 256u, fs = lay ? W_FA : W_ST;
        auto ask = [&]() { asm volatile("" ::: "memory"); return __builtin_amdgcn_raw_buffer_load_b128(rwin, fv, fs, 16); };
        auto miss = [&](u32x4 f) { return __ballot(((f[0] ^ tag[0]) | (f[1] ^ tag[1]) | (f[2] ^ tag[0]) | (f[3] ^ tag[1])) != 0u); };
        if (G < 0 && w >= 4) for (unsigned spins = 0; spins < SPIN_CAP && miss(ask()); ++spins) __builtin_amdgcn_s_sleep(ASLP);
        BAR();
        if (G >= 0) {
            unsigned long long m = ~0ull;
            unsigned e = ~0u;
            a2_unit(rws, G, w, lane, lay == 0, [&](int r) {
                if (r == 6) { if (lay) e = __builtin_amdgcn_raw_buffer_load_b32(rwin, (lane & 1) * 128u, W_E + (E_A1 + 2 * (w & 3)) * 128u, 16); else if (DFA) __builtin_amdgcn_s_sleep(DFA); }
                if (r >= 7 && lay) { if (r == 7) await(E_A1 + 2 * (w & 3), 2, 0, e); }
                else for (unsigned spins = 0; spins < SPIN_CAP && (r == 6 ? m & 0x3FFFu : m); ++spins) {
                    if (spins) __builtin_amdgcn_s_sleep(ASLP);
                    m = miss(ask());
                }
                if (r == 7) SST(5);
            }, [&](int k) {
                if (k == 2 && w == 3 && blockIdx.x < 136) due(E_A2 + (blockIdx.x & 7), lay ? DA2 : DA20);
                if (k == 7 && YSLP) __builtin_amdgcn_s_sleep(YSLP);
                if (bid < NREC) SST(8 + k);
            });
            SST(6);
            VMWAIT();
            BAR0();
            if (tid == 0) __builtin_amdgcn_raw_buffer_store_b32(0u, rwin, 0u, W_FB + fb + (unsigned)((G >> 2) * 128 + (G & 3) * 4), 16);
            SST(7);
        }
        const RowTw tw = row_tw(lane);
        auto columns = [&](int all) {
            if (w == 7) await(E_A2 + fb / 128u, 8, all);
            if (w == 7 && all) for (unsigned spins = 0;;) {
                asm volatile("" ::: "memory");
                const u32x4 f = __builtin_amdgcn_raw_buffer_load_b128(rwin, (unsigned)min(lane, 20) * 128u, W_FB + fb, 16);
                if (!__any((f[0] | (lane < 20 ? f[1] | f[2] | f[3] : 0u)) != 0u)) break;
                if (spin_fail(spins, rws)) break;
                __builtin_amdgcn_s_sleep(ASLP);
            }
            BAR0();
            if (!all || !first) SST(3);
        };
        const __amdgpu_buffer_rsrc_t rx = mkrsrc(lay ? karg<0>() + OFF_X2 : karg<3>(), S_ * D_ * 2);
        if (first) {
            const unsigned fo = fb + (unsigned)((fi >> 2) * 128 + (fi & 3) * 4);
            a3_units<4>(rws, rx, fi * 8, 0, lay, smem, tw, w, lane, [&](int k) {
                if (k < 0) { if (G < 0) columns(0); } else if (fi < 16) SST(16 + k);
                if (k == 0 && w == 7 && fi < 4) due(E_A3 + fi, lay ? DA3 : DA30);
            }, [&](int) { if (tid == 0) __builtin_amdgcn_raw_buffer_store_b32(0u, rwin, 0u, W_H + fo, 16); });
            SST(4);
        }
                if (!first || (WK3 && bid >= NWRK)) {
            const bool two = bid >= NREC && bid < NREC + 32;
            const int u0 = two ? 32 + 16 * WK3 + (bid - NREC) / 8 + 4 * ((bid - NREC) % 8) : bid < NREC ? fi - 24 : bid < NWRK ? bid - NREC - 32 : 32 + fi;
            columns(two ? DFB - (32 + 16 * WK3) * DST + u0 * DST : DFB0);
            a3_units<8>(rws, rx, (16 + u0) * 16, two ? 512 : S_, lay, smem, tw, w, lane, [](int) {}, [&](int r) {
                if (tid == 0) __builtin_amdgcn_raw_buffer_store_b64((u32x2){0u, 0u}, rwin, 0u, W_H + fb + (unsigned)((r >> 5) * 128 + (r & 16) / 2), 16);
                if (r < 1536 && two) SST(2);
            });
            VMWAIT();
        }
        if (lay == 0 && bid < NREC) { config0(); config(); }
        SST(1);
        places();
    }
    const __amdgpu_buffer_rsrc_t rout = mkrsrc(lay ? karg<4>() : karg<0>() + OFF_X2, S_ * D_ * 2);
    if (bid < NREC) {
        LANES();
        const int o = bid - 96, hg = w & 3;
        const unsigned ztoff = (unsigned)((o * H_ + 8 * hg) * 64 + lane) * 16u;
        const unsigned zoff = zpair(o / 10, o % 10) + (unsigned)(lane * 16);
        const unsigned poff = (4u * (mc >> 1) + 2u * (hg >> 1) + (mc & 1u)) * PAIR + nh + (unsigned)((hg & 1) * 4096 + (fq >> 1) * 512 + (fr >> 2) * 128 + (fr & 1) * 64 + (fq & 1) * 32 + (fr & 2) * 8);
        const unsigned pmoff = (8u + (mc & 1u)) * PAIR + nh + (mc >> 1) * 4096u + (unsigned)(fq * 256 + (fr >> 3) * 128 + (fr & 2) * 32 + (fr & 4) * 8 + (fr & 1) * 16);
        const unsigned fzo = (unsigned)(8 * hg + (lane & 7)) * 128u + (unsigned)(o / 10) * PAIR + nh + (lane & 8) * 512u;
        const unsigned moff = (o < 32 ? (unsigned)o * 256u : 4u * PAIR + (unsigned)(o - 32) * 256u) + nh + (unsigned)(lane * 4);
        const unsigned mc2 = o < 32 ? 2u * PAIR : 2048u;
        char* op = smem + 8192;
        SST(2);
        u32x8 zf[4];
        asm volatile("" : "={v[160:167]}"(zf[0]), "={v[168:175]}"(zf[1]), "={v[176:183]}"(zf[2]), "={v[184:191]}"(zf[3]));
        int zr = -1;
        const unsigned ra = (o < 32 ? (unsigned)fq * PAIR + (unsigned)o * 256u : (4u + (fq & 1)) * PAIR + (unsigned)(fq >> 1) * 2048u + (unsigned)(o - 32) * 256u) + nh + (unsigned)(fr * 16);
        auto zt_wait = [&](int s) {
            if (__builtin_expect(zr < s, 0)) for (unsigned spins = 0;;) {
                asm volatile("" ::: "memory");
                if (!__any((int)__builtin_amdgcn_raw_buffer_load_b32(rwin, fzo, W_FZ + fb, 16) < s && ((lane >> 3) & 1) == (int)((MXV >> s) & 1))) break;
                FC(fc2);
                if (spin_fail(spins, rws)) break;
                __builtin_amdgcn_s_sleep(ZPS);
            }
        };
        auto sent = [&](int s) {
            for (;;) {
                const u32x4 x = *(volatile LAS u32x4*)(smem + 4096);
                zr = __builtin_amdgcn_readfirstlane(x[0]);
                if (__builtin_amdgcn_readfirstlane(min((int)x[1], max((int)x[2], (int)x[0] - 1))) == s) break;
                __builtin_amdgcn_s_sleep(2);
            }
        };
        if (w < 4) {
            await(E_ZT + fb / 128u, 4, 0);
            for (int s = 0; s + 1 < NSEG; ++s) {
                const unsigned pb = W_P + (unsigned)(s & 3) * 10u * PAIR;
                zt_wait(s);
                if (s == 0) SST(1);
#define ZTL \
                    "buffer_load_dwordx4 v[160:163], %[zt], %[rs], %[so] offen sc1\n\t" \
                    "buffer_load_dwordx4 v[164:167], %[zt], %[rs], %[so] offen offset:1024 sc1\n\t" \
                    "buffer_load_dwordx4 v[168:171], %[zt], %[rs], %[so] offen offset:2048 sc1\n\t" \
                    "buffer_load_dwordx4 v[172:175], %[zt], %[rs], %[so] offen offset:3072 sc1\n\t" \
                    "buffer_load_dwordx4 v[176:179], %[zt], %[rs], %[s4] offen sc1\n\t" \
                    "buffer_load_dwordx4 v[180:183], %[zt], %[rs], %[s4] offen offset:1024 sc1\n\t" \
                    "buffer_load_dwordx4 v[184:187], %[zt], %[rs], %[s4] offen offset:2048 sc1\n\t" \
                    "buffer_load_dwordx4 v[188:191], %[zt], %[rs], %[s4] offen offset:3072 sc1\n\t"
                const unsigned zso = O_Z + (unsigned)(s * ZTSEG) * 2u;
                if (__builtin_expect(budget <= 0, 0)) __builtin_amdgcn_raw_buffer_store_b32(1u, rws, 0u, 0u, 16);
                RT(6);
                const unsigned spa = (unsigned)((s & 1) * 2048 + (w < 3 ? w * 512 : 0) + lane * 8);
#define OST(r)
                unsigned long long t0_, t1_, t2_, t3_, t4_ = 0, bad;
                int tries = budget;
                unsigned po = poff, pm = pmoff, x0 = 4u * PAIR, x1 = 4096u;
                const unsigned mb = W_MEM + (unsigned)((s + 1) & 3) * 6u * PAIR;
                if (w < 3) asm volatile("s_nop 4\n\t" ZTL AL
                    "s_barrier\n\t"
                    OST("%[t0]")
                    "v_mov_b64 v[96:97], 0\n\t"
                    "v_mov_b64 v[98:99], 0\n\t"
                    "s_barrier\n\t"
                    "1:\n\t"
                    "buffer_load_dwordx4 v[208:211], %[po], %[rw], %[pb] offen sc1\n\t"
                    "buffer_load_dwordx4 v[212:215], %[po], %[rw], %[pb] offen offset:1024 sc1\n\t"
                    "buffer_load_dwordx4 v[216:219], %[po], %[rw], %[pb] offen offset:2048 sc1\n\t"
                    "buffer_load_dwordx4 v[220:223], %[po], %[rw], %[pb] offen offset:3072 sc1\n\t"
                    OST("%[t1]")
                    "s_waitcnt vmcnt(3)\n\t"
                    OST("%[t3]")
                    "v_smfmac_f32_16x16x64_bf16 v[96:99], v[208:211], v[160:167], %[ix]\n\t"
                    "s_waitcnt vmcnt(2)\n\t"
                    "v_smfmac_f32_16x16x64_bf16 v[96:99], v[212:215], v[168:175], %[ix]\n\t"
                    "s_waitcnt vmcnt(1)\n\t"
                    "v_smfmac_f32_16x16x64_bf16 v[96:99], v[216:219], v[176:183], %[ix]\n\t"
                    "s_waitcnt vmcnt(0)\n\t"
                    "v_smfmac_f32_16x16x64_bf16 v[96:99], v[220:223], v[184:191], %[ix]\n\t"
                    OST("%[t2]")
                    "s_nop 7\n\t"
                    "v_pk_add_f32 v[96:97], v[96:97], v[98:99]\n\t"
                    "ds_write_b64 %[sp], v[96:97]\n\t"
                    "v_cmp_u_f32 vcc, v96, v97\n\t"
                    "s_cbranch_vccz 2f\n\t"
                    "v_max3_u32 v100, v208, v208, v209\n\t"
                    "v_max3_u32 v100, v100, v210, v211\n\t"
                    "v_max3_u32 v100, v100, v212, v213\n\t"
                    "v_max3_u32 v100, v100, v214, v215\n\t"
                    "v_max3_u32 v100, v100, v216, v217\n\t"
                    "v_max3_u32 v100, v100, v218, v219\n\t"
                    "v_max3_u32 v100, v100, v220, v221\n\t"
                    "v_max3_u32 v100, v100, v222, v223\n\t"
                    "v_cmp_eq_u32 vcc, -1, v100\n\t"
                    "s_cbranch_vccz 2f\n\t"
                    "s_sub_i32 %[tr], %[tr], 1\n\t"
                    "s_cmp_lt_i32 %[tr], 1\n\t"
                    "s_cbranch_scc1 2f\n\t"
                    "v_xor_b32 %[po], %[x0], %[po]\n\t"
                    "s_xor_b32 %[x0], %[x0], 0x14000\n\t"
                    "s_waitcnt lgkmcnt(0)\n\t"
                    "v_mov_b64 v[96:97], 0\n\t"
                    "v_mov_b64 v[98:99], 0\n\t"
                    "s_branch 1b\n\t"
                    "2:\n\t"
                    "s_waitcnt lgkmcnt(0)\n\t"
                    "s_barrier\n\t"
                    "s_sleep 3\n\t"
                    : [tr] "+&s"(tries), [po] "+&v"(po), [x0] "+&s"(x0), [t0] "=&s"(t0_), [t1] "=&s"(t1_), [t2] "=&s"(t2_), [t3] "=&s"(t3_), "=&{v[160:167]}"(zf[0]), "=&{v[168:175]}"(zf[1]), "=&{v[176:183]}"(zf[2]), "=&{v[184:191]}"(zf[3])
                    : [zt] "v"(ztoff), [rs] "s"(rws), [so] "s"(zso), [s4] "s"(zso + 4096u), [sp] "v"(spa), [ix] "v"(sidx), [rw] "s"(rwin), [pb] "s"(pb)
                    : "v96", "v97", "v98", "v99", "v100", "v208", "v209", "v210", "v211", "v212", "v213", "v214", "v215", "v216", "v217", "v218", "v219", "v220", "v221", "v222", "v223", "vcc", "scc", "memory");
                else asm volatile("s_nop 4\n\t" ZTL AL
                    "s_barrier\n\t"
                    "buffer_load_dwordx4 v[128:131], %[zo], %[rw], %[zb] offen sc1\n\t"
                    "buffer_load_dwordx4 v[132:135], %[zo], %[rw], %[zb] offen offset:1024 sc1\n\t"
                    "buffer_load_dwordx4 v[136:139], %[zo], %[rw], %[zb] offen offset:2048 sc1\n\t"
                    "buffer_load_dwordx4 v[140:143], %[zo], %[rw], %[zb] offen offset:3072 sc1\n\t"
                    "buffer_load_dwordx4 v[144:147], %[zo], %[rw], %[z4] offen sc1\n\t"
                    "buffer_load_dwordx4 v[148:151], %[zo], %[rw], %[z4] offen offset:1024 sc1\n\t"
                    "buffer_load_dwordx4 v[152:155], %[zo], %[rw], %[z4] offen offset:2048 sc1\n\t"
                    "buffer_load_dwordx4 v[156:159], %[zo], %[rw], %[z4] offen offset:3072 sc1\n\t"
                    OST("%[t0]")
                    "v_mov_b64 v[96:97], 0\n\t"
                    "v_mov_b64 v[98:99], 0\n\t"
                    "s_barrier\n\t"
                    "s_sleep %[fd]\n\t"
                    "buffer_load_dwordx4 v[208:211], %[po], %[rw], %[pb] offen sc1\n\t"
                    "buffer_load_dwordx4 v[212:215], %[po], %[rw], %[pb] offen offset:1024 sc1\n\t"
                    "buffer_load_dwordx4 v[216:219], %[po], %[rw], %[pb] offen offset:2048 sc1\n\t"
                    "buffer_load_dwordx4 v[220:223], %[po], %[rw], %[pb] offen offset:3072 sc1\n\t"
                    "buffer_load_dwordx4 v[224:227], %[pm], %[rw], %[pb] offen sc1\n\t"
                    "buffer_load_dwordx4 v[228:231], %[pm], %[rw], %[pb] offen offset:1024 sc1\n\t"
                    "buffer_load_dwordx4 v[232:235], %[pm], %[rw], %[pb] offen offset:2048 sc1\n\t"
                    "buffer_load_dwordx4 v[236:239], %[pm], %[rw], %[pb] offen offset:3072 sc1\n\t"
                    OST("%[t1]")
                    "s_waitcnt vmcnt(8)\n\t"
                    "v_max3_u32 v101, v129, v131, v133\n\t"
                    "v_max3_u32 v101, v101, v135, v137\n\t"
                    "v_max3_u32 v101, v101, v139, v141\n\t"
                    "v_max3_u32 v101, v101, v143, v145\n\t"
                    "v_max3_u32 v101, v101, v147, v149\n\t"
                    "v_max3_u32 v101, v101, v151, v153\n\t"
                    "v_max3_u32 v101, v101, v155, v157\n\t"
                    "v_max_u32 v101, v101, v159\n\t"
                    "v_cmp_eq_u32 vcc, -1, v101\n\t"
                    "s_cbranch_vccz 3f\n\t"
                    "buffer_load_dwordx4 v[128:131], %[zo], %[rw], %[zb] offen sc1\n\t"
                    "buffer_load_dwordx4 v[132:135], %[zo], %[rw], %[zb] offen offset:1024 sc1\n\t"
                    "buffer_load_dwordx4 v[136:139], %[zo], %[rw], %[zb] offen offset:2048 sc1\n\t"
                    "buffer_load_dwordx4 v[140:143], %[zo], %[rw], %[zb] offen offset:3072 sc1\n\t"
                    "buffer_load_dwordx4 v[144:147], %[zo], %[rw], %[z4] offen sc1\n\t"
                    "buffer_load_dwordx4 v[148:151], %[zo], %[rw], %[z4] offen offset:1024 sc1\n\t"
                    "buffer_load_dwordx4 v[152:155], %[zo], %[rw], %[z4] offen offset:2048 sc1\n\t"
                    "buffer_load_dwordx4 v[156:159], %[zo], %[rw], %[z4] offen offset:3072 sc1\n\t"
                    "s_waitcnt vmcnt(0)\n\t"
                    "v_max3_u32 v101, v129, v131, v133\n\t"
                    "v_max3_u32 v101, v101, v135, v137\n\t"
                    "v_max3_u32 v101, v101, v139, v141\n\t"
                    "v_max3_u32 v101, v101, v143, v145\n\t"
                    "v_max3_u32 v101, v101, v147, v149\n\t"
                    "v_max3_u32 v101, v101, v151, v153\n\t"
                    "v_max3_u32 v101, v101, v155, v157\n\t"
                    "v_max_u32 v101, v101, v159\n\t"
                    "3:\n\t"
                    "v_permlane16_swap_b32 v128, v129\n\t"
                    "v_permlane16_swap_b32 v130, v131\n\t"
                    "v_permlane16_swap_b32 v132, v133\n\t"
                    "v_permlane16_swap_b32 v134, v135\n\t"
                    "v_permlane16_swap_b32 v136, v137\n\t"
                    "v_permlane16_swap_b32 v138, v139\n\t"
                    "v_permlane16_swap_b32 v140, v141\n\t"
                    "v_permlane16_swap_b32 v142, v143\n\t"
                    "v_permlane16_swap_b32 v144, v145\n\t"
                    "v_permlane16_swap_b32 v146, v147\n\t"
                    "v_permlane16_swap_b32 v148, v149\n\t"
                    "v_permlane16_swap_b32 v150, v151\n\t"
                    "v_permlane16_swap_b32 v152, v153\n\t"
                    "v_permlane16_swap_b32 v154, v155\n\t"
                    "v_permlane16_swap_b32 v156, v157\n\t"
                    "v_permlane16_swap_b32 v158, v159\n\t"
                    "s_mov_b32 vcc_lo, 0x00ff00ff\n\t"
                    "s_mov_b32 vcc_hi, 0x00ff00ff\n\t"
                    "s_waitcnt vmcnt(7)\n\t"
                    OST("%[t3]")
                    "v_smfmac_f32_16x16x64_bf16 v[96:99], v[208:211], v[160:167], %[ix]\n\t"
                    "s_waitcnt vmcnt(6)\n\t"
                    "v_smfmac_f32_16x16x64_bf16 v[96:99], v[212:215], v[168:175], %[ix]\n\t"
                    "s_waitcnt vmcnt(5)\n\t"
                    "v_smfmac_f32_16x16x64_bf16 v[96:99], v[216:219], v[176:183], %[ix]\n\t"
                    "s_waitcnt vmcnt(4)\n\t"
                    "v_smfmac_f32_16x16x64_bf16 v[96:99], v[220:223], v[184:191], %[ix]\n\t"
                    OB(4)
                    "s_waitcnt vmcnt(3)\n\t"
                    "v_smfmac_f32_16x16x64_bf16 v[96:99], v[224:227], v[128:135], %[ix]\n\t"
                    OB(5)
                    "s_waitcnt vmcnt(2)\n\t"
                    "v_smfmac_f32_16x16x64_bf16 v[96:99], v[228:231], v[136:143], %[ix]\n\t"
                    OB(6)
                    "s_waitcnt vmcnt(1)\n\t"
                    "v_smfmac_f32_16x16x64_bf16 v[96:99], v[232:235], v[144:151], %[ix]\n\t"
                    "s_waitcnt vmcnt(0)\n\t"
                    "v_smfmac_f32_16x16x64_bf16 v[96:99], v[236:239], v[152:159], %[ix]\n\t"
                    OST("%[t2]")
                    "s_waitcnt lgkmcnt(0)\n\t"
                    "v_pk_add_f32 v[102:103], v[102:103], v[104:105]\n\t"
                    "v_pk_add_f32 v[102:103], v[102:103], v[106:107]\n\t"
                    "s_nop 4\n\t"
                    "6:\n\t"
                    "v_pk_add_f32 v[96:97], v[96:97], v[102:103]\n\t"
                    "v_pk_add_f32 v[96:97], v[96:97], v[98:99]\n\t"
                    "v_cmp_u_f32 %[bad], v96, v97\n\t"
                    "s_nop 0\n\t"
                    "v_cndmask_b32_dpp v98, v97, v96, vcc row_ror:8 row_mask:0xf bank_mask:0xf\n\t"
                    "s_not_b64 vcc, vcc\n\t"
                    "v_cndmask_b32_dpp v99, v96, v97, vcc row_ror:8 row_mask:0xf bank_mask:0xf\n\t"
                    "s_cmp_lg_u64 %[bad], 0\n\t"
                    "v_cvt_pk_bf16_f32 v98, v98, v99\n\t"
                    "s_cbranch_scc1 4f\n\t"
                    "2:\n\t"
                    "buffer_store_dword v98, %[mo], %[rw], %[m0] offen sc1\n\t"
                    "buffer_store_dword v98, %[mo], %[rw], %[m1] offen sc1\n\t"
                    "buffer_store_dword v98, %[mo], %[rw], %[m2] offen sc1\n\t"
                    "buffer_store_dword v98, %[mo], %[rw], %[m3] offen sc1\n\t"
                    OST("%[t4]")
                    "s_branch 5f\n\t"
                    "4:\n\t"
                    "v_min_u32 v98, -2, v98\n\t"
                    "v_max3_u32 v100, v101, v208, v209\n\t"
                    "v_max3_u32 v100, v100, v210, v211\n\t"
                    "v_max3_u32 v100, v100, v212, v213\n\t"
                    "v_max3_u32 v100, v100, v214, v215\n\t"
                    "v_max3_u32 v100, v100, v216, v217\n\t"
                    "v_max3_u32 v100, v100, v218, v219\n\t"
                    "v_max3_u32 v100, v100, v220, v221\n\t"
                    "v_max3_u32 v100, v100, v222, v223\n\t"
                    "v_max3_u32 v100, v100, v224, v225\n\t"
                    "v_max3_u32 v100, v100, v226, v227\n\t"
                    "v_max3_u32 v100, v100, v228, v229\n\t"
                    "v_max3_u32 v100, v100, v230, v231\n\t"
                    "v_max3_u32 v100, v100, v232, v233\n\t"
                    "v_max3_u32 v100, v100, v234, v235\n\t"
                    "v_max3_u32 v100, v100, v236, v237\n\t"
                    "v_max3_u32 v100, v100, v238, v239\n\t"
                    "v_cmp_eq_u32 %[bad], -1, v100\n\t"
                    "s_not_b64 vcc, vcc\n\t"
                    "s_cmp_eq_u64 %[bad], 0\n\t"
                    "s_cbranch_scc1 2b\n\t"
                    "s_sub_i32 %[tr], %[tr], 1\n\t"
                    "s_cmp_lt_i32 %[tr], 1\n\t"
                    "s_cbranch_scc1 2b\n\t"
                    "v_xor_b32 %[po], %[x0], %[po]\n\t"
                    "s_xor_b32 %[x0], %[x0], 0x14000\n\t"
                    "v_xor_b32 %[pm], %[x1], %[pm]\n\t"
                    "s_xor_b32 %[x1], %[x1], 0x5000\n\t"
                    "buffer_load_dwordx4 v[128:131], %[zo], %[rw], %[zb] offen sc1\n\t"
                    "buffer_load_dwordx4 v[132:135], %[zo], %[rw], %[zb] offen offset:1024 sc1\n\t"
                    "buffer_load_dwordx4 v[136:139], %[zo], %[rw], %[zb] offen offset:2048 sc1\n\t"
                    "buffer_load_dwordx4 v[140:143], %[zo], %[rw], %[zb] offen offset:3072 sc1\n\t"
                    "buffer_load_dwordx4 v[144:147], %[zo], %[rw], %[z4] offen sc1\n\t"
                    "buffer_load_dwordx4 v[148:151], %[zo], %[rw], %[z4] offen offset:1024 sc1\n\t"
                    "buffer_load_dwordx4 v[152:155], %[zo], %[rw], %[z4] offen offset:2048 sc1\n\t"
                    "buffer_load_dwordx4 v[156:159], %[zo], %[rw], %[z4] offen offset:3072 sc1\n\t"
                    "buffer_load_dwordx4 v[208:211], %[po], %[rw], %[pb] offen sc1\n\t"
                    "buffer_load_dwordx4 v[212:215], %[po], %[rw], %[pb] offen offset:1024 sc1\n\t"
                    "buffer_load_dwordx4 v[216:219], %[po], %[rw], %[pb] offen offset:2048 sc1\n\t"
                    "buffer_load_dwordx4 v[220:223], %[po], %[rw], %[pb] offen offset:3072 sc1\n\t"
                    "buffer_load_dwordx4 v[224:227], %[pm], %[rw], %[pb] offen sc1\n\t"
                    "buffer_load_dwordx4 v[228:231], %[pm], %[rw], %[pb] offen offset:1024 sc1\n\t"
                    "buffer_load_dwordx4 v[232:235], %[pm], %[rw], %[pb] offen offset:2048 sc1\n\t"
                    "buffer_load_dwordx4 v[236:239], %[pm], %[rw], %[pb] offen offset:3072 sc1\n\t"
                    "v_mov_b64 v[96:97], 0\n\t"
                    "v_mov_b64 v[98:99], 0\n\t"
                    "s_waitcnt vmcnt(0)\n\t"
                    "v_max3_u32 v101, v129, v131, v133\n\t"
                    "v_max3_u32 v101, v101, v135, v137\n\t"
                    "v_max3_u32 v101, v101, v139, v141\n\t"
                    "v_max3_u32 v101, v101, v143, v145\n\t"
                    "v_max3_u32 v101, v101, v147, v149\n\t"
                    "v_max3_u32 v101, v101, v151, v153\n\t"
                    "v_max3_u32 v101, v101, v155, v157\n\t"
                    "v_max_u32 v101, v101, v159\n\t"
                    "v_permlane16_swap_b32 v128, v129\n\t"
                    "v_permlane16_swap_b32 v130, v131\n\t"
                    "v_permlane16_swap_b32 v132, v133\n\t"
                    "v_permlane16_swap_b32 v134, v135\n\t"
                    "v_permlane16_swap_b32 v136, v137\n\t"
                    "v_permlane16_swap_b32 v138, v139\n\t"
                    "v_permlane16_swap_b32 v140, v141\n\t"
                    "v_permlane16_swap_b32 v142, v143\n\t"
                    "v_permlane16_swap_b32 v144, v145\n\t"
                    "v_permlane16_swap_b32 v146, v147\n\t"
                    "v_permlane16_swap_b32 v148, v149\n\t"
                    "v_permlane16_swap_b32 v150, v151\n\t"
                    "v_permlane16_swap_b32 v152, v153\n\t"
                    "v_permlane16_swap_b32 v154, v155\n\t"
                    "v_permlane16_swap_b32 v156, v157\n\t"
                    "v_permlane16_swap_b32 v158, v159\n\t"
                    "s_nop 1\n\t"
                    "v_smfmac_f32_16x16x64_bf16 v[96:99], v[208:211], v[160:167], %[ix]\n\t"
                    "v_smfmac_f32_16x16x64_bf16 v[96:99], v[212:215], v[168:175], %[ix]\n\t"
                    "v_smfmac_f32_16x16x64_bf16 v[96:99], v[216:219], v[176:183], %[ix]\n\t"
                    "v_smfmac_f32_16x16x64_bf16 v[96:99], v[220:223], v[184:191], %[ix]\n\t"
                    "v_smfmac_f32_16x16x64_bf16 v[96:99], v[224:227], v[128:135], %[ix]\n\t"
                    "v_smfmac_f32_16x16x64_bf16 v[96:99], v[228:231], v[136:143], %[ix]\n\t"
                    "v_smfmac_f32_16x16x64_bf16 v[96:99], v[232:235], v[144:151], %[ix]\n\t"
                    "v_smfmac_f32_16x16x64_bf16 v[96:99], v[236:239], v[152:159], %[ix]\n\t"
                    "s_nop 7\n\t"
                    "s_branch 6b\n\t"
                    "5:\n\t"
                    : [tr] "+&s"(tries), [po] "+&v"(po), [pm] "+&v"(pm), [x0] "+&s"(x0), [x1] "+&s"(x1), [bad] "=&s"(bad), [t0] "=&s"(t0_), [t1] "=&s"(t1_), [t2] "=&s"(t2_), [t3] "=&s"(t3_), [t4] "=&s"(t4_), "=&{v[160:167]}"(zf[0]), "=&{v[168:175]}"(zf[1]), "=&{v[176:183]}"(zf[2]), "=&{v[184:191]}"(zf[3])
                    : [zt] "v"(ztoff), [rs] "s"(rws), [so] "s"(zso), [s4] "s"(zso + 4096u), [zo] "v"(zoff), [sp] "v"(spa), [ix] "v"(sidx), [rw] "s"(rwin), [zb] "s"(zb), [z4] "s"(zb + 4096u), [pb] "s"(pb), [mo] "v"(moff), [fd] "n"(FDL),
                      [m0] "s"(mb), [m1] "s"(mb + PAIR), [m2] "s"(mb + mc2), [m3] "s"(mb + mc2 + PAIR)
                    : "v96", "v97", "v98", "v99", "v100", "v101", "v102", "v103", "v104", "v105", "v106", "v107", "v128", "v129", "v130", "v131", "v132", "v133", "v134", "v135", "v136", "v137", "v138", "v139", "v140", "v141", "v142", "v143", "v144", "v145", "v146", "v147", "v148", "v149", "v150", "v151", "v152", "v153", "v154", "v155", "v156", "v157", "v158", "v159", "v208", "v209", "v210", "v211", "v212", "v213", "v214", "v215", "v216", "v217", "v218", "v219", "v220", "v221", "v222", "v223", "v224", "v225", "v226", "v227", "v228", "v229", "v230", "v231", "v232", "v233", "v234", "v235", "v236", "v237", "v238", "v239", "vcc", "scc", "memory");
                budget = tries;
                if (w == 3) *(volatile LAS int*)(smem + 4100) = s;
                __builtin_amdgcn_s_sleep(DPS - 4);
                if (w == 3) {
                    st128(sent4(), rwin, ra, W_MEM + (unsigned)(s & 3) * 6u * PAIR, 16);
                    if (lane == 0) __builtin_amdgcn_raw_buffer_store_b64((u32x2){(unsigned)s - 1u, (unsigned)max(zr, s)}, rwin, 0u, W_GO + fb + (unsigned)o * 128u, 16);
                }
                sent(s);
                zb = zb == W_ZM + 2u * NOWN * PAIR ? W_ZM : zb + NOWN * PAIR;
            }
        } else if (w == 4) {
            const unsigned fo = (unsigned)lane * 128u + (unsigned)(o / 10) * PAIR + nh;
            int zk = -1;
            if (lane < 3) *(volatile LAS int*)(smem + 4096 + lane * 4) = -1;
            for (int s = 0; s + 1 < NSEG; ++s) {
                asm volatile("s_waitcnt lgkmcnt(0)\n\ts_barrier\n\ts_barrier" ::: "memory");
                int f = 0;
                const bool look = zk < min(s + ZLA, NSEG - 1);
                if (look) { __builtin_amdgcn_s_sleep(6); f = (int)__builtin_amdgcn_raw_buffer_load_b32(rwin, fo, W_FZ + fb, 16); }
                SB();
                BAR0();
                if (look) for (int again = 0;; ++again) {
                    f = min(f, __builtin_amdgcn_update_dpp(f, f, 0xB1, 0xF, 0xF, false)); f = min(f, __builtin_amdgcn_update_dpp(f, f, 0x4E, 0xF, 0xF, false));
                    f = min(f, __builtin_amdgcn_update_dpp(f, f, 0x141, 0xF, 0xF, false)); f = min(f, __builtin_amdgcn_update_dpp(f, f, 0x140, 0xF, 0xF, false));
                    zk = seg_done(MXV, min(__builtin_amdgcn_readlane(f, 0), __builtin_amdgcn_readlane(f, 16)), min(__builtin_amdgcn_readlane(f, 32), __builtin_amdgcn_readlane(f, 48)));
                    *(volatile LAS int*)(smem + 4096) = zk;
                    if (zk > s || again) break;
                    __builtin_amdgcn_s_sleep(2);
                    f = (int)__builtin_amdgcn_raw_buffer_load_b32(rwin, fo, W_FZ + fb, 16);
                }
                *(volatile LAS int*)(smem + 4104) = s;
            }
        } else if (w < 7) {
            for (int s = 0; s < 3 * NSEG - 3; ++s) BAR0();
        } else {
            Tk tk = tk_init();
            __builtin_amdgcn_s_setprio(3);
            for (int s = -1; s + 1 < NSEG; ++s) {
                if (s >= 0) {
                    const unsigned ta0 = ck.ta, mm0 = ck.ma, tp = s ? tpp + lt + xs + M1 : tpp - M2 - XS0 - (lay ? PEN0 : PEN00);
                    wait_fine_bar(tp - DZP, ck);
                    wait_cyc_bar(cyc_at(ck, sx24(tp - ck.ta)));
                    clk_rate(ck, ta0, mm0);
                    asm volatile("s_barrier ; 7" ::: "memory");
                }
                if (s) sched(tk, max(s, 0));
                if (s < 0) continue;
                RTS();
            }
            __builtin_amdgcn_s_setprio(0);
            if (o < 2) due(E_OUT + o, (sx24(tpp + ML - 4u * (unsigned)__builtin_amdgcn_s_memrealtime()) >> 2) + DOUT);
        }
        {
            const int e = w >> 2;
            const unsigned ooff = (unsigned)((S_ - 2 * SEG + 8 * w + (lane >> 3)) * D_ + o * 16 + (lane & 7) * 2) * 2u;
            unsigned pl = W_PL + fb + e * 5u * PAIR, zs = W_ZM + (NSEG - 2 + e + lay * NSEG) % 3 * (NOWN * PAIR);
            asm volatile("" : "+s"(pl), "+s"(zs));
            if (e) {
                if (w < 7) __builtin_amdgcn_s_sleep(DPS);
                sent(NSEG - 2);
                zt_wait(NSEG - 1);
                const unsigned zl = O_Z + (unsigned)((NSEG - 1) * ZTSEG) * 2u;
#pragma unroll
                for (int q = 0; q < 4; ++q) zf[q] = cat8(__builtin_amdgcn_raw_buffer_load_b128(rws, ztoff + q * 2048u, zl, 16), __builtin_amdgcn_raw_buffer_load_b128(rws, ztoff + q * 2048u + 1024u, zl, 16));
            }
            if (DR0 && !e) __builtin_amdgcn_s_sleep(DR0);
            const unsigned xr = __builtin_amdgcn_raw_buffer_load_b32(rws, ooff, O_X1, 16);
            SB();
            if (w == 7) wait_fine_bar(tpp + ML, ck); else if (e) BAR0();
            f32x4 c[2];
            for (unsigned spins = 0;;) {
                asm volatile("" ::: "memory");
                u32x4 pt[16], ptm[2][2], zm[2];
#pragma unroll
                for (int q = 0; q < 16; ++q) pt[q] = __builtin_amdgcn_raw_buffer_load_b128(rwin, (unsigned)(16 * hg) * 1024u + lane * 16u + (q & 3) * 1024u, pl + (q >> 2) * 4096u, 16);
#pragma unroll
                for (int q = 0; q < 2; ++q) zm[q] = __builtin_amdgcn_raw_buffer_load_b128(rwin, zoff + hg * 2048u + q * 1024u, zs, 16);
#pragma unroll
                for (int q = 0; q < 4; ++q) ptm[q >> 1][q & 1] = __builtin_amdgcn_raw_buffer_load_b128(rwin, (unsigned)(hg * 2048 + lane * 16) + (q & 1) * 1024u, pl + 65536u + (q >> 1) * 8192u, 16);
                SB();
                if (!e && !spins) BAR0();
                unsigned m = max(smax(zm[0]), smax(zm[1]));
                #pragma unroll
                for (int q = 0; q < 16; ++q) { c[q & 1] = mfma32(half8(zf[q >> 2], q >> 1 & 1), pt[q], q < 2 ? z4 : c[q & 1]); m = max(m, smax(pt[q])); }
#pragma unroll
                for (int q = 0; q < 4; ++q) { c[q >> 1] = mfma32((u32x4){zm[q & 1][0], zm[q & 1][2], zm[q & 1][1], zm[q & 1][3]}, ptm[q >> 1][q & 1], c[q >> 1]); m = max(m, smax(ptm[q >> 1][q & 1])); }
                if (!__any(m == 0xFFFFFFFFu)) break;
                if (spin_fail(spins, rws)) break;
            }
#pragma unroll
            for (int t = 0; t < 2; ++t)
#pragma unroll
                for (int r = 0; r < 4; ++r) *(float*)(op + w * 2048 + (16 * t + fr) * 64 + pi16(4 * fq + r) * 4) = c[t][r];
            BAR();
            const char* src = op + e * 8192 + hg * 512 + lane * 8;
            const f32x2 y = (*(const f32x2*)src + *(const f32x2*)(src + 2048)) + (*(const f32x2*)(src + 4096) + *(const f32x2*)(src + 6144));
            __builtin_amdgcn_raw_buffer_store_b32(nosent(pack2(lo2f(xr) + bfr(y[0]), hi2f(xr) + bfr(y[1]))), rout, ooff, 0u, 16);
            __builtin_amdgcn_s_sleep(4);
            const u32x4 sv = sent4();
            st128(sv, rwin, zoff + hg * 2048u, zs, 16); st128(sv, rwin, zoff + hg * 2048u + 1024u, zs, 16);
            if (w == 3) st128(sv, rwin, ra, W_MEM + 18u * PAIR, 16);
            if (lay && o == 0 && w == 0) __builtin_amdgcn_raw_buffer_store_b64(get_tag(), rws, 0u, 8u, 16);
        }
        SST(23);
    } else if (bid < NREC + 30) {
        LANES();
        const int cg = (bid - NREC) % 10;
        const unsigned ztoff = (unsigned)((4 * cg * H_ + 4 * w) * 64 + lane) * 16u;
        const unsigned hoff4 = fq * HS + fr * 16u;
        const int tk = (w & 3) * 8 + (lane >> 3);
        const unsigned ooff = (unsigned)(tk * D_ + cg * 64 + (lane & 7) * 8) * 2u;
        const unsigned gooff = W_GO + (unsigned)((4 * cg + (lane & 2 ? 3 : 0)) * 128 + (lane & 1) * 4);
        int rdy = -1, go = -1;
        auto ask = [&]() {
            asm volatile("" ::: "memory");
            return (int)__builtin_amdgcn_raw_buffer_load_b32(rwin, gooff, fb, 16);
        };
        auto look = [&](int f) {
            go = min(__builtin_amdgcn_readlane(f, 33), __builtin_amdgcn_readlane(f, 35));
            rdy = max(rdy, max(__builtin_amdgcn_readlane(f, 32), __builtin_amdgcn_readlane(f, 34)));
        };
        auto wait_rdy = [&](int& x, int v, int s) {
            for (unsigned spins = 0; x < v;) {
                look(ask());
                if (x >= v) break;
                if (spin_fail(spins, rws)) break;
                if (s >= NSEG - 5) __builtin_amdgcn_s_sleep(OSLP2); else __builtin_amdgcn_s_sleep(OSLP);
            }
        };
        if (w >= 4) wait_h(rws, rwin, fb, 3, hrdy);
        u32x4 zf[4][4];
        auto load_z = [&](int s) {
#pragma unroll
            for (int q = 0; q < 16; ++q) { zf[q >> 2][q & 3] = __builtin_amdgcn_raw_buffer_load_b128(rws, ztoff + (q & 3) * 1024u, O_Z + (unsigned)(s * ZTSEG) * 2u + (q >> 2) * (H_ * 1024u), 16); }
            SB();
        };
        wait_rdy(go, (bid - NREC) / 10, 0);
        load_z((bid - NREC) / 10);
        for (int s = (bid - NREC) / 10;; s += 3) {
            u32x4 xr, zx[4], pt[8], ptm[2];
            asm volatile("" : "=v"(xr));
            RT(0);
            wait_rdy(rdy, s, s);
            RT(1);
            if (s >= NSEG - 8 && cg == 0 && w == 0) due(E_OUT + NSEG - 1 - s, s + 3 < NSEG ? DOU3 : DOU2);
            const unsigned ptb = O_PT + (unsigned)(4 * w * HS + s * 2560);
#pragma unroll
            for (int q = 0; q < 8; ++q) pt[q] = __builtin_amdgcn_raw_buffer_load_b128(rws, lane * 16u + (q & 1) * 1024u, ptb + (q >> 1) * HS, 16);
#pragma unroll
            for (int q = 0; q < 2; ++q) ptm[q] = __builtin_amdgcn_raw_buffer_load_b128(rws, hoff4 + 2048u + q * 256u, ptb, 16);
#pragma unroll
            for (int q = 0; q < 4; ++q) zx[q] = __builtin_amdgcn_raw_buffer_load_b128(rws, hoff4 + q * 256u, O_Z2 + (unsigned)(4 * w * HS + s * 10240 + cg * 1024), 16);
            if (w >= 4) xr = __builtin_amdgcn_raw_buffer_load_b128(rws, ooff, O_X1 + (unsigned)(s * SEG * D_ * 2), 16);
            const int f = ask();
            SB();
            char* op = smem + (s & 1) * 65536;
            f32x4 c[8];
#pragma unroll
            for (int q = 0; q < 4; ++q)
#pragma unroll
                for (int u = 0; u < 8; ++u) c[u] = mfma32(zf[u >> 1][q], pt[2 * q + (u & 1)], q ? c[u] : z4);
#pragma unroll
            for (int u = 0; u < 8; ++u) {
                const int tq = u & 1, t = u >> 1;
                *(f32x4*)(op + w * 8192 + (16 * tq + fr) * 256 + (((t * 4 + fq) ^ (fr & 7)) << 4)) = mfma32((u32x4){zx[t][0], zx[t][2], zx[t][1], zx[t][3]}, ptm[tq], c[u]);
            }
            look(f);
            const bool pre = go >= s + 3 && s < NSEG - 14;
            if (pre) load_z(s + 3);
            BAR();
            if (lay == 0 && s >= 3 && s < NSEG - 11 && tid == 0) __builtin_amdgcn_raw_buffer_store_b32(0u, rwin, 0u, W_OUT + (unsigned)((s - 3) * 128 + cg * 4), 16);
            if (w >= 4) {
                f32x2 x[4];
#pragma unroll
                for (int q = 0; q < 4; ++q) {
                    const char* sp = op + tk * 256 + ((((lane & 6) * 2 + q) ^ (tk & 7)) << 4) + (lane & 1) * 8;
                    x[q] = *(const f32x2*)sp;
#pragma unroll
                    for (int ww = 1; ww < 8; ++ww) x[q] += *(const f32x2*)(sp + ww * 8192);
                }
                const u32x4 r = {pack2(lo2f(xr[0]) + bfr(x[0][0]), hi2f(xr[0]) + bfr(x[2][0])), pack2(lo2f(xr[1]) + bfr(x[1][0]), hi2f(xr[1]) + bfr(x[3][0])),
                                 pack2(lo2f(xr[2]) + bfr(x[0][1]), hi2f(xr[2]) + bfr(x[2][1])), pack2(lo2f(xr[3]) + bfr(x[1][1]), hi2f(xr[3]) + bfr(x[3][1]))};
                st128(((u32x4){nosent(r[0]), nosent(r[1]), nosent(r[2]), nosent(r[3])}), rout, ooff, (unsigned)(s * SEG * D_ * 2), 16);
            } else if (!pre) __builtin_amdgcn_s_sleep(ODL);
            if (lay == 0 && s >= NSEG - 14 && s < NSEG - 8) {
                VMWAIT();
                BAR0();
                if (tid == 0) __builtin_amdgcn_raw_buffer_store_b32(0u, rwin, 0u, W_OUT + (unsigned)(s * 128 + cg * 4), 16);
            }
            RT(2);
            RTS();
            if (s < 8) SST(8 + s);
            if (s >= 56) SST(s - 40);
            if (s + 3 >= NSEG - 2) break;
            if (!pre) { wait_rdy(go, s + 3, 0); load_z(s + 3); }
        }
    } else if (lay == 0 && bid < NWRK) {
        if (w == 0) for (unsigned spins = 0; (int)__builtin_amdgcn_raw_buffer_load_b32(rwin, 0u, W_E + (E_OUT + 3) * 128u, 16) == -1 && !spin_fail(spins, rws);) {
            __builtin_amdgcn_s_sleep(64);
            asm volatile("" ::: "memory");
        }
    } else if (lay == 0) {
        LANES();
        const RowTw tw = row_tw(lane);
        for (int u = (256 - bid) & 15; u < 256; u += 16) {
            if (w == 0) {
                if (u >= 224) await(E_OUT + (255 - u >> 2), 1, 0);
                else for (unsigned spins = 0;;) {
                    asm volatile("" ::: "memory");
                    const unsigned f = __builtin_amdgcn_raw_buffer_load_b32(rwin, (unsigned)min(lane, 9) * 4u, W_OUT + (unsigned)(u >> 2) * 128u, 16);
                    if (!__any(f != 0u)) break;
                    if (spin_fail(spins, rws)) break;
                    __builtin_amdgcn_s_sleep(WSLP);
                }
            }
            BAR0();
            SST(24);
            const unsigned fo = W_FA + (unsigned)((u >> 2) * 128 + (u & 3) * 4);
            a1_unit<4, 512>(rout, u * 8, u >= 224, rws, smem, tw, w, lane, [&](int k) {
                if (k == 0 && w == 0 && u >= 224 && !(u & 3)) due(E_A1 + (u - 224 >> 2), DA1);
                if (k >= 0) SST(8 + k);
            });
            SST(25);
            if (u >= 224) continue;
            VMWAIT();
            BAR0();
            if (tid == 0) __builtin_amdgcn_raw_buffer_store_b32(0u, rwin, 0u, fo, 16);
        }
    }
    if (lay == 0 && w == 0 && bid >= NREC && bid <= NWRK) await(E_OUT + 1, 1, bid < NREC + 32 ? -DOW : -60);
    SST(31);
    }
}

constexpr int L_DBG = 0;

extern "C" void kernel_launch(void* const* d_in, const int* in_sizes, int n_in, void* d_out, int out_size, void* d_ws, size_t ws_size,
                              hipStream_t stream) {
    char* ws = (char*)(((uintptr_t)d_ws + 0x1FFFFFu) & ~(uintptr_t)0x1FFFFFu);
    hipFuncSetAttribute((const void*)b3_kernel, hipFuncAttributeMaxDynamicSharedMemorySize, L_END + L_DBG);
    char* win = ws + OFF_WIN;
    RecArgs ra{(unsigned*)ws, win, (const u16*)d_in[4], (const u16*)d_in[0], (u16*)d_out, {(const u16*)d_in[5], (const u16*)d_in[6], (const u16*)d_in[7], (const u16*)d_in[8], (const u16*)d_in[1], (const u16*)d_in[2]}};
    b3_kernel<<<256, 512, L_END + L_DBG, stream>>>(ra);
}
```
